```python
import torch

def _softmax(x, axis=-1):
    return torch.softmax(x, dim=axis)
def _gelu(x):
    return torch.nn.functional.gelu(x, approximate="tanh")
def _silu(x):
    return torch.nn.functional.silu(x)
def _sigmoid(x):
    return torch.sigmoid(x)
def _layernorm(x, axis=-1, eps=1e-5):
    m = x.mean(dim=axis, keepdim=True)
    v = x.var(dim=axis, keepdim=True, unbiased=False)
    return (x - m) / torch.sqrt(v + eps)
def _scatter_add(tgt, idx, src, axis=0):
    return tgt.clone().index_add_(axis, idx, src)
def _segment_sum(src, seg, n):
    out = torch.zeros((n,) + src.shape[1:], dtype=src.dtype)
    return out.index_add_(0, seg, src)
def _pad(x, pw):
    flat = tuple(v for pair in reversed(pw) for v in pair)
    return torch.nn.functional.pad(x, flat)

kernel_name = 'concat_heads__mha_block_cached__mha_block_cached__near_ridge'

def setup_inputs():

    _w = (lambda a: (lambda t: t.to(torch.bfloat16) if t.is_floating_point() else t)(torch.as_tensor(a)))
    def _A_setup():
        B, Sq, D = 8, 288, 448
        H, Sk, Dh = 16, 1024, 256
        return {
            "x": _w((lambda *s: torch.randn(*s, dtype=torch.bfloat16) * 0.8)(B, Sq, D)),
            "k_cache": _w((lambda *s: torch.randn(*s, dtype=torch.bfloat16) * 0.8)(B, H, Sk, Dh)),
            "v_cache": _w((lambda *s: torch.randn(*s, dtype=torch.bfloat16) * 0.05)(B, H, Sk, Dh)),
            "wq": _w((lambda *s: torch.randn(*s, dtype=torch.bfloat16) * 0.8)(D, H * Dh) * (D ** -0.5)),
            "wo": _w((lambda *s: torch.randn(*s, dtype=torch.bfloat16) * 0.05)(H * Dh, D) * ((H * Dh) ** -0.5)),
        }
    _A_in = _A_setup()

    def _B_setup():
        B, Sq, D = 8, 288, 448
        H, Sk, Dh = 16, 1024, 256
        return {
            "x": _w((lambda *s: torch.randn(*s, dtype=torch.bfloat16) * 0.8)(B, Sq, D)),
            "k_cache": _w((lambda *s: torch.randn(*s, dtype=torch.bfloat16) * 0.8)(B, H, Sk, Dh)),
            "v_cache": _w((lambda *s: torch.randn(*s, dtype=torch.bfloat16) * 0.05)(B, H, Sk, Dh)),
            "wq": _w((lambda *s: torch.randn(*s, dtype=torch.bfloat16) * 0.8)(D, H * Dh) * (D ** -0.5)),
            "wo": _w((lambda *s: torch.randn(*s, dtype=torch.bfloat16) * 0.05)(H * Dh, D) * ((H * Dh) ** -0.5)),
        }
    _B_in = _B_setup()

    return {
        "A_x": _A_in["x"],
        "A_k_cache": _A_in["k_cache"],
        "A_v_cache": _A_in["v_cache"],
        "A_wq": _A_in["wq"],
        "A_wo": _A_in["wo"],
        "B_x": _B_in["x"],
        "B_k_cache": _B_in["k_cache"],
        "B_v_cache": _B_in["v_cache"],
        "B_wq": _B_in["wq"],
        "B_wo": _B_in["wo"],
    }

def reference(A_x, A_k_cache, A_v_cache, A_wq, A_wo, B_x, B_k_cache, B_v_cache, B_wq, B_wo):
    def _A_ref(x, k_cache, v_cache, wq, wo):
        B, Sq, D = x.shape
        H, Dh = 16, 256
        q = (lambda x, *d: x.permute(*d))((x @ wq).reshape(B, Sq, H, Dh), 0, 2, 1, 3)
        scores = torch.einsum("bhqd,bhkd->bhqk", q, k_cache) * (Dh ** -0.5)
        p = _softmax(scores, axis=-1)
        o = torch.einsum("bhqk,bhkd->bhqd", p, v_cache)
        return (lambda x, *d: x.permute(*d))(o, 0, 2, 1, 3).reshape(B, Sq, H * Dh) @ wo

    def _B_ref(x, k_cache, v_cache, wq, wo):
        B, Sq, D = x.shape
        H, Dh = 16, 256
        q = (lambda x, *d: x.permute(*d))((x @ wq).reshape(B, Sq, H, Dh), 0, 2, 1, 3)
        scores = torch.einsum("bhqd,bhkd->bhqk", q, k_cache) * (Dh ** -0.5)
        p = _softmax(scores, axis=-1)
        o = torch.einsum("bhqk,bhkd->bhqd", p, v_cache)
        return (lambda x, *d: x.permute(*d))(o, 0, 2, 1, 3).reshape(B, Sq, H * Dh) @ wo

    a = _A_ref(A_x, A_k_cache, A_v_cache, A_wq, A_wo)
    b = _B_ref(B_x, B_k_cache, B_v_cache, B_wq, B_wo)
    return torch.concatenate([a, b], axis=-1)
```

```cpp
#include <hip/hip_runtime.h>
#include <hip/hip_bf16.h>
typedef unsigned short u16;
__device__ __forceinline__ float bf2f(u16 x){ return __uint_as_float(((unsigned)x)<<16); }
__device__ __forceinline__ u16 f2bf(float f){ unsigned u=__float_as_uint(f); u += 0x7fffu + ((u>>16)&1u); return (u16)(u>>16); }
constexpr int B=8,SQ=288,D=448,H=16,SK=1024,DH=256;
__global__ void qproj(const u16* x,const u16* wq,u16* q){
  int row=blockIdx.x;
  __shared__ float xs[D];
  for(int i=threadIdx.x;i<D;i+=256) xs[i]=bf2f(x[row*D+i]);
  __syncthreads();
  for(int n=threadIdx.x;n<H*DH;n+=256){ float a=0; for(int k=0;k<D;k++) a+=xs[k]*bf2f(wq[(size_t)k*H*DH+n]); q[(size_t)row*H*DH+n]=f2bf(a);}
}
__global__ void attn(const u16* q,const u16* kc,const u16* vc,u16* o){
  int s=blockIdx.x, h=blockIdx.y, b=blockIdx.z;
  __shared__ float qs[DH]; __shared__ float p[SK]; __shared__ float red[256];
  int t=threadIdx.x;
  qs[t]=bf2f(q[((size_t)(b*SQ+s))*H*DH+h*DH+t]);
  __syncthreads();
  const u16* K=kc+((size_t)(b*H+h))*SK*DH; const u16* V=vc+((size_t)(b*H+h))*SK*DH;
  float mx=-1e30f;
  for(int k=t;k<SK;k+=256){ float a=0; for(int d=0;d<DH;d++) a+=qs[d]*bf2f(K[(size_t)k*DH+d]); a*=0.0625f; p[k]=a; mx=fmaxf(mx,a);}
  red[t]=mx; __syncthreads();
  for(int o2=128;o2>0;o2>>=1){ if(t<o2) red[t]=fmaxf(red[t],red[t+o2]); __syncthreads(); }
  mx=red[0]; __syncthreads();
  float sm=0; for(int k=t;k<SK;k+=256){ float e=__expf(p[k]-mx); p[k]=e; sm+=e; }
  red[t]=sm; __syncthreads();
  for(int o2=128;o2>0;o2>>=1){ if(t<o2) red[t]+=red[t+o2]; __syncthreads(); }
  sm=red[0];
  float a=0; for(int k=0;k<SK;k++) a+=p[k]*bf2f(V[(size_t)k*DH+t]);
  o[((size_t)(b*SQ+s))*H*DH+h*DH+t]=f2bf(a/sm);
}
__global__ void oproj(const u16* o,const u16* wo,u16* out,int br){
  int row=blockIdx.x;
  __shared__ float os[H*DH];
  for(int i=threadIdx.x;i<H*DH;i+=256) os[i]=bf2f(o[(size_t)row*H*DH+i]);
  __syncthreads();
  for(int n=threadIdx.x;n<D;n+=256){ float a=0; for(int k=0;k<H*DH;k++) a+=os[k]*bf2f(wo[(size_t)k*D+n]); out[(size_t)row*2*D+br*D+n]=f2bf(a);}
}
extern "C" void kernel_launch(void* const* d_in, const int* in_sizes, int n_in, void* d_out, int out_size, void* d_ws, size_t ws_size, hipStream_t stream) {
  for(int br=0;br<2;br++){
    const u16* x=(const u16*)d_in[br*5+0]; const u16* k=(const u16*)d_in[br*5+1]; const u16* v=(const u16*)d_in[br*5+2];
    const u16* wq=(const u16*)d_in[br*5+3]; const u16* wo=(const u16*)d_in[br*5+4];
    u16* q=(u16*)d_ws + (size_t)br*2*B*SQ*H*DH; u16* o=q+(size_t)B*SQ*H*DH;
    qproj<<<B*SQ,256,0,stream>>>(x,wq,q);
    attn<<<dim3(SQ,H,B),256,0,stream>>>(q,k,v,o);
    oproj<<<B*SQ,256,0,stream>>>(o,wo,(u16*)d_out,br);
  }
}
```

```cpp
#include <hip/hip_runtime.h>
#include <hip/hip_bf16.h>

typedef unsigned short u16;
typedef unsigned u32x4 __attribute__((ext_vector_type(4)));
typedef unsigned u32x2 __attribute__((ext_vector_type(2)));

__global__ __launch_bounds__(256, 1) __attribute__((aligned(4096))) void mha_fused(const char* xA, const char* kA, const char* vA, const char* wqA, const char* woA,
                                                    const char* xB, const char* kB, const char* vB, const char* wqB, const char* woB,
                                                    char* ws) {
  extern __shared__ __attribute__((aligned(16))) char lds[];
  const unsigned bid = blockIdx.x;
  const unsigned xcd = bid & 7u, idx = bid >> 3;
  const unsigned br = xcd >> 2, b = idx >> 2, h = (xcd & 3u) * 4u + (idx & 3u);
  const char* x = (br ? xB : xA) + (size_t)b * (288 * 448 * 2);
  const char* wq = (br ? wqB : wqA) + (size_t)h * 512;
  const char* k = (br ? kB : kA) + (size_t)(b * 16u + h) * (1024 * 512);
  const char* v = (br ? vB : vA) + (size_t)(b * 16u + h) * (1024 * 512);
  const char* wo = (br ? woB : woA) + (size_t)(h & ~1u) * (256 * 896);
  char* po = ws + (size_t)((br * 8u + b) * 16u + (h & ~1u)) * 4096;
  const unsigned w = __builtin_amdgcn_readfirstlane(threadIdx.x >> 6);
  const unsigned wqrem = 448u * 4096u * 2u - h * 512u;
  asm volatile(
"s_mov_b64 s[44:45], %1\n"
"s_and_b32 s45, s45, 0xffff\n"
"s_mov_b32 s46, %7\n"
"s_mov_b32 s47, 0x20000\n"
"s_mov_b64 s[40:41], %0\n"
"s_and_b32 s41, s41, 0xffff\n"
"s_mov_b32 s42, 0x3f000\n"
"s_mov_b32 s43, 0x20000\n"
"s_mov_b32 s64, %6\n"
"s_lshl_b32 s85, s64, 12\n"
"s_lshl_b32 s90, s64, 13\n"
"s_and_b32 s71, s64, 1\n"
"s_lshr_b32 s72, s64, 1\n"
"s_lshl_b32 s73, s71, 5\n"
"s_lshl_b32 s74, s72, 7\n"
"s_add_u32 s69, s85, s73\n"
"s_add_u32 s69, s69, s74\n"
"s_lshl_b32 s86, s71, 8\n"
"s_bfe_u32 s24, %7, 0x10009\n"
"s_mul_i32 s25, s24, 0x7000\n"
"s_lshl_b32 s73, s24, 1\n"
"s_xor_b32 s73, s73, s64\n"
"s_mul_i32 s83, s73, 0x1c00\n"
"s_add_u32 s83, s83, 0x38000\n"
"s_mul_i32 s73, s64, 0xe000\n"
"s_sub_u32 s83, s83, s73\n"
"s_lshl_b32 s84, s64, 10\n"
"s_add_u32 s84, s84, 0x8000\n"
"s_lshl_b32 s79, %8, 1\n"
"s_lshr_b32 s78, %9, 2\n"
"s_add_u32 s79, s79, s78\n"
"s_and_b32 s78, %9, 3\n"
"s_lshl_b32 s78, s78, 1\n"
"s_add_u32 s79, s79, s78\n"
"s_and_b32 s79, s79, 1\n"
"s_mul_i32 s79, s79, 3\n"
"s_sub_u32 s78, s79, 7\n"
"s_cselect_b32 s79, s79, s78\n"
"s_lshl_b32 s78, s79, 19\n"
"s_lshl_b32 s79, s79, 7\n"
"s_add_u32 s80, s79, s25\n"
"s_sub_u32 s81, s79, s25\n"
"s_add_u32 s75, s79, s83\n"
"v_mbcnt_lo_u32_b32 v250, -1, 0\n"
"v_mbcnt_hi_u32_b32 v250, -1, v250\n"
"s_and_b32 s71, s64, 1\n"
"s_lshr_b32 s72, s64, 1\n"
"s_lshl_b32 s71, s71, 14\n"
"s_lshl_b32 s72, s72, 16\n"
"s_add_u32 s71, s71, s72\n"
"v_bfe_u32 v251, v250, 2, 1\n"
"v_mul_u32_u24 v240, 0x2000, v251\n"
"v_bfe_u32 v251, v250, 5, 1\n"
"v_mul_u32_u24 v251, 0x40000, v251\n"
"v_add_u32 v240, v240, v251\n"
"v_bfe_u32 v251, v250, 3, 2\n"
"v_mul_u32_u24 v251, 64, v251\n"
"v_add_u32 v240, v240, v251\n"
"v_bfe_u32 v251, v250, 0, 2\n"
"v_mul_u32_u24 v251, 16, v251\n"
"v_add_u32 v240, v240, v251\n"
"v_add_u32 v240, s71, v240\n"
"s_add_u32 m0, s69, 0x1f600\n"
"s_add_u32 s92, s78, 0\n"
"buffer_load_dwordx4 v240, s[44:47], s92 offen lds\n"
"s_add_u32 s93, s78, 0x1fc00\n"
"buffer_load_dwordx4 v240, s[44:47], s93 offen offset:1024 lds\n"
"s_add_u32 s94, s78, 0x7800\n"
"buffer_load_dwordx4 v240, s[44:47], s94 offen offset:2048 lds\n"
"s_add_u32 s91, s78, 0x27400\n"
"buffer_load_dwordx4 v240, s[44:47], s91 offen offset:3072 lds\n"
"s_mov_b32 s88, %8\n"
"s_mov_b32 s89, %9\n"
"s_getpc_b64 s[36:37]\n"
"Lpc0:\n"
"s_and_b32 s37, s37, 0xffff\n"
"s_mov_b32 s38, Lend-Lpc0+1280\n"
"s_mov_b32 s39, 0x20000\n"
"s_lshl_b32 s35, s88, 3\n"
"s_add_u32 s35, s35, s89\n"
"s_lshl_b32 s35, s35, 2\n"
"s_add_u32 s35, s35, s64\n"
"s_lshl_b32 s35, s35, 10\n"
"v_lshlrev_b32 v248, 4, v250\n"
"v_lshlrev_b32 v4, 20, v248\n"
"v_and_b32 v4, 0x38000000, v4\n"
"v_lshl_or_b32 v4, v248, 3, v4\n"
"s_mov_b32 m0, 0x27800\n"
"s_nop 0\n"
"buffer_load_dword v4, s[36:39], s35 offen lds\n"
"s_mul_i32 s73, s64, 0xe000\n"
"v_lshrrev_b32 v251, 4, v250\n"
"v_xor_b32 v241, v251, v250\n"
"v_and_b32 v241, 7, v241\n"
"v_lshlrev_b32 v241, 4, v241\n"
"v_xor_b32 v244, 64, v241\n"
"s_lshr_b32 s74, s86, 2\n"
"v_xor_b32 v245, s74, v241\n"
"v_lshrrev_b32 v251, 3, v250\n"
"v_mul_u32_u24 v251, 0x380, v251\n"
"v_add_u32 v251, s73, v251\n"
"v_add_u32 v241, v241, v251\n"
"v_add_u32 v244, v244, v251\n"
"v_add_u32 v245, v245, v251\n"
"s_add_u32 m0, s90, 0xe400\n"
"s_add_u32 s92, s80, 0\n"
"buffer_load_dwordx4 v241, s[40:43], s92 offen lds\n"
"s_add_u32 s93, s80, 0x1800\n"
"buffer_load_dwordx4 v244, s[40:43], s93 offen offset:1024 lds\n"
"s_add_u32 s94, s80, 0x3000\n"
"buffer_load_dwordx4 v241, s[40:43], s94 offen offset:2048 lds\n"
"s_add_u32 s91, s80, 0x4800\n"
"buffer_load_dwordx4 v244, s[40:43], s91 offen offset:3072 lds\n"
"s_add_u32 m0, s90, 0xf400\n"
"s_add_u32 s92, s81, 0x7000\n"
"buffer_load_dwordx4 v241, s[40:43], s92 offen lds\n"
"s_add_u32 s93, s81, 0x8800\n"
"buffer_load_dwordx4 v244, s[40:43], s93 offen offset:1024 lds\n"
"s_add_u32 s94, s81, 0xa000\n"
"buffer_load_dwordx4 v241, s[40:43], s94 offen offset:2048 lds\n"
"s_add_u32 s91, s81, 0xb800\n"
"buffer_load_dwordx4 v244, s[40:43], s91 offen offset:3072 lds\n"
"s_add_u32 m0, s84, 0xe400\n"
"s_mov_b32 s92, s75\n"
"buffer_load_dwordx4 v245, s[40:43], s92 offen lds\n"
"s_add_u32 s79, s79, 0x80\n"
"s_cmp_eq_u32 s79, 0x380\n"
"s_cselect_b32 s79, 0, s79\n"
"s_add_u32 s80, s79, s25\n"
"s_sub_u32 s81, s79, s25\n"
"s_add_u32 s75, s79, s83\n"
"s_add_u32 m0, s69, 0x23700\n"
"s_add_u32 s93, s78, 0x100\n"
"buffer_load_dwordx4 v240, s[44:47], s93 offen lds\n"
"s_add_u32 s94, s78, 0x1fd00\n"
"buffer_load_dwordx4 v240, s[44:47], s94 offen offset:1024 lds\n"
"s_add_u32 s91, s78, 0x7900\n"
"buffer_load_dwordx4 v240, s[44:47], s91 offen offset:2048 lds\n"
"s_add_u32 s92, s78, 0x27500\n"
"buffer_load_dwordx4 v240, s[44:47], s92 offen offset:3072 lds\n"
"s_add_u32 s78, s78, 0x80000\n"
"s_cmp_eq_u32 s78, 0x380000\n"
"s_cselect_b32 s78, 0, s78\n"
"s_mov_b64 s[48:49], %2\n"
"s_and_b32 s49, s49, 0xffff\n"
"s_mov_b32 s50, 0x80000\n"
"s_mov_b32 s51, 0x20000\n"
"s_mov_b64 s[52:53], %3\n"
"s_and_b32 s53, s53, 0xffff\n"
"s_mov_b32 s54, 0x80000\n"
"s_mov_b32 s55, 0x20000\n"
"s_mov_b64 s[56:57], %4\n"
"s_and_b32 s57, s57, 0xffff\n"
"s_mov_b32 s58, 0x70000\n"
"s_mov_b32 s59, 0x20000\n"
"s_mov_b64 s[60:61], %5\n"
"s_and_b32 s61, s61, 0xffff\n"
"s_mov_b32 s62, 0x3401000\n"
"s_mov_b32 s63, 0x20000\n"
"s_mov_b32 s65, 0x3db8aa3b\n"
"s_mov_b32 s66, 0x41000000\n"
"s_mov_b32 s95, 0xbdb8aa3b\n"
"s_mul_i32 s82, s64, 0x700\n"
"s_add_u32 s82, s82, 0x8000\n"
"s_mov_b64 s[76:77], 0\n"
"s_mov_b32 s70, 15\n"
"s_add_u32 s31, s69, 0x5400\n"
"s_add_u32 s33, s69, 0x1f600\n"
"s_mov_b32 s34, 0x8200\n"
"s_mov_b32 s30, 0x8200\n"
"s_mul_i32 s26, s24, 0x38000\n"
"s_sub_u32 s27, 0x38000, s26\n"
"s_lshl_b32 s87, s64, 10\n"
"s_lshl_b32 s73, s24, 12\n"
"s_add_u32 s87, s87, s73\n"
"s_xor_b32 s28, s87, 0x1000\n"
"s_add_u32 s73, %10, 0x2000000\n"
"s_add_u32 s35, s87, s73\n"
"s_add_u32 s36, s28, s73\n"
"s_and_b32 s37, s28, 0x1000\n"
"s_add_u32 s37, s37, s73\n"
"s_lshr_b32 s38, s87, 2\n"
"s_add_u32 s38, s38, 0x3400000\n"
"s_xor_b32 s39, s38, 0x400\n"
"s_mov_b32 s67, 0\n"
"s_mov_b32 s68, 0\n"
"v_bfe_u32 v251, v250, 2, 1\n"
"v_mul_u32_u24 v249, 0x200, v251\n"
"v_bfe_u32 v251, v250, 3, 3\n"
"v_mul_u32_u24 v251, 64, v251\n"
"v_add_u32 v249, v249, v251\n"
"v_bfe_u32 v251, v250, 0, 2\n"
"v_mul_u32_u24 v251, 16, v251\n"
"v_add_u32 v249, v249, v251\n"
"v_add_u32 v249, s85, v249\n"
"v_bfe_u32 v251, v250, 3, 1\n"
"v_mul_u32_u24 v236, 0x1020, v251\n"
"v_bfe_u32 v251, v250, 4, 1\n"
"v_mul_u32_u24 v251, 0x2080, v251\n"
"v_add_u32 v236, v236, v251\n"
"v_bfe_u32 v251, v250, 5, 1\n"
"v_mul_u32_u24 v251, 0x400, v251\n"
"v_add_u32 v236, v236, v251\n"
"v_bfe_u32 v251, v250, 2, 1\n"
"v_mul_u32_u24 v251, 64, v251\n"
"v_add_u32 v236, v236, v251\n"
"v_bfe_u32 v251, v250, 0, 2\n"
"v_mul_u32_u24 v251, 16, v251\n"
"v_add_u32 v236, v236, v251\n"
"v_add_u32 v236, 0x17400, v236\n"
"s_lshl_b32 s71, s64, 7\n"
"v_add_u32 v237, s71, v236\n"
"v_bfe_u32 v239, v250, 1, 3\n"
"v_lshrrev_b32 v251, 4, v250\n"
"v_xor_b32 v239, v239, v251\n"
"v_lshlrev_b32 v239, 4, v239\n"
"v_and_b32 v251, 15, v250\n"
"v_lshlrev_b32 v251, 7, v251\n"
"v_add_u32 v239, v239, v251\n"
"v_xor_b32 v243, 64, v239\n"
"v_add_u32 v239, 0x5400, v239\n"
"v_add_u32 v238, s90, v239\n"
"v_add_u32 v239, 0x8000, v239\n"
"v_add_u32 v243, 0x5400, v243\n"
"v_add_u32 v242, s90, v243\n"
"v_add_u32 v243, 0x8000, v243\n"
"v_lshlrev_b32 v248, 4, v250\n"
"v_mov_b32 v8, 0\n"
"v_mov_b32 v9, 0\n"
"v_mov_b32 v10, 0\n"
"v_mov_b32 v11, 0\n"
"ds_write_b128 v248, v[8:11] offset:0\n"
"s_nop 0\n"
"s_mov_b64 exec, 0\n"
"s_cmp_eq_u32 s64, 1\n"
"s_cbranch_scc1 Lpada_1\n"
"s_cmp_eq_u32 s64, 2\n"
"s_cbranch_scc1 Lpada_2\n"
"s_cmp_eq_u32 s64, 3\n"
"s_cbranch_scc1 Lpada_3\n"
"s_branch Lpada_0\n"
"Lpadxa:\n"
"s_mov_b64 exec, -1\n"
"s_nop 4\n"
"s_add_u32 m0, s69, 0x17400\n"
"s_add_u32 s93, s78, 0\n"
"buffer_load_dwordx4 v240, s[44:47], s93 offen lds\n"
"s_add_u32 s94, s78, 0x1fc00\n"
"buffer_load_dwordx4 v240, s[44:47], s94 offen offset:1024 lds\n"
"s_add_u32 s91, s78, 0x7800\n"
"buffer_load_dwordx4 v240, s[44:47], s91 offen offset:2048 lds\n"
"s_add_u32 s92, s78, 0x27400\n"
"buffer_load_dwordx4 v240, s[44:47], s92 offen offset:3072 lds\n"
"s_waitcnt vmcnt(8)\n"
"s_barrier\n"
"ds_read_b128 a[160:163], v238 offset:36864\n"
"ds_read_b64_tr_b16 v[212:213], v236 offset:33280\n"
"ds_read_b64_tr_b16 v[214:215], v236 offset:35328\n"
"s_waitcnt lgkmcnt(0)\n"
"v_mfma_f32_16x16x32_bf16 v[0:3], v[212:215], a[160:163], 0\n"
"s_add_u32 m0, s90, 0x5400\n"
"ds_read_b128 a[184:187], v242 offset:36864\n"
"ds_read_b64_tr_b16 v[216:217], v236 offset:33792\n"
"ds_read_b64_tr_b16 v[218:219], v236 offset:35840\n"
"ds_read_b128 a[188:191], v242 offset:38912\n"
"ds_read_b128 a[164:167], v238 offset:38912\n"
"ds_read_b128 a[168:171], v238 offset:40960\n"
"ds_read_b128 a[192:195], v242 offset:40960\n"
"s_waitcnt lgkmcnt(4)\n"
"v_mfma_f32_16x16x32_bf16 v[0:3], v[216:219], a[184:187], v[0:3]\n"
"s_waitcnt lgkmcnt(3)\n"
"ds_read_b128 a[196:199], v242 offset:43008\n"
"ds_read_b128 a[172:175], v238 offset:43008\n"
"v_mfma_f32_16x16x32_bf16 v[8:11], v[216:219], a[188:191], 0\n"
"s_waitcnt lgkmcnt(4)\n"
"ds_read_b64_tr_b16 v[220:221], v236 offset:33288\n"
"ds_read_b64_tr_b16 v[222:223], v236 offset:35336\n"
"v_mfma_f32_16x16x32_bf16 v[8:11], v[212:215], a[164:167], v[8:11]\n"
"s_waitcnt lgkmcnt(5)\n"
"ds_read_b64_tr_b16 v[224:225], v236 offset:33800\n"
"ds_read_b64_tr_b16 v[226:227], v236 offset:35848\n"
"v_mfma_f32_16x16x32_bf16 v[16:19], v[212:215], a[168:171], 0\n"
"s_waitcnt lgkmcnt(6)\n"
"ds_read_b128 a[176:179], v239 offset:36864\n"
"ds_read_b64_tr_b16 v[228:229], v237 offset:33280\n"
"v_mfma_f32_16x16x32_bf16 v[16:19], v[216:219], a[192:195], v[16:19]\n"
"s_waitcnt lgkmcnt(7)\n"
"ds_read_b64_tr_b16 v[230:231], v237 offset:35328\n"
"ds_read_b128 a[200:203], v243 offset:36864\n"
"v_mfma_f32_16x16x32_bf16 v[24:27], v[216:219], a[196:199], 0\n"
"s_waitcnt lgkmcnt(8)\n"
"ds_read_b64_tr_b16 v[232:233], v237 offset:33792\n"
"ds_read_b64_tr_b16 v[234:235], v237 offset:35840\n"
"v_mfma_f32_16x16x32_bf16 v[24:27], v[212:215], a[172:175], v[24:27]\n"
"s_waitcnt lgkmcnt(8)\n"
"ds_read_b128 a[204:207], v243 offset:38912\n"
"ds_read_b128 a[180:183], v239 offset:38912\n"
"v_mfma_f32_16x16x32_bf16 v[28:31], v[220:223], a[172:175], 0\n"
"s_waitcnt lgkmcnt(8)\n"
"ds_read_b64_tr_b16 v[216:217], v236 offset:33408\n"
"ds_read_b64_tr_b16 v[218:219], v236 offset:35456\n"
"v_mfma_f32_16x16x32_bf16 v[28:31], v[224:227], a[196:199], v[28:31]\n"
"s_add_u32 s93, s80, 0\n"
"ds_read_b64_tr_b16 v[212:213], v236 offset:33920\n"
"ds_read_b64_tr_b16 v[214:215], v236 offset:35968\n"
"v_mfma_f32_16x16x32_bf16 v[20:23], v[224:227], a[192:195], 0\n"
"s_add_u32 s94, s80, 0x1800\n"
"buffer_load_dwordx4 v241, s[40:43], s93 offen lds\n"
"v_mfma_f32_16x16x32_bf16 v[20:23], v[220:223], a[168:171], v[20:23]\n"
"s_add_u32 s91, s80, 0x3000\n"
"s_add_u32 s92, s80, 0x4800\n"
"v_lshlrev_b32 v208, 22, v248\n"
"v_mfma_f32_16x16x32_bf16 v[12:15], v[220:223], a[164:167], 0\n"
"s_add_u32 s93, s81, 0x7000\n"
"v_mbcnt_lo_u32_b32 v250, -1, 0\n"
"v_mbcnt_hi_u32_b32 v250, -1, v250\n"
"v_mfma_f32_16x16x32_bf16 v[12:15], v[224:227], a[188:191], v[12:15]\n"
"s_add_u32 s79, s79, 0x80\n"
"s_cmp_eq_u32 s79, 0x380\n"
"s_cselect_b32 s79, 0, s79\n"
"buffer_load_dwordx4 v244, s[40:43], s94 offen offset:1024 lds\n"
"v_mfma_f32_16x16x32_bf16 v[4:7], v[224:227], a[184:187], 0\n"
"s_add_u32 s94, s81, 0x8800\n"
"ds_read_b64_tr_b16 v[224:225], v236 offset:33416\n"
"ds_read_b64_tr_b16 v[226:227], v236 offset:35464\n"
"v_mfma_f32_16x16x32_bf16 v[4:7], v[220:223], a[160:163], v[4:7]\n"
"s_waitcnt lgkmcnt(11)\n"
"ds_read_b64_tr_b16 v[220:221], v236 offset:33928\n"
"ds_read_b64_tr_b16 v[222:223], v236 offset:35976\n"
"v_mfma_f32_16x16x32_bf16 v[128:131], v[228:231], a[176:179], 0\n"
"s_waitcnt lgkmcnt(10)\n"
"buffer_load_dwordx4 v241, s[40:43], s91 offen offset:2048 lds\n"
"v_mfma_f32_16x16x32_bf16 v[128:131], v[232:235], a[200:203], v[128:131]\n"
"s_waitcnt lgkmcnt(9)\n"
"v_bfe_u32 v251, v250, 1, 1\n"
"v_mul_u32_u24 v246, 0x1020, v251\n"
"v_mfma_f32_16x16x32_bf16 v[132:135], v[232:235], a[204:207], 0\n"
"s_waitcnt lgkmcnt(8)\n"
"ds_read_b64_tr_b16 v[232:233], v236 offset:33536\n"
"ds_read_b64_tr_b16 v[234:235], v236 offset:35584\n"
"v_mfma_f32_16x16x32_bf16 v[132:135], v[228:231], a[180:183], v[132:135]\n"
"s_waitcnt lgkmcnt(8)\n"
"buffer_load_dwordx4 v244, s[40:43], s92 offen offset:3072 lds\n"
"v_mfma_f32_16x16x32_bf16 v[32:35], v[216:219], a[160:163], 0\n"
"s_waitcnt lgkmcnt(6)\n"
"s_add_u32 m0, s90, 0x6400\n"
"s_add_u32 s91, s81, 0xa000\n"
"v_mfma_f32_16x16x32_bf16 v[32:35], v[212:215], a[184:187], v[32:35]\n"
"s_add_u32 s92, s81, 0xb800\n"
"ds_read_b64_tr_b16 v[228:229], v236 offset:34048\n"
"ds_read_b64_tr_b16 v[230:231], v236 offset:36096\n"
"v_mfma_f32_16x16x32_bf16 v[40:43], v[212:215], a[188:191], 0\n"
"s_add_u32 s80, s79, s25\n"
"buffer_load_dwordx4 v241, s[40:43], s93 offen lds\n"
"v_mfma_f32_16x16x32_bf16 v[40:43], v[216:219], a[164:167], v[40:43]\n"
"s_mov_b32 s93, s75\n"
"v_bfe_u32 v251, v250, 2, 1\n"
"v_mul_u32_u24 v251, 0x2080, v251\n"
"v_mfma_f32_16x16x32_bf16 v[48:51], v[216:219], a[168:171], 0\n"
"s_sub_u32 s81, s79, s25\n"
"v_add_u32 v246, v246, v251\n"
"v_bfe_u32 v251, v250, 3, 1\n"
"v_mfma_f32_16x16x32_bf16 v[48:51], v[212:215], a[192:195], v[48:51]\n"
"s_add_u32 s75, s79, s83\n"
"buffer_load_dwordx4 v244, s[40:43], s94 offen offset:1024 lds\n"
"v_mfma_f32_16x16x32_bf16 v[56:59], v[212:215], a[196:199], 0\n"
"s_add_u32 s94, s78, 0x100\n"
"ds_read_b64_tr_b16 v[212:213], v236 offset:33544\n"
"ds_read_b64_tr_b16 v[214:215], v236 offset:35592\n"
"v_mfma_f32_16x16x32_bf16 v[56:59], v[216:219], a[172:175], v[56:59]\n"
"s_waitcnt lgkmcnt(8)\n"
"ds_read_b64_tr_b16 v[216:217], v236 offset:34056\n"
"ds_read_b64_tr_b16 v[218:219], v236 offset:36104\n"
"v_mfma_f32_16x16x32_bf16 v[60:63], v[224:227], a[172:175], 0\n"
"s_waitcnt lgkmcnt(8)\n"
"buffer_load_dwordx4 v241, s[40:43], s91 offen offset:2048 lds\n"
"v_mfma_f32_16x16x32_bf16 v[60:63], v[220:223], a[196:199], v[60:63]\n"
"s_add_u32 s91, s78, 0x1fd00\n"
"v_mul_u32_u24 v251, 0x400, v251\n"
"v_add_u32 v246, v246, v251\n"
"v_mfma_f32_16x16x32_bf16 v[52:55], v[220:223], a[192:195], 0\n"
"s_add_u32 s79, s79, 0x80\n"
"s_cmp_eq_u32 s79, 0x380\n"
"s_cselect_b32 s79, 0, s79\n"
"v_bfe_u32 v251, v250, 0, 1\n"
"v_mul_u32_u24 v251, 64, v251\n"
"v_mfma_f32_16x16x32_bf16 v[52:55], v[224:227], a[168:171], v[52:55]\n"
"s_lshl_b32 s71, s64, 10\n"
"buffer_load_dwordx4 v244, s[40:43], s92 offen offset:3072 lds\n"
"v_mfma_f32_16x16x32_bf16 v[44:47], v[224:227], a[164:167], 0\n"
"s_add_u32 m0, s84, 0x5400\n"
"v_add_u32 v246, v246, v251\n"
"v_bfe_u32 v251, v250, 4, 2\n"
"v_mfma_f32_16x16x32_bf16 v[44:47], v[220:223], a[188:191], v[44:47]\n"
"s_add_u32 s92, s78, 0x7900\n"
"v_mul_u32_u24 v251, 16, v251\n"
"v_add_u32 v246, v246, v251\n"
"v_mfma_f32_16x16x32_bf16 v[36:39], v[220:223], a[184:187], 0\n"
"s_lshl_b32 s74, s86, 5\n"
"buffer_load_dwordx4 v245, s[40:43], s93 offen lds\n"
"v_mfma_f32_16x16x32_bf16 v[36:39], v[224:227], a[160:163], v[36:39]\n"
"s_waitcnt lgkmcnt(6)\n"
"s_add_u32 m0, s69, 0x1b500\n"
"ds_read_b64_tr_b16 v[220:221], v237 offset:33288\n"
"v_mfma_f32_16x16x32_bf16 v[64:67], v[232:235], a[160:163], 0\n"
"s_waitcnt lgkmcnt(5)\n"
"ds_read_b64_tr_b16 v[222:223], v237 offset:35336\n"
"ds_read_b64_tr_b16 v[224:225], v237 offset:33800\n"
"v_mfma_f32_16x16x32_bf16 v[64:67], v[228:231], a[184:187], v[64:67]\n"
"s_add_u32 s93, s78, 0x27500\n"
"ds_read_b64_tr_b16 v[226:227], v237 offset:35848\n"
"v_add_u32 v246, 0x5400, v246\n"
"v_mfma_f32_16x16x32_bf16 v[72:75], v[228:231], a[188:191], 0\n"
"s_add_u32 s78, s78, 0x80000\n"
"s_cmp_eq_u32 s78, 0x380000\n"
"s_cselect_b32 s78, 0, s78\n"
"v_bfe_u32 v251, v250, 3, 1\n"
"v_mul_u32_u24 v247, 0x1020, v251\n"
"v_mfma_f32_16x16x32_bf16 v[72:75], v[232:235], a[164:167], v[72:75]\n"
"s_lshr_b32 s72, s64, 1\n"
"v_bfe_u32 v251, v250, 4, 1\n"
"v_mul_u32_u24 v251, 0x2080, v251\n"
"v_mfma_f32_16x16x32_bf16 v[80:83], v[232:235], a[168:171], 0\n"
"s_lshl_b32 s72, s72, 11\n"
"v_add_u32 v247, v247, v251\n"
"v_bfe_u32 v251, v250, 5, 1\n"
"v_mfma_f32_16x16x32_bf16 v[80:83], v[228:231], a[192:195], v[80:83]\n"
"s_lshl_b32 s73, s64, 8\n"
"v_mul_u32_u24 v251, 0x400, v251\n"
"v_add_u32 v247, v247, v251\n"
"v_mfma_f32_16x16x32_bf16 v[88:91], v[228:231], a[196:199], 0\n"
"s_waitcnt vmcnt(13)\n"
"s_barrier\n"
"v_mfma_f32_16x16x32_bf16 v[88:91], v[232:235], a[172:175], v[88:91]\n"
"s_waitcnt lgkmcnt(6)\n"
"buffer_load_dwordx4 v240, s[44:47], s94 offen lds\n"
"v_mfma_f32_16x16x32_bf16 v[92:95], v[212:215], a[172:175], 0\n"
"s_waitcnt lgkmcnt(4)\n"
"ds_read_b64_tr_b16 v[228:229], v236 offset:33664\n"
"ds_read_b64_tr_b16 v[230:231], v236 offset:35712\n"
"v_mfma_f32_16x16x32_bf16 v[92:95], v[216:219], a[196:199], v[92:95]\n"
"s_add_u32 s94, s80, 0\n"
"ds_read_b64_tr_b16 v[232:233], v236 offset:34176\n"
"ds_read_b64_tr_b16 v[234:235], v236 offset:36224\n"
"v_mfma_f32_16x16x32_bf16 v[84:87], v[216:219], a[192:195], 0\n"
"buffer_load_dwordx4 v240, s[44:47], s91 offen offset:1024 lds\n"
"v_mfma_f32_16x16x32_bf16 v[84:87], v[212:215], a[168:171], v[84:87]\n"
"s_add_u32 s91, s80, 0x1800\n"
"v_bfe_u32 v251, v250, 2, 1\n"
"v_mul_u32_u24 v251, 64, v251\n"
"v_mfma_f32_16x16x32_bf16 v[76:79], v[212:215], a[164:167], 0\n"
"v_add_u32 v247, v247, v251\n"
"v_bfe_u32 v251, v250, 0, 2\n"
"v_mfma_f32_16x16x32_bf16 v[76:79], v[216:219], a[188:191], v[76:79]\n"
"buffer_load_dwordx4 v240, s[44:47], s92 offen offset:2048 lds\n"
"v_mfma_f32_16x16x32_bf16 v[68:71], v[216:219], a[184:187], 0\n"
"s_add_u32 s92, s80, 0x3000\n"
"ds_read_b64_tr_b16 v[216:217], v236 offset:33672\n"
"ds_read_b64_tr_b16 v[218:219], v236 offset:35720\n"
"v_mfma_f32_16x16x32_bf16 v[68:71], v[212:215], a[160:163], v[68:71]\n"
"s_waitcnt lgkmcnt(8)\n"
"ds_read_b64_tr_b16 v[212:213], v236 offset:34184\n"
"ds_read_b64_tr_b16 v[214:215], v236 offset:36232\n"
"v_mfma_f32_16x16x32_bf16 v[136:139], v[220:223], a[176:179], 0\n"
"s_waitcnt lgkmcnt(8)\n"
"buffer_load_dwordx4 v240, s[44:47], s93 offen offset:3072 lds\n"
"v_mfma_f32_16x16x32_bf16 v[136:139], v[224:227], a[200:203], v[136:139]\n"
"s_add_u32 m0, s90, 0xe400\n"
"v_mul_u32_u24 v251, 8, v251\n"
"v_add_u32 v247, v247, v251\n"
"v_mfma_f32_16x16x32_bf16 v[140:143], v[224:227], a[204:207], 0\n"
"s_add_u32 s93, s80, 0x4800\n"
"ds_read_b64_tr_b16 v[224:225], v236 offset:49920\n"
"ds_read_b64_tr_b16 v[226:227], v236 offset:51968\n"
"v_mfma_f32_16x16x32_bf16 v[140:143], v[220:223], a[180:183], v[140:143]\n"
"s_waitcnt lgkmcnt(8)\n"
"buffer_load_dwordx4 v241, s[40:43], s94 offen lds\n"
"v_mfma_f32_16x16x32_bf16 v[96:99], v[228:231], a[160:163], 0\n"
"s_waitcnt lgkmcnt(6)\n"
"ds_read_b64_tr_b16 v[220:221], v236 offset:50432\n"
"ds_read_b64_tr_b16 v[222:223], v236 offset:52480\n"
"v_mfma_f32_16x16x32_bf16 v[96:99], v[232:235], a[184:187], v[96:99]\n"
"s_add_u32 s94, s81, 0x7000\n"
"v_add_u32 v251, s71, v248\n"
"v_add_u32 v252, s74, v248\n"
"v_mfma_f32_16x16x32_bf16 v[104:107], v[232:235], a[188:191], 0\n"
"s_add_u32 s80, s79, s25\n"
"buffer_load_dwordx4 v244, s[40:43], s91 offen offset:1024 lds\n"
"v_mfma_f32_16x16x32_bf16 v[104:107], v[228:231], a[164:167], v[104:107]\n"
"s_add_u32 s91, s81, 0x8800\n"
"v_add_u32 v254, s72, v246\n"
"v_add_u32 v247, 0x17400, v247\n"
"v_mfma_f32_16x16x32_bf16 v[112:115], v[228:231], a[168:171], 0\n"
"s_lshr_b32 s72, s64, 1\n"
"v_add_u32 v255, s73, v247\n"
"v_mfma_f32_16x16x32_bf16 v[112:115], v[232:235], a[192:195], v[112:115]\n"
"s_lshl_b32 s73, s86, 2\n"
"buffer_load_dwordx4 v241, s[40:43], s92 offen offset:2048 lds\n"
"v_mfma_f32_16x16x32_bf16 v[120:123], v[232:235], a[196:199], 0\n"
"s_add_u32 s92, s81, 0xa000\n"
"ds_read_b64_tr_b16 v[232:233], v236 offset:49928\n"
"ds_read_b64_tr_b16 v[234:235], v236 offset:51976\n"
"v_mfma_f32_16x16x32_bf16 v[120:123], v[228:231], a[172:175], v[120:123]\n"
"s_waitcnt lgkmcnt(8)\n"
"ds_read_b64_tr_b16 v[228:229], v236 offset:50440\n"
"ds_read_b64_tr_b16 v[230:231], v236 offset:52488\n"
"v_mfma_f32_16x16x32_bf16 v[124:127], v[216:219], a[172:175], 0\n"
"s_waitcnt lgkmcnt(8)\n"
"buffer_load_dwordx4 v244, s[40:43], s93 offen offset:3072 lds\n"
"v_mfma_f32_16x16x32_bf16 v[124:127], v[212:215], a[196:199], v[124:127]\n"
"s_add_u32 m0, s90, 0xf400\n"
"v_mfma_f32_16x16x32_bf16 v[116:119], v[212:215], a[192:195], 0\n"
"s_add_u32 s93, s81, 0xb800\n"
"v_mfma_f32_16x16x32_bf16 v[116:119], v[216:219], a[168:171], v[116:119]\n"
"s_sub_u32 s81, s79, s25\n"
"v_mfma_f32_16x16x32_bf16 v[108:111], v[216:219], a[164:167], 0\n"
"s_lshl_b32 s72, s72, 3\n"
"v_mfma_f32_16x16x32_bf16 v[108:111], v[212:215], a[188:191], v[108:111]\n"
"s_add_u32 s73, s73, s72\n"
"buffer_load_dwordx4 v241, s[40:43], s94 offen lds\n"
"v_mfma_f32_16x16x32_bf16 v[100:103], v[212:215], a[184:187], 0\n"
"s_mov_b32 s94, s75\n"
"ds_read_b64_tr_b16 v[212:213], v237 offset:49920\n"
"ds_read_b64_tr_b16 v[214:215], v237 offset:51968\n"
"v_mfma_f32_16x16x32_bf16 v[100:103], v[216:219], a[160:163], v[100:103]\n"
"s_waitcnt lgkmcnt(8)\n"
"ds_read_b64_tr_b16 v[216:217], v237 offset:50432\n"
"ds_read_b64_tr_b16 v[218:219], v237 offset:52480\n"
"v_mfma_f32_16x16x32_bf16 v[160:163], v[224:227], a[160:163], 0\n"
"s_waitcnt lgkmcnt(8)\n"
"v_add_u32 v253, s73, v248\n"
"v_mfma_f32_16x16x32_bf16 v[160:163], v[220:223], a[184:187], v[160:163]\n"
"s_add_u32 s75, s79, s83\n"
"v_mfma_f32_16x16x32_bf16 v[168:171], v[220:223], a[188:191], 0\n"
"s_add_u32 s79, s79, 0x80\n"
"s_cmp_eq_u32 s79, 0x380\n"
"s_cselect_b32 s79, 0, s79\n"
"buffer_load_dwordx4 v244, s[40:43], s91 offen offset:1024 lds\n"
"v_mfma_f32_16x16x32_bf16 v[168:171], v[224:227], a[164:167], v[168:171]\n"
"s_add_u32 s91, s78, 0\n"
"v_mfma_f32_16x16x32_bf16 v[176:179], v[224:227], a[168:171], 0\n"
"v_mfma_f32_16x16x32_bf16 v[176:179], v[220:223], a[192:195], v[176:179]\n"
"v_mfma_f32_16x16x32_bf16 v[184:187], v[220:223], a[196:199], 0\n"
"ds_read_b64_tr_b16 v[220:221], v236 offset:50048\n"
"ds_read_b64_tr_b16 v[222:223], v236 offset:52096\n"
"v_mfma_f32_16x16x32_bf16 v[184:187], v[224:227], a[172:175], v[184:187]\n"
"s_waitcnt lgkmcnt(8)\n"
"buffer_load_dwordx4 v241, s[40:43], s92 offen offset:2048 lds\n"
"v_mfma_f32_16x16x32_bf16 v[188:191], v[232:235], a[172:175], 0\n"
"s_waitcnt lgkmcnt(6)\n"
"ds_read_b64_tr_b16 v[224:225], v236 offset:50560\n"
"ds_read_b64_tr_b16 v[226:227], v236 offset:52608\n"
"v_mfma_f32_16x16x32_bf16 v[188:191], v[228:231], a[196:199], v[188:191]\n"
"s_add_u32 s92, s78, 0x1fc00\n"
"v_mfma_f32_16x16x32_bf16 v[180:183], v[228:231], a[192:195], 0\n"
"v_mfma_f32_16x16x32_bf16 v[180:183], v[232:235], a[168:171], v[180:183]\n"
"v_mfma_f32_16x16x32_bf16 v[172:175], v[232:235], a[164:167], 0\n"
"buffer_load_dwordx4 v244, s[40:43], s93 offen offset:3072 lds\n"
"v_mfma_f32_16x16x32_bf16 v[172:175], v[228:231], a[188:191], v[172:175]\n"
"s_add_u32 m0, s84, 0xe400\n"
"v_mfma_f32_16x16x32_bf16 v[164:167], v[228:231], a[184:187], 0\n"
"s_add_u32 s93, s78, 0x7800\n"
"ds_read_b64_tr_b16 v[228:229], v236 offset:50056\n"
"ds_read_b64_tr_b16 v[230:231], v236 offset:52104\n"
"v_mfma_f32_16x16x32_bf16 v[164:167], v[232:235], a[160:163], v[164:167]\n"
"s_waitcnt lgkmcnt(8)\n"
"ds_read_b64_tr_b16 v[232:233], v236 offset:50568\n"
"ds_read_b64_tr_b16 v[234:235], v236 offset:52616\n"
"v_mfma_f32_16x16x32_bf16 v[144:147], v[212:215], a[176:179], 0\n"
"s_waitcnt lgkmcnt(8)\n"
"v_mfma_f32_16x16x32_bf16 v[144:147], v[216:219], a[200:203], v[144:147]\n"
"buffer_load_dwordx4 v245, s[40:43], s94 offen lds\n"
"v_mfma_f32_16x16x32_bf16 v[148:151], v[216:219], a[204:207], 0\n"
"s_add_u32 s94, s78, 0x27400\n"
"ds_read_b64_tr_b16 v[216:217], v236 offset:50176\n"
"ds_read_b64_tr_b16 v[218:219], v236 offset:52224\n"
"v_mfma_f32_16x16x32_bf16 v[148:151], v[212:215], a[180:183], v[148:151]\n"
"s_waitcnt lgkmcnt(8)\n"
"ds_read_b64_tr_b16 v[212:213], v236 offset:50688\n"
"ds_read_b64_tr_b16 v[214:215], v236 offset:52736\n"
"v_mfma_f32_16x16x32_bf16 v[192:195], v[220:223], a[160:163], 0\n"
"s_waitcnt lgkmcnt(8)\n"
"v_mfma_f32_16x16x32_bf16 v[192:195], v[224:227], a[184:187], v[192:195]\n"
"v_mfma_f32_16x16x32_bf16 v[200:203], v[224:227], a[188:191], 0\n"
"v_mfma_f32_16x16x32_bf16 v[200:203], v[220:223], a[164:167], v[200:203]\n"
"v_mfma_f32_16x16x32_bf16 a[80:83], v[220:223], a[168:171], 0\n"
"v_mfma_f32_16x16x32_bf16 a[80:83], v[224:227], a[192:195], a[80:83]\n"
"Lpada_0:\n"
"s_cbranch_execz Lpada_4\n"
"v_mfma_f32_16x16x32_bf16 a[88:91], v[224:227], a[196:199], 0\n"
"ds_read_b64_tr_b16 v[224:225], v236 offset:50184\n"
"ds_read_b64_tr_b16 v[226:227], v236 offset:52232\n"
"v_mfma_f32_16x16x32_bf16 a[88:91], v[220:223], a[172:175], a[88:91]\n"
"s_waitcnt lgkmcnt(8)\n"
"ds_read_b64_tr_b16 v[220:221], v236 offset:50696\n"
"ds_read_b64_tr_b16 v[222:223], v236 offset:52744\n"
"v_mfma_f32_16x16x32_bf16 a[92:95], v[228:231], a[172:175], 0\n"
"s_waitcnt lgkmcnt(8)\n"
"v_accvgpr_write_b32 a0, 0\n"
"v_mfma_f32_16x16x32_bf16 a[92:95], v[232:235], a[196:199], a[92:95]\n"
"v_mfma_f32_16x16x32_bf16 a[84:87], v[232:235], a[192:195], 0\n"
"s_waitcnt vmcnt(13)\n"
"s_barrier\n"
"v_mfma_f32_16x16x32_bf16 a[84:87], v[228:231], a[168:171], a[84:87]\n"
"s_add_u32 m0, s69, 0x1f600\n"
"ds_read_b128 a[208:211], v238 offset:0\n"
"ds_read_b128 a[232:235], v242 offset:0\n"
"v_mfma_f32_16x16x32_bf16 v[204:207], v[228:231], a[164:167], 0\n"
"buffer_load_dwordx4 v240, s[44:47], s91 offen lds\n"
"v_mfma_f32_16x16x32_bf16 v[204:207], v[232:235], a[188:191], v[204:207]\n"
"s_add_u32 s91, s78, 0x100\n"
"ds_read_b128 a[236:239], v242 offset:2048\n"
"ds_read_b128 a[212:215], v238 offset:2048\n"
"v_mfma_f32_16x16x32_bf16 v[196:199], v[232:235], a[184:187], 0\n"
"ds_read_b64_tr_b16 v[232:233], v237 offset:49928\n"
"ds_read_b64_tr_b16 v[234:235], v237 offset:51976\n"
"v_mfma_f32_16x16x32_bf16 v[196:199], v[228:231], a[160:163], v[196:199]\n"
"s_waitcnt lgkmcnt(12)\n"
"ds_read_b64_tr_b16 v[228:229], v237 offset:50440\n"
"ds_read_b64_tr_b16 v[230:231], v237 offset:52488\n"
"v_mfma_f32_16x16x32_bf16 a[96:99], v[216:219], a[160:163], 0\n"
"s_waitcnt lgkmcnt(12)\n"
"buffer_load_dwordx4 v240, s[44:47], s92 offen offset:1024 lds\n"
"v_mfma_f32_16x16x32_bf16 a[96:99], v[212:215], a[184:187], a[96:99]\n"
"s_add_u32 s92, s78, 0x1fd00\n"
"ds_read_b128 a[216:219], v238 offset:4096\n"
"ds_read_b128 a[240:243], v242 offset:4096\n"
"v_mfma_f32_16x16x32_bf16 a[104:107], v[212:215], a[188:191], 0\n"
"ds_read_b128 a[244:247], v242 offset:6144\n"
"ds_read_b128 a[220:223], v238 offset:6144\n"
"v_mfma_f32_16x16x32_bf16 a[104:107], v[216:219], a[164:167], a[104:107]\n"
"ds_read_b128 a[224:227], v239 offset:0\n"
"ds_read_b128 a[248:251], v243 offset:0\n"
"v_mfma_f32_16x16x32_bf16 a[112:115], v[216:219], a[168:171], 0\n"
"buffer_load_dwordx4 v240, s[44:47], s93 offen offset:2048 lds\n"
"v_mfma_f32_16x16x32_bf16 a[112:115], v[212:215], a[192:195], a[112:115]\n"
"s_add_u32 s93, s78, 0x7900\n"
"ds_read_b128 a[252:255], v243 offset:2048\n"
"ds_read_b128 a[228:231], v239 offset:2048\n"
"v_mfma_f32_16x16x32_bf16 a[120:123], v[212:215], a[196:199], 0\n"
"ds_read_b64_tr_b16 v[212:213], v236 offset:50304\n"
"ds_read_b64_tr_b16 v[214:215], v236 offset:52352\n"
"v_mfma_f32_16x16x32_bf16 a[120:123], v[216:219], a[172:175], a[120:123]\n"
"s_waitcnt lgkmcnt(15)\n"
"ds_read_b64_tr_b16 v[216:217], v236 offset:50816\n"
"ds_read_b64_tr_b16 v[218:219], v236 offset:52864\n"
"v_mfma_f32_16x16x32_bf16 a[124:127], v[224:227], a[172:175], 0\n"
"buffer_load_dwordx4 v240, s[44:47], s94 offen offset:3072 lds\n"
"v_mfma_f32_16x16x32_bf16 a[124:127], v[220:223], a[196:199], a[124:127]\n"
"s_add_u32 s94, s78, 0x27500\n"
"v_accvgpr_write_b32 a1, 0\n"
"v_accvgpr_write_b32 a2, 0\n"
"v_mfma_f32_16x16x32_bf16 a[116:119], v[220:223], a[192:195], 0\n"
"s_add_u32 s78, s78, 0x80000\n"
"s_cmp_eq_u32 s78, 0x380000\n"
"s_cselect_b32 s78, 0, s78\n"
"v_accvgpr_write_b32 a3, 0\n"
"v_mfma_f32_16x16x32_bf16 a[116:119], v[224:227], a[168:171], a[116:119]\n"
"v_mfma_f32_16x16x32_bf16 a[108:111], v[224:227], a[164:167], 0\n"
"v_accvgpr_write_b32 a4, 0\n"
"v_mfma_f32_16x16x32_bf16 a[108:111], v[220:223], a[188:191], a[108:111]\n"
"v_mfma_f32_16x16x32_bf16 a[100:103], v[220:223], a[184:187], 0\n"
"ds_read_b64_tr_b16 v[220:221], v236 offset:50312\n"
"ds_read_b64_tr_b16 v[222:223], v236 offset:52360\n"
"v_mfma_f32_16x16x32_bf16 a[100:103], v[224:227], a[160:163], a[100:103]\n"
"s_waitcnt lgkmcnt(15)\n"
"ds_read_b64_tr_b16 v[224:225], v236 offset:50824\n"
"ds_read_b64_tr_b16 v[226:227], v236 offset:52872\n"
"v_mfma_f32_16x16x32_bf16 v[152:155], v[232:235], a[176:179], 0\n"
"s_waitcnt lgkmcnt(15)\n"
"v_mfma_f32_16x16x32_bf16 v[152:155], v[228:231], a[200:203], v[152:155]\n"
"v_accvgpr_write_b32 a5, 0\n"
"v_mfma_f32_16x16x32_bf16 v[156:159], v[228:231], a[204:207], 0\n"
"ds_read_b64_tr_b16 v[228:229], v236 offset:0\n"
"ds_read_b64_tr_b16 v[230:231], v236 offset:2048\n"
"v_mfma_f32_16x16x32_bf16 v[156:159], v[232:235], a[180:183], v[156:159]\n"
"s_waitcnt lgkmcnt(8)\n"
"ds_read_b64_tr_b16 v[232:233], v236 offset:512\n"
"ds_read_b64_tr_b16 v[234:235], v236 offset:2560\n"
"v_mfma_f32_16x16x32_bf16 a[128:131], v[212:215], a[160:163], 0\n"
"s_waitcnt lgkmcnt(8)\n"
"v_mfma_f32_16x16x32_bf16 a[128:131], v[216:219], a[184:187], a[128:131]\n"
"v_mfma_f32_16x16x32_bf16 a[136:139], v[216:219], a[188:191], 0\n"
"v_accvgpr_write_b32 a6, 0\n"
"v_mfma_f32_16x16x32_bf16 a[136:139], v[212:215], a[164:167], a[136:139]\n"
"v_mfma_f32_16x16x32_bf16 a[144:147], v[212:215], a[168:171], 0\n"
"v_mfma_f32_16x16x32_bf16 a[144:147], v[216:219], a[192:195], a[144:147]\n"
"v_mfma_f32_16x16x32_bf16 a[152:155], v[216:219], a[196:199], 0\n"
"ds_read_b64_tr_b16 v[216:217], v236 offset:8\n"
"ds_read_b64_tr_b16 v[218:219], v236 offset:2056\n"
"v_mfma_f32_16x16x32_bf16 a[152:155], v[212:215], a[172:175], a[152:155]\n"
"s_waitcnt lgkmcnt(8)\n"
"ds_read_b64_tr_b16 v[212:213], v236 offset:520\n"
"ds_read_b64_tr_b16 v[214:215], v236 offset:2568\n"
"v_mfma_f32_16x16x32_bf16 a[156:159], v[220:223], a[172:175], 0\n"
"s_waitcnt lgkmcnt(8)\n"
"v_accvgpr_write_b32 a7, 0\n"
"v_mfma_f32_16x16x32_bf16 a[156:159], v[224:227], a[196:199], a[156:159]\n"
"v_mfma_f32_16x16x32_bf16 a[148:151], v[224:227], a[192:195], 0\n"
"v_mfma_f32_16x16x32_bf16 a[148:151], v[220:223], a[168:171], a[148:151]\n"
"v_mfma_f32_16x16x32_bf16 a[140:143], v[220:223], a[164:167], 0\n"
"v_accvgpr_write_b32 a8, 0\n"
"v_mfma_f32_16x16x32_bf16 a[140:143], v[224:227], a[188:191], a[140:143]\n"
"v_mfma_f32_16x16x32_bf16 a[132:135], v[224:227], a[184:187], 0\n"
"ds_read_b64_tr_b16 v[224:225], v237 offset:0\n"
"ds_read_b64_tr_b16 v[226:227], v237 offset:2048\n"
"v_mfma_f32_16x16x32_bf16 a[132:135], v[220:223], a[160:163], a[132:135]\n"
"s_waitcnt lgkmcnt(8)\n"
"ds_read_b64_tr_b16 v[220:221], v237 offset:512\n"
"ds_read_b64_tr_b16 v[222:223], v237 offset:2560\n"
"v_mfma_f32_16x16x32_bf16 v[0:3], v[228:231], a[208:211], v[0:3]\n"
"s_waitcnt lgkmcnt(8)\n"
"v_mfma_f32_16x16x32_bf16 v[0:3], v[232:235], a[232:235], v[0:3]\n"
"v_accvgpr_write_b32 a9, 0\n"
"v_mfma_f32_16x16x32_bf16 v[8:11], v[232:235], a[236:239], v[8:11]\n"
"v_mfma_f32_16x16x32_bf16 v[8:11], v[228:231], a[212:215], v[8:11]\n"
"v_mfma_f32_16x16x32_bf16 v[16:19], v[228:231], a[216:219], v[16:19]\n"
"v_mfma_f32_16x16x32_bf16 v[16:19], v[232:235], a[240:243], v[16:19]\n"
"v_mfma_f32_16x16x32_bf16 v[24:27], v[232:235], a[244:247], v[24:27]\n"
"ds_read_b64_tr_b16 v[232:233], v236 offset:128\n"
"ds_read_b64_tr_b16 v[234:235], v236 offset:2176\n"
"v_mfma_f32_16x16x32_bf16 v[24:27], v[228:231], a[220:223], v[24:27]\n"
"s_waitcnt lgkmcnt(8)\n"
"ds_read_b64_tr_b16 v[228:229], v236 offset:640\n"
"ds_read_b64_tr_b16 v[230:231], v236 offset:2688\n"
"v_mfma_f32_16x16x32_bf16 v[28:31], v[216:219], a[220:223], v[28:31]\n"
"s_waitcnt lgkmcnt(8)\n"
"buffer_load_dword v209, v208, s[60:63], s38 offen sc1\n"
"v_mfma_f32_16x16x32_bf16 v[28:31], v[212:215], a[244:247], v[28:31]\n"
"v_accvgpr_write_b32 a10, 0\n"
"v_mfma_f32_16x16x32_bf16 v[20:23], v[212:215], a[240:243], v[20:23]\n"
"v_mfma_f32_16x16x32_bf16 v[20:23], v[216:219], a[216:219], v[20:23]\n"
"v_accvgpr_write_b32 a11, 0\n"
"v_mfma_f32_16x16x32_bf16 v[12:15], v[216:219], a[212:215], v[12:15]\n"
"v_mfma_f32_16x16x32_bf16 v[12:15], v[212:215], a[236:239], v[12:15]\n"
"v_mfma_f32_16x16x32_bf16 v[4:7], v[212:215], a[232:235], v[4:7]\n"
"ds_read_b64_tr_b16 v[212:213], v236 offset:136\n"
"ds_read_b64_tr_b16 v[214:215], v236 offset:2184\n"
"v_mfma_f32_16x16x32_bf16 v[4:7], v[216:219], a[208:211], v[4:7]\n"
"s_waitcnt lgkmcnt(8)\n"
"ds_read_b64_tr_b16 v[216:217], v236 offset:648\n"
"ds_read_b64_tr_b16 v[218:219], v236 offset:2696\n"
"v_mfma_f32_16x16x32_bf16 v[128:131], v[224:227], a[224:227], v[128:131]\n"
"s_waitcnt lgkmcnt(8)\n"
"v_accvgpr_write_b32 a12, 0\n"
"v_mfma_f32_16x16x32_bf16 v[128:131], v[220:223], a[248:251], v[128:131]\n"
"v_mfma_f32_16x16x32_bf16 v[132:135], v[220:223], a[252:255], v[132:135]\n"
"ds_read_b64_tr_b16 v[220:221], v236 offset:256\n"
"ds_read_b64_tr_b16 v[222:223], v236 offset:2304\n"
"v_mfma_f32_16x16x32_bf16 v[132:135], v[224:227], a[228:231], v[132:135]\n"
"s_waitcnt lgkmcnt(8)\n"
"ds_read_b64_tr_b16 v[224:225], v236 offset:768\n"
"ds_read_b64_tr_b16 v[226:227], v236 offset:2816\n"
"v_mfma_f32_16x16x32_bf16 v[32:35], v[232:235], a[208:211], v[32:35]\n"
"s_waitcnt lgkmcnt(8)\n"
"v_mfma_f32_16x16x32_bf16 v[32:35], v[228:231], a[232:235], v[32:35]\n"
"v_accvgpr_write_b32 a13, 0\n"
"v_mfma_f32_16x16x32_bf16 v[40:43], v[228:231], a[236:239], v[40:43]\n"
"v_mfma_f32_16x16x32_bf16 v[40:43], v[232:235], a[212:215], v[40:43]\n"
"v_mfma_f32_16x16x32_bf16 v[48:51], v[232:235], a[216:219], v[48:51]\n"
"v_mfma_f32_16x16x32_bf16 v[48:51], v[228:231], a[240:243], v[48:51]\n"
"v_mfma_f32_16x16x32_bf16 v[56:59], v[228:231], a[244:247], v[56:59]\n"
"ds_read_b64_tr_b16 v[228:229], v236 offset:264\n"
"ds_read_b64_tr_b16 v[230:231], v236 offset:2312\n"
"v_mfma_f32_16x16x32_bf16 v[56:59], v[232:235], a[220:223], v[56:59]\n"
"s_waitcnt lgkmcnt(8)\n"
"ds_read_b64_tr_b16 v[232:233], v236 offset:776\n"
"ds_read_b64_tr_b16 v[234:235], v236 offset:2824\n"
"v_mfma_f32_16x16x32_bf16 v[60:63], v[212:215], a[220:223], v[60:63]\n"
"s_waitcnt lgkmcnt(8)\n"
"v_accvgpr_write_b32 a14, 0\n"
"v_mfma_f32_16x16x32_bf16 v[60:63], v[216:219], a[244:247], v[60:63]\n"
"v_mfma_f32_16x16x32_bf16 v[52:55], v[216:219], a[240:243], v[52:55]\n"
"s_waitcnt vmcnt(14)\n"
"s_barrier\n"
"v_mfma_f32_16x16x32_bf16 v[52:55], v[212:215], a[216:219], v[52:55]\n"
"s_add_u32 m0, s69, 0x23700\n"
"v_accvgpr_write_b32 a15, 0\n"
"v_mfma_f32_16x16x32_bf16 v[44:47], v[212:215], a[212:215], v[44:47]\n"
"buffer_load_dwordx4 v240, s[44:47], s91 offen lds\n"
"v_mfma_f32_16x16x32_bf16 v[44:47], v[216:219], a[236:239], v[44:47]\n"
"s_add_u32 s91, s80, 0\n"
"v_mfma_f32_16x16x32_bf16 v[36:39], v[216:219], a[232:235], v[36:39]\n"
"ds_read_b64_tr_b16 v[216:217], v237 offset:8\n"
"ds_read_b64_tr_b16 v[218:219], v237 offset:2056\n"
"v_mfma_f32_16x16x32_bf16 v[36:39], v[212:215], a[208:211], v[36:39]\n"
"s_waitcnt lgkmcnt(8)\n"
"ds_read_b64_tr_b16 v[212:213], v237 offset:520\n"
"ds_read_b64_tr_b16 v[214:215], v237 offset:2568\n"
"v_mfma_f32_16x16x32_bf16 v[64:67], v[220:223], a[208:211], v[64:67]\n"
"s_waitcnt lgkmcnt(8)\n"
"buffer_load_dwordx4 v240, s[44:47], s92 offen offset:1024 lds\n"
"v_mfma_f32_16x16x32_bf16 v[64:67], v[224:227], a[232:235], v[64:67]\n"
"s_add_u32 s92, s80, 0x1800\n"
"v_accvgpr_write_b32 a16, 0\n"
"v_mfma_f32_16x16x32_bf16 v[72:75], v[224:227], a[236:239], v[72:75]\n"
"v_mfma_f32_16x16x32_bf16 v[72:75], v[220:223], a[212:215], v[72:75]\n"
"v_mfma_f32_16x16x32_bf16 v[80:83], v[220:223], a[216:219], v[80:83]\n"
"buffer_load_dwordx4 v240, s[44:47], s93 offen offset:2048 lds\n"
"v_mfma_f32_16x16x32_bf16 v[80:83], v[224:227], a[240:243], v[80:83]\n"
"s_add_u32 s93, s80, 0x3000\n"
"v_accvgpr_write_b32 a17, 0\n"
"v_mfma_f32_16x16x32_bf16 v[88:91], v[224:227], a[244:247], v[88:91]\n"
"ds_read_b64_tr_b16 v[224:225], v236 offset:384\n"
"ds_read_b64_tr_b16 v[226:227], v236 offset:2432\n"
"v_mfma_f32_16x16x32_bf16 v[88:91], v[220:223], a[220:223], v[88:91]\n"
"s_waitcnt lgkmcnt(8)\n"
"ds_read_b64_tr_b16 v[220:221], v236 offset:896\n"
"ds_read_b64_tr_b16 v[222:223], v236 offset:2944\n"
"v_mfma_f32_16x16x32_bf16 v[92:95], v[228:231], a[220:223], v[92:95]\n"
"s_waitcnt lgkmcnt(8)\n"
"buffer_load_dwordx4 v240, s[44:47], s94 offen offset:3072 lds\n"
"v_mfma_f32_16x16x32_bf16 v[92:95], v[232:235], a[244:247], v[92:95]\n"
"s_add_u32 s94, s80, 0x4800\n"
"v_mfma_f32_16x16x32_bf16 v[84:87], v[232:235], a[240:243], v[84:87]\n"
"s_add_u32 s80, s79, s25\n"
"v_accvgpr_write_b32 a18, 0\n"
"v_mfma_f32_16x16x32_bf16 v[84:87], v[228:231], a[216:219], v[84:87]\n"
"v_mfma_f32_16x16x32_bf16 v[76:79], v[228:231], a[212:215], v[76:79]\n"
"s_add_u32 m0, s90, 0x5400\n"
"v_mfma_f32_16x16x32_bf16 v[76:79], v[232:235], a[236:239], v[76:79]\n"
"buffer_load_dwordx4 v241, s[40:43], s91 offen lds\n"
"v_mfma_f32_16x16x32_bf16 v[68:71], v[232:235], a[232:235], v[68:71]\n"
"s_add_u32 s91, s81, 0x7000\n"
"ds_read_b64_tr_b16 v[232:233], v236 offset:392\n"
"ds_read_b64_tr_b16 v[234:235], v236 offset:2440\n"
"v_mfma_f32_16x16x32_bf16 v[68:71], v[228:231], a[208:211], v[68:71]\n"
"s_waitcnt lgkmcnt(8)\n"
"ds_read_b64_tr_b16 v[228:229], v236 offset:904\n"
"ds_read_b64_tr_b16 v[230:231], v236 offset:2952\n"
"v_mfma_f32_16x16x32_bf16 v[136:139], v[216:219], a[224:227], v[136:139]\n"
"s_waitcnt lgkmcnt(8)\n"
"v_accvgpr_write_b32 a19, 0\n"
"v_mfma_f32_16x16x32_bf16 v[136:139], v[212:215], a[248:251], v[136:139]\n"
"v_mfma_f32_16x16x32_bf16 v[140:143], v[212:215], a[252:255], v[140:143]\n"
"buffer_load_dwordx4 v244, s[40:43], s92 offen offset:1024 lds\n"
"v_mfma_f32_16x16x32_bf16 v[140:143], v[216:219], a[228:231], v[140:143]\n"
"s_waitcnt lgkmcnt(6)\n"
"ds_read_b64_tr_b16 v[212:213], v236 offset:16640\n"
"ds_read_b64_tr_b16 v[214:215], v236 offset:18688\n"
"v_mfma_f32_16x16x32_bf16 v[96:99], v[224:227], a[208:211], v[96:99]\n"
"s_waitcnt lgkmcnt(6)\n"
"ds_read_b64_tr_b16 v[216:217], v236 offset:17152\n"
"ds_read_b64_tr_b16 v[218:219], v236 offset:19200\n"
"v_mfma_f32_16x16x32_bf16 v[96:99], v[220:223], a[232:235], v[96:99]\n"
"s_add_u32 s92, s81, 0x8800\n"
"v_accvgpr_write_b32 a20, 0\n"
"v_mfma_f32_16x16x32_bf16 v[104:107], v[220:223], a[236:239], v[104:107]\n"
"v_mfma_f32_16x16x32_bf16 v[104:107], v[224:227], a[212:215], v[104:107]\n"
"buffer_load_dwordx4 v241, s[40:43], s93 offen offset:2048 lds\n"
"v_mfma_f32_16x16x32_bf16 v[112:115], v[224:227], a[216:219], v[112:115]\n"
"s_add_u32 s93, s81, 0xa000\n"
"v_mfma_f32_16x16x32_bf16 v[112:115], v[220:223], a[240:243], v[112:115]\n"
"v_accvgpr_write_b32 a21, 0\n"
"v_mfma_f32_16x16x32_bf16 v[120:123], v[220:223], a[244:247], v[120:123]\n"
"ds_read_b64_tr_b16 v[220:221], v236 offset:16648\n"
"ds_read_b64_tr_b16 v[222:223], v236 offset:18696\n"
"v_mfma_f32_16x16x32_bf16 v[120:123], v[224:227], a[220:223], v[120:123]\n"
"s_waitcnt lgkmcnt(8)\n"
"ds_read_b64_tr_b16 v[224:225], v236 offset:17160\n"
"ds_read_b64_tr_b16 v[226:227], v236 offset:19208\n"
"v_mfma_f32_16x16x32_bf16 v[124:127], v[232:235], a[220:223], v[124:127]\n"
"s_waitcnt lgkmcnt(8)\n"
"buffer_load_dwordx4 v244, s[40:43], s94 offen offset:3072 lds\n"
"v_mfma_f32_16x16x32_bf16 v[124:127], v[228:231], a[244:247], v[124:127]\n"
"s_add_u32 m0, s90, 0x6400\n"
"v_mfma_f32_16x16x32_bf16 v[116:119], v[228:231], a[240:243], v[116:119]\n"
"s_add_u32 s94, s81, 0xb800\n"
"v_accvgpr_write_b32 a22, 0\n"
"v_mfma_f32_16x16x32_bf16 v[116:119], v[232:235], a[216:219], v[116:119]\n"
"s_sub_u32 s81, s79, s25\n"
"v_mfma_f32_16x16x32_bf16 v[108:111], v[232:235], a[212:215], v[108:111]\n"
"v_mfma_f32_16x16x32_bf16 v[108:111], v[228:231], a[236:239], v[108:111]\n"
"buffer_load_dwordx4 v241, s[40:43], s91 offen lds\n"
"v_mfma_f32_16x16x32_bf16 v[100:103], v[228:231], a[232:235], v[100:103]\n"
"s_mov_b32 s91, s75\n"
"ds_read_b64_tr_b16 v[228:229], v237 offset:16640\n"
"ds_read_b64_tr_b16 v[230:231], v237 offset:18688\n"
"v_mfma_f32_16x16x32_bf16 v[100:103], v[232:235], a[208:211], v[100:103]\n"
"s_waitcnt lgkmcnt(8)\n"
"ds_read_b64_tr_b16 v[232:233], v237 offset:17152\n"
"ds_read_b64_tr_b16 v[234:235], v237 offset:19200\n"
"v_mfma_f32_16x16x32_bf16 v[160:163], v[212:215], a[208:211], v[160:163]\n"
"s_waitcnt lgkmcnt(8)\n"
"v_accvgpr_write_b32 a23, 0\n"
"v_mfma_f32_16x16x32_bf16 v[160:163], v[216:219], a[232:235], v[160:163]\n"
"s_add_u32 s75, s79, s83\n"
"v_mfma_f32_16x16x32_bf16 v[168:171], v[216:219], a[236:239], v[168:171]\n"
"s_add_u32 s79, s79, 0x80\n"
"s_cmp_eq_u32 s79, 0x380\n"
"s_cselect_b32 s79, 0, s79\n"
"buffer_load_dwordx4 v244, s[40:43], s92 offen offset:1024 lds\n"
"v_mfma_f32_16x16x32_bf16 v[168:171], v[212:215], a[212:215], v[168:171]\n"
"s_add_u32 s92, s78, 0\n"
"v_mfma_f32_16x16x32_bf16 v[176:179], v[212:215], a[216:219], v[176:179]\n"
"v_accvgpr_write_b32 a24, 0\n"
"v_mfma_f32_16x16x32_bf16 v[176:179], v[216:219], a[240:243], v[176:179]\n"
"v_mfma_f32_16x16x32_bf16 v[184:187], v[216:219], a[244:247], v[184:187]\n"
"ds_read_b64_tr_b16 v[216:217], v236 offset:16768\n"
"ds_read_b64_tr_b16 v[218:219], v236 offset:18816\n"
"v_mfma_f32_16x16x32_bf16 v[184:187], v[212:215], a[220:223], v[184:187]\n"
"s_waitcnt lgkmcnt(8)\n"
"buffer_load_dwordx4 v241, s[40:43], s93 offen offset:2048 lds\n"
"v_mfma_f32_16x16x32_bf16 v[188:191], v[220:223], a[220:223], v[188:191]\n"
"s_waitcnt lgkmcnt(6)\n"
"ds_read_b64_tr_b16 v[212:213], v236 offset:17280\n"
"ds_read_b64_tr_b16 v[214:215], v236 offset:19328\n"
"v_mfma_f32_16x16x32_bf16 v[188:191], v[224:227], a[244:247], v[188:191]\n"
"s_add_u32 s93, s78, 0x1fc00\n"
"v_accvgpr_write_b32 a25, 0\n"
"v_mfma_f32_16x16x32_bf16 v[180:183], v[224:227], a[240:243], v[180:183]\n"
"v_mfma_f32_16x16x32_bf16 v[180:183], v[220:223], a[216:219], v[180:183]\n"
"v_mfma_f32_16x16x32_bf16 v[172:175], v[220:223], a[212:215], v[172:175]\n"
"buffer_load_dwordx4 v244, s[40:43], s94 offen offset:3072 lds\n"
"v_mfma_f32_16x16x32_bf16 v[172:175], v[224:227], a[236:239], v[172:175]\n"
"s_add_u32 m0, s84, 0x5400\n"
"v_mfma_f32_16x16x32_bf16 v[164:167], v[224:227], a[232:235], v[164:167]\n"
"s_add_u32 s94, s78, 0x7800\n"
"ds_read_b64_tr_b16 v[224:225], v236 offset:16776\n"
"ds_read_b64_tr_b16 v[226:227], v236 offset:18824\n"
"v_mfma_f32_16x16x32_bf16 v[164:167], v[220:223], a[208:211], v[164:167]\n"
"s_waitcnt lgkmcnt(8)\n"
"ds_read_b64_tr_b16 v[220:221], v236 offset:17288\n"
"ds_read_b64_tr_b16 v[222:223], v236 offset:19336\n"
"v_mfma_f32_16x16x32_bf16 v[144:147], v[228:231], a[224:227], v[144:147]\n"
"s_waitcnt lgkmcnt(8)\n"
"v_accvgpr_write_b32 a26, 0\n"
"v_mfma_f32_16x16x32_bf16 v[144:147], v[232:235], a[248:251], v[144:147]\n"
"buffer_load_dwordx4 v245, s[40:43], s91 offen lds\n"
"v_mfma_f32_16x16x32_bf16 v[148:151], v[232:235], a[252:255], v[148:151]\n"
"s_add_u32 s91, s78, 0x27400\n"
"ds_read_b64_tr_b16 v[232:233], v236 offset:16896\n"
"ds_read_b64_tr_b16 v[234:235], v236 offset:18944\n"
"v_mfma_f32_16x16x32_bf16 v[148:151], v[228:231], a[228:231], v[148:151]\n"
"s_waitcnt lgkmcnt(8)\n"
"ds_read_b64_tr_b16 v[228:229], v236 offset:17408\n"
"ds_read_b64_tr_b16 v[230:231], v236 offset:19456\n"
"v_mfma_f32_16x16x32_bf16 v[192:195], v[216:219], a[208:211], v[192:195]\n"
"s_waitcnt lgkmcnt(8)\n"
"v_accvgpr_write_b32 a27, 0\n"
"v_mfma_f32_16x16x32_bf16 v[192:195], v[212:215], a[232:235], v[192:195]\n"
"v_mfma_f32_16x16x32_bf16 v[200:203], v[212:215], a[236:239], v[200:203]\n"
"v_mfma_f32_16x16x32_bf16 v[200:203], v[216:219], a[212:215], v[200:203]\n"
"v_mfma_f32_16x16x32_bf16 a[80:83], v[216:219], a[216:219], a[80:83]\n"
"v_accvgpr_write_b32 a28, 0\n"
"v_mfma_f32_16x16x32_bf16 a[80:83], v[212:215], a[240:243], a[80:83]\n"
"v_mfma_f32_16x16x32_bf16 a[88:91], v[212:215], a[244:247], a[88:91]\n"
"ds_read_b64_tr_b16 v[212:213], v236 offset:16904\n"
"ds_read_b64_tr_b16 v[214:215], v236 offset:18952\n"
"v_mfma_f32_16x16x32_bf16 a[88:91], v[216:219], a[220:223], a[88:91]\n"
"s_waitcnt lgkmcnt(8)\n"
"ds_read_b64_tr_b16 v[216:217], v236 offset:17416\n"
"ds_read_b64_tr_b16 v[218:219], v236 offset:19464\n"
"v_mfma_f32_16x16x32_bf16 a[92:95], v[224:227], a[220:223], a[92:95]\n"
"s_waitcnt lgkmcnt(8)\n"
"v_mfma_f32_16x16x32_bf16 a[92:95], v[220:223], a[244:247], a[92:95]\n"
"v_accvgpr_write_b32 a29, 0\n"
"v_mfma_f32_16x16x32_bf16 a[84:87], v[220:223], a[240:243], a[84:87]\n"
"s_waitcnt vmcnt(14)\n"
"s_barrier\n"
"v_mfma_f32_16x16x32_bf16 a[84:87], v[224:227], a[216:219], a[84:87]\n"
"s_add_u32 m0, s69, 0x17400\n"
"ds_read_b128 a[160:163], v238 offset:36864\n"
"ds_read_b128 a[184:187], v242 offset:36864\n"
"v_mfma_f32_16x16x32_bf16 v[204:207], v[224:227], a[212:215], v[204:207]\n"
"buffer_load_dwordx4 v240, s[44:47], s92 offen lds\n"
"v_mfma_f32_16x16x32_bf16 v[204:207], v[220:223], a[236:239], v[204:207]\n"
"s_add_u32 s92, s78, 0x100\n"
"ds_read_b128 a[188:191], v242 offset:38912\n"
"ds_read_b128 a[164:167], v238 offset:38912\n"
"v_mfma_f32_16x16x32_bf16 v[196:199], v[220:223], a[232:235], v[196:199]\n"
"ds_read_b64_tr_b16 v[220:221], v237 offset:16648\n"
"ds_read_b64_tr_b16 v[222:223], v237 offset:18696\n"
"v_mfma_f32_16x16x32_bf16 v[196:199], v[224:227], a[208:211], v[196:199]\n"
"s_waitcnt lgkmcnt(12)\n"
"ds_read_b64_tr_b16 v[224:225], v237 offset:17160\n"
"ds_read_b64_tr_b16 v[226:227], v237 offset:19208\n"
"v_mfma_f32_16x16x32_bf16 a[96:99], v[232:235], a[208:211], a[96:99]\n"
"s_waitcnt lgkmcnt(12)\n"
"buffer_load_dwordx4 v240, s[44:47], s93 offen offset:1024 lds\n"
"v_mfma_f32_16x16x32_bf16 a[96:99], v[228:231], a[232:235], a[96:99]\n"
"s_add_u32 s93, s78, 0x1fd00\n"
"ds_read_b128 a[168:171], v238 offset:40960\n"
"ds_read_b128 a[192:195], v242 offset:40960\n"
"v_mfma_f32_16x16x32_bf16 a[104:107], v[228:231], a[236:239], a[104:107]\n"
"ds_read_b128 a[196:199], v242 offset:43008\n"
"ds_read_b128 a[172:175], v238 offset:43008\n"
"v_mfma_f32_16x16x32_bf16 a[104:107], v[232:235], a[212:215], a[104:107]\n"
"ds_read_b128 a[176:179], v239 offset:36864\n"
"ds_read_b128 a[200:203], v243 offset:36864\n"
"v_mfma_f32_16x16x32_bf16 a[112:115], v[232:235], a[216:219], a[112:115]\n"
"buffer_load_dwordx4 v240, s[44:47], s94 offen offset:2048 lds\n"
"v_mfma_f32_16x16x32_bf16 a[112:115], v[228:231], a[240:243], a[112:115]\n"
"s_add_u32 s94, s78, 0x7900\n"
"ds_read_b128 a[204:207], v243 offset:38912\n"
"ds_read_b128 a[180:183], v239 offset:38912\n"
"v_mfma_f32_16x16x32_bf16 a[120:123], v[228:231], a[244:247], a[120:123]\n"
"ds_read_b64_tr_b16 v[228:229], v236 offset:17024\n"
"ds_read_b64_tr_b16 v[230:231], v236 offset:19072\n"
"v_mfma_f32_16x16x32_bf16 a[120:123], v[232:235], a[220:223], a[120:123]\n"
"s_waitcnt lgkmcnt(15)\n"
"ds_read_b64_tr_b16 v[232:233], v236 offset:17536\n"
"ds_read_b64_tr_b16 v[234:235], v236 offset:19584\n"
"v_mfma_f32_16x16x32_bf16 a[124:127], v[212:215], a[220:223], a[124:127]\n"
"buffer_load_dwordx4 v240, s[44:47], s91 offen offset:3072 lds\n"
"v_mfma_f32_16x16x32_bf16 a[124:127], v[216:219], a[244:247], a[124:127]\n"
"s_add_u32 s91, s78, 0x27500\n"
"v_accvgpr_write_b32 a30, 0\n"
"v_accvgpr_write_b32 a31, 0\n"
"v_mfma_f32_16x16x32_bf16 a[116:119], v[216:219], a[240:243], a[116:119]\n"
"s_add_u32 s78, s78, 0x80000\n"
"s_cmp_eq_u32 s78, 0x380000\n"
"s_cselect_b32 s78, 0, s78\n"
"v_accvgpr_write_b32 a32, 0\n"
"v_mfma_f32_16x16x32_bf16 a[116:119], v[212:215], a[216:219], a[116:119]\n"
"v_mfma_f32_16x16x32_bf16 a[108:111], v[212:215], a[212:215], a[108:111]\n"
"v_mfma_f32_16x16x32_bf16 a[108:111], v[216:219], a[236:239], a[108:111]\n"
"v_accvgpr_write_b32 a33, 0\n"
"v_mfma_f32_16x16x32_bf16 a[100:103], v[216:219], a[232:235], a[100:103]\n"
"ds_read_b64_tr_b16 v[216:217], v236 offset:17032\n"
"ds_read_b64_tr_b16 v[218:219], v236 offset:19080\n"
"v_mfma_f32_16x16x32_bf16 a[100:103], v[212:215], a[208:211], a[100:103]\n"
"s_waitcnt lgkmcnt(15)\n"
"ds_read_b64_tr_b16 v[212:213], v236 offset:17544\n"
"ds_read_b64_tr_b16 v[214:215], v236 offset:19592\n"
"v_mfma_f32_16x16x32_bf16 v[152:155], v[220:223], a[224:227], v[152:155]\n"
"s_waitcnt lgkmcnt(15)\n"
"v_mfma_f32_16x16x32_bf16 v[152:155], v[224:227], a[248:251], v[152:155]\n"
"v_mfma_f32_16x16x32_bf16 v[156:159], v[224:227], a[252:255], v[156:159]\n"
"ds_read_b64_tr_b16 v[224:225], v236 offset:33280\n"
"ds_read_b64_tr_b16 v[226:227], v236 offset:35328\n"
"v_mfma_f32_16x16x32_bf16 v[156:159], v[220:223], a[228:231], v[156:159]\n"
"s_waitcnt lgkmcnt(8)\n"
"ds_read_b64_tr_b16 v[220:221], v236 offset:33792\n"
"ds_read_b64_tr_b16 v[222:223], v236 offset:35840\n"
"v_mfma_f32_16x16x32_bf16 a[128:131], v[228:231], a[208:211], a[128:131]\n"
"s_waitcnt lgkmcnt(8)\n"
"v_accvgpr_write_b32 a34, 0\n"
"v_mfma_f32_16x16x32_bf16 a[128:131], v[232:235], a[232:235], a[128:131]\n"
"v_mfma_f32_16x16x32_bf16 a[136:139], v[232:235], a[236:239], a[136:139]\n"
"v_mfma_f32_16x16x32_bf16 a[136:139], v[228:231], a[212:215], a[136:139]\n"
"v_accvgpr_write_b32 a35, 0\n"
"v_mfma_f32_16x16x32_bf16 a[144:147], v[228:231], a[216:219], a[144:147]\n"
"v_mfma_f32_16x16x32_bf16 a[144:147], v[232:235], a[240:243], a[144:147]\n"
"v_mfma_f32_16x16x32_bf16 a[152:155], v[232:235], a[244:247], a[152:155]\n"
"ds_read_b64_tr_b16 v[232:233], v236 offset:33288\n"
"ds_read_b64_tr_b16 v[234:235], v236 offset:35336\n"
"v_mfma_f32_16x16x32_bf16 a[152:155], v[228:231], a[220:223], a[152:155]\n"
"s_waitcnt lgkmcnt(8)\n"
"ds_read_b64_tr_b16 v[228:229], v236 offset:33800\n"
"ds_read_b64_tr_b16 v[230:231], v236 offset:35848\n"
"v_mfma_f32_16x16x32_bf16 a[156:159], v[216:219], a[220:223], a[156:159]\n"
"s_waitcnt lgkmcnt(8)\n"
"v_accvgpr_write_b32 a36, 0\n"
"v_mfma_f32_16x16x32_bf16 a[156:159], v[212:215], a[244:247], a[156:159]\n"
"v_mfma_f32_16x16x32_bf16 a[148:151], v[212:215], a[240:243], a[148:151]\n"
"v_mfma_f32_16x16x32_bf16 a[148:151], v[216:219], a[216:219], a[148:151]\n"
"v_mfma_f32_16x16x32_bf16 a[140:143], v[216:219], a[212:215], a[140:143]\n"
"v_mfma_f32_16x16x32_bf16 a[140:143], v[212:215], a[236:239], a[140:143]\n"
"v_accvgpr_write_b32 a37, 0\n"
"v_mfma_f32_16x16x32_bf16 a[132:135], v[212:215], a[232:235], a[132:135]\n"
"ds_read_b64_tr_b16 v[212:213], v237 offset:33280\n"
"ds_read_b64_tr_b16 v[214:215], v237 offset:35328\n"
"v_mfma_f32_16x16x32_bf16 a[132:135], v[216:219], a[208:211], a[132:135]\n"
"s_waitcnt lgkmcnt(8)\n"
"ds_read_b64_tr_b16 v[216:217], v237 offset:33792\n"
"ds_read_b64_tr_b16 v[218:219], v237 offset:35840\n"
"v_mfma_f32_16x16x32_bf16 v[0:3], v[224:227], a[160:163], v[0:3]\n"
"s_waitcnt lgkmcnt(8)\n"
"v_mfma_f32_16x16x32_bf16 v[0:3], v[220:223], a[184:187], v[0:3]\n"
"v_mfma_f32_16x16x32_bf16 v[8:11], v[220:223], a[188:191], v[8:11]\n"
"v_accvgpr_write_b32 a38, 0\n"
"v_mfma_f32_16x16x32_bf16 v[8:11], v[224:227], a[164:167], v[8:11]\n"
"v_mfma_f32_16x16x32_bf16 v[16:19], v[224:227], a[168:171], v[16:19]\n"
"v_mfma_f32_16x16x32_bf16 v[16:19], v[220:223], a[192:195], v[16:19]\n"
"v_mfma_f32_16x16x32_bf16 v[24:27], v[220:223], a[196:199], v[24:27]\n"
"ds_read_b64_tr_b16 v[220:221], v236 offset:33408\n"
"ds_read_b64_tr_b16 v[222:223], v236 offset:35456\n"
"v_mfma_f32_16x16x32_bf16 v[24:27], v[224:227], a[172:175], v[24:27]\n"
"s_waitcnt lgkmcnt(8)\n"
"ds_read_b64_tr_b16 v[224:225], v236 offset:33920\n"
"ds_read_b64_tr_b16 v[226:227], v236 offset:35968\n"
"v_mfma_f32_16x16x32_bf16 v[28:31], v[232:235], a[172:175], v[28:31]\n"
"s_waitcnt lgkmcnt(8)\n"
"v_accvgpr_write_b32 a39, 0\n"
"v_mfma_f32_16x16x32_bf16 v[28:31], v[228:231], a[196:199], v[28:31]\n"
"v_mfma_f32_16x16x32_bf16 v[20:23], v[228:231], a[192:195], v[20:23]\n"
"v_mfma_f32_16x16x32_bf16 v[20:23], v[232:235], a[168:171], v[20:23]\n"
"v_mfma_f32_16x16x32_bf16 v[12:15], v[232:235], a[164:167], v[12:15]\n"
"v_accvgpr_write_b32 a40, 0\n"
"v_mfma_f32_16x16x32_bf16 v[12:15], v[228:231], a[188:191], v[12:15]\n"
"v_mfma_f32_16x16x32_bf16 v[4:7], v[228:231], a[184:187], v[4:7]\n"
"ds_read_b64_tr_b16 v[228:229], v236 offset:33416\n"
"ds_read_b64_tr_b16 v[230:231], v236 offset:35464\n"
"v_mfma_f32_16x16x32_bf16 v[4:7], v[232:235], a[160:163], v[4:7]\n"
"s_waitcnt lgkmcnt(8)\n"
"ds_read_b64_tr_b16 v[232:233], v236 offset:33928\n"
"ds_read_b64_tr_b16 v[234:235], v236 offset:35976\n"
"v_mfma_f32_16x16x32_bf16 v[128:131], v[212:215], a[176:179], v[128:131]\n"
"s_waitcnt lgkmcnt(8)\n"
"v_mfma_f32_16x16x32_bf16 v[128:131], v[216:219], a[200:203], v[128:131]\n"
"v_accvgpr_write_b32 a41, 0\n"
"v_mfma_f32_16x16x32_bf16 v[132:135], v[216:219], a[204:207], v[132:135]\n"
"ds_read_b64_tr_b16 v[216:217], v236 offset:33536\n"
"ds_read_b64_tr_b16 v[218:219], v236 offset:35584\n"
"v_mfma_f32_16x16x32_bf16 v[132:135], v[212:215], a[180:183], v[132:135]\n"
"s_waitcnt lgkmcnt(8)\n"
"ds_read_b64_tr_b16 v[212:213], v236 offset:34048\n"
"ds_read_b64_tr_b16 v[214:215], v236 offset:36096\n"
"v_mfma_f32_16x16x32_bf16 v[32:35], v[220:223], a[160:163], v[32:35]\n"
"s_waitcnt lgkmcnt(8)\n"
"v_mfma_f32_16x16x32_bf16 v[32:35], v[224:227], a[184:187], v[32:35]\n"
"v_mfma_f32_16x16x32_bf16 v[40:43], v[224:227], a[188:191], v[40:43]\n"
"v_accvgpr_write_b32 a42, 0\n"
"v_mfma_f32_16x16x32_bf16 v[40:43], v[220:223], a[164:167], v[40:43]\n"
"v_mfma_f32_16x16x32_bf16 v[48:51], v[220:223], a[168:171], v[48:51]\n"
"v_mfma_f32_16x16x32_bf16 v[48:51], v[224:227], a[192:195], v[48:51]\n"
"v_mfma_f32_16x16x32_bf16 v[56:59], v[224:227], a[196:199], v[56:59]\n"
"ds_read_b64_tr_b16 v[224:225], v236 offset:33544\n"
"ds_read_b64_tr_b16 v[226:227], v236 offset:35592\n"
"v_mfma_f32_16x16x32_bf16 v[56:59], v[220:223], a[172:175], v[56:59]\n"
"s_waitcnt lgkmcnt(8)\n"
"ds_read_b64_tr_b16 v[220:221], v236 offset:34056\n"
"ds_read_b64_tr_b16 v[222:223], v236 offset:36104\n"
"v_mfma_f32_16x16x32_bf16 v[60:63], v[228:231], a[172:175], v[60:63]\n"
"s_waitcnt lgkmcnt(8)\n"
"v_accvgpr_write_b32 a43, 0\n"
"v_mfma_f32_16x16x32_bf16 v[60:63], v[232:235], a[196:199], v[60:63]\n"
"v_mfma_f32_16x16x32_bf16 v[52:55], v[232:235], a[192:195], v[52:55]\n"
"s_waitcnt vmcnt(13)\n"
"s_barrier\n"
"v_mfma_f32_16x16x32_bf16 v[52:55], v[228:231], a[168:171], v[52:55]\n"
"s_add_u32 m0, s69, 0x1b500\n"
"v_mfma_f32_16x16x32_bf16 v[44:47], v[228:231], a[164:167], v[44:47]\n"
"buffer_load_dwordx4 v240, s[44:47], s92 offen lds\n"
"v_mfma_f32_16x16x32_bf16 v[44:47], v[232:235], a[188:191], v[44:47]\n"
"s_add_u32 s92, s80, 0\n"
"v_accvgpr_write_b32 a44, 0\n"
"v_mfma_f32_16x16x32_bf16 v[36:39], v[232:235], a[184:187], v[36:39]\n"
"Lpada_1:\n"
"s_cbranch_execz Lpada_5\n"
"ds_read_b64_tr_b16 v[232:233], v237 offset:33288\n"
"ds_read_b64_tr_b16 v[234:235], v237 offset:35336\n"
"v_mfma_f32_16x16x32_bf16 v[36:39], v[228:231], a[160:163], v[36:39]\n"
"s_waitcnt lgkmcnt(8)\n"
"ds_read_b64_tr_b16 v[228:229], v237 offset:33800\n"
"ds_read_b64_tr_b16 v[230:231], v237 offset:35848\n"
"v_mfma_f32_16x16x32_bf16 v[64:67], v[216:219], a[160:163], v[64:67]\n"
"s_waitcnt lgkmcnt(8)\n"
"buffer_load_dwordx4 v240, s[44:47], s93 offen offset:1024 lds\n"
"v_mfma_f32_16x16x32_bf16 v[64:67], v[212:215], a[184:187], v[64:67]\n"
"s_add_u32 s93, s80, 0x1800\n"
"v_accvgpr_write_b32 a45, 0\n"
"v_mfma_f32_16x16x32_bf16 v[72:75], v[212:215], a[188:191], v[72:75]\n"
"v_mfma_f32_16x16x32_bf16 v[72:75], v[216:219], a[164:167], v[72:75]\n"
"v_mfma_f32_16x16x32_bf16 v[80:83], v[216:219], a[168:171], v[80:83]\n"
"buffer_load_dwordx4 v240, s[44:47], s94 offen offset:2048 lds\n"
"v_mfma_f32_16x16x32_bf16 v[80:83], v[212:215], a[192:195], v[80:83]\n"
"s_add_u32 s94, s80, 0x3000\n"
"v_mfma_f32_16x16x32_bf16 v[88:91], v[212:215], a[196:199], v[88:91]\n"
"ds_read_b64_tr_b16 v[212:213], v236 offset:33664\n"
"ds_read_b64_tr_b16 v[214:215], v236 offset:35712\n"
"v_mfma_f32_16x16x32_bf16 v[88:91], v[216:219], a[172:175], v[88:91]\n"
"s_waitcnt lgkmcnt(8)\n"
"ds_read_b64_tr_b16 v[216:217], v236 offset:34176\n"
"ds_read_b64_tr_b16 v[218:219], v236 offset:36224\n"
"v_mfma_f32_16x16x32_bf16 v[92:95], v[224:227], a[172:175], v[92:95]\n"
"s_waitcnt lgkmcnt(8)\n"
"buffer_load_dwordx4 v240, s[44:47], s91 offen offset:3072 lds\n"
"v_mfma_f32_16x16x32_bf16 v[92:95], v[220:223], a[196:199], v[92:95]\n"
"s_add_u32 s91, s80, 0x4800\n"
"v_accvgpr_write_b32 a46, 0\n"
"v_mfma_f32_16x16x32_bf16 v[84:87], v[220:223], a[192:195], v[84:87]\n"
"s_add_u32 s80, s79, s25\n"
"v_mfma_f32_16x16x32_bf16 v[84:87], v[224:227], a[168:171], v[84:87]\n"
"v_accvgpr_write_b32 a47, 0\n"
"v_mfma_f32_16x16x32_bf16 v[76:79], v[224:227], a[164:167], v[76:79]\n"
"s_add_u32 m0, s90, 0xe400\n"
"v_mfma_f32_16x16x32_bf16 v[76:79], v[220:223], a[188:191], v[76:79]\n"
"buffer_load_dwordx4 v241, s[40:43], s92 offen lds\n"
"v_mfma_f32_16x16x32_bf16 v[68:71], v[220:223], a[184:187], v[68:71]\n"
"s_add_u32 s92, s81, 0x7000\n"
"ds_read_b64_tr_b16 v[220:221], v236 offset:33672\n"
"ds_read_b64_tr_b16 v[222:223], v236 offset:35720\n"
"v_mfma_f32_16x16x32_bf16 v[68:71], v[224:227], a[160:163], v[68:71]\n"
"s_waitcnt lgkmcnt(8)\n"
"ds_read_b64_tr_b16 v[224:225], v236 offset:34184\n"
"ds_read_b64_tr_b16 v[226:227], v236 offset:36232\n"
"v_mfma_f32_16x16x32_bf16 v[136:139], v[232:235], a[176:179], v[136:139]\n"
"s_waitcnt lgkmcnt(8)\n"
"v_accvgpr_write_b32 a48, 0\n"
"v_mfma_f32_16x16x32_bf16 v[136:139], v[228:231], a[200:203], v[136:139]\n"
"v_mfma_f32_16x16x32_bf16 v[140:143], v[228:231], a[204:207], v[140:143]\n"
"buffer_load_dwordx4 v244, s[40:43], s93 offen offset:1024 lds\n"
"v_mfma_f32_16x16x32_bf16 v[140:143], v[232:235], a[180:183], v[140:143]\n"
"s_waitcnt lgkmcnt(6)\n"
"ds_read_b64_tr_b16 v[228:229], v236 offset:49920\n"
"ds_read_b64_tr_b16 v[230:231], v236 offset:51968\n"
"v_mfma_f32_16x16x32_bf16 v[96:99], v[212:215], a[160:163], v[96:99]\n"
"s_waitcnt lgkmcnt(6)\n"
"ds_read_b64_tr_b16 v[232:233], v236 offset:50432\n"
"ds_read_b64_tr_b16 v[234:235], v236 offset:52480\n"
"v_mfma_f32_16x16x32_bf16 v[96:99], v[216:219], a[184:187], v[96:99]\n"
"s_add_u32 s93, s81, 0x8800\n"
"v_accvgpr_write_b32 a49, 0\n"
"v_mfma_f32_16x16x32_bf16 v[104:107], v[216:219], a[188:191], v[104:107]\n"
"v_mfma_f32_16x16x32_bf16 v[104:107], v[212:215], a[164:167], v[104:107]\n"
"buffer_load_dwordx4 v241, s[40:43], s94 offen offset:2048 lds\n"
"v_mfma_f32_16x16x32_bf16 v[112:115], v[212:215], a[168:171], v[112:115]\n"
"s_add_u32 s94, s81, 0xa000\n"
"v_mfma_f32_16x16x32_bf16 v[112:115], v[216:219], a[192:195], v[112:115]\n"
"v_mfma_f32_16x16x32_bf16 v[120:123], v[216:219], a[196:199], v[120:123]\n"
"ds_read_b64_tr_b16 v[216:217], v236 offset:49928\n"
"ds_read_b64_tr_b16 v[218:219], v236 offset:51976\n"
"v_mfma_f32_16x16x32_bf16 v[120:123], v[212:215], a[172:175], v[120:123]\n"
"s_waitcnt lgkmcnt(8)\n"
"ds_read_b64_tr_b16 v[212:213], v236 offset:50440\n"
"ds_read_b64_tr_b16 v[214:215], v236 offset:52488\n"
"v_mfma_f32_16x16x32_bf16 v[124:127], v[220:223], a[172:175], v[124:127]\n"
"s_waitcnt lgkmcnt(8)\n"
"buffer_load_dwordx4 v244, s[40:43], s91 offen offset:3072 lds\n"
"v_mfma_f32_16x16x32_bf16 v[124:127], v[224:227], a[196:199], v[124:127]\n"
"s_add_u32 m0, s90, 0xf400\n"
"v_accvgpr_write_b32 a50, 0\n"
"v_mfma_f32_16x16x32_bf16 v[116:119], v[224:227], a[192:195], v[116:119]\n"
"s_add_u32 s91, s81, 0xb800\n"
"v_mfma_f32_16x16x32_bf16 v[116:119], v[220:223], a[168:171], v[116:119]\n"
"s_sub_u32 s81, s79, s25\n"
"v_accvgpr_write_b32 a51, 0\n"
"v_mfma_f32_16x16x32_bf16 v[108:111], v[220:223], a[164:167], v[108:111]\n"
"v_mfma_f32_16x16x32_bf16 v[108:111], v[224:227], a[188:191], v[108:111]\n"
"buffer_load_dwordx4 v241, s[40:43], s92 offen lds\n"
"v_mfma_f32_16x16x32_bf16 v[100:103], v[224:227], a[184:187], v[100:103]\n"
"s_mov_b32 s92, s75\n"
"ds_read_b64_tr_b16 v[224:225], v237 offset:49920\n"
"ds_read_b64_tr_b16 v[226:227], v237 offset:51968\n"
"v_mfma_f32_16x16x32_bf16 v[100:103], v[220:223], a[160:163], v[100:103]\n"
"s_waitcnt lgkmcnt(8)\n"
"ds_read_b64_tr_b16 v[220:221], v237 offset:50432\n"
"ds_read_b64_tr_b16 v[222:223], v237 offset:52480\n"
"v_mfma_f32_16x16x32_bf16 v[160:163], v[228:231], a[160:163], v[160:163]\n"
"s_waitcnt lgkmcnt(8)\n"
"v_accvgpr_write_b32 a52, 0\n"
"v_mfma_f32_16x16x32_bf16 v[160:163], v[232:235], a[184:187], v[160:163]\n"
"s_add_u32 s75, s79, s83\n"
"v_mfma_f32_16x16x32_bf16 v[168:171], v[232:235], a[188:191], v[168:171]\n"
"s_add_u32 s79, s79, 0x80\n"
"s_cmp_eq_u32 s79, 0x380\n"
"s_cselect_b32 s79, 0, s79\n"
"buffer_load_dwordx4 v244, s[40:43], s93 offen offset:1024 lds\n"
"v_mfma_f32_16x16x32_bf16 v[168:171], v[228:231], a[164:167], v[168:171]\n"
"s_add_u32 s93, s78, 0\n"
"v_mfma_f32_16x16x32_bf16 v[176:179], v[228:231], a[168:171], v[176:179]\n"
"v_mfma_f32_16x16x32_bf16 v[176:179], v[232:235], a[192:195], v[176:179]\n"
"v_accvgpr_write_b32 a53, 0\n"
"v_mfma_f32_16x16x32_bf16 v[184:187], v[232:235], a[196:199], v[184:187]\n"
"ds_read_b64_tr_b16 v[232:233], v236 offset:50048\n"
"ds_read_b64_tr_b16 v[234:235], v236 offset:52096\n"
"v_mfma_f32_16x16x32_bf16 v[184:187], v[228:231], a[172:175], v[184:187]\n"
"s_waitcnt lgkmcnt(8)\n"
"buffer_load_dwordx4 v241, s[40:43], s94 offen offset:2048 lds\n"
"v_mfma_f32_16x16x32_bf16 v[188:191], v[216:219], a[172:175], v[188:191]\n"
"s_waitcnt lgkmcnt(6)\n"
"ds_read_b64_tr_b16 v[228:229], v236 offset:50560\n"
"ds_read_b64_tr_b16 v[230:231], v236 offset:52608\n"
"v_mfma_f32_16x16x32_bf16 v[188:191], v[212:215], a[196:199], v[188:191]\n"
"s_add_u32 s94, s78, 0x1fc00\n"
"v_mfma_f32_16x16x32_bf16 v[180:183], v[212:215], a[192:195], v[180:183]\n"
"v_accvgpr_write_b32 a54, 0\n"
"v_mfma_f32_16x16x32_bf16 v[180:183], v[216:219], a[168:171], v[180:183]\n"
"v_mfma_f32_16x16x32_bf16 v[172:175], v[216:219], a[164:167], v[172:175]\n"
"buffer_load_dwordx4 v244, s[40:43], s91 offen offset:3072 lds\n"
"v_mfma_f32_16x16x32_bf16 v[172:175], v[212:215], a[188:191], v[172:175]\n"
"s_add_u32 m0, s84, 0xe400\n"
"v_mfma_f32_16x16x32_bf16 v[164:167], v[212:215], a[184:187], v[164:167]\n"
"s_add_u32 s91, s78, 0x7800\n"
"ds_read_b64_tr_b16 v[212:213], v236 offset:50056\n"
"ds_read_b64_tr_b16 v[214:215], v236 offset:52104\n"
"v_mfma_f32_16x16x32_bf16 v[164:167], v[216:219], a[160:163], v[164:167]\n"
"s_waitcnt lgkmcnt(8)\n"
"ds_read_b64_tr_b16 v[216:217], v236 offset:50568\n"
"ds_read_b64_tr_b16 v[218:219], v236 offset:52616\n"
"v_mfma_f32_16x16x32_bf16 v[144:147], v[224:227], a[176:179], v[144:147]\n"
"s_waitcnt lgkmcnt(8)\n"
"v_accvgpr_write_b32 a55, 0\n"
"v_mfma_f32_16x16x32_bf16 v[144:147], v[220:223], a[200:203], v[144:147]\n"
"buffer_load_dwordx4 v245, s[40:43], s92 offen lds\n"
"v_mfma_f32_16x16x32_bf16 v[148:151], v[220:223], a[204:207], v[148:151]\n"
"s_add_u32 s92, s78, 0x27400\n"
"ds_read_b64_tr_b16 v[220:221], v236 offset:50176\n"
"ds_read_b64_tr_b16 v[222:223], v236 offset:52224\n"
"v_mfma_f32_16x16x32_bf16 v[148:151], v[224:227], a[180:183], v[148:151]\n"
"s_waitcnt lgkmcnt(8)\n"
"ds_read_b64_tr_b16 v[224:225], v236 offset:50688\n"
"ds_read_b64_tr_b16 v[226:227], v236 offset:52736\n"
"v_mfma_f32_16x16x32_bf16 v[192:195], v[232:235], a[160:163], v[192:195]\n"
"s_waitcnt lgkmcnt(8)\n"
"v_accvgpr_write_b32 a56, 0\n"
"v_mfma_f32_16x16x32_bf16 v[192:195], v[228:231], a[184:187], v[192:195]\n"
"v_mfma_f32_16x16x32_bf16 v[200:203], v[228:231], a[188:191], v[200:203]\n"
"v_mfma_f32_16x16x32_bf16 v[200:203], v[232:235], a[164:167], v[200:203]\n"
"v_mfma_f32_16x16x32_bf16 a[80:83], v[232:235], a[168:171], a[80:83]\n"
"v_mfma_f32_16x16x32_bf16 a[80:83], v[228:231], a[192:195], a[80:83]\n"
"v_accvgpr_write_b32 a57, 0\n"
"v_mfma_f32_16x16x32_bf16 a[88:91], v[228:231], a[196:199], a[88:91]\n"
"ds_read_b64_tr_b16 v[228:229], v236 offset:50184\n"
"ds_read_b64_tr_b16 v[230:231], v236 offset:52232\n"
"v_mfma_f32_16x16x32_bf16 a[88:91], v[232:235], a[172:175], a[88:91]\n"
"s_waitcnt lgkmcnt(8)\n"
"ds_read_b64_tr_b16 v[232:233], v236 offset:50696\n"
"ds_read_b64_tr_b16 v[234:235], v236 offset:52744\n"
"v_mfma_f32_16x16x32_bf16 a[92:95], v[212:215], a[172:175], a[92:95]\n"
"s_waitcnt lgkmcnt(8)\n"
"v_mfma_f32_16x16x32_bf16 a[92:95], v[216:219], a[196:199], a[92:95]\n"
"v_mfma_f32_16x16x32_bf16 a[84:87], v[216:219], a[192:195], a[84:87]\n"
"s_waitcnt vmcnt(13)\n"
"s_barrier\n"
"v_mfma_f32_16x16x32_bf16 a[84:87], v[212:215], a[168:171], a[84:87]\n"
"s_add_u32 m0, s69, 0x1f600\n"
"ds_read_b128 a[208:211], v238 offset:0\n"
"ds_read_b128 a[232:235], v242 offset:0\n"
"v_mfma_f32_16x16x32_bf16 v[204:207], v[212:215], a[164:167], v[204:207]\n"
"buffer_load_dwordx4 v240, s[44:47], s93 offen lds\n"
"v_mfma_f32_16x16x32_bf16 v[204:207], v[216:219], a[188:191], v[204:207]\n"
"s_add_u32 s93, s78, 0x100\n"
"ds_read_b128 a[236:239], v242 offset:2048\n"
"ds_read_b128 a[212:215], v238 offset:2048\n"
"v_mfma_f32_16x16x32_bf16 v[196:199], v[216:219], a[184:187], v[196:199]\n"
"ds_read_b64_tr_b16 v[216:217], v237 offset:49928\n"
"ds_read_b64_tr_b16 v[218:219], v237 offset:51976\n"
"v_mfma_f32_16x16x32_bf16 v[196:199], v[212:215], a[160:163], v[196:199]\n"
"s_waitcnt lgkmcnt(12)\n"
"ds_read_b64_tr_b16 v[212:213], v237 offset:50440\n"
"ds_read_b64_tr_b16 v[214:215], v237 offset:52488\n"
"v_mfma_f32_16x16x32_bf16 a[96:99], v[220:223], a[160:163], a[96:99]\n"
"s_waitcnt lgkmcnt(12)\n"
"buffer_load_dwordx4 v240, s[44:47], s94 offen offset:1024 lds\n"
"v_mfma_f32_16x16x32_bf16 a[96:99], v[224:227], a[184:187], a[96:99]\n"
"s_add_u32 s94, s78, 0x1fd00\n"
"ds_read_b128 a[216:219], v238 offset:4096\n"
"ds_read_b128 a[240:243], v242 offset:4096\n"
"v_mfma_f32_16x16x32_bf16 a[104:107], v[224:227], a[188:191], a[104:107]\n"
"ds_read_b128 a[244:247], v242 offset:6144\n"
"ds_read_b128 a[220:223], v238 offset:6144\n"
"v_mfma_f32_16x16x32_bf16 a[104:107], v[220:223], a[164:167], a[104:107]\n"
"ds_read_b128 a[224:227], v239 offset:0\n"
"ds_read_b128 a[248:251], v243 offset:0\n"
"v_mfma_f32_16x16x32_bf16 a[112:115], v[220:223], a[168:171], a[112:115]\n"
"buffer_load_dwordx4 v240, s[44:47], s91 offen offset:2048 lds\n"
"v_mfma_f32_16x16x32_bf16 a[112:115], v[224:227], a[192:195], a[112:115]\n"
"s_add_u32 s91, s78, 0x7900\n"
"ds_read_b128 a[252:255], v243 offset:2048\n"
"ds_read_b128 a[228:231], v239 offset:2048\n"
"v_mfma_f32_16x16x32_bf16 a[120:123], v[224:227], a[196:199], a[120:123]\n"
"ds_read_b64_tr_b16 v[224:225], v236 offset:50304\n"
"ds_read_b64_tr_b16 v[226:227], v236 offset:52352\n"
"v_mfma_f32_16x16x32_bf16 a[120:123], v[220:223], a[172:175], a[120:123]\n"
"s_waitcnt lgkmcnt(15)\n"
"ds_read_b64_tr_b16 v[220:221], v236 offset:50816\n"
"ds_read_b64_tr_b16 v[222:223], v236 offset:52864\n"
"v_mfma_f32_16x16x32_bf16 a[124:127], v[228:231], a[172:175], a[124:127]\n"
"buffer_load_dwordx4 v240, s[44:47], s92 offen offset:3072 lds\n"
"v_mfma_f32_16x16x32_bf16 a[124:127], v[232:235], a[196:199], a[124:127]\n"
"s_add_u32 s92, s78, 0x27500\n"
"v_accvgpr_write_b32 a58, 0\n"
"v_accvgpr_write_b32 a59, 0\n"
"v_mfma_f32_16x16x32_bf16 a[116:119], v[232:235], a[192:195], a[116:119]\n"
"s_add_u32 s78, s78, 0x80000\n"
"s_cmp_eq_u32 s78, 0x380000\n"
"s_cselect_b32 s78, 0, s78\n"
"v_accvgpr_write_b32 a60, 0\n"
"v_accvgpr_write_b32 a61, 0\n"
"v_mfma_f32_16x16x32_bf16 a[116:119], v[228:231], a[168:171], a[116:119]\n"
"v_mfma_f32_16x16x32_bf16 a[108:111], v[228:231], a[164:167], a[108:111]\n"
"v_mfma_f32_16x16x32_bf16 a[108:111], v[232:235], a[188:191], a[108:111]\n"
"v_mfma_f32_16x16x32_bf16 a[100:103], v[232:235], a[184:187], a[100:103]\n"
"ds_read_b64_tr_b16 v[232:233], v236 offset:50312\n"
"ds_read_b64_tr_b16 v[234:235], v236 offset:52360\n"
"v_mfma_f32_16x16x32_bf16 a[100:103], v[228:231], a[160:163], a[100:103]\n"
"s_waitcnt lgkmcnt(15)\n"
"ds_read_b64_tr_b16 v[228:229], v236 offset:50824\n"
"ds_read_b64_tr_b16 v[230:231], v236 offset:52872\n"
"v_mfma_f32_16x16x32_bf16 v[152:155], v[216:219], a[176:179], v[152:155]\n"
"s_waitcnt lgkmcnt(15)\n"
"v_accvgpr_write_b32 a62, 0\n"
"v_mfma_f32_16x16x32_bf16 v[152:155], v[212:215], a[200:203], v[152:155]\n"
"v_mfma_f32_16x16x32_bf16 v[156:159], v[212:215], a[204:207], v[156:159]\n"
"ds_read_b64_tr_b16 v[212:213], v236 offset:0\n"
"ds_read_b64_tr_b16 v[214:215], v236 offset:2048\n"
"v_mfma_f32_16x16x32_bf16 v[156:159], v[216:219], a[180:183], v[156:159]\n"
"s_waitcnt lgkmcnt(8)\n"
"ds_read_b64_tr_b16 v[216:217], v236 offset:512\n"
"ds_read_b64_tr_b16 v[218:219], v236 offset:2560\n"
"v_mfma_f32_16x16x32_bf16 a[128:131], v[224:227], a[160:163], a[128:131]\n"
"s_waitcnt lgkmcnt(8)\n"
"v_accvgpr_write_b32 a63, 0\n"
"v_mfma_f32_16x16x32_bf16 a[128:131], v[220:223], a[184:187], a[128:131]\n"
"v_mfma_f32_16x16x32_bf16 a[136:139], v[220:223], a[188:191], a[136:139]\n"
"v_mfma_f32_16x16x32_bf16 a[136:139], v[224:227], a[164:167], a[136:139]\n"
"v_mfma_f32_16x16x32_bf16 a[144:147], v[224:227], a[168:171], a[144:147]\n"
"v_readfirstlane_b32 s29, v209\n"
"v_accvgpr_write_b32 a64, 0\n"
"v_mfma_f32_16x16x32_bf16 a[144:147], v[220:223], a[192:195], a[144:147]\n"
"s_add_u32 s29, s29, 1\n"
"v_mov_b32 v209, s29\n"
"v_mfma_f32_16x16x32_bf16 a[152:155], v[220:223], a[196:199], a[152:155]\n"
"ds_read_b64_tr_b16 v[220:221], v236 offset:8\n"
"ds_read_b64_tr_b16 v[222:223], v236 offset:2056\n"
"v_mfma_f32_16x16x32_bf16 a[152:155], v[224:227], a[172:175], a[152:155]\n"
"s_waitcnt lgkmcnt(8)\n"
"ds_read_b64_tr_b16 v[224:225], v236 offset:520\n"
"ds_read_b64_tr_b16 v[226:227], v236 offset:2568\n"
"v_mfma_f32_16x16x32_bf16 a[156:159], v[232:235], a[172:175], a[156:159]\n"
"s_waitcnt lgkmcnt(8)\n"
"buffer_store_dword v209, v208, s[60:63], s38 offen offset:128 sc1\n"
"v_mfma_f32_16x16x32_bf16 a[156:159], v[228:231], a[196:199], a[156:159]\n"
"v_accvgpr_write_b32 a65, 0\n"
"v_mfma_f32_16x16x32_bf16 a[148:151], v[228:231], a[192:195], a[148:151]\n"
"v_mfma_f32_16x16x32_bf16 a[148:151], v[232:235], a[168:171], a[148:151]\n"
"v_mfma_f32_16x16x32_bf16 a[140:143], v[232:235], a[164:167], a[140:143]\n"
"v_mfma_f32_16x16x32_bf16 a[140:143], v[228:231], a[188:191], a[140:143]\n"
"v_mfma_f32_16x16x32_bf16 a[132:135], v[228:231], a[184:187], a[132:135]\n"
"ds_read_b64_tr_b16 v[228:229], v237 offset:0\n"
"ds_read_b64_tr_b16 v[230:231], v237 offset:2048\n"
"v_mfma_f32_16x16x32_bf16 a[132:135], v[232:235], a[160:163], a[132:135]\n"
"s_waitcnt lgkmcnt(8)\n"
"ds_read_b64_tr_b16 v[232:233], v237 offset:512\n"
"ds_read_b64_tr_b16 v[234:235], v237 offset:2560\n"
"v_mfma_f32_16x16x32_bf16 v[0:3], v[212:215], a[208:211], v[0:3]\n"
"s_waitcnt lgkmcnt(8)\n"
"v_accvgpr_write_b32 a66, 0\n"
"v_mfma_f32_16x16x32_bf16 v[0:3], v[216:219], a[232:235], v[0:3]\n"
"v_mfma_f32_16x16x32_bf16 v[8:11], v[216:219], a[236:239], v[8:11]\n"
"v_mfma_f32_16x16x32_bf16 v[8:11], v[212:215], a[212:215], v[8:11]\n"
"v_accvgpr_write_b32 a67, 0\n"
"v_mfma_f32_16x16x32_bf16 v[16:19], v[212:215], a[216:219], v[16:19]\n"
"v_mfma_f32_16x16x32_bf16 v[16:19], v[216:219], a[240:243], v[16:19]\n"
"v_mfma_f32_16x16x32_bf16 v[24:27], v[216:219], a[244:247], v[24:27]\n"
"ds_read_b64_tr_b16 v[216:217], v236 offset:128\n"
"ds_read_b64_tr_b16 v[218:219], v236 offset:2176\n"
"v_mfma_f32_16x16x32_bf16 v[24:27], v[212:215], a[220:223], v[24:27]\n"
"s_waitcnt lgkmcnt(8)\n"
"ds_read_b64_tr_b16 v[212:213], v236 offset:640\n"
"ds_read_b64_tr_b16 v[214:215], v236 offset:2688\n"
"v_mfma_f32_16x16x32_bf16 v[28:31], v[220:223], a[220:223], v[28:31]\n"
"s_waitcnt lgkmcnt(8)\n"
"v_accvgpr_write_b32 a68, 0\n"
"v_mfma_f32_16x16x32_bf16 v[28:31], v[224:227], a[244:247], v[28:31]\n"
"v_mfma_f32_16x16x32_bf16 v[20:23], v[224:227], a[240:243], v[20:23]\n"
"v_mfma_f32_16x16x32_bf16 v[20:23], v[220:223], a[216:219], v[20:23]\n"
"v_mfma_f32_16x16x32_bf16 v[12:15], v[220:223], a[212:215], v[12:15]\n"
"v_mfma_f32_16x16x32_bf16 v[12:15], v[224:227], a[236:239], v[12:15]\n"
"v_accvgpr_write_b32 a69, 0\n"
"v_mfma_f32_16x16x32_bf16 v[4:7], v[224:227], a[232:235], v[4:7]\n"
"ds_read_b64_tr_b16 v[224:225], v236 offset:136\n"
"ds_read_b64_tr_b16 v[226:227], v236 offset:2184\n"
"v_mfma_f32_16x16x32_bf16 v[4:7], v[220:223], a[208:211], v[4:7]\n"
"s_waitcnt lgkmcnt(8)\n"
"ds_read_b64_tr_b16 v[220:221], v236 offset:648\n"
"ds_read_b64_tr_b16 v[222:223], v236 offset:2696\n"
"v_mfma_f32_16x16x32_bf16 v[128:131], v[228:231], a[224:227], v[128:131]\n"
"s_waitcnt lgkmcnt(8)\n"
"v_mfma_f32_16x16x32_bf16 v[128:131], v[232:235], a[248:251], v[128:131]\n"
"v_mfma_f32_16x16x32_bf16 v[132:135], v[232:235], a[252:255], v[132:135]\n"
"ds_read_b64_tr_b16 v[232:233], v236 offset:256\n"
"ds_read_b64_tr_b16 v[234:235], v236 offset:2304\n"
"v_mfma_f32_16x16x32_bf16 v[132:135], v[228:231], a[228:231], v[132:135]\n"
"s_waitcnt lgkmcnt(8)\n"
"ds_read_b64_tr_b16 v[228:229], v236 offset:768\n"
"ds_read_b64_tr_b16 v[230:231], v236 offset:2816\n"
"v_mfma_f32_16x16x32_bf16 v[32:35], v[216:219], a[208:211], v[32:35]\n"
"s_waitcnt lgkmcnt(8)\n"
"v_accvgpr_write_b32 a70, 0\n"
"v_mfma_f32_16x16x32_bf16 v[32:35], v[212:215], a[232:235], v[32:35]\n"
"v_mfma_f32_16x16x32_bf16 v[40:43], v[212:215], a[236:239], v[40:43]\n"
"v_mfma_f32_16x16x32_bf16 v[40:43], v[216:219], a[212:215], v[40:43]\n"
"v_accvgpr_write_b32 a71, 0\n"
"v_mfma_f32_16x16x32_bf16 v[48:51], v[216:219], a[216:219], v[48:51]\n"
"v_mfma_f32_16x16x32_bf16 v[48:51], v[212:215], a[240:243], v[48:51]\n"
"v_mfma_f32_16x16x32_bf16 v[56:59], v[212:215], a[244:247], v[56:59]\n"
"ds_read_b64_tr_b16 v[212:213], v236 offset:264\n"
"ds_read_b64_tr_b16 v[214:215], v236 offset:2312\n"
"v_mfma_f32_16x16x32_bf16 v[56:59], v[216:219], a[220:223], v[56:59]\n"
"s_waitcnt lgkmcnt(8)\n"
"ds_read_b64_tr_b16 v[216:217], v236 offset:776\n"
"ds_read_b64_tr_b16 v[218:219], v236 offset:2824\n"
"v_mfma_f32_16x16x32_bf16 v[60:63], v[224:227], a[220:223], v[60:63]\n"
"s_waitcnt lgkmcnt(8)\n"
"v_accvgpr_write_b32 a72, 0\n"
"v_mfma_f32_16x16x32_bf16 v[60:63], v[220:223], a[244:247], v[60:63]\n"
"v_mfma_f32_16x16x32_bf16 v[52:55], v[220:223], a[240:243], v[52:55]\n"
"s_waitcnt vmcnt(14)\n"
"s_barrier\n"
"v_mfma_f32_16x16x32_bf16 v[52:55], v[224:227], a[216:219], v[52:55]\n"
"s_add_u32 m0, s69, 0x23700\n"
"v_mfma_f32_16x16x32_bf16 v[44:47], v[224:227], a[212:215], v[44:47]\n"
"buffer_load_dwordx4 v240, s[44:47], s93 offen lds\n"
"v_mfma_f32_16x16x32_bf16 v[44:47], v[220:223], a[236:239], v[44:47]\n"
"s_add_u32 s93, s80, 0\n"
"v_accvgpr_write_b32 a73, 0\n"
"v_mfma_f32_16x16x32_bf16 v[36:39], v[220:223], a[232:235], v[36:39]\n"
"ds_read_b64_tr_b16 v[220:221], v237 offset:8\n"
"ds_read_b64_tr_b16 v[222:223], v237 offset:2056\n"
"v_mfma_f32_16x16x32_bf16 v[36:39], v[224:227], a[208:211], v[36:39]\n"
"s_waitcnt lgkmcnt(8)\n"
"ds_read_b64_tr_b16 v[224:225], v237 offset:520\n"
"ds_read_b64_tr_b16 v[226:227], v237 offset:2568\n"
"v_mfma_f32_16x16x32_bf16 v[64:67], v[232:235], a[208:211], v[64:67]\n"
"s_waitcnt lgkmcnt(8)\n"
"buffer_load_dwordx4 v240, s[44:47], s94 offen offset:1024 lds\n"
"v_mfma_f32_16x16x32_bf16 v[64:67], v[228:231], a[232:235], v[64:67]\n"
"s_add_u32 s94, s80, 0x1800\n"
"v_mfma_f32_16x16x32_bf16 v[72:75], v[228:231], a[236:239], v[72:75]\n"
"v_accvgpr_write_b32 a74, 0\n"
"v_mfma_f32_16x16x32_bf16 v[72:75], v[232:235], a[212:215], v[72:75]\n"
"v_mfma_f32_16x16x32_bf16 v[80:83], v[232:235], a[216:219], v[80:83]\n"
"buffer_load_dwordx4 v240, s[44:47], s91 offen offset:2048 lds\n"
"v_mfma_f32_16x16x32_bf16 v[80:83], v[228:231], a[240:243], v[80:83]\n"
"s_add_u32 s91, s80, 0x3000\n"
"v_mfma_f32_16x16x32_bf16 v[88:91], v[228:231], a[244:247], v[88:91]\n"
"ds_read_b64_tr_b16 v[228:229], v236 offset:384\n"
"ds_read_b64_tr_b16 v[230:231], v236 offset:2432\n"
"v_mfma_f32_16x16x32_bf16 v[88:91], v[232:235], a[220:223], v[88:91]\n"
"s_waitcnt lgkmcnt(8)\n"
"ds_read_b64_tr_b16 v[232:233], v236 offset:896\n"
"ds_read_b64_tr_b16 v[234:235], v236 offset:2944\n"
"v_mfma_f32_16x16x32_bf16 v[92:95], v[212:215], a[220:223], v[92:95]\n"
"s_waitcnt lgkmcnt(8)\n"
"buffer_load_dwordx4 v240, s[44:47], s92 offen offset:3072 lds\n"
"v_mfma_f32_16x16x32_bf16 v[92:95], v[216:219], a[244:247], v[92:95]\n"
"s_add_u32 s92, s80, 0x4800\n"
"v_accvgpr_write_b32 a75, 0\n"
"v_mfma_f32_16x16x32_bf16 v[84:87], v[216:219], a[240:243], v[84:87]\n"
"s_add_u32 s80, s79, s25\n"
"v_mfma_f32_16x16x32_bf16 v[84:87], v[212:215], a[216:219], v[84:87]\n"
"v_mfma_f32_16x16x32_bf16 v[76:79], v[212:215], a[212:215], v[76:79]\n"
"s_add_u32 m0, s90, 0x5400\n"
"v_accvgpr_write_b32 a76, 0\n"
"v_mfma_f32_16x16x32_bf16 v[76:79], v[216:219], a[236:239], v[76:79]\n"
"buffer_load_dwordx4 v241, s[40:43], s93 offen lds\n"
"v_mfma_f32_16x16x32_bf16 v[68:71], v[216:219], a[232:235], v[68:71]\n"
"s_add_u32 s93, s81, 0x7000\n"
"ds_read_b64_tr_b16 v[216:217], v236 offset:392\n"
"ds_read_b64_tr_b16 v[218:219], v236 offset:2440\n"
"v_mfma_f32_16x16x32_bf16 v[68:71], v[212:215], a[208:211], v[68:71]\n"
"s_waitcnt lgkmcnt(8)\n"
"ds_read_b64_tr_b16 v[212:213], v236 offset:904\n"
"ds_read_b64_tr_b16 v[214:215], v236 offset:2952\n"
"v_mfma_f32_16x16x32_bf16 v[136:139], v[220:223], a[224:227], v[136:139]\n"
"s_waitcnt lgkmcnt(8)\n"
"v_mfma_f32_16x16x32_bf16 v[136:139], v[224:227], a[248:251], v[136:139]\n"
"v_accvgpr_write_b32 a77, 0\n"
"v_mfma_f32_16x16x32_bf16 v[140:143], v[224:227], a[252:255], v[140:143]\n"
"buffer_load_dwordx4 v244, s[40:43], s94 offen offset:1024 lds\n"
"v_mfma_f32_16x16x32_bf16 v[140:143], v[220:223], a[228:231], v[140:143]\n"
"s_waitcnt lgkmcnt(6)\n"
"ds_read_b64_tr_b16 v[224:225], v236 offset:16640\n"
"ds_read_b64_tr_b16 v[226:227], v236 offset:18688\n"
"v_mfma_f32_16x16x32_bf16 v[96:99], v[228:231], a[208:211], v[96:99]\n"
"s_waitcnt lgkmcnt(6)\n"
"ds_read_b64_tr_b16 v[220:221], v236 offset:17152\n"
"ds_read_b64_tr_b16 v[222:223], v236 offset:19200\n"
"v_mfma_f32_16x16x32_bf16 v[96:99], v[232:235], a[232:235], v[96:99]\n"
"s_add_u32 s94, s81, 0x8800\n"
"v_mfma_f32_16x16x32_bf16 v[104:107], v[232:235], a[236:239], v[104:107]\n"
"v_accvgpr_write_b32 a78, 0\n"
"v_mfma_f32_16x16x32_bf16 v[104:107], v[228:231], a[212:215], v[104:107]\n"
"buffer_load_dwordx4 v241, s[40:43], s91 offen offset:2048 lds\n"
"v_mfma_f32_16x16x32_bf16 v[112:115], v[228:231], a[216:219], v[112:115]\n"
"s_add_u32 s91, s81, 0xa000\n"
"v_mfma_f32_16x16x32_bf16 v[112:115], v[232:235], a[240:243], v[112:115]\n"
"v_mfma_f32_16x16x32_bf16 v[120:123], v[232:235], a[244:247], v[120:123]\n"
"ds_read_b64_tr_b16 v[232:233], v236 offset:16648\n"
"ds_read_b64_tr_b16 v[234:235], v236 offset:18696\n"
"v_mfma_f32_16x16x32_bf16 v[120:123], v[228:231], a[220:223], v[120:123]\n"
"s_waitcnt lgkmcnt(8)\n"
"ds_read_b64_tr_b16 v[228:229], v236 offset:17160\n"
"ds_read_b64_tr_b16 v[230:231], v236 offset:19208\n"
"v_mfma_f32_16x16x32_bf16 v[124:127], v[216:219], a[220:223], v[124:127]\n"
"s_waitcnt lgkmcnt(8)\n"
"buffer_load_dwordx4 v244, s[40:43], s92 offen offset:3072 lds\n"
"v_mfma_f32_16x16x32_bf16 v[124:127], v[212:215], a[244:247], v[124:127]\n"
"s_add_u32 m0, s90, 0x6400\n"
"v_accvgpr_write_b32 a79, 0\n"
"v_mfma_f32_16x16x32_bf16 v[116:119], v[212:215], a[240:243], v[116:119]\n"
"s_add_u32 s92, s81, 0xb800\n"
"v_mfma_f32_16x16x32_bf16 v[116:119], v[216:219], a[216:219], v[116:119]\n"
"s_sub_u32 s81, s79, s25\n"
"v_mfma_f32_16x16x32_bf16 v[108:111], v[216:219], a[212:215], v[108:111]\n"
"v_mfma_f32_16x16x32_bf16 v[108:111], v[212:215], a[236:239], v[108:111]\n"
"buffer_load_dwordx4 v241, s[40:43], s93 offen lds\n"
"v_mfma_f32_16x16x32_bf16 v[100:103], v[212:215], a[232:235], v[100:103]\n"
"s_mov_b32 s93, s75\n"
"ds_read_b64_tr_b16 v[212:213], v237 offset:16640\n"
"ds_read_b64_tr_b16 v[214:215], v237 offset:18688\n"
"v_mfma_f32_16x16x32_bf16 v[100:103], v[216:219], a[208:211], v[100:103]\n"
"s_waitcnt lgkmcnt(8)\n"
"ds_read_b64_tr_b16 v[216:217], v237 offset:17152\n"
"ds_read_b64_tr_b16 v[218:219], v237 offset:19200\n"
"v_mfma_f32_16x16x32_bf16 v[160:163], v[224:227], a[208:211], v[160:163]\n"
"s_waitcnt lgkmcnt(8)\n"
"v_mfma_f32_16x16x32_bf16 v[160:163], v[220:223], a[232:235], v[160:163]\n"
"s_add_u32 s75, s79, s83\n"
"v_mfma_f32_16x16x32_bf16 v[168:171], v[220:223], a[236:239], v[168:171]\n"
"s_add_u32 s79, s79, 0x80\n"
"s_cmp_eq_u32 s79, 0x380\n"
"s_cselect_b32 s79, 0, s79\n"
"buffer_load_dwordx4 v244, s[40:43], s94 offen offset:1024 lds\n"
"v_mfma_f32_16x16x32_bf16 v[168:171], v[224:227], a[212:215], v[168:171]\n"
"s_add_u32 s94, s78, 0\n"
"v_mfma_f32_16x16x32_bf16 v[176:179], v[224:227], a[216:219], v[176:179]\n"
"v_mfma_f32_16x16x32_bf16 v[176:179], v[220:223], a[240:243], v[176:179]\n"
"v_mfma_f32_16x16x32_bf16 v[184:187], v[220:223], a[244:247], v[184:187]\n"
"ds_read_b64_tr_b16 v[220:221], v236 offset:16768\n"
"ds_read_b64_tr_b16 v[222:223], v236 offset:18816\n"
"v_mfma_f32_16x16x32_bf16 v[184:187], v[224:227], a[220:223], v[184:187]\n"
"s_waitcnt lgkmcnt(8)\n"
"buffer_load_dwordx4 v241, s[40:43], s91 offen offset:2048 lds\n"
"v_mfma_f32_16x16x32_bf16 v[188:191], v[232:235], a[220:223], v[188:191]\n"
"s_waitcnt lgkmcnt(6)\n"
"ds_read_b64_tr_b16 v[224:225], v236 offset:17280\n"
"ds_read_b64_tr_b16 v[226:227], v236 offset:19328\n"
"v_mfma_f32_16x16x32_bf16 v[188:191], v[228:231], a[244:247], v[188:191]\n"
"s_add_u32 s91, s78, 0x1fc00\n"
"v_mfma_f32_16x16x32_bf16 v[180:183], v[228:231], a[240:243], v[180:183]\n"
"v_mfma_f32_16x16x32_bf16 v[180:183], v[232:235], a[216:219], v[180:183]\n"
"v_mfma_f32_16x16x32_bf16 v[172:175], v[232:235], a[212:215], v[172:175]\n"
"buffer_load_dwordx4 v244, s[40:43], s92 offen offset:3072 lds\n"
"v_mfma_f32_16x16x32_bf16 v[172:175], v[228:231], a[236:239], v[172:175]\n"
"s_add_u32 m0, s84, 0x5400\n"
"v_mfma_f32_16x16x32_bf16 v[164:167], v[228:231], a[232:235], v[164:167]\n"
"s_add_u32 s92, s78, 0x7800\n"
"ds_read_b64_tr_b16 v[228:229], v236 offset:16776\n"
"ds_read_b64_tr_b16 v[230:231], v236 offset:18824\n"
"v_mfma_f32_16x16x32_bf16 v[164:167], v[232:235], a[208:211], v[164:167]\n"
"s_waitcnt lgkmcnt(8)\n"
"ds_read_b64_tr_b16 v[232:233], v236 offset:17288\n"
"ds_read_b64_tr_b16 v[234:235], v236 offset:19336\n"
"v_mfma_f32_16x16x32_bf16 v[144:147], v[212:215], a[224:227], v[144:147]\n"
"s_waitcnt lgkmcnt(8)\n"
"v_mfma_f32_16x16x32_bf16 v[144:147], v[216:219], a[248:251], v[144:147]\n"
"buffer_load_dwordx4 v245, s[40:43], s93 offen lds\n"
"v_mfma_f32_16x16x32_bf16 v[148:151], v[216:219], a[252:255], v[148:151]\n"
"s_add_u32 s93, s78, 0x27400\n"
"ds_read_b64_tr_b16 v[216:217], v236 offset:16896\n"
"ds_read_b64_tr_b16 v[218:219], v236 offset:18944\n"
"v_mfma_f32_16x16x32_bf16 v[148:151], v[212:215], a[228:231], v[148:151]\n"
"s_waitcnt lgkmcnt(8)\n"
"ds_read_b64_tr_b16 v[212:213], v236 offset:17408\n"
"ds_read_b64_tr_b16 v[214:215], v236 offset:19456\n"
"v_mfma_f32_16x16x32_bf16 v[192:195], v[220:223], a[208:211], v[192:195]\n"
"s_waitcnt lgkmcnt(8)\n"
"v_mfma_f32_16x16x32_bf16 v[192:195], v[224:227], a[232:235], v[192:195]\n"
"v_mfma_f32_16x16x32_bf16 v[200:203], v[224:227], a[236:239], v[200:203]\n"
"v_mfma_f32_16x16x32_bf16 v[200:203], v[220:223], a[212:215], v[200:203]\n"
"v_mfma_f32_16x16x32_bf16 a[80:83], v[220:223], a[216:219], a[80:83]\n"
"v_mfma_f32_16x16x32_bf16 a[80:83], v[224:227], a[240:243], a[80:83]\n"
"v_mfma_f32_16x16x32_bf16 a[88:91], v[224:227], a[244:247], a[88:91]\n"
"ds_read_b64_tr_b16 v[224:225], v236 offset:16904\n"
"ds_read_b64_tr_b16 v[226:227], v236 offset:18952\n"
"v_mfma_f32_16x16x32_bf16 a[88:91], v[220:223], a[220:223], a[88:91]\n"
"s_waitcnt lgkmcnt(8)\n"
"ds_read_b64_tr_b16 v[220:221], v236 offset:17416\n"
"ds_read_b64_tr_b16 v[222:223], v236 offset:19464\n"
"v_mfma_f32_16x16x32_bf16 a[92:95], v[228:231], a[220:223], a[92:95]\n"
"s_waitcnt lgkmcnt(8)\n"
"v_mfma_f32_16x16x32_bf16 a[92:95], v[232:235], a[244:247], a[92:95]\n"
"v_mfma_f32_16x16x32_bf16 a[84:87], v[232:235], a[240:243], a[84:87]\n"
"s_waitcnt vmcnt(14)\n"
"s_barrier\n"
"v_mfma_f32_16x16x32_bf16 a[84:87], v[228:231], a[216:219], a[84:87]\n"
"s_add_u32 m0, s69, 0x17400\n"
"ds_read_b128 a[160:163], v238 offset:36864\n"
"ds_read_b128 a[184:187], v242 offset:36864\n"
"v_mfma_f32_16x16x32_bf16 v[204:207], v[228:231], a[212:215], v[204:207]\n"
"buffer_load_dwordx4 v240, s[44:47], s94 offen lds\n"
"v_mfma_f32_16x16x32_bf16 v[204:207], v[232:235], a[236:239], v[204:207]\n"
"s_add_u32 s94, s78, 0x100\n"
"ds_read_b128 a[188:191], v242 offset:38912\n"
"ds_read_b128 a[164:167], v238 offset:38912\n"
"v_mfma_f32_16x16x32_bf16 v[196:199], v[232:235], a[232:235], v[196:199]\n"
"ds_read_b64_tr_b16 v[232:233], v237 offset:16648\n"
"ds_read_b64_tr_b16 v[234:235], v237 offset:18696\n"
"v_mfma_f32_16x16x32_bf16 v[196:199], v[228:231], a[208:211], v[196:199]\n"
"s_waitcnt lgkmcnt(12)\n"
"ds_read_b64_tr_b16 v[228:229], v237 offset:17160\n"
"ds_read_b64_tr_b16 v[230:231], v237 offset:19208\n"
"v_mfma_f32_16x16x32_bf16 a[96:99], v[216:219], a[208:211], a[96:99]\n"
"s_waitcnt lgkmcnt(12)\n"
"buffer_load_dwordx4 v240, s[44:47], s91 offen offset:1024 lds\n"
"v_mfma_f32_16x16x32_bf16 a[96:99], v[212:215], a[232:235], a[96:99]\n"
"s_add_u32 s91, s78, 0x1fd00\n"
"ds_read_b128 a[168:171], v238 offset:40960\n"
"ds_read_b128 a[192:195], v242 offset:40960\n"
"v_mfma_f32_16x16x32_bf16 a[104:107], v[212:215], a[236:239], a[104:107]\n"
"Lpada_2:\n"
"s_cbranch_execz Lpada_6\n"
"ds_read_b128 a[196:199], v242 offset:43008\n"
"ds_read_b128 a[172:175], v238 offset:43008\n"
"v_mfma_f32_16x16x32_bf16 a[104:107], v[216:219], a[212:215], a[104:107]\n"
"ds_read_b128 a[176:179], v239 offset:36864\n"
"ds_read_b128 a[200:203], v243 offset:36864\n"
"v_mfma_f32_16x16x32_bf16 a[112:115], v[216:219], a[216:219], a[112:115]\n"
"buffer_load_dwordx4 v240, s[44:47], s92 offen offset:2048 lds\n"
"v_mfma_f32_16x16x32_bf16 a[112:115], v[212:215], a[240:243], a[112:115]\n"
"s_add_u32 s92, s78, 0x7900\n"
"ds_read_b128 a[204:207], v243 offset:38912\n"
"ds_read_b128 a[180:183], v239 offset:38912\n"
"v_mfma_f32_16x16x32_bf16 a[120:123], v[212:215], a[244:247], a[120:123]\n"
"ds_read_b64_tr_b16 v[212:213], v236 offset:17024\n"
"ds_read_b64_tr_b16 v[214:215], v236 offset:19072\n"
"v_mfma_f32_16x16x32_bf16 a[120:123], v[216:219], a[220:223], a[120:123]\n"
"s_waitcnt lgkmcnt(15)\n"
"ds_read_b64_tr_b16 v[216:217], v236 offset:17536\n"
"ds_read_b64_tr_b16 v[218:219], v236 offset:19584\n"
"v_mfma_f32_16x16x32_bf16 a[124:127], v[224:227], a[220:223], a[124:127]\n"
"buffer_load_dwordx4 v240, s[44:47], s93 offen offset:3072 lds\n"
"v_mfma_f32_16x16x32_bf16 a[124:127], v[220:223], a[244:247], a[124:127]\n"
"s_add_u32 s93, s78, 0x27500\n"
"v_mfma_f32_16x16x32_bf16 a[116:119], v[220:223], a[240:243], a[116:119]\n"
"s_add_u32 s78, s78, 0x80000\n"
"s_cmp_eq_u32 s78, 0x380000\n"
"s_cselect_b32 s78, 0, s78\n"
"v_mfma_f32_16x16x32_bf16 a[116:119], v[224:227], a[216:219], a[116:119]\n"
"v_mfma_f32_16x16x32_bf16 a[108:111], v[224:227], a[212:215], a[108:111]\n"
"v_mfma_f32_16x16x32_bf16 a[108:111], v[220:223], a[236:239], a[108:111]\n"
"v_mfma_f32_16x16x32_bf16 a[100:103], v[220:223], a[232:235], a[100:103]\n"
"ds_read_b64_tr_b16 v[220:221], v236 offset:17032\n"
"ds_read_b64_tr_b16 v[222:223], v236 offset:19080\n"
"v_mfma_f32_16x16x32_bf16 a[100:103], v[224:227], a[208:211], a[100:103]\n"
"s_waitcnt lgkmcnt(15)\n"
"ds_read_b64_tr_b16 v[224:225], v236 offset:17544\n"
"ds_read_b64_tr_b16 v[226:227], v236 offset:19592\n"
"v_mfma_f32_16x16x32_bf16 v[152:155], v[232:235], a[224:227], v[152:155]\n"
"s_waitcnt lgkmcnt(15)\n"
"v_mfma_f32_16x16x32_bf16 v[152:155], v[228:231], a[248:251], v[152:155]\n"
"v_mfma_f32_16x16x32_bf16 v[156:159], v[228:231], a[252:255], v[156:159]\n"
"ds_read_b64_tr_b16 v[228:229], v236 offset:33280\n"
"ds_read_b64_tr_b16 v[230:231], v236 offset:35328\n"
"v_mfma_f32_16x16x32_bf16 v[156:159], v[232:235], a[228:231], v[156:159]\n"
"s_waitcnt lgkmcnt(8)\n"
"ds_read_b64_tr_b16 v[232:233], v236 offset:33792\n"
"ds_read_b64_tr_b16 v[234:235], v236 offset:35840\n"
"v_mfma_f32_16x16x32_bf16 a[128:131], v[212:215], a[208:211], a[128:131]\n"
"s_waitcnt lgkmcnt(8)\n"
"v_mfma_f32_16x16x32_bf16 a[128:131], v[216:219], a[232:235], a[128:131]\n"
"v_mfma_f32_16x16x32_bf16 a[136:139], v[216:219], a[236:239], a[136:139]\n"
"v_mfma_f32_16x16x32_bf16 a[136:139], v[212:215], a[212:215], a[136:139]\n"
"v_mfma_f32_16x16x32_bf16 a[144:147], v[212:215], a[216:219], a[144:147]\n"
"v_mfma_f32_16x16x32_bf16 a[144:147], v[216:219], a[240:243], a[144:147]\n"
"v_mfma_f32_16x16x32_bf16 a[152:155], v[216:219], a[244:247], a[152:155]\n"
"ds_read_b64_tr_b16 v[216:217], v236 offset:33288\n"
"ds_read_b64_tr_b16 v[218:219], v236 offset:35336\n"
"v_mfma_f32_16x16x32_bf16 a[152:155], v[212:215], a[220:223], a[152:155]\n"
"s_waitcnt lgkmcnt(8)\n"
"ds_read_b64_tr_b16 v[212:213], v236 offset:33800\n"
"ds_read_b64_tr_b16 v[214:215], v236 offset:35848\n"
"v_mfma_f32_16x16x32_bf16 a[156:159], v[220:223], a[220:223], a[156:159]\n"
"s_waitcnt lgkmcnt(8)\n"
"v_mfma_f32_16x16x32_bf16 a[156:159], v[224:227], a[244:247], a[156:159]\n"
"v_mfma_f32_16x16x32_bf16 a[148:151], v[224:227], a[240:243], a[148:151]\n"
"v_mfma_f32_16x16x32_bf16 a[148:151], v[220:223], a[216:219], a[148:151]\n"
"v_mfma_f32_16x16x32_bf16 a[140:143], v[220:223], a[212:215], a[140:143]\n"
"v_mfma_f32_16x16x32_bf16 a[140:143], v[224:227], a[236:239], a[140:143]\n"
"v_mfma_f32_16x16x32_bf16 a[132:135], v[224:227], a[232:235], a[132:135]\n"
"ds_read_b64_tr_b16 v[224:225], v237 offset:33280\n"
"ds_read_b64_tr_b16 v[226:227], v237 offset:35328\n"
"v_mfma_f32_16x16x32_bf16 a[132:135], v[220:223], a[208:211], a[132:135]\n"
"s_waitcnt lgkmcnt(8)\n"
"ds_read_b64_tr_b16 v[220:221], v237 offset:33792\n"
"ds_read_b64_tr_b16 v[222:223], v237 offset:35840\n"
"v_mfma_f32_16x16x32_bf16 v[0:3], v[228:231], a[160:163], v[0:3]\n"
"s_waitcnt lgkmcnt(8)\n"
"v_mfma_f32_16x16x32_bf16 v[0:3], v[232:235], a[184:187], v[0:3]\n"
"v_mfma_f32_16x16x32_bf16 v[8:11], v[232:235], a[188:191], v[8:11]\n"
"v_mfma_f32_16x16x32_bf16 v[8:11], v[228:231], a[164:167], v[8:11]\n"
"v_mfma_f32_16x16x32_bf16 v[16:19], v[228:231], a[168:171], v[16:19]\n"
"v_mfma_f32_16x16x32_bf16 v[16:19], v[232:235], a[192:195], v[16:19]\n"
"v_mfma_f32_16x16x32_bf16 v[24:27], v[232:235], a[196:199], v[24:27]\n"
"ds_read_b64_tr_b16 v[232:233], v236 offset:33408\n"
"ds_read_b64_tr_b16 v[234:235], v236 offset:35456\n"
"v_mfma_f32_16x16x32_bf16 v[24:27], v[228:231], a[172:175], v[24:27]\n"
"s_waitcnt lgkmcnt(8)\n"
"ds_read_b64_tr_b16 v[228:229], v236 offset:33920\n"
"ds_read_b64_tr_b16 v[230:231], v236 offset:35968\n"
"v_mfma_f32_16x16x32_bf16 v[28:31], v[216:219], a[172:175], v[28:31]\n"
"s_waitcnt lgkmcnt(8)\n"
"v_mfma_f32_16x16x32_bf16 v[28:31], v[212:215], a[196:199], v[28:31]\n"
"v_mfma_f32_16x16x32_bf16 v[20:23], v[212:215], a[192:195], v[20:23]\n"
"v_mfma_f32_16x16x32_bf16 v[20:23], v[216:219], a[168:171], v[20:23]\n"
"v_mfma_f32_16x16x32_bf16 v[12:15], v[216:219], a[164:167], v[12:15]\n"
"v_mfma_f32_16x16x32_bf16 v[12:15], v[212:215], a[188:191], v[12:15]\n"
"v_mfma_f32_16x16x32_bf16 v[4:7], v[212:215], a[184:187], v[4:7]\n"
"ds_read_b64_tr_b16 v[212:213], v236 offset:33416\n"
"ds_read_b64_tr_b16 v[214:215], v236 offset:35464\n"
"v_mfma_f32_16x16x32_bf16 v[4:7], v[216:219], a[160:163], v[4:7]\n"
"s_waitcnt lgkmcnt(8)\n"
"ds_read_b64_tr_b16 v[216:217], v236 offset:33928\n"
"ds_read_b64_tr_b16 v[218:219], v236 offset:35976\n"
"v_mfma_f32_16x16x32_bf16 v[128:131], v[224:227], a[176:179], v[128:131]\n"
"s_waitcnt lgkmcnt(8)\n"
"v_mfma_f32_16x16x32_bf16 v[128:131], v[220:223], a[200:203], v[128:131]\n"
"v_mfma_f32_16x16x32_bf16 v[132:135], v[220:223], a[204:207], v[132:135]\n"
"ds_read_b64_tr_b16 v[220:221], v236 offset:33536\n"
"ds_read_b64_tr_b16 v[222:223], v236 offset:35584\n"
"v_mfma_f32_16x16x32_bf16 v[132:135], v[224:227], a[180:183], v[132:135]\n"
"s_waitcnt lgkmcnt(8)\n"
"ds_read_b64_tr_b16 v[224:225], v236 offset:34048\n"
"ds_read_b64_tr_b16 v[226:227], v236 offset:36096\n"
"v_mfma_f32_16x16x32_bf16 v[32:35], v[232:235], a[160:163], v[32:35]\n"
"s_waitcnt lgkmcnt(8)\n"
"v_mfma_f32_16x16x32_bf16 v[32:35], v[228:231], a[184:187], v[32:35]\n"
"v_mfma_f32_16x16x32_bf16 v[40:43], v[228:231], a[188:191], v[40:43]\n"
"v_mfma_f32_16x16x32_bf16 v[40:43], v[232:235], a[164:167], v[40:43]\n"
"v_mfma_f32_16x16x32_bf16 v[48:51], v[232:235], a[168:171], v[48:51]\n"
"v_mfma_f32_16x16x32_bf16 v[48:51], v[228:231], a[192:195], v[48:51]\n"
"v_mfma_f32_16x16x32_bf16 v[56:59], v[228:231], a[196:199], v[56:59]\n"
"ds_read_b64_tr_b16 v[228:229], v236 offset:33544\n"
"ds_read_b64_tr_b16 v[230:231], v236 offset:35592\n"
"v_mfma_f32_16x16x32_bf16 v[56:59], v[232:235], a[172:175], v[56:59]\n"
"s_waitcnt lgkmcnt(8)\n"
"ds_read_b64_tr_b16 v[232:233], v236 offset:34056\n"
"ds_read_b64_tr_b16 v[234:235], v236 offset:36104\n"
"v_mfma_f32_16x16x32_bf16 v[60:63], v[212:215], a[172:175], v[60:63]\n"
"s_waitcnt lgkmcnt(8)\n"
"v_mfma_f32_16x16x32_bf16 v[60:63], v[216:219], a[196:199], v[60:63]\n"
"v_mfma_f32_16x16x32_bf16 v[52:55], v[216:219], a[192:195], v[52:55]\n"
"s_waitcnt vmcnt(13)\n"
"s_barrier\n"
"v_mfma_f32_16x16x32_bf16 v[52:55], v[212:215], a[168:171], v[52:55]\n"
"s_add_u32 m0, s69, 0x1b500\n"
"v_mfma_f32_16x16x32_bf16 v[44:47], v[212:215], a[164:167], v[44:47]\n"
"buffer_load_dwordx4 v240, s[44:47], s94 offen lds\n"
"v_mfma_f32_16x16x32_bf16 v[44:47], v[216:219], a[188:191], v[44:47]\n"
"s_add_u32 s94, s80, 0\n"
"v_mfma_f32_16x16x32_bf16 v[36:39], v[216:219], a[184:187], v[36:39]\n"
"ds_read_b64_tr_b16 v[216:217], v237 offset:33288\n"
"ds_read_b64_tr_b16 v[218:219], v237 offset:35336\n"
"v_mfma_f32_16x16x32_bf16 v[36:39], v[212:215], a[160:163], v[36:39]\n"
"s_waitcnt lgkmcnt(8)\n"
"ds_read_b64_tr_b16 v[212:213], v237 offset:33800\n"
"ds_read_b64_tr_b16 v[214:215], v237 offset:35848\n"
"v_mfma_f32_16x16x32_bf16 v[64:67], v[220:223], a[160:163], v[64:67]\n"
"s_waitcnt lgkmcnt(8)\n"
"buffer_load_dwordx4 v240, s[44:47], s91 offen offset:1024 lds\n"
"v_mfma_f32_16x16x32_bf16 v[64:67], v[224:227], a[184:187], v[64:67]\n"
"s_add_u32 s91, s80, 0x1800\n"
"v_mfma_f32_16x16x32_bf16 v[72:75], v[224:227], a[188:191], v[72:75]\n"
"v_mfma_f32_16x16x32_bf16 v[72:75], v[220:223], a[164:167], v[72:75]\n"
"v_mfma_f32_16x16x32_bf16 v[80:83], v[220:223], a[168:171], v[80:83]\n"
"buffer_load_dwordx4 v240, s[44:47], s92 offen offset:2048 lds\n"
"v_mfma_f32_16x16x32_bf16 v[80:83], v[224:227], a[192:195], v[80:83]\n"
"s_add_u32 s92, s80, 0x3000\n"
"v_mfma_f32_16x16x32_bf16 v[88:91], v[224:227], a[196:199], v[88:91]\n"
"ds_read_b64_tr_b16 v[224:225], v236 offset:33664\n"
"ds_read_b64_tr_b16 v[226:227], v236 offset:35712\n"
"v_mfma_f32_16x16x32_bf16 v[88:91], v[220:223], a[172:175], v[88:91]\n"
"s_waitcnt lgkmcnt(8)\n"
"ds_read_b64_tr_b16 v[220:221], v236 offset:34176\n"
"ds_read_b64_tr_b16 v[222:223], v236 offset:36224\n"
"v_mfma_f32_16x16x32_bf16 v[92:95], v[228:231], a[172:175], v[92:95]\n"
"s_waitcnt lgkmcnt(8)\n"
"buffer_load_dwordx4 v240, s[44:47], s93 offen offset:3072 lds\n"
"v_mfma_f32_16x16x32_bf16 v[92:95], v[232:235], a[196:199], v[92:95]\n"
"s_add_u32 s93, s80, 0x4800\n"
"v_mfma_f32_16x16x32_bf16 v[84:87], v[232:235], a[192:195], v[84:87]\n"
"s_add_u32 s80, s79, s25\n"
"v_mfma_f32_16x16x32_bf16 v[84:87], v[228:231], a[168:171], v[84:87]\n"
"v_mfma_f32_16x16x32_bf16 v[76:79], v[228:231], a[164:167], v[76:79]\n"
"s_add_u32 m0, s90, 0xe400\n"
"v_mfma_f32_16x16x32_bf16 v[76:79], v[232:235], a[188:191], v[76:79]\n"
"buffer_load_dwordx4 v241, s[40:43], s94 offen lds\n"
"v_mfma_f32_16x16x32_bf16 v[68:71], v[232:235], a[184:187], v[68:71]\n"
"s_add_u32 s94, s81, 0x7000\n"
"ds_read_b64_tr_b16 v[232:233], v236 offset:33672\n"
"ds_read_b64_tr_b16 v[234:235], v236 offset:35720\n"
"v_mfma_f32_16x16x32_bf16 v[68:71], v[228:231], a[160:163], v[68:71]\n"
"s_waitcnt lgkmcnt(8)\n"
"ds_read_b64_tr_b16 v[228:229], v236 offset:34184\n"
"ds_read_b64_tr_b16 v[230:231], v236 offset:36232\n"
"v_mfma_f32_16x16x32_bf16 v[136:139], v[216:219], a[176:179], v[136:139]\n"
"s_waitcnt lgkmcnt(8)\n"
"v_mfma_f32_16x16x32_bf16 v[136:139], v[212:215], a[200:203], v[136:139]\n"
"v_mfma_f32_16x16x32_bf16 v[140:143], v[212:215], a[204:207], v[140:143]\n"
"buffer_load_dwordx4 v244, s[40:43], s91 offen offset:1024 lds\n"
"v_mfma_f32_16x16x32_bf16 v[140:143], v[216:219], a[180:183], v[140:143]\n"
"s_waitcnt lgkmcnt(6)\n"
"ds_read_b64_tr_b16 v[212:213], v236 offset:49920\n"
"ds_read_b64_tr_b16 v[214:215], v236 offset:51968\n"
"v_mfma_f32_16x16x32_bf16 v[96:99], v[224:227], a[160:163], v[96:99]\n"
"s_waitcnt lgkmcnt(6)\n"
"ds_read_b64_tr_b16 v[216:217], v236 offset:50432\n"
"ds_read_b64_tr_b16 v[218:219], v236 offset:52480\n"
"v_mfma_f32_16x16x32_bf16 v[96:99], v[220:223], a[184:187], v[96:99]\n"
"s_add_u32 s91, s81, 0x8800\n"
"v_mfma_f32_16x16x32_bf16 v[104:107], v[220:223], a[188:191], v[104:107]\n"
"v_mfma_f32_16x16x32_bf16 v[104:107], v[224:227], a[164:167], v[104:107]\n"
"buffer_load_dwordx4 v241, s[40:43], s92 offen offset:2048 lds\n"
"v_mfma_f32_16x16x32_bf16 v[112:115], v[224:227], a[168:171], v[112:115]\n"
"s_add_u32 s92, s81, 0xa000\n"
"v_mfma_f32_16x16x32_bf16 v[112:115], v[220:223], a[192:195], v[112:115]\n"
"v_mfma_f32_16x16x32_bf16 v[120:123], v[220:223], a[196:199], v[120:123]\n"
"ds_read_b64_tr_b16 v[220:221], v236 offset:49928\n"
"ds_read_b64_tr_b16 v[222:223], v236 offset:51976\n"
"v_mfma_f32_16x16x32_bf16 v[120:123], v[224:227], a[172:175], v[120:123]\n"
"s_waitcnt lgkmcnt(8)\n"
"ds_read_b64_tr_b16 v[224:225], v236 offset:50440\n"
"ds_read_b64_tr_b16 v[226:227], v236 offset:52488\n"
"v_mfma_f32_16x16x32_bf16 v[124:127], v[232:235], a[172:175], v[124:127]\n"
"s_waitcnt lgkmcnt(8)\n"
"buffer_load_dwordx4 v244, s[40:43], s93 offen offset:3072 lds\n"
"v_mfma_f32_16x16x32_bf16 v[124:127], v[228:231], a[196:199], v[124:127]\n"
"s_add_u32 m0, s90, 0xf400\n"
"v_mfma_f32_16x16x32_bf16 v[116:119], v[228:231], a[192:195], v[116:119]\n"
"s_add_u32 s93, s81, 0xb800\n"
"v_mfma_f32_16x16x32_bf16 v[116:119], v[232:235], a[168:171], v[116:119]\n"
"s_sub_u32 s81, s79, s25\n"
"v_mfma_f32_16x16x32_bf16 v[108:111], v[232:235], a[164:167], v[108:111]\n"
"v_mfma_f32_16x16x32_bf16 v[108:111], v[228:231], a[188:191], v[108:111]\n"
"buffer_load_dwordx4 v241, s[40:43], s94 offen lds\n"
"v_mfma_f32_16x16x32_bf16 v[100:103], v[228:231], a[184:187], v[100:103]\n"
"s_mov_b32 s94, s75\n"
"ds_read_b64_tr_b16 v[228:229], v237 offset:49920\n"
"ds_read_b64_tr_b16 v[230:231], v237 offset:51968\n"
"v_mfma_f32_16x16x32_bf16 v[100:103], v[232:235], a[160:163], v[100:103]\n"
"s_waitcnt lgkmcnt(8)\n"
"ds_read_b64_tr_b16 v[232:233], v237 offset:50432\n"
"ds_read_b64_tr_b16 v[234:235], v237 offset:52480\n"
"v_mfma_f32_16x16x32_bf16 v[160:163], v[212:215], a[160:163], v[160:163]\n"
"s_waitcnt lgkmcnt(8)\n"
"v_mfma_f32_16x16x32_bf16 v[160:163], v[216:219], a[184:187], v[160:163]\n"
"s_add_u32 s75, s79, s83\n"
"v_mfma_f32_16x16x32_bf16 v[168:171], v[216:219], a[188:191], v[168:171]\n"
"buffer_load_dwordx4 v244, s[40:43], s91 offen offset:1024 lds\n"
"v_mfma_f32_16x16x32_bf16 v[168:171], v[212:215], a[164:167], v[168:171]\n"
"s_add_u32 s91, s78, 0\n"
"v_mfma_f32_16x16x32_bf16 v[176:179], v[212:215], a[168:171], v[176:179]\n"
"v_mfma_f32_16x16x32_bf16 v[176:179], v[216:219], a[192:195], v[176:179]\n"
"v_mfma_f32_16x16x32_bf16 v[184:187], v[216:219], a[196:199], v[184:187]\n"
"ds_read_b64_tr_b16 v[216:217], v236 offset:50048\n"
"ds_read_b64_tr_b16 v[218:219], v236 offset:52096\n"
"v_mfma_f32_16x16x32_bf16 v[184:187], v[212:215], a[172:175], v[184:187]\n"
"s_waitcnt lgkmcnt(8)\n"
"buffer_load_dwordx4 v241, s[40:43], s92 offen offset:2048 lds\n"
"v_mfma_f32_16x16x32_bf16 v[188:191], v[220:223], a[172:175], v[188:191]\n"
"s_waitcnt lgkmcnt(6)\n"
"ds_read_b64_tr_b16 v[212:213], v236 offset:50560\n"
"ds_read_b64_tr_b16 v[214:215], v236 offset:52608\n"
"v_mfma_f32_16x16x32_bf16 v[188:191], v[224:227], a[196:199], v[188:191]\n"
"s_add_u32 s92, s78, 0x1fc00\n"
"v_mfma_f32_16x16x32_bf16 v[180:183], v[224:227], a[192:195], v[180:183]\n"
"v_mfma_f32_16x16x32_bf16 v[180:183], v[220:223], a[168:171], v[180:183]\n"
"v_mfma_f32_16x16x32_bf16 v[172:175], v[220:223], a[164:167], v[172:175]\n"
"buffer_load_dwordx4 v244, s[40:43], s93 offen offset:3072 lds\n"
"v_mfma_f32_16x16x32_bf16 v[172:175], v[224:227], a[188:191], v[172:175]\n"
"s_add_u32 m0, s84, 0xe400\n"
"v_mfma_f32_16x16x32_bf16 v[164:167], v[224:227], a[184:187], v[164:167]\n"
"s_add_u32 s93, s78, 0x7800\n"
"ds_read_b64_tr_b16 v[224:225], v236 offset:50056\n"
"ds_read_b64_tr_b16 v[226:227], v236 offset:52104\n"
"v_mfma_f32_16x16x32_bf16 v[164:167], v[220:223], a[160:163], v[164:167]\n"
"s_waitcnt lgkmcnt(8)\n"
"ds_read_b64_tr_b16 v[220:221], v236 offset:50568\n"
"ds_read_b64_tr_b16 v[222:223], v236 offset:52616\n"
"v_mfma_f32_16x16x32_bf16 v[144:147], v[228:231], a[176:179], v[144:147]\n"
"s_waitcnt lgkmcnt(8)\n"
"v_mfma_f32_16x16x32_bf16 v[144:147], v[232:235], a[200:203], v[144:147]\n"
"buffer_load_dwordx4 v245, s[40:43], s94 offen lds\n"
"v_mfma_f32_16x16x32_bf16 v[148:151], v[232:235], a[204:207], v[148:151]\n"
"s_add_u32 s94, s78, 0x27400\n"
"ds_read_b64_tr_b16 v[232:233], v236 offset:50176\n"
"ds_read_b64_tr_b16 v[234:235], v236 offset:52224\n"
"v_mfma_f32_16x16x32_bf16 v[148:151], v[228:231], a[180:183], v[148:151]\n"
"s_waitcnt lgkmcnt(8)\n"
"ds_read_b64_tr_b16 v[228:229], v236 offset:50688\n"
"ds_read_b64_tr_b16 v[230:231], v236 offset:52736\n"
"v_mfma_f32_16x16x32_bf16 v[192:195], v[216:219], a[160:163], v[192:195]\n"
"s_waitcnt lgkmcnt(8)\n"
"v_mfma_f32_16x16x32_bf16 v[192:195], v[212:215], a[184:187], v[192:195]\n"
"v_mfma_f32_16x16x32_bf16 v[200:203], v[212:215], a[188:191], v[200:203]\n"
"v_mfma_f32_16x16x32_bf16 v[200:203], v[216:219], a[164:167], v[200:203]\n"
"v_mfma_f32_16x16x32_bf16 a[80:83], v[216:219], a[168:171], a[80:83]\n"
"v_mfma_f32_16x16x32_bf16 a[80:83], v[212:215], a[192:195], a[80:83]\n"
"v_mfma_f32_16x16x32_bf16 a[88:91], v[212:215], a[196:199], a[88:91]\n"
"ds_read_b64_tr_b16 v[212:213], v236 offset:50184\n"
"ds_read_b64_tr_b16 v[214:215], v236 offset:52232\n"
"v_mfma_f32_16x16x32_bf16 a[88:91], v[216:219], a[172:175], a[88:91]\n"
"s_waitcnt lgkmcnt(8)\n"
"ds_read_b64_tr_b16 v[216:217], v236 offset:50696\n"
"ds_read_b64_tr_b16 v[218:219], v236 offset:52744\n"
"v_mfma_f32_16x16x32_bf16 a[92:95], v[224:227], a[172:175], a[92:95]\n"
"s_waitcnt lgkmcnt(8)\n"
"v_mfma_f32_16x16x32_bf16 a[92:95], v[220:223], a[196:199], a[92:95]\n"
"v_mfma_f32_16x16x32_bf16 a[84:87], v[220:223], a[192:195], a[84:87]\n"
"s_waitcnt vmcnt(13)\n"
"s_barrier\n"
"v_mfma_f32_16x16x32_bf16 a[84:87], v[224:227], a[168:171], a[84:87]\n"
"s_add_u32 m0, s69, 0x1f600\n"
"ds_read_b128 a[208:211], v238 offset:0\n"
"ds_read_b128 a[232:235], v242 offset:0\n"
"v_mfma_f32_16x16x32_bf16 v[204:207], v[224:227], a[164:167], v[204:207]\n"
"buffer_load_dwordx4 v240, s[44:47], s91 offen lds\n"
"v_mfma_f32_16x16x32_bf16 v[204:207], v[220:223], a[188:191], v[204:207]\n"
"s_add_u32 s91, s78, 0x100\n"
"ds_read_b128 a[236:239], v242 offset:2048\n"
"ds_read_b128 a[212:215], v238 offset:2048\n"
"v_mfma_f32_16x16x32_bf16 v[196:199], v[220:223], a[184:187], v[196:199]\n"
"ds_read_b64_tr_b16 v[220:221], v237 offset:49928\n"
"ds_read_b64_tr_b16 v[222:223], v237 offset:51976\n"
"v_mfma_f32_16x16x32_bf16 v[196:199], v[224:227], a[160:163], v[196:199]\n"
"s_waitcnt lgkmcnt(12)\n"
"ds_read_b64_tr_b16 v[224:225], v237 offset:50440\n"
"ds_read_b64_tr_b16 v[226:227], v237 offset:52488\n"
"v_mfma_f32_16x16x32_bf16 a[96:99], v[232:235], a[160:163], a[96:99]\n"
"s_waitcnt lgkmcnt(12)\n"
"buffer_load_dwordx4 v240, s[44:47], s92 offen offset:1024 lds\n"
"v_mfma_f32_16x16x32_bf16 a[96:99], v[228:231], a[184:187], a[96:99]\n"
"s_add_u32 s92, s78, 0x1fd00\n"
"ds_read_b128 a[216:219], v238 offset:4096\n"
"ds_read_b128 a[240:243], v242 offset:4096\n"
"v_mfma_f32_16x16x32_bf16 a[104:107], v[228:231], a[188:191], a[104:107]\n"
"ds_read_b128 a[244:247], v242 offset:6144\n"
"ds_read_b128 a[220:223], v238 offset:6144\n"
"v_mfma_f32_16x16x32_bf16 a[104:107], v[232:235], a[164:167], a[104:107]\n"
"ds_read_b128 a[224:227], v239 offset:0\n"
"ds_read_b128 a[248:251], v243 offset:0\n"
"v_mfma_f32_16x16x32_bf16 a[112:115], v[232:235], a[168:171], a[112:115]\n"
"buffer_load_dwordx4 v240, s[44:47], s93 offen offset:2048 lds\n"
"v_mfma_f32_16x16x32_bf16 a[112:115], v[228:231], a[192:195], a[112:115]\n"
"s_add_u32 s93, s78, 0x7900\n"
"ds_read_b128 a[252:255], v243 offset:2048\n"
"ds_read_b128 a[228:231], v239 offset:2048\n"
"v_mfma_f32_16x16x32_bf16 a[120:123], v[228:231], a[196:199], a[120:123]\n"
"ds_read_b64_tr_b16 v[228:229], v236 offset:50304\n"
"ds_read_b64_tr_b16 v[230:231], v236 offset:52352\n"
"v_mfma_f32_16x16x32_bf16 a[120:123], v[232:235], a[172:175], a[120:123]\n"
"s_waitcnt lgkmcnt(15)\n"
"ds_read_b64_tr_b16 v[232:233], v236 offset:50816\n"
"ds_read_b64_tr_b16 v[234:235], v236 offset:52864\n"
"v_mfma_f32_16x16x32_bf16 a[124:127], v[212:215], a[172:175], a[124:127]\n"
"buffer_load_dwordx4 v240, s[44:47], s94 offen offset:3072 lds\n"
"v_mfma_f32_16x16x32_bf16 a[124:127], v[216:219], a[196:199], a[124:127]\n"
"s_add_u32 s94, s78, 0x27500\n"
"v_mfma_f32_16x16x32_bf16 a[116:119], v[216:219], a[192:195], a[116:119]\n"
"s_add_u32 s78, s78, 0x80000\n"
"s_cmp_eq_u32 s78, 0x380000\n"
"s_cselect_b32 s78, 0, s78\n"
"v_mfma_f32_16x16x32_bf16 a[116:119], v[212:215], a[168:171], a[116:119]\n"
"v_mfma_f32_16x16x32_bf16 a[108:111], v[212:215], a[164:167], a[108:111]\n"
"v_mfma_f32_16x16x32_bf16 a[108:111], v[216:219], a[188:191], a[108:111]\n"
"v_mfma_f32_16x16x32_bf16 a[100:103], v[216:219], a[184:187], a[100:103]\n"
"ds_read_b64_tr_b16 v[216:217], v236 offset:50312\n"
"ds_read_b64_tr_b16 v[218:219], v236 offset:52360\n"
"v_mfma_f32_16x16x32_bf16 a[100:103], v[212:215], a[160:163], a[100:103]\n"
"s_waitcnt lgkmcnt(15)\n"
"ds_read_b64_tr_b16 v[212:213], v236 offset:50824\n"
"ds_read_b64_tr_b16 v[214:215], v236 offset:52872\n"
"v_mfma_f32_16x16x32_bf16 v[152:155], v[220:223], a[176:179], v[152:155]\n"
"s_waitcnt lgkmcnt(15)\n"
"v_mfma_f32_16x16x32_bf16 v[152:155], v[224:227], a[200:203], v[152:155]\n"
"v_mfma_f32_16x16x32_bf16 v[156:159], v[224:227], a[204:207], v[156:159]\n"
"ds_read_b64_tr_b16 v[224:225], v236 offset:0\n"
"ds_read_b64_tr_b16 v[226:227], v236 offset:2048\n"
"v_mfma_f32_16x16x32_bf16 v[156:159], v[220:223], a[180:183], v[156:159]\n"
"s_waitcnt lgkmcnt(8)\n"
"ds_read_b64_tr_b16 v[220:221], v236 offset:512\n"
"ds_read_b64_tr_b16 v[222:223], v236 offset:2560\n"
"v_mfma_f32_16x16x32_bf16 a[128:131], v[228:231], a[160:163], a[128:131]\n"
"s_waitcnt lgkmcnt(8)\n"
"v_mfma_f32_16x16x32_bf16 a[128:131], v[232:235], a[184:187], a[128:131]\n"
"v_mfma_f32_16x16x32_bf16 a[136:139], v[232:235], a[188:191], a[136:139]\n"
"v_mfma_f32_16x16x32_bf16 a[136:139], v[228:231], a[164:167], a[136:139]\n"
"v_mfma_f32_16x16x32_bf16 a[144:147], v[228:231], a[168:171], a[144:147]\n"
"v_mfma_f32_16x16x32_bf16 a[144:147], v[232:235], a[192:195], a[144:147]\n"
"v_mfma_f32_16x16x32_bf16 a[152:155], v[232:235], a[196:199], a[152:155]\n"
"ds_read_b64_tr_b16 v[232:233], v236 offset:8\n"
"ds_read_b64_tr_b16 v[234:235], v236 offset:2056\n"
"v_mfma_f32_16x16x32_bf16 a[152:155], v[228:231], a[172:175], a[152:155]\n"
"s_waitcnt lgkmcnt(8)\n"
"ds_read_b64_tr_b16 v[228:229], v236 offset:520\n"
"ds_read_b64_tr_b16 v[230:231], v236 offset:2568\n"
"v_mfma_f32_16x16x32_bf16 a[156:159], v[216:219], a[172:175], a[156:159]\n"
"s_waitcnt lgkmcnt(8)\n"
"v_mfma_f32_16x16x32_bf16 a[156:159], v[212:215], a[196:199], a[156:159]\n"
"v_mfma_f32_16x16x32_bf16 a[148:151], v[212:215], a[192:195], a[148:151]\n"
"v_mfma_f32_16x16x32_bf16 a[148:151], v[216:219], a[168:171], a[148:151]\n"
"v_mfma_f32_16x16x32_bf16 a[140:143], v[216:219], a[164:167], a[140:143]\n"
"v_mfma_f32_16x16x32_bf16 a[140:143], v[212:215], a[188:191], a[140:143]\n"
"v_mfma_f32_16x16x32_bf16 a[132:135], v[212:215], a[184:187], a[132:135]\n"
"ds_read_b64_tr_b16 v[212:213], v237 offset:0\n"
"ds_read_b64_tr_b16 v[214:215], v237 offset:2048\n"
"v_mfma_f32_16x16x32_bf16 a[132:135], v[216:219], a[160:163], a[132:135]\n"
"s_waitcnt lgkmcnt(8)\n"
"ds_read_b64_tr_b16 v[216:217], v237 offset:512\n"
"ds_read_b64_tr_b16 v[218:219], v237 offset:2560\n"
"v_mfma_f32_16x16x32_bf16 v[0:3], v[224:227], a[208:211], v[0:3]\n"
"s_waitcnt lgkmcnt(8)\n"
"v_mfma_f32_16x16x32_bf16 v[0:3], v[220:223], a[232:235], v[0:3]\n"
"v_mfma_f32_16x16x32_bf16 v[8:11], v[220:223], a[236:239], v[8:11]\n"
"v_mfma_f32_16x16x32_bf16 v[8:11], v[224:227], a[212:215], v[8:11]\n"
"v_mfma_f32_16x16x32_bf16 v[16:19], v[224:227], a[216:219], v[16:19]\n"
"v_mfma_f32_16x16x32_bf16 v[16:19], v[220:223], a[240:243], v[16:19]\n"
"v_mfma_f32_16x16x32_bf16 v[24:27], v[220:223], a[244:247], v[24:27]\n"
"ds_read_b64_tr_b16 v[220:221], v236 offset:128\n"
"ds_read_b64_tr_b16 v[222:223], v236 offset:2176\n"
"v_mfma_f32_16x16x32_bf16 v[24:27], v[224:227], a[220:223], v[24:27]\n"
"s_waitcnt lgkmcnt(8)\n"
"ds_read_b64_tr_b16 v[224:225], v236 offset:640\n"
"ds_read_b64_tr_b16 v[226:227], v236 offset:2688\n"
"v_mfma_f32_16x16x32_bf16 v[28:31], v[232:235], a[220:223], v[28:31]\n"
"s_waitcnt lgkmcnt(8)\n"
"v_mfma_f32_16x16x32_bf16 v[28:31], v[228:231], a[244:247], v[28:31]\n"
"v_mfma_f32_16x16x32_bf16 v[20:23], v[228:231], a[240:243], v[20:23]\n"
"v_mfma_f32_16x16x32_bf16 v[20:23], v[232:235], a[216:219], v[20:23]\n"
"v_mfma_f32_16x16x32_bf16 v[12:15], v[232:235], a[212:215], v[12:15]\n"
"v_mfma_f32_16x16x32_bf16 v[12:15], v[228:231], a[236:239], v[12:15]\n"
"v_mfma_f32_16x16x32_bf16 v[4:7], v[228:231], a[232:235], v[4:7]\n"
"ds_read_b64_tr_b16 v[228:229], v236 offset:136\n"
"ds_read_b64_tr_b16 v[230:231], v236 offset:2184\n"
"v_mfma_f32_16x16x32_bf16 v[4:7], v[232:235], a[208:211], v[4:7]\n"
"s_waitcnt lgkmcnt(8)\n"
"ds_read_b64_tr_b16 v[232:233], v236 offset:648\n"
"ds_read_b64_tr_b16 v[234:235], v236 offset:2696\n"
"v_mfma_f32_16x16x32_bf16 v[128:131], v[212:215], a[224:227], v[128:131]\n"
"s_waitcnt lgkmcnt(8)\n"
"v_mfma_f32_16x16x32_bf16 v[128:131], v[216:219], a[248:251], v[128:131]\n"
"v_mfma_f32_16x16x32_bf16 v[132:135], v[216:219], a[252:255], v[132:135]\n"
"ds_read_b64_tr_b16 v[216:217], v236 offset:256\n"
"ds_read_b64_tr_b16 v[218:219], v236 offset:2304\n"
"v_mfma_f32_16x16x32_bf16 v[132:135], v[212:215], a[228:231], v[132:135]\n"
"s_waitcnt lgkmcnt(8)\n"
"ds_read_b64_tr_b16 v[212:213], v236 offset:768\n"
"ds_read_b64_tr_b16 v[214:215], v236 offset:2816\n"
"v_mfma_f32_16x16x32_bf16 v[32:35], v[220:223], a[208:211], v[32:35]\n"
"s_waitcnt lgkmcnt(8)\n"
"v_mfma_f32_16x16x32_bf16 v[32:35], v[224:227], a[232:235], v[32:35]\n"
"v_mfma_f32_16x16x32_bf16 v[40:43], v[224:227], a[236:239], v[40:43]\n"
"v_mfma_f32_16x16x32_bf16 v[40:43], v[220:223], a[212:215], v[40:43]\n"
"v_mfma_f32_16x16x32_bf16 v[48:51], v[220:223], a[216:219], v[48:51]\n"
"v_mfma_f32_16x16x32_bf16 v[48:51], v[224:227], a[240:243], v[48:51]\n"
"v_mfma_f32_16x16x32_bf16 v[56:59], v[224:227], a[244:247], v[56:59]\n"
"ds_read_b64_tr_b16 v[224:225], v236 offset:264\n"
"ds_read_b64_tr_b16 v[226:227], v236 offset:2312\n"
"v_mfma_f32_16x16x32_bf16 v[56:59], v[220:223], a[220:223], v[56:59]\n"
"s_waitcnt lgkmcnt(8)\n"
"ds_read_b64_tr_b16 v[220:221], v236 offset:776\n"
"ds_read_b64_tr_b16 v[222:223], v236 offset:2824\n"
"v_mfma_f32_16x16x32_bf16 v[60:63], v[228:231], a[220:223], v[60:63]\n"
"s_waitcnt lgkmcnt(8)\n"
"v_mfma_f32_16x16x32_bf16 v[60:63], v[232:235], a[244:247], v[60:63]\n"
"v_mfma_f32_16x16x32_bf16 v[52:55], v[232:235], a[240:243], v[52:55]\n"
"s_waitcnt vmcnt(13)\n"
"s_barrier\n"
"v_mfma_f32_16x16x32_bf16 v[52:55], v[228:231], a[216:219], v[52:55]\n"
"s_add_u32 m0, s69, 0x23700\n"
"v_mfma_f32_16x16x32_bf16 v[44:47], v[228:231], a[212:215], v[44:47]\n"
"buffer_load_dwordx4 v240, s[44:47], s91 offen lds\n"
"v_mfma_f32_16x16x32_bf16 v[44:47], v[232:235], a[236:239], v[44:47]\n"
"v_mfma_f32_16x16x32_bf16 v[36:39], v[232:235], a[232:235], v[36:39]\n"
"ds_read_b64_tr_b16 v[232:233], v237 offset:8\n"
"ds_read_b64_tr_b16 v[234:235], v237 offset:2056\n"
"v_mfma_f32_16x16x32_bf16 v[36:39], v[228:231], a[208:211], v[36:39]\n"
"s_waitcnt lgkmcnt(8)\n"
"ds_read_b64_tr_b16 v[228:229], v237 offset:520\n"
"ds_read_b64_tr_b16 v[230:231], v237 offset:2568\n"
"v_mfma_f32_16x16x32_bf16 v[64:67], v[216:219], a[208:211], v[64:67]\n"
"s_waitcnt lgkmcnt(8)\n"
"buffer_load_dwordx4 v240, s[44:47], s92 offen offset:1024 lds\n"
"v_mfma_f32_16x16x32_bf16 v[64:67], v[212:215], a[232:235], v[64:67]\n"
"v_mfma_f32_16x16x32_bf16 v[72:75], v[212:215], a[236:239], v[72:75]\n"
"v_mfma_f32_16x16x32_bf16 v[72:75], v[216:219], a[212:215], v[72:75]\n"
"v_mfma_f32_16x16x32_bf16 v[80:83], v[216:219], a[216:219], v[80:83]\n"
"buffer_load_dwordx4 v240, s[44:47], s93 offen offset:2048 lds\n"
"v_mfma_f32_16x16x32_bf16 v[80:83], v[212:215], a[240:243], v[80:83]\n"
"v_mfma_f32_16x16x32_bf16 v[88:91], v[212:215], a[244:247], v[88:91]\n"
"ds_read_b64_tr_b16 v[212:213], v236 offset:384\n"
"ds_read_b64_tr_b16 v[214:215], v236 offset:2432\n"
"v_mfma_f32_16x16x32_bf16 v[88:91], v[216:219], a[220:223], v[88:91]\n"
"s_waitcnt lgkmcnt(8)\n"
"ds_read_b64_tr_b16 v[216:217], v236 offset:896\n"
"ds_read_b64_tr_b16 v[218:219], v236 offset:2944\n"
"v_mfma_f32_16x16x32_bf16 v[92:95], v[224:227], a[220:223], v[92:95]\n"
"s_waitcnt lgkmcnt(8)\n"
"buffer_load_dwordx4 v240, s[44:47], s94 offen offset:3072 lds\n"
"v_mfma_f32_16x16x32_bf16 v[92:95], v[220:223], a[244:247], v[92:95]\n"
"v_mfma_f32_16x16x32_bf16 v[84:87], v[220:223], a[240:243], v[84:87]\n"
"v_mfma_f32_16x16x32_bf16 v[84:87], v[224:227], a[216:219], v[84:87]\n"
"v_mfma_f32_16x16x32_bf16 v[76:79], v[224:227], a[212:215], v[76:79]\n"
"s_add_u32 m0, s69, 0x5400\n"
"v_mfma_f32_16x16x32_bf16 v[76:79], v[220:223], a[236:239], v[76:79]\n"
"buffer_load_dwordx4 v249, s[48:51], s67 offen nt lds\n"
"v_mfma_f32_16x16x32_bf16 v[68:71], v[220:223], a[232:235], v[68:71]\n"
"ds_read_b64_tr_b16 v[220:221], v236 offset:392\n"
"ds_read_b64_tr_b16 v[222:223], v236 offset:2440\n"
"v_mfma_f32_16x16x32_bf16 v[68:71], v[224:227], a[208:211], v[68:71]\n"
"s_waitcnt lgkmcnt(8)\n"
"ds_read_b64_tr_b16 v[224:225], v236 offset:904\n"
"ds_read_b64_tr_b16 v[226:227], v236 offset:2952\n"
"v_mfma_f32_16x16x32_bf16 v[136:139], v[232:235], a[224:227], v[136:139]\n"
"s_waitcnt lgkmcnt(8)\n"
"v_mfma_f32_16x16x32_bf16 v[136:139], v[228:231], a[248:251], v[136:139]\n"
"buffer_load_dwordx4 v249, s[48:51], s67 offen offset:1024 nt lds\n"
"v_mfma_f32_16x16x32_bf16 v[140:143], v[228:231], a[252:255], v[140:143]\n"
"ds_read_b64_tr_b16 v[228:229], v236 offset:16640\n"
"ds_read_b64_tr_b16 v[230:231], v236 offset:18688\n"
"v_mfma_f32_16x16x32_bf16 v[140:143], v[232:235], a[228:231], v[140:143]\n"
"s_waitcnt lgkmcnt(8)\n"
"ds_read_b64_tr_b16 v[232:233], v236 offset:17152\n"
"ds_read_b64_tr_b16 v[234:235], v236 offset:19200\n"
"v_mfma_f32_16x16x32_bf16 v[96:99], v[212:215], a[208:211], v[96:99]\n"
"s_waitcnt lgkmcnt(8)\n"
"v_mfma_f32_16x16x32_bf16 v[96:99], v[216:219], a[232:235], v[96:99]\n"
"buffer_load_dwordx4 v249, s[48:51], s67 offen offset:2048 nt lds\n"
"v_mfma_f32_16x16x32_bf16 v[104:107], v[216:219], a[236:239], v[104:107]\n"
"v_mfma_f32_16x16x32_bf16 v[104:107], v[212:215], a[212:215], v[104:107]\n"
"v_mfma_f32_16x16x32_bf16 v[112:115], v[212:215], a[216:219], v[112:115]\n"
"v_mfma_f32_16x16x32_bf16 v[112:115], v[216:219], a[240:243], v[112:115]\n"
"buffer_load_dwordx4 v249, s[48:51], s67 offen offset:3072 nt lds\n"
"v_mfma_f32_16x16x32_bf16 v[120:123], v[216:219], a[244:247], v[120:123]\n"
"s_add_u32 s67, s67, 0x4000\n"
"ds_read_b64_tr_b16 v[216:217], v236 offset:16648\n"
"ds_read_b64_tr_b16 v[218:219], v236 offset:18696\n"
"v_mfma_f32_16x16x32_bf16 v[120:123], v[212:215], a[220:223], v[120:123]\n"
"s_waitcnt lgkmcnt(8)\n"
"ds_read_b64_tr_b16 v[212:213], v236 offset:17160\n"
"ds_read_b64_tr_b16 v[214:215], v236 offset:19208\n"
"v_mfma_f32_16x16x32_bf16 v[124:127], v[220:223], a[220:223], v[124:127]\n"
"s_waitcnt lgkmcnt(8)\n"
"v_mfma_f32_16x16x32_bf16 v[124:127], v[224:227], a[244:247], v[124:127]\n"
"v_mfma_f32_16x16x32_bf16 v[116:119], v[224:227], a[240:243], v[116:119]\n"
"v_mfma_f32_16x16x32_bf16 v[116:119], v[220:223], a[216:219], v[116:119]\n"
"v_mfma_f32_16x16x32_bf16 v[108:111], v[220:223], a[212:215], v[108:111]\n"
"v_mfma_f32_16x16x32_bf16 v[108:111], v[224:227], a[236:239], v[108:111]\n"
"v_mfma_f32_16x16x32_bf16 v[100:103], v[224:227], a[232:235], v[100:103]\n"
"ds_read_b64_tr_b16 v[224:225], v237 offset:16640\n"
"ds_read_b64_tr_b16 v[226:227], v237 offset:18688\n"
"v_mfma_f32_16x16x32_bf16 v[100:103], v[220:223], a[208:211], v[100:103]\n"
"s_waitcnt lgkmcnt(8)\n"
"ds_read_b64_tr_b16 v[220:221], v237 offset:17152\n"
"ds_read_b64_tr_b16 v[222:223], v237 offset:19200\n"
"v_mfma_f32_16x16x32_bf16 v[160:163], v[228:231], a[208:211], v[160:163]\n"
"s_waitcnt lgkmcnt(8)\n"
"v_mfma_f32_16x16x32_bf16 v[160:163], v[232:235], a[232:235], v[160:163]\n"
"v_mfma_f32_16x16x32_bf16 v[168:171], v[232:235], a[236:239], v[168:171]\n"
"v_mfma_f32_16x16x32_bf16 v[168:171], v[228:231], a[212:215], v[168:171]\n"
"v_mfma_f32_16x16x32_bf16 v[176:179], v[228:231], a[216:219], v[176:179]\n"
"v_mfma_f32_16x16x32_bf16 v[176:179], v[232:235], a[240:243], v[176:179]\n"
"v_mfma_f32_16x16x32_bf16 v[184:187], v[232:235], a[244:247], v[184:187]\n"
"ds_read_b64_tr_b16 v[232:233], v236 offset:16768\n"
"ds_read_b64_tr_b16 v[234:235], v236 offset:18816\n"
"v_mfma_f32_16x16x32_bf16 v[184:187], v[228:231], a[220:223], v[184:187]\n"
"Lpada_3:\n"
"s_cbranch_execz Lpada_7\n"
"s_waitcnt lgkmcnt(8)\n"
"ds_read_b64_tr_b16 v[228:229], v236 offset:17280\n"
"ds_read_b64_tr_b16 v[230:231], v236 offset:19328\n"
"v_mfma_f32_16x16x32_bf16 v[188:191], v[216:219], a[220:223], v[188:191]\n"
"s_waitcnt lgkmcnt(8)\n"
"v_mfma_f32_16x16x32_bf16 v[188:191], v[212:215], a[244:247], v[188:191]\n"
"v_mfma_f32_16x16x32_bf16 v[180:183], v[212:215], a[240:243], v[180:183]\n"
"v_mfma_f32_16x16x32_bf16 v[180:183], v[216:219], a[216:219], v[180:183]\n"
"v_mfma_f32_16x16x32_bf16 v[172:175], v[216:219], a[212:215], v[172:175]\n"
"v_mfma_f32_16x16x32_bf16 v[172:175], v[212:215], a[236:239], v[172:175]\n"
"v_mfma_f32_16x16x32_bf16 v[164:167], v[212:215], a[232:235], v[164:167]\n"
"ds_read_b64_tr_b16 v[212:213], v236 offset:16776\n"
"ds_read_b64_tr_b16 v[214:215], v236 offset:18824\n"
"v_mfma_f32_16x16x32_bf16 v[164:167], v[216:219], a[208:211], v[164:167]\n"
"s_waitcnt lgkmcnt(8)\n"
"ds_read_b64_tr_b16 v[216:217], v236 offset:17288\n"
"ds_read_b64_tr_b16 v[218:219], v236 offset:19336\n"
"v_mfma_f32_16x16x32_bf16 v[144:147], v[224:227], a[224:227], v[144:147]\n"
"s_waitcnt lgkmcnt(8)\n"
"v_mfma_f32_16x16x32_bf16 v[144:147], v[220:223], a[248:251], v[144:147]\n"
"v_mfma_f32_16x16x32_bf16 v[148:151], v[220:223], a[252:255], v[148:151]\n"
"ds_read_b64_tr_b16 v[220:221], v236 offset:16896\n"
"ds_read_b64_tr_b16 v[222:223], v236 offset:18944\n"
"v_mfma_f32_16x16x32_bf16 v[148:151], v[224:227], a[228:231], v[148:151]\n"
"s_waitcnt lgkmcnt(8)\n"
"ds_read_b64_tr_b16 v[224:225], v236 offset:17408\n"
"ds_read_b64_tr_b16 v[226:227], v236 offset:19456\n"
"v_mfma_f32_16x16x32_bf16 v[192:195], v[232:235], a[208:211], v[192:195]\n"
"s_waitcnt lgkmcnt(8)\n"
"v_mfma_f32_16x16x32_bf16 v[192:195], v[228:231], a[232:235], v[192:195]\n"
"v_mfma_f32_16x16x32_bf16 v[200:203], v[228:231], a[236:239], v[200:203]\n"
"v_mfma_f32_16x16x32_bf16 v[200:203], v[232:235], a[212:215], v[200:203]\n"
"v_mfma_f32_16x16x32_bf16 a[80:83], v[232:235], a[216:219], a[80:83]\n"
"v_mfma_f32_16x16x32_bf16 a[80:83], v[228:231], a[240:243], a[80:83]\n"
"v_mfma_f32_16x16x32_bf16 a[88:91], v[228:231], a[244:247], a[88:91]\n"
"ds_read_b64_tr_b16 v[228:229], v236 offset:16904\n"
"ds_read_b64_tr_b16 v[230:231], v236 offset:18952\n"
"v_mfma_f32_16x16x32_bf16 a[88:91], v[232:235], a[220:223], a[88:91]\n"
"s_waitcnt lgkmcnt(8)\n"
"ds_read_b64_tr_b16 v[232:233], v236 offset:17416\n"
"ds_read_b64_tr_b16 v[234:235], v236 offset:19464\n"
"v_mfma_f32_16x16x32_bf16 a[92:95], v[212:215], a[220:223], a[92:95]\n"
"s_waitcnt lgkmcnt(8)\n"
"v_mfma_f32_16x16x32_bf16 a[92:95], v[216:219], a[244:247], a[92:95]\n"
"v_mfma_f32_16x16x32_bf16 a[84:87], v[216:219], a[240:243], a[84:87]\n"
"s_waitcnt vmcnt(8)\n"
"s_barrier\n"
"v_mfma_f32_16x16x32_bf16 a[84:87], v[212:215], a[216:219], a[84:87]\n"
"s_add_u32 m0, s69, 0x9500\n"
"ds_read_b128 a[160:163], v238 offset:36864\n"
"ds_read_b128 a[184:187], v242 offset:36864\n"
"v_mfma_f32_16x16x32_bf16 v[204:207], v[212:215], a[212:215], v[204:207]\n"
"buffer_load_dwordx4 v249, s[48:51], s67 offen nt lds\n"
"v_mfma_f32_16x16x32_bf16 v[204:207], v[216:219], a[236:239], v[204:207]\n"
"ds_read_b128 a[188:191], v242 offset:38912\n"
"ds_read_b128 a[164:167], v238 offset:38912\n"
"v_mfma_f32_16x16x32_bf16 v[196:199], v[216:219], a[232:235], v[196:199]\n"
"ds_read_b64_tr_b16 v[216:217], v237 offset:16648\n"
"ds_read_b64_tr_b16 v[218:219], v237 offset:18696\n"
"v_mfma_f32_16x16x32_bf16 v[196:199], v[212:215], a[208:211], v[196:199]\n"
"s_waitcnt lgkmcnt(12)\n"
"ds_read_b64_tr_b16 v[212:213], v237 offset:17160\n"
"ds_read_b64_tr_b16 v[214:215], v237 offset:19208\n"
"v_mfma_f32_16x16x32_bf16 a[96:99], v[220:223], a[208:211], a[96:99]\n"
"s_waitcnt lgkmcnt(12)\n"
"buffer_load_dwordx4 v249, s[48:51], s67 offen offset:1024 nt lds\n"
"v_mfma_f32_16x16x32_bf16 a[96:99], v[224:227], a[232:235], a[96:99]\n"
"ds_read_b128 a[168:171], v238 offset:40960\n"
"ds_read_b128 a[192:195], v242 offset:40960\n"
"v_mfma_f32_16x16x32_bf16 a[104:107], v[224:227], a[236:239], a[104:107]\n"
"ds_read_b128 a[196:199], v242 offset:43008\n"
"ds_read_b128 a[172:175], v238 offset:43008\n"
"v_mfma_f32_16x16x32_bf16 a[104:107], v[220:223], a[212:215], a[104:107]\n"
"ds_read_b128 a[176:179], v239 offset:36864\n"
"ds_read_b128 a[200:203], v243 offset:36864\n"
"v_mfma_f32_16x16x32_bf16 a[112:115], v[220:223], a[216:219], a[112:115]\n"
"buffer_load_dwordx4 v249, s[48:51], s67 offen offset:2048 nt lds\n"
"v_mfma_f32_16x16x32_bf16 a[112:115], v[224:227], a[240:243], a[112:115]\n"
"ds_read_b128 a[204:207], v243 offset:38912\n"
"ds_read_b128 a[180:183], v239 offset:38912\n"
"v_mfma_f32_16x16x32_bf16 a[120:123], v[224:227], a[244:247], a[120:123]\n"
"ds_read_b64_tr_b16 v[224:225], v236 offset:17024\n"
"ds_read_b64_tr_b16 v[226:227], v236 offset:19072\n"
"v_mfma_f32_16x16x32_bf16 a[120:123], v[220:223], a[220:223], a[120:123]\n"
"s_waitcnt lgkmcnt(15)\n"
"ds_read_b64_tr_b16 v[220:221], v236 offset:17536\n"
"ds_read_b64_tr_b16 v[222:223], v236 offset:19584\n"
"v_mfma_f32_16x16x32_bf16 a[124:127], v[228:231], a[220:223], a[124:127]\n"
"buffer_load_dwordx4 v249, s[48:51], s67 offen offset:3072 nt lds\n"
"v_mfma_f32_16x16x32_bf16 a[124:127], v[232:235], a[244:247], a[124:127]\n"
"s_add_u32 s67, s67, 0x4000\n"
"v_mfma_f32_16x16x32_bf16 a[116:119], v[232:235], a[240:243], a[116:119]\n"
"v_mfma_f32_16x16x32_bf16 a[116:119], v[228:231], a[216:219], a[116:119]\n"
"v_mfma_f32_16x16x32_bf16 a[108:111], v[228:231], a[212:215], a[108:111]\n"
"s_add_u32 m0, s69, 0x17400\n"
"v_mfma_f32_16x16x32_bf16 a[108:111], v[232:235], a[236:239], a[108:111]\n"
"buffer_load_dwordx4 v249, s[52:55], s68 offen nt lds\n"
"v_mfma_f32_16x16x32_bf16 a[100:103], v[232:235], a[232:235], a[100:103]\n"
"ds_read_b64_tr_b16 v[232:233], v236 offset:17032\n"
"ds_read_b64_tr_b16 v[234:235], v236 offset:19080\n"
"v_mfma_f32_16x16x32_bf16 a[100:103], v[228:231], a[208:211], a[100:103]\n"
"s_waitcnt lgkmcnt(15)\n"
"ds_read_b64_tr_b16 v[228:229], v236 offset:17544\n"
"ds_read_b64_tr_b16 v[230:231], v236 offset:19592\n"
"v_mfma_f32_16x16x32_bf16 v[152:155], v[216:219], a[224:227], v[152:155]\n"
"s_waitcnt lgkmcnt(15)\n"
"v_accvgpr_write_b32 a224, 0\n"
"v_accvgpr_write_b32 a225, 0\n"
"v_mfma_f32_16x16x32_bf16 v[152:155], v[212:215], a[248:251], v[152:155]\n"
"buffer_load_dwordx4 v249, s[52:55], s68 offen offset:1024 nt lds\n"
"v_mfma_f32_16x16x32_bf16 v[156:159], v[212:215], a[252:255], v[156:159]\n"
"ds_read_b64_tr_b16 v[212:213], v236 offset:33280\n"
"ds_read_b64_tr_b16 v[214:215], v236 offset:35328\n"
"v_mfma_f32_16x16x32_bf16 v[156:159], v[216:219], a[228:231], v[156:159]\n"
"s_waitcnt lgkmcnt(8)\n"
"ds_read_b64_tr_b16 v[216:217], v236 offset:33792\n"
"ds_read_b64_tr_b16 v[218:219], v236 offset:35840\n"
"v_mfma_f32_16x16x32_bf16 a[128:131], v[224:227], a[208:211], a[128:131]\n"
"s_waitcnt lgkmcnt(8)\n"
"v_accvgpr_write_b32 a226, 0\n"
"v_accvgpr_write_b32 a227, 0\n"
"v_mfma_f32_16x16x32_bf16 a[128:131], v[220:223], a[232:235], a[128:131]\n"
"buffer_load_dwordx4 v249, s[52:55], s68 offen offset:2048 nt lds\n"
"v_mfma_f32_16x16x32_bf16 a[136:139], v[220:223], a[236:239], a[136:139]\n"
"v_accvgpr_write_b32 a228, 0\n"
"v_accvgpr_write_b32 a229, 0\n"
"v_mfma_f32_16x16x32_bf16 a[136:139], v[224:227], a[212:215], a[136:139]\n"
"v_accvgpr_write_b32 a230, 0\n"
"v_accvgpr_write_b32 a231, 0\n"
"v_mfma_f32_16x16x32_bf16 a[144:147], v[224:227], a[216:219], a[144:147]\n"
"v_accvgpr_write_b32 a248, 0\n"
"v_accvgpr_write_b32 a249, 0\n"
"v_mfma_f32_16x16x32_bf16 a[144:147], v[220:223], a[240:243], a[144:147]\n"
"buffer_load_dwordx4 v249, s[52:55], s68 offen offset:3072 nt lds\n"
"v_mfma_f32_16x16x32_bf16 a[152:155], v[220:223], a[244:247], a[152:155]\n"
"s_add_u32 s68, s68, 0x4000\n"
"ds_read_b64_tr_b16 v[220:221], v236 offset:33288\n"
"ds_read_b64_tr_b16 v[222:223], v236 offset:35336\n"
"v_mfma_f32_16x16x32_bf16 a[152:155], v[224:227], a[220:223], a[152:155]\n"
"s_waitcnt lgkmcnt(8)\n"
"ds_read_b64_tr_b16 v[224:225], v236 offset:33800\n"
"ds_read_b64_tr_b16 v[226:227], v236 offset:35848\n"
"v_mfma_f32_16x16x32_bf16 a[156:159], v[232:235], a[220:223], a[156:159]\n"
"s_waitcnt lgkmcnt(8)\n"
"v_accvgpr_write_b32 a220, 0\n"
"v_accvgpr_write_b32 a221, 0\n"
"v_mfma_f32_16x16x32_bf16 a[156:159], v[228:231], a[244:247], a[156:159]\n"
"v_accvgpr_write_b32 a222, 0\n"
"v_accvgpr_write_b32 a223, 0\n"
"v_mfma_f32_16x16x32_bf16 a[148:151], v[228:231], a[240:243], a[148:151]\n"
"v_accvgpr_write_b32 a240, 0\n"
"v_accvgpr_write_b32 a241, 0\n"
"v_mfma_f32_16x16x32_bf16 a[148:151], v[232:235], a[216:219], a[148:151]\n"
"v_accvgpr_write_b32 a216, 0\n"
"v_accvgpr_write_b32 a217, 0\n"
"v_mfma_f32_16x16x32_bf16 a[140:143], v[232:235], a[212:215], a[140:143]\n"
"v_accvgpr_write_b32 a212, 0\n"
"v_accvgpr_write_b32 a213, 0\n"
"v_mfma_f32_16x16x32_bf16 a[140:143], v[228:231], a[236:239], a[140:143]\n"
"v_accvgpr_write_b32 a214, 0\n"
"v_accvgpr_write_b32 a215, 0\n"
"v_mfma_f32_16x16x32_bf16 a[132:135], v[228:231], a[232:235], a[132:135]\n"
"ds_read_b64_tr_b16 v[228:229], v237 offset:33280\n"
"ds_read_b64_tr_b16 v[230:231], v237 offset:35328\n"
"v_mfma_f32_16x16x32_bf16 a[132:135], v[232:235], a[208:211], a[132:135]\n"
"s_waitcnt lgkmcnt(8)\n"
"ds_read_b64_tr_b16 v[232:233], v237 offset:33792\n"
"ds_read_b64_tr_b16 v[234:235], v237 offset:35840\n"
"v_mfma_f32_16x16x32_bf16 v[0:3], v[212:215], a[160:163], v[0:3]\n"
"s_waitcnt lgkmcnt(8)\n"
"v_accvgpr_write_b32 a208, 0\n"
"v_accvgpr_write_b32 a209, 0\n"
"v_mfma_f32_16x16x32_bf16 v[0:3], v[216:219], a[184:187], v[0:3]\n"
"v_accvgpr_write_b32 a210, 0\n"
"v_accvgpr_write_b32 a211, 0\n"
"v_mfma_f32_16x16x32_bf16 v[8:11], v[216:219], a[188:191], v[8:11]\n"
"v_accvgpr_write_b32 a218, 0\n"
"v_accvgpr_write_b32 a219, 0\n"
"v_mfma_f32_16x16x32_bf16 v[8:11], v[212:215], a[164:167], v[8:11]\n"
"v_accvgpr_write_b32 a232, 0\n"
"v_accvgpr_write_b32 a233, 0\n"
"v_mfma_f32_16x16x32_bf16 v[16:19], v[212:215], a[168:171], v[16:19]\n"
"v_cvt_pk_bf16_f32 v0, v0, v1\n"
"v_cvt_pk_bf16_f32 v1, v2, v3\n"
"v_mfma_f32_16x16x32_bf16 v[16:19], v[216:219], a[192:195], v[16:19]\n"
"v_accvgpr_write_b32 a234, 0\n"
"v_accvgpr_write_b32 a235, 0\n"
"v_mfma_f32_16x16x32_bf16 v[24:27], v[216:219], a[196:199], v[24:27]\n"
"ds_read_b64_tr_b16 v[216:217], v236 offset:33408\n"
"ds_read_b64_tr_b16 v[218:219], v236 offset:35456\n"
"v_mfma_f32_16x16x32_bf16 v[24:27], v[212:215], a[172:175], v[24:27]\n"
"s_waitcnt lgkmcnt(8)\n"
"ds_read_b64_tr_b16 v[212:213], v236 offset:33920\n"
"ds_read_b64_tr_b16 v[214:215], v236 offset:35968\n"
"v_mfma_f32_16x16x32_bf16 v[28:31], v[220:223], a[172:175], v[28:31]\n"
"s_waitcnt lgkmcnt(8)\n"
"v_cvt_pk_bf16_f32 v8, v8, v9\n"
"v_cvt_pk_bf16_f32 v9, v10, v11\n"
"v_mfma_f32_16x16x32_bf16 v[28:31], v[224:227], a[196:199], v[28:31]\n"
"v_cvt_pk_bf16_f32 v16, v16, v17\n"
"v_cvt_pk_bf16_f32 v17, v18, v19\n"
"v_mfma_f32_16x16x32_bf16 v[20:23], v[224:227], a[192:195], v[20:23]\n"
"v_cvt_pk_bf16_f32 v24, v24, v25\n"
"v_cvt_pk_bf16_f32 v25, v26, v27\n"
"v_mfma_f32_16x16x32_bf16 v[20:23], v[220:223], a[168:171], v[20:23]\n"
"v_accvgpr_write_b32 a236, 0\n"
"v_accvgpr_write_b32 a237, 0\n"
"v_mfma_f32_16x16x32_bf16 v[12:15], v[220:223], a[164:167], v[12:15]\n"
"v_cvt_pk_bf16_f32 v26, v28, v29\n"
"v_cvt_pk_bf16_f32 v27, v30, v31\n"
"v_mfma_f32_16x16x32_bf16 v[12:15], v[224:227], a[188:191], v[12:15]\n"
"v_accvgpr_write_b32 a238, 0\n"
"v_accvgpr_write_b32 a239, 0\n"
"v_mfma_f32_16x16x32_bf16 v[4:7], v[224:227], a[184:187], v[4:7]\n"
"ds_read_b64_tr_b16 v[224:225], v236 offset:33416\n"
"ds_read_b64_tr_b16 v[226:227], v236 offset:35464\n"
"v_mfma_f32_16x16x32_bf16 v[4:7], v[220:223], a[160:163], v[4:7]\n"
"s_waitcnt lgkmcnt(8)\n"
"ds_read_b64_tr_b16 v[220:221], v236 offset:33928\n"
"ds_read_b64_tr_b16 v[222:223], v236 offset:35976\n"
"v_mfma_f32_16x16x32_bf16 v[128:131], v[228:231], a[176:179], v[128:131]\n"
"s_waitcnt lgkmcnt(8)\n"
"v_cvt_pk_bf16_f32 v10, v12, v13\n"
"v_cvt_pk_bf16_f32 v11, v14, v15\n"
"v_mfma_f32_16x16x32_bf16 v[128:131], v[232:235], a[200:203], v[128:131]\n"
"v_cvt_pk_bf16_f32 v2, v4, v5\n"
"v_cvt_pk_bf16_f32 v3, v6, v7\n"
"v_mfma_f32_16x16x32_bf16 v[132:135], v[232:235], a[204:207], v[132:135]\n"
"ds_read_b64_tr_b16 v[232:233], v236 offset:33536\n"
"ds_read_b64_tr_b16 v[234:235], v236 offset:35584\n"
"v_mfma_f32_16x16x32_bf16 v[132:135], v[228:231], a[180:183], v[132:135]\n"
"s_waitcnt lgkmcnt(8)\n"
"ds_read_b64_tr_b16 v[228:229], v236 offset:34048\n"
"ds_read_b64_tr_b16 v[230:231], v236 offset:36096\n"
"v_mfma_f32_16x16x32_bf16 v[32:35], v[216:219], a[160:163], v[32:35]\n"
"s_waitcnt lgkmcnt(8)\n"
"v_cvt_pk_bf16_f32 v18, v20, v21\n"
"v_cvt_pk_bf16_f32 v19, v22, v23\n"
"v_mfma_f32_16x16x32_bf16 v[32:35], v[212:215], a[184:187], v[32:35]\n"
"v_cvt_pk_bf16_f32 v128, v128, v129\n"
"v_cvt_pk_bf16_f32 v132, v132, v133\n"
"v_mfma_f32_16x16x32_bf16 v[40:43], v[212:215], a[188:191], v[40:43]\n"
"v_cvt_pk_bf16_f32 v129, v130, v131\n"
"v_cvt_pk_bf16_f32 v133, v134, v135\n"
"v_mfma_f32_16x16x32_bf16 v[40:43], v[216:219], a[164:167], v[40:43]\n"
"v_accvgpr_write_b32 a242, 0\n"
"v_accvgpr_write_b32 a243, 0\n"
"v_mfma_f32_16x16x32_bf16 v[48:51], v[216:219], a[168:171], v[48:51]\n"
"v_cvt_pk_bf16_f32 v32, v32, v33\n"
"v_cvt_pk_bf16_f32 v33, v34, v35\n"
"v_mfma_f32_16x16x32_bf16 v[48:51], v[212:215], a[192:195], v[48:51]\n"
"v_accvgpr_write_b32 a244, 0\n"
"v_accvgpr_write_b32 a245, 0\n"
"v_mfma_f32_16x16x32_bf16 v[56:59], v[212:215], a[196:199], v[56:59]\n"
"ds_read_b64_tr_b16 v[212:213], v236 offset:33544\n"
"ds_read_b64_tr_b16 v[214:215], v236 offset:35592\n"
"v_mfma_f32_16x16x32_bf16 v[56:59], v[216:219], a[172:175], v[56:59]\n"
"s_waitcnt lgkmcnt(8)\n"
"ds_read_b64_tr_b16 v[216:217], v236 offset:34056\n"
"ds_read_b64_tr_b16 v[218:219], v236 offset:36104\n"
"v_mfma_f32_16x16x32_bf16 v[60:63], v[224:227], a[172:175], v[60:63]\n"
"s_waitcnt lgkmcnt(8)\n"
"v_cvt_pk_bf16_f32 v40, v40, v41\n"
"v_cvt_pk_bf16_f32 v41, v42, v43\n"
"v_mfma_f32_16x16x32_bf16 v[60:63], v[220:223], a[196:199], v[60:63]\n"
"v_cvt_pk_bf16_f32 v48, v48, v49\n"
"v_cvt_pk_bf16_f32 v49, v50, v51\n"
"v_mfma_f32_16x16x32_bf16 v[52:55], v[220:223], a[192:195], v[52:55]\n"
"s_waitcnt vmcnt(12)\n"
"s_barrier\n"
"v_mfma_f32_16x16x32_bf16 v[52:55], v[224:227], a[168:171], v[52:55]\n"
"s_add_u32 m0, s69, 0xd600\n"
"v_cvt_pk_bf16_f32 v56, v56, v57\n"
"v_cvt_pk_bf16_f32 v57, v58, v59\n"
"v_mfma_f32_16x16x32_bf16 v[44:47], v[224:227], a[164:167], v[44:47]\n"
"buffer_load_dwordx4 v249, s[48:51], s67 offen nt lds\n"
"v_mfma_f32_16x16x32_bf16 v[44:47], v[220:223], a[188:191], v[44:47]\n"
"v_cvt_pk_bf16_f32 v58, v60, v61\n"
"v_cvt_pk_bf16_f32 v59, v62, v63\n"
"v_mfma_f32_16x16x32_bf16 v[36:39], v[220:223], a[184:187], v[36:39]\n"
"ds_read_b64_tr_b16 v[220:221], v237 offset:33288\n"
"ds_read_b64_tr_b16 v[222:223], v237 offset:35336\n"
"v_mfma_f32_16x16x32_bf16 v[36:39], v[224:227], a[160:163], v[36:39]\n"
"s_waitcnt lgkmcnt(8)\n"
"ds_read_b64_tr_b16 v[224:225], v237 offset:33800\n"
"ds_read_b64_tr_b16 v[226:227], v237 offset:35848\n"
"v_mfma_f32_16x16x32_bf16 v[64:67], v[232:235], a[160:163], v[64:67]\n"
"s_waitcnt lgkmcnt(8)\n"
"buffer_load_dwordx4 v249, s[48:51], s67 offen offset:1024 nt lds\n"
"v_mfma_f32_16x16x32_bf16 v[64:67], v[228:231], a[184:187], v[64:67]\n"
"v_cvt_pk_bf16_f32 v42, v44, v45\n"
"v_cvt_pk_bf16_f32 v34, v36, v37\n"
"v_mfma_f32_16x16x32_bf16 v[72:75], v[228:231], a[188:191], v[72:75]\n"
"v_cvt_pk_bf16_f32 v35, v38, v39\n"
"v_cvt_pk_bf16_f32 v43, v46, v47\n"
"v_mfma_f32_16x16x32_bf16 v[72:75], v[232:235], a[164:167], v[72:75]\n"
"v_cvt_pk_bf16_f32 v50, v52, v53\n"
"v_cvt_pk_bf16_f32 v51, v54, v55\n"
"v_mfma_f32_16x16x32_bf16 v[80:83], v[232:235], a[168:171], v[80:83]\n"
"buffer_load_dwordx4 v249, s[48:51], s67 offen offset:2048 nt lds\n"
"v_mfma_f32_16x16x32_bf16 v[80:83], v[228:231], a[192:195], v[80:83]\n"
"v_cvt_pk_bf16_f32 v64, v64, v65\n"
"v_cvt_pk_bf16_f32 v65, v66, v67\n"
"v_mfma_f32_16x16x32_bf16 v[88:91], v[228:231], a[196:199], v[88:91]\n"
"ds_read_b64_tr_b16 v[228:229], v236 offset:33664\n"
"ds_read_b64_tr_b16 v[230:231], v236 offset:35712\n"
"v_mfma_f32_16x16x32_bf16 v[88:91], v[232:235], a[172:175], v[88:91]\n"
"s_waitcnt lgkmcnt(8)\n"
"ds_read_b64_tr_b16 v[232:233], v236 offset:34176\n"
"ds_read_b64_tr_b16 v[234:235], v236 offset:36224\n"
"v_mfma_f32_16x16x32_bf16 v[92:95], v[212:215], a[172:175], v[92:95]\n"
"s_waitcnt lgkmcnt(8)\n"
"buffer_load_dwordx4 v249, s[48:51], s67 offen offset:3072 nt lds\n"
"v_mfma_f32_16x16x32_bf16 v[92:95], v[216:219], a[196:199], v[92:95]\n"
"s_add_u32 s67, s67, 0x4000\n"
"v_cvt_pk_bf16_f32 v72, v72, v73\n"
"v_cvt_pk_bf16_f32 v73, v74, v75\n"
"v_mfma_f32_16x16x32_bf16 v[84:87], v[216:219], a[192:195], v[84:87]\n"
"v_cvt_pk_bf16_f32 v80, v80, v81\n"
"v_cvt_pk_bf16_f32 v81, v82, v83\n"
"v_mfma_f32_16x16x32_bf16 v[84:87], v[212:215], a[168:171], v[84:87]\n"
"v_cvt_pk_bf16_f32 v88, v88, v89\n"
"v_cvt_pk_bf16_f32 v89, v90, v91\n"
"v_mfma_f32_16x16x32_bf16 v[76:79], v[212:215], a[164:167], v[76:79]\n"
"s_add_u32 m0, s69, 0x1b500\n"
"v_cvt_pk_bf16_f32 v90, v92, v93\n"
"v_cvt_pk_bf16_f32 v91, v94, v95\n"
"v_mfma_f32_16x16x32_bf16 v[76:79], v[216:219], a[188:191], v[76:79]\n"
"buffer_load_dwordx4 v249, s[52:55], s68 offen nt lds\n"
"v_mfma_f32_16x16x32_bf16 v[68:71], v[216:219], a[184:187], v[68:71]\n"
"ds_read_b64_tr_b16 v[216:217], v236 offset:33672\n"
"ds_read_b64_tr_b16 v[218:219], v236 offset:35720\n"
"v_mfma_f32_16x16x32_bf16 v[68:71], v[212:215], a[160:163], v[68:71]\n"
"s_waitcnt lgkmcnt(8)\n"
"ds_read_b64_tr_b16 v[212:213], v236 offset:34184\n"
"ds_read_b64_tr_b16 v[214:215], v236 offset:36232\n"
"v_mfma_f32_16x16x32_bf16 v[136:139], v[220:223], a[176:179], v[136:139]\n"
"s_waitcnt lgkmcnt(8)\n"
"v_cvt_pk_bf16_f32 v74, v76, v77\n"
"v_cvt_pk_bf16_f32 v75, v78, v79\n"
"v_mfma_f32_16x16x32_bf16 v[136:139], v[224:227], a[200:203], v[136:139]\n"
"buffer_load_dwordx4 v249, s[52:55], s68 offen offset:1024 nt lds\n"
"v_mfma_f32_16x16x32_bf16 v[140:143], v[224:227], a[204:207], v[140:143]\n"
"ds_read_b64_tr_b16 v[224:225], v236 offset:49920\n"
"ds_read_b64_tr_b16 v[226:227], v236 offset:51968\n"
"v_mfma_f32_16x16x32_bf16 v[140:143], v[220:223], a[180:183], v[140:143]\n"
"s_waitcnt lgkmcnt(8)\n"
"ds_read_b64_tr_b16 v[220:221], v236 offset:50432\n"
"ds_read_b64_tr_b16 v[222:223], v236 offset:52480\n"
"v_mfma_f32_16x16x32_bf16 v[96:99], v[228:231], a[160:163], v[96:99]\n"
"s_waitcnt lgkmcnt(8)\n"
"v_cvt_pk_bf16_f32 v66, v68, v69\n"
"v_cvt_pk_bf16_f32 v67, v70, v71\n"
"v_mfma_f32_16x16x32_bf16 v[96:99], v[232:235], a[184:187], v[96:99]\n"
"buffer_load_dwordx4 v249, s[52:55], s68 offen offset:2048 nt lds\n"
"v_mfma_f32_16x16x32_bf16 v[104:107], v[232:235], a[188:191], v[104:107]\n"
"v_cvt_pk_bf16_f32 v82, v84, v85\n"
"v_cvt_pk_bf16_f32 v83, v86, v87\n"
"v_mfma_f32_16x16x32_bf16 v[104:107], v[228:231], a[164:167], v[104:107]\n"
"v_cvt_pk_bf16_f32 v130, v136, v137\n"
"v_cvt_pk_bf16_f32 v131, v138, v139\n"
"v_mfma_f32_16x16x32_bf16 v[112:115], v[228:231], a[168:171], v[112:115]\n"
"v_cvt_pk_bf16_f32 v96, v96, v97\n"
"v_cvt_pk_bf16_f32 v97, v98, v99\n"
"v_mfma_f32_16x16x32_bf16 v[112:115], v[232:235], a[192:195], v[112:115]\n"
"buffer_load_dwordx4 v249, s[52:55], s68 offen offset:3072 nt lds\n"
"v_mfma_f32_16x16x32_bf16 v[120:123], v[232:235], a[196:199], v[120:123]\n"
"s_add_u32 s68, s68, 0x4000\n"
"ds_read_b64_tr_b16 v[232:233], v237 offset:49920\n"
"ds_read_b64_tr_b16 v[234:235], v237 offset:51968\n"
"v_mfma_f32_16x16x32_bf16 v[120:123], v[228:231], a[172:175], v[120:123]\n"
"s_waitcnt lgkmcnt(8)\n"
"ds_read_b64_tr_b16 v[228:229], v237 offset:50432\n"
"ds_read_b64_tr_b16 v[230:231], v237 offset:52480\n"
"v_mfma_f32_16x16x32_bf16 v[124:127], v[216:219], a[172:175], v[124:127]\n"
"s_waitcnt lgkmcnt(8)\n"
"v_cvt_pk_bf16_f32 v104, v104, v105\n"
"v_cvt_pk_bf16_f32 v105, v106, v107\n"
"v_mfma_f32_16x16x32_bf16 v[124:127], v[212:215], a[196:199], v[124:127]\n"
"v_cvt_pk_bf16_f32 v112, v112, v113\n"
"v_cvt_pk_bf16_f32 v113, v114, v115\n"
"v_mfma_f32_16x16x32_bf16 v[116:119], v[212:215], a[192:195], v[116:119]\n"
"v_cvt_pk_bf16_f32 v120, v120, v121\n"
"v_cvt_pk_bf16_f32 v121, v122, v123\n"
"v_mfma_f32_16x16x32_bf16 v[116:119], v[216:219], a[168:171], v[116:119]\n"
"v_cvt_pk_bf16_f32 v134, v140, v141\n"
"v_cvt_pk_bf16_f32 v135, v142, v143\n"
"v_mfma_f32_16x16x32_bf16 v[108:111], v[216:219], a[164:167], v[108:111]\n"
"v_cvt_pk_bf16_f32 v122, v124, v125\n"
"v_cvt_pk_bf16_f32 v123, v126, v127\n"
"v_mfma_f32_16x16x32_bf16 v[108:111], v[212:215], a[188:191], v[108:111]\n"
"ds_write_b128 v251, v[128:131] offset:5120\n"
"v_mfma_f32_16x16x32_bf16 v[100:103], v[212:215], a[184:187], v[100:103]\n"
"ds_read_b64_tr_b16 v[212:213], v236 offset:49928\n"
"ds_read_b64_tr_b16 v[214:215], v236 offset:51976\n"
"v_mfma_f32_16x16x32_bf16 v[100:103], v[216:219], a[160:163], v[100:103]\n"
"s_waitcnt lgkmcnt(9)\n"
"ds_read_b64_tr_b16 v[216:217], v236 offset:50440\n"
"ds_read_b64_tr_b16 v[218:219], v236 offset:52488\n"
"v_mfma_f32_16x16x32_bf16 v[160:163], v[224:227], a[160:163], v[160:163]\n"
"s_waitcnt lgkmcnt(9)\n"
"v_cvt_pk_bf16_f32 v106, v108, v109\n"
"v_cvt_pk_bf16_f32 v107, v110, v111\n"
"v_mfma_f32_16x16x32_bf16 v[160:163], v[220:223], a[184:187], v[160:163]\n"
"v_cvt_pk_bf16_f32 v98, v100, v101\n"
"v_cvt_pk_bf16_f32 v99, v102, v103\n"
"v_mfma_f32_16x16x32_bf16 v[168:171], v[220:223], a[188:191], v[168:171]\n"
"v_cvt_pk_bf16_f32 v114, v116, v117\n"
"v_cvt_pk_bf16_f32 v115, v118, v119\n"
"v_mfma_f32_16x16x32_bf16 v[168:171], v[224:227], a[164:167], v[168:171]\n"
"ds_write_b128 v251, v[132:135] offset:13312\n"
"v_mfma_f32_16x16x32_bf16 v[176:179], v[224:227], a[168:171], v[176:179]\n"
"v_cvt_pk_bf16_f32 v4, v160, v161\n"
"v_cvt_pk_bf16_f32 v5, v162, v163\n"
"v_mfma_f32_16x16x32_bf16 v[176:179], v[220:223], a[192:195], v[176:179]\n"
"v_accvgpr_write_b32 a246, 0\n"
"v_accvgpr_write_b32 a247, 0\n"
"v_mfma_f32_16x16x32_bf16 v[184:187], v[220:223], a[196:199], v[184:187]\n"
"ds_read_b64_tr_b16 v[220:221], v237 offset:49928\n"
"ds_read_b64_tr_b16 v[222:223], v237 offset:51976\n"
"v_mfma_f32_16x16x32_bf16 v[184:187], v[224:227], a[172:175], v[184:187]\n"
"s_waitcnt lgkmcnt(10)\n"
"ds_read_b64_tr_b16 v[224:225], v237 offset:50440\n"
"ds_read_b64_tr_b16 v[226:227], v237 offset:52488\n"
"v_mfma_f32_16x16x32_bf16 v[144:147], v[232:235], a[176:179], v[144:147]\n"
"s_waitcnt lgkmcnt(10)\n"
"v_cvt_pk_bf16_f32 v12, v168, v169\n"
"v_cvt_pk_bf16_f32 v13, v170, v171\n"
"v_mfma_f32_16x16x32_bf16 v[144:147], v[228:231], a[200:203], v[144:147]\n"
"v_cvt_pk_bf16_f32 v20, v176, v177\n"
"v_cvt_pk_bf16_f32 v21, v178, v179\n"
"v_mfma_f32_16x16x32_bf16 v[148:151], v[228:231], a[204:207], v[148:151]\n"
"ds_read_b64_tr_b16 v[228:229], v236 offset:50048\n"
"ds_read_b64_tr_b16 v[230:231], v236 offset:52096\n"
"v_mfma_f32_16x16x32_bf16 v[148:151], v[232:235], a[180:183], v[148:151]\n"
"s_waitcnt lgkmcnt(9)\n"
"ds_read_b64_tr_b16 v[232:233], v236 offset:50560\n"
"ds_read_b64_tr_b16 v[234:235], v236 offset:52608\n"
"v_mfma_f32_16x16x32_bf16 v[188:191], v[212:215], a[172:175], v[188:191]\n"
"s_waitcnt lgkmcnt(9)\n"
"v_cvt_pk_bf16_f32 v28, v184, v185\n"
"v_cvt_pk_bf16_f32 v29, v186, v187\n"
"v_mfma_f32_16x16x32_bf16 v[188:191], v[216:219], a[196:199], v[188:191]\n"
"v_cvt_pk_bf16_f32 v144, v144, v145\n"
"v_cvt_pk_bf16_f32 v148, v148, v149\n"
"v_mfma_f32_16x16x32_bf16 v[180:183], v[216:219], a[192:195], v[180:183]\n"
"v_cvt_pk_bf16_f32 v145, v146, v147\n"
"v_cvt_pk_bf16_f32 v149, v150, v151\n"
"v_mfma_f32_16x16x32_bf16 v[180:183], v[212:215], a[168:171], v[180:183]\n"
"v_accvgpr_write_b32 a250, 0\n"
"v_accvgpr_write_b32 a251, 0\n"
"v_mfma_f32_16x16x32_bf16 v[172:175], v[212:215], a[164:167], v[172:175]\n"
"v_cvt_pk_bf16_f32 v30, v188, v189\n"
"v_cvt_pk_bf16_f32 v31, v190, v191\n"
"v_mfma_f32_16x16x32_bf16 v[172:175], v[216:219], a[188:191], v[172:175]\n"
"v_accvgpr_write_b32 a252, 0\n"
"v_accvgpr_write_b32 a253, 0\n"
"v_mfma_f32_16x16x32_bf16 v[164:167], v[216:219], a[184:187], v[164:167]\n"
"ds_read_b64_tr_b16 v[216:217], v236 offset:50056\n"
"ds_read_b64_tr_b16 v[218:219], v236 offset:52104\n"
"v_mfma_f32_16x16x32_bf16 v[164:167], v[212:215], a[160:163], v[164:167]\n"
"s_waitcnt lgkmcnt(8)\n"
"ds_read_b64_tr_b16 v[212:213], v236 offset:50568\n"
"ds_read_b64_tr_b16 v[214:215], v236 offset:52616\n"
"v_mfma_f32_16x16x32_bf16 v[152:155], v[220:223], a[176:179], v[152:155]\n"
"s_waitcnt lgkmcnt(8)\n"
"v_cvt_pk_bf16_f32 v14, v172, v173\n"
"v_cvt_pk_bf16_f32 v15, v174, v175\n"
"v_mfma_f32_16x16x32_bf16 v[152:155], v[224:227], a[200:203], v[152:155]\n"
"v_cvt_pk_bf16_f32 v6, v164, v165\n"
"v_cvt_pk_bf16_f32 v7, v166, v167\n"
"v_mfma_f32_16x16x32_bf16 v[156:159], v[224:227], a[204:207], v[156:159]\n"
"ds_read_b64_tr_b16 v[224:225], v236 offset:50176\n"
"ds_read_b64_tr_b16 v[226:227], v236 offset:52224\n"
"v_mfma_f32_16x16x32_bf16 v[156:159], v[220:223], a[180:183], v[156:159]\n"
"s_waitcnt lgkmcnt(8)\n"
"ds_read_b64_tr_b16 v[220:221], v236 offset:50688\n"
"ds_read_b64_tr_b16 v[222:223], v236 offset:52736\n"
"v_mfma_f32_16x16x32_bf16 v[192:195], v[228:231], a[160:163], v[192:195]\n"
"s_waitcnt lgkmcnt(8)\n"
"v_cvt_pk_bf16_f32 v22, v180, v181\n"
"v_cvt_pk_bf16_f32 v23, v182, v183\n"
"v_mfma_f32_16x16x32_bf16 v[192:195], v[232:235], a[184:187], v[192:195]\n"
"v_cvt_pk_bf16_f32 v146, v152, v153\n"
"v_cvt_pk_bf16_f32 v147, v154, v155\n"
"v_mfma_f32_16x16x32_bf16 v[200:203], v[232:235], a[188:191], v[200:203]\n"
"v_cvt_pk_bf16_f32 v150, v156, v157\n"
"v_cvt_pk_bf16_f32 v151, v158, v159\n"
"v_mfma_f32_16x16x32_bf16 v[200:203], v[228:231], a[164:167], v[200:203]\n"
"ds_write_b128 v251, v[144:147] offset:9216\n"
"v_mfma_f32_16x16x32_bf16 a[80:83], v[228:231], a[168:171], a[80:83]\n"
"v_cvt_pk_bf16_f32 v36, v192, v193\n"
"v_cvt_pk_bf16_f32 v37, v194, v195\n"
"v_mfma_f32_16x16x32_bf16 a[80:83], v[232:235], a[192:195], a[80:83]\n"
"ds_write_b128 v251, v[148:151] offset:17408\n"
"v_mfma_f32_16x16x32_bf16 a[88:91], v[232:235], a[196:199], a[88:91]\n"
"s_waitcnt vmcnt(16)\n"
"s_barrier\n"
"v_mfma_f32_16x16x32_bf16 a[88:91], v[228:231], a[172:175], a[88:91]\n"
"s_waitcnt lgkmcnt(8)\n"
"ds_read_b64_tr_b16 v[232:233], v236 offset:50184\n"
"ds_read_b64_tr_b16 v[234:235], v236 offset:52232\n"
"v_mfma_f32_16x16x32_bf16 a[92:95], v[216:219], a[172:175], a[92:95]\n"
"s_waitcnt lgkmcnt(8)\n"
"ds_read_b64_tr_b16 v[228:229], v236 offset:50696\n"
"ds_read_b64_tr_b16 v[230:231], v236 offset:52744\n"
"v_mfma_f32_16x16x32_bf16 a[92:95], v[212:215], a[196:199], a[92:95]\n"
"s_add_u32 m0, s69, 0x11700\n"
"v_accvgpr_read_b32 v52, a80\n"
"v_accvgpr_read_b32 v53, a81\n"
"v_mfma_f32_16x16x32_bf16 a[84:87], v[212:215], a[192:195], a[84:87]\n"
"buffer_load_dwordx4 v249, s[48:51], s67 offen nt lds\n"
"v_mfma_f32_16x16x32_bf16 a[84:87], v[216:219], a[168:171], a[84:87]\n"
"v_accvgpr_read_b32 v54, a82\n"
"v_accvgpr_read_b32 v55, a83\n"
"v_mfma_f32_16x16x32_bf16 v[204:207], v[216:219], a[164:167], v[204:207]\n"
"v_cvt_pk_bf16_f32 v52, v52, v53\n"
"v_cvt_pk_bf16_f32 v44, v200, v201\n"
"v_mfma_f32_16x16x32_bf16 v[204:207], v[212:215], a[188:191], v[204:207]\n"
"buffer_load_dwordx4 v249, s[48:51], s67 offen offset:1024 nt lds\n"
"v_mfma_f32_16x16x32_bf16 v[196:199], v[212:215], a[184:187], v[196:199]\n"
"ds_read_b64_tr_b16 v[212:213], v236 offset:50304\n"
"ds_read_b64_tr_b16 v[214:215], v236 offset:52352\n"
"v_mfma_f32_16x16x32_bf16 v[196:199], v[216:219], a[160:163], v[196:199]\n"
"s_waitcnt lgkmcnt(10)\n"
"ds_read_b64_tr_b16 v[216:217], v236 offset:50816\n"
"ds_read_b64_tr_b16 v[218:219], v236 offset:52864\n"
"v_mfma_f32_16x16x32_bf16 a[96:99], v[224:227], a[160:163], a[96:99]\n"
"s_waitcnt lgkmcnt(10)\n"
"buffer_load_dwordx4 v249, s[48:51], s67 offen offset:2048 nt lds\n"
"v_mfma_f32_16x16x32_bf16 a[96:99], v[220:223], a[184:187], a[96:99]\n"
"v_cvt_pk_bf16_f32 v45, v202, v203\n"
"v_cvt_pk_bf16_f32 v38, v196, v197\n"
"v_cvt_pk_bf16_f32 v39, v198, v199\n"
"v_mfma_f32_16x16x32_bf16 a[104:107], v[220:223], a[188:191], a[104:107]\n"
"v_cvt_pk_bf16_f32 v46, v204, v205\n"
"v_cvt_pk_bf16_f32 v47, v206, v207\n"
"v_cvt_pk_bf16_f32 v53, v54, v55\n"
"v_mfma_f32_16x16x32_bf16 a[104:107], v[224:227], a[164:167], a[104:107]\n"
"Lpada_4:\n"
"s_cbranch_execz Lpada_8\n"
"v_accvgpr_read_b32 v54, a84\n"
"v_accvgpr_read_b32 v55, a85\n"
"v_mfma_f32_16x16x32_bf16 a[112:115], v[224:227], a[168:171], a[112:115]\n"
"buffer_load_dwordx4 v249, s[48:51], s67 offen offset:3072 nt lds\n"
"v_cvt_pk_bf16_f32 v54, v54, v55\n"
"v_mfma_f32_16x16x32_bf16 a[112:115], v[220:223], a[192:195], a[112:115]\n"
"s_add_u32 s67, s67, 0x4000\n"
"v_accvgpr_read_b32 v60, a88\n"
"v_accvgpr_read_b32 v61, a89\n"
"v_accvgpr_read_b32 v208, a87\n"
"v_mfma_f32_16x16x32_bf16 a[120:123], v[220:223], a[196:199], a[120:123]\n"
"ds_read_b64_tr_b16 v[220:221], v236 offset:50312\n"
"ds_read_b64_tr_b16 v[222:223], v236 offset:52360\n"
"v_accvgpr_read_b32 v55, a86\n"
"v_mfma_f32_16x16x32_bf16 a[120:123], v[224:227], a[172:175], a[120:123]\n"
"s_waitcnt lgkmcnt(8)\n"
"ds_read_b64_tr_b16 v[224:225], v236 offset:50824\n"
"ds_read_b64_tr_b16 v[226:227], v236 offset:52872\n"
"v_mfma_f32_16x16x32_bf16 a[124:127], v[232:235], a[172:175], a[124:127]\n"
"s_waitcnt lgkmcnt(8)\n"
"v_accvgpr_read_b32 v62, a90\n"
"v_accvgpr_read_b32 v63, a91\n"
"v_mfma_f32_16x16x32_bf16 a[124:127], v[228:231], a[196:199], a[124:127]\n"
"v_cvt_pk_bf16_f32 v60, v60, v61\n"
"v_cvt_pk_bf16_f32 v55, v55, v208\n"
"v_mfma_f32_16x16x32_bf16 a[116:119], v[228:231], a[192:195], a[116:119]\n"
"v_cvt_pk_bf16_f32 v61, v62, v63\n"
"v_accvgpr_read_b32 v62, a92\n"
"v_mfma_f32_16x16x32_bf16 a[116:119], v[232:235], a[168:171], a[116:119]\n"
"v_accvgpr_read_b32 v63, a93\n"
"v_cvt_pk_bf16_f32 v62, v62, v63\n"
"v_accvgpr_read_b32 v68, a96\n"
"v_mfma_f32_16x16x32_bf16 a[108:111], v[232:235], a[164:167], a[108:111]\n"
"v_accvgpr_read_b32 v69, a97\n"
"v_accvgpr_read_b32 v209, a95\n"
"v_mfma_f32_16x16x32_bf16 a[108:111], v[228:231], a[188:191], a[108:111]\n"
"v_accvgpr_read_b32 v63, a94\n"
"v_accvgpr_read_b32 v70, a98\n"
"v_mfma_f32_16x16x32_bf16 a[100:103], v[228:231], a[184:187], a[100:103]\n"
"ds_read_b128 v[228:231], v246 offset:0\n"
"v_accvgpr_read_b32 v71, a99\n"
"v_mfma_f32_16x16x32_bf16 a[100:103], v[232:235], a[160:163], a[100:103]\n"
"s_waitcnt lgkmcnt(7)\n"
"ds_read_b128 v[232:235], v246 offset:128\n"
"v_cvt_pk_bf16_f32 v68, v68, v69\n"
"v_mfma_f32_16x16x32_bf16 a[128:131], v[212:215], a[160:163], a[128:131]\n"
"s_waitcnt lgkmcnt(6)\n"
"v_cvt_pk_bf16_f32 v63, v63, v209\n"
"v_cvt_pk_bf16_f32 v69, v70, v71\n"
"v_mfma_f32_16x16x32_bf16 a[128:131], v[216:219], a[184:187], a[128:131]\n"
"v_accvgpr_read_b32 v70, a100\n"
"v_accvgpr_read_b32 v71, a101\n"
"v_cvt_pk_bf16_f32 v70, v70, v71\n"
"v_mfma_f32_16x16x32_bf16 a[136:139], v[216:219], a[188:191], a[136:139]\n"
"v_accvgpr_read_b32 v76, a104\n"
"v_accvgpr_read_b32 v77, a105\n"
"v_mfma_f32_16x16x32_bf16 a[136:139], v[212:215], a[164:167], a[136:139]\n"
"v_accvgpr_read_b32 v210, a103\n"
"v_accvgpr_read_b32 v71, a102\n"
"v_mfma_f32_16x16x32_bf16 a[144:147], v[212:215], a[168:171], a[144:147]\n"
"v_accvgpr_read_b32 v78, a106\n"
"v_accvgpr_read_b32 v79, a107\n"
"v_mfma_f32_16x16x32_bf16 a[144:147], v[216:219], a[192:195], a[144:147]\n"
"v_cvt_pk_bf16_f32 v76, v76, v77\n"
"v_cvt_pk_bf16_f32 v71, v71, v210\n"
"v_mfma_f32_16x16x32_bf16 a[152:155], v[216:219], a[196:199], a[152:155]\n"
"ds_read_b128 v[216:219], v246 offset:256\n"
"v_cvt_pk_bf16_f32 v77, v78, v79\n"
"v_mfma_f32_16x16x32_bf16 a[152:155], v[212:215], a[172:175], a[152:155]\n"
"s_waitcnt lgkmcnt(5)\n"
"ds_read_b128 v[212:215], v246 offset:384\n"
"v_accvgpr_read_b32 v78, a108\n"
"v_mfma_f32_16x16x32_bf16 a[156:159], v[220:223], a[172:175], a[156:159]\n"
"s_waitcnt lgkmcnt(4)\n"
"v_accvgpr_read_b32 v79, a109\n"
"v_cvt_pk_bf16_f32 v78, v78, v79\n"
"v_accvgpr_read_b32 v84, a112\n"
"v_mfma_f32_16x16x32_bf16 a[156:159], v[224:227], a[196:199], a[156:159]\n"
"v_accvgpr_read_b32 v85, a113\n"
"v_accvgpr_read_b32 v211, a111\n"
"v_mfma_f32_16x16x32_bf16 a[148:151], v[224:227], a[192:195], a[148:151]\n"
"v_accvgpr_read_b32 v79, a110\n"
"v_accvgpr_read_b32 v86, a114\n"
"v_mfma_f32_16x16x32_bf16 a[148:151], v[220:223], a[168:171], a[148:151]\n"
"v_accvgpr_read_b32 v87, a115\n"
"v_cvt_pk_bf16_f32 v84, v84, v85\n"
"v_mfma_f32_16x16x32_bf16 a[140:143], v[220:223], a[164:167], a[140:143]\n"
"v_cvt_pk_bf16_f32 v79, v79, v211\n"
"v_cvt_pk_bf16_f32 v85, v86, v87\n"
"v_mfma_f32_16x16x32_bf16 a[140:143], v[224:227], a[188:191], a[140:143]\n"
"v_accvgpr_read_b32 v86, a116\n"
"v_accvgpr_read_b32 v87, a117\n"
"v_mfma_f32_16x16x32_bf16 a[132:135], v[224:227], a[184:187], a[132:135]\n"
"ds_read_b128 v[224:227], v246 offset:512\n"
"v_cvt_pk_bf16_f32 v86, v86, v87\n"
"v_mfma_f32_16x16x32_bf16 a[132:135], v[220:223], a[160:163], a[132:135]\n"
"s_waitcnt lgkmcnt(4)\n"
"ds_read_b128 v[220:223], v246 offset:640\n"
"v_accvgpr_read_b32 v92, a120\n"
"v_accvgpr_read_b32 v93, a121\n"
"v_mfma_f32_16x16x32_bf16 v[160:163], v[228:231], v[0:3], 0\n"
"v_accvgpr_read_b32 v208, a119\n"
"v_accvgpr_read_b32 v87, a118\n"
"v_mfma_f32_16x16x32_bf16 v[164:167], v[228:231], v[8:11], 0\n"
"v_accvgpr_read_b32 v94, a122\n"
"v_accvgpr_read_b32 v95, a123\n"
"v_mfma_f32_16x16x32_bf16 v[168:171], v[228:231], v[16:19], 0\n"
"v_cvt_pk_bf16_f32 v92, v92, v93\n"
"v_cvt_pk_bf16_f32 v87, v87, v208\n"
"v_mfma_f32_16x16x32_bf16 v[172:175], v[228:231], v[24:27], 0\n"
"s_waitcnt lgkmcnt(4)\n"
"ds_read_b128 v[228:231], v246 offset:768\n"
"v_cvt_pk_bf16_f32 v93, v94, v95\n"
"v_mfma_f32_16x16x32_bf16 v[160:163], v[232:235], v[32:35], v[160:163]\n"
"v_accvgpr_read_b32 v94, a124\n"
"v_accvgpr_read_b32 v95, a125\n"
"v_mfma_f32_16x16x32_bf16 v[164:167], v[232:235], v[40:43], v[164:167]\n"
"v_cvt_pk_bf16_f32 v94, v94, v95\n"
"v_accvgpr_read_b32 v100, a128\n"
"v_accvgpr_read_b32 v101, a129\n"
"v_mfma_f32_16x16x32_bf16 v[168:171], v[232:235], v[48:51], v[168:171]\n"
"v_accvgpr_read_b32 v209, a127\n"
"v_accvgpr_read_b32 v95, a126\n"
"v_mfma_f32_16x16x32_bf16 v[172:175], v[232:235], v[56:59], v[172:175]\n"
"s_waitcnt lgkmcnt(4)\n"
"ds_read_b128 v[232:235], v246 offset:896\n"
"v_accvgpr_read_b32 v102, a130\n"
"v_mfma_f32_16x16x32_bf16 v[160:163], v[216:219], v[64:67], v[160:163]\n"
"v_accvgpr_read_b32 v103, a131\n"
"v_cvt_pk_bf16_f32 v100, v100, v101\n"
"v_mfma_f32_16x16x32_bf16 v[164:167], v[216:219], v[72:75], v[164:167]\n"
"v_cvt_pk_bf16_f32 v95, v95, v209\n"
"v_cvt_pk_bf16_f32 v101, v102, v103\n"
"v_mfma_f32_16x16x32_bf16 v[168:171], v[216:219], v[80:83], v[168:171]\n"
"v_accvgpr_read_b32 v102, a132\n"
"v_accvgpr_read_b32 v103, a133\n"
"v_mfma_f32_16x16x32_bf16 v[172:175], v[216:219], v[88:91], v[172:175]\n"
"s_waitcnt lgkmcnt(4)\n"
"ds_read_b128 v[216:219], v246 offset:2048\n"
"v_cvt_pk_bf16_f32 v102, v102, v103\n"
"v_mfma_f32_16x16x32_bf16 v[160:163], v[212:215], v[96:99], v[160:163]\n"
"v_accvgpr_read_b32 v108, a136\n"
"v_accvgpr_read_b32 v109, a137\n"
"v_accvgpr_read_b32 v210, a135\n"
"v_mfma_f32_16x16x32_bf16 v[164:167], v[212:215], v[104:107], v[164:167]\n"
"v_accvgpr_read_b32 v103, a134\n"
"v_accvgpr_read_b32 v110, a138\n"
"v_mfma_f32_16x16x32_bf16 v[168:171], v[212:215], v[112:115], v[168:171]\n"
"v_accvgpr_read_b32 v111, a139\n"
"v_cvt_pk_bf16_f32 v108, v108, v109\n"
"v_mfma_f32_16x16x32_bf16 v[172:175], v[212:215], v[120:123], v[172:175]\n"
"s_waitcnt lgkmcnt(4)\n"
"v_cvt_pk_bf16_f32 v103, v103, v210\n"
"v_cvt_pk_bf16_f32 v109, v110, v111\n"
"v_mfma_f32_16x16x32_bf16 v[160:163], v[224:227], v[4:7], v[160:163]\n"
"v_accvgpr_read_b32 v110, a140\n"
"v_accvgpr_read_b32 v111, a141\n"
"v_mfma_f32_16x16x32_bf16 v[164:167], v[224:227], v[12:15], v[164:167]\n"
"v_cvt_pk_bf16_f32 v110, v110, v111\n"
"v_accvgpr_read_b32 v116, a144\n"
"v_mfma_f32_16x16x32_bf16 v[168:171], v[224:227], v[20:23], v[168:171]\n"
"v_accvgpr_read_b32 v117, a145\n"
"v_accvgpr_read_b32 v211, a143\n"
"v_mfma_f32_16x16x32_bf16 v[172:175], v[224:227], v[28:31], v[172:175]\n"
"s_waitcnt lgkmcnt(3)\n"
"s_barrier\n"
"v_accvgpr_read_b32 v111, a142\n"
"v_mfma_f32_16x16x32_bf16 v[160:163], v[220:223], v[36:39], v[160:163]\n"
"v_accvgpr_read_b32 v118, a146\n"
"v_accvgpr_read_b32 v119, a147\n"
"v_cvt_pk_bf16_f32 v116, v116, v117\n"
"v_mfma_f32_16x16x32_bf16 v[164:167], v[220:223], v[44:47], v[164:167]\n"
"ds_read_b128 v[212:215], v252 offset:5120\n"
"v_cvt_pk_bf16_f32 v111, v111, v211\n"
"v_mfma_f32_16x16x32_bf16 v[168:171], v[220:223], v[52:55], v[168:171]\n"
"v_cvt_pk_bf16_f32 v117, v118, v119\n"
"v_accvgpr_read_b32 v118, a148\n"
"v_accvgpr_read_b32 v119, a149\n"
"v_mfma_f32_16x16x32_bf16 v[172:175], v[220:223], v[60:63], v[172:175]\n"
"s_waitcnt lgkmcnt(3)\n"
"v_cvt_pk_bf16_f32 v118, v118, v119\n"
"v_accvgpr_read_b32 v124, a152\n"
"v_mfma_f32_16x16x32_bf16 v[160:163], v[228:231], v[68:71], v[160:163]\n"
"v_accvgpr_read_b32 v125, a153\n"
"v_accvgpr_read_b32 v208, a151\n"
"v_accvgpr_read_b32 v119, a150\n"
"v_mfma_f32_16x16x32_bf16 v[164:167], v[228:231], v[76:79], v[164:167]\n"
"ds_read_b128 v[224:227], v254 offset:0\n"
"v_accvgpr_read_b32 v126, a154\n"
"v_mfma_f32_16x16x32_bf16 v[168:171], v[228:231], v[84:87], v[168:171]\n"
"ds_read_b128 v[220:223], v246 offset:2176\n"
"v_accvgpr_read_b32 v127, a155\n"
"v_cvt_pk_bf16_f32 v124, v124, v125\n"
"v_mfma_f32_16x16x32_bf16 v[172:175], v[228:231], v[92:95], v[172:175]\n"
"s_waitcnt lgkmcnt(4)\n"
"v_cvt_pk_bf16_f32 v119, v119, v208\n"
"v_cvt_pk_bf16_f32 v125, v126, v127\n"
"v_accvgpr_read_b32 v126, a156\n"
"v_mfma_f32_16x16x32_bf16 v[160:163], v[232:235], v[100:103], v[160:163]\n"
"v_accvgpr_read_b32 v127, a157\n"
"v_cvt_pk_bf16_f32 v126, v126, v127\n"
"v_mfma_f32_16x16x32_bf16 v[164:167], v[232:235], v[108:111], v[164:167]\n"
"v_accvgpr_read_b32 v209, a159\n"
"v_accvgpr_read_b32 v127, a158\n"
"v_mfma_f32_16x16x32_bf16 v[168:171], v[232:235], v[116:119], v[168:171]\n"
"ds_read_b128 v[228:231], v252 offset:6144\n"
"v_cvt_pk_bf16_f32 v127, v127, v209\n"
"s_nop 1\n"
"v_mfma_f32_16x16x32_bf16 v[172:175], v[232:235], v[124:127], v[172:175]\n"
"s_waitcnt lgkmcnt(4)\n"
"ds_read_b128 v[232:235], v254 offset:128\n"
"v_accvgpr_write_b32 a254, 0\n"
"v_mfma_f32_16x16x32_bf16 v[176:179], v[216:219], v[0:3], 0\n"
"v_accvgpr_write_b32 a255, 0\n"
"v_max3_f32 v236, v160, v161, v162\n"
"v_mfma_f32_16x16x32_bf16 v[180:183], v[216:219], v[8:11], 0\n"
"v_max_f32 v236, v236, v163\n"
"v_max3_f32 v239, v172, v173, v174\n"
"v_mfma_f32_16x16x32_bf16 v[184:187], v[216:219], v[16:19], 0\n"
"v_max_f32 v239, v239, v175\n"
"v_mov_b32 v241, v236\n"
"v_mfma_f32_16x16x32_bf16 v[188:191], v[216:219], v[24:27], 0\n"
"s_waitcnt lgkmcnt(4)\n"
"v_max3_f32 v238, v168, v169, v170\n"
"v_mov_b32 v244, v239\n"
"v_mfma_f32_16x16x32_bf16 v[204:207], v[216:219], v[212:215], 0\n"
"s_waitcnt lgkmcnt(3)\n"
"ds_read_b128 v[216:219], v246 offset:2304\n"
"v_permlane32_swap_b32 v236, v241\n"
"v_mfma_f32_16x16x32_bf16 v[208:211], v[224:227], v[212:215], 0\n"
"s_waitcnt lgkmcnt(3)\n"
"ds_read_b128 v[212:215], v252 offset:7168\n"
"ds_read_b128 v[224:227], v254 offset:256\n"
"v_mfma_f32_16x16x32_bf16 v[176:179], v[220:223], v[32:35], v[176:179]\n"
"v_max_f32 v238, v238, v171\n"
"v_permlane32_swap_b32 v239, v244\n"
"v_mfma_f32_16x16x32_bf16 v[180:183], v[220:223], v[40:43], v[180:183]\n"
"v_max_f32 v236, v236, v241\n"
"v_max3_f32 v237, v164, v165, v166\n"
"v_mfma_f32_16x16x32_bf16 v[184:187], v[220:223], v[48:51], v[184:187]\n"
"v_mov_b32 v243, v238\n"
"v_max_f32 v239, v239, v244\n"
"v_mfma_f32_16x16x32_bf16 v[188:191], v[220:223], v[56:59], v[188:191]\n"
"s_waitcnt lgkmcnt(4)\n"
"v_mov_b32 v241, v236\n"
"v_max_f32 v237, v237, v167\n"
"v_mfma_f32_16x16x32_bf16 v[204:207], v[220:223], v[228:231], v[204:207]\n"
"s_waitcnt lgkmcnt(3)\n"
"ds_read_b128 v[220:223], v246 offset:2432\n"
"v_permlane32_swap_b32 v238, v243\n"
"v_mfma_f32_16x16x32_bf16 v[208:211], v[232:235], v[228:231], v[208:211]\n"
"s_waitcnt lgkmcnt(3)\n"
"ds_read_b128 v[228:231], v252 offset:8192\n"
"ds_read_b128 v[232:235], v254 offset:384\n"
"v_mfma_f32_16x16x32_bf16 v[176:179], v[216:219], v[64:67], v[176:179]\n"
"v_mov_b32 v244, v239\n"
"v_permlane16_swap_b32 v236, v241\n"
"v_mfma_f32_16x16x32_bf16 v[180:183], v[216:219], v[72:75], v[180:183]\n"
"v_mov_b32 v242, v237\n"
"v_max_f32 v238, v238, v243\n"
"v_mfma_f32_16x16x32_bf16 v[184:187], v[216:219], v[80:83], v[184:187]\n"
"v_permlane16_swap_b32 v239, v244\n"
"v_max_f32 v236, v236, v241\n"
"v_mfma_f32_16x16x32_bf16 v[188:191], v[216:219], v[88:91], v[188:191]\n"
"s_waitcnt lgkmcnt(4)\n"
"v_permlane32_swap_b32 v237, v242\n"
"v_mov_b32 v243, v238\n"
"v_mfma_f32_16x16x32_bf16 v[204:207], v[216:219], v[212:215], v[204:207]\n"
"s_waitcnt lgkmcnt(3)\n"
"ds_read_b128 v[216:219], v246 offset:2560\n"
"v_max_f32 v239, v239, v244\n"
"v_mfma_f32_16x16x32_bf16 v[208:211], v[224:227], v[212:215], v[208:211]\n"
"s_waitcnt lgkmcnt(3)\n"
"ds_read_b128 v[212:215], v252 offset:9216\n"
"ds_read_b128 v[224:227], v254 offset:512\n"
"v_mfma_f32_16x16x32_bf16 v[176:179], v[220:223], v[96:99], v[176:179]\n"
"v_mul_f32 v236, s95, v236\n"
"v_max_f32 v237, v237, v242\n"
"v_mfma_f32_16x16x32_bf16 v[180:183], v[220:223], v[104:107], v[180:183]\n"
"v_permlane16_swap_b32 v238, v243\n"
"v_mul_f32 v239, s95, v239\n"
"v_mfma_f32_16x16x32_bf16 v[184:187], v[220:223], v[112:115], v[184:187]\n"
"v_fma_f32 v163, v163, s65, v236\n"
"v_mov_b32 v242, v237\n"
"v_mfma_f32_16x16x32_bf16 v[188:191], v[220:223], v[120:123], v[188:191]\n"
"s_waitcnt lgkmcnt(4)\n"
"v_max_f32 v238, v238, v243\n"
"v_mov_b32 v251, 0\n"
"v_mfma_f32_16x16x32_bf16 v[204:207], v[220:223], v[228:231], v[204:207]\n"
"s_waitcnt lgkmcnt(3)\n"
"ds_read_b128 v[220:223], v246 offset:2688\n"
"v_permlane16_swap_b32 v237, v242\n"
"v_mfma_f32_16x16x32_bf16 v[208:211], v[232:235], v[228:231], v[208:211]\n"
"s_waitcnt lgkmcnt(3)\n"
"ds_read_b128 v[228:231], v252 offset:10240\n"
"ds_read_b128 v[232:235], v254 offset:640\n"
"v_mfma_f32_16x16x32_bf16 v[176:179], v[216:219], v[4:7], v[176:179]\n"
"v_mul_f32 v238, s95, v238\n"
"v_max_f32 v237, v237, v242\n"
"v_mfma_f32_16x16x32_bf16 v[180:183], v[216:219], v[12:15], v[180:183]\n"
"v_mul_f32 v237, s95, v237\n"
"v_mov_b32 v241, 0\n"
"v_mfma_f32_16x16x32_bf16 v[184:187], v[216:219], v[20:23], v[184:187]\n"
"v_fma_f32 v160, v160, s65, v236\n"
"v_fma_f32 v161, v161, s65, v236\n"
"v_mfma_f32_16x16x32_bf16 v[188:191], v[216:219], v[28:31], v[188:191]\n"
"s_waitcnt lgkmcnt(4)\n"
"v_fma_f32 v162, v162, s65, v236\n"
"v_mov_b32 v242, 0\n"
"v_mfma_f32_16x16x32_bf16 v[204:207], v[216:219], v[212:215], v[204:207]\n"
"s_waitcnt lgkmcnt(3)\n"
"ds_read_b128 v[216:219], v246 offset:2816\n"
"v_fma_f32 v164, v164, s65, v237\n"
"v_mfma_f32_16x16x32_bf16 v[208:211], v[224:227], v[212:215], v[208:211]\n"
"s_waitcnt lgkmcnt(3)\n"
"ds_read_b128 v[212:215], v252 offset:11264\n"
"ds_read_b128 v[224:227], v254 offset:768\n"
"v_mfma_f32_16x16x32_bf16 v[176:179], v[220:223], v[36:39], v[176:179]\n"
"v_fma_f32 v165, v165, s65, v237\n"
"v_fma_f32 v166, v166, s65, v237\n"
"v_mfma_f32_16x16x32_bf16 v[180:183], v[220:223], v[44:47], v[180:183]\n"
"v_fma_f32 v167, v167, s65, v237\n"
"v_mov_b32 v243, 0\n"
"v_mfma_f32_16x16x32_bf16 v[184:187], v[220:223], v[52:55], v[184:187]\n"
"v_fma_f32 v168, v168, s65, v238\n"
"v_fma_f32 v169, v169, s65, v238\n"
"v_mfma_f32_16x16x32_bf16 v[188:191], v[220:223], v[60:63], v[188:191]\n"
"s_waitcnt lgkmcnt(4)\n"
"v_fma_f32 v170, v170, s65, v238\n"
"v_fma_f32 v171, v171, s65, v238\n"
"v_mfma_f32_16x16x32_bf16 v[204:207], v[220:223], v[228:231], v[204:207]\n"
"s_waitcnt lgkmcnt(3)\n"
"ds_read_b128 v[220:223], v246 offset:2944\n"
"v_mov_b32 v244, 0\n"
"v_mfma_f32_16x16x32_bf16 v[208:211], v[232:235], v[228:231], v[208:211]\n"
"s_waitcnt lgkmcnt(3)\n"
"ds_read_b128 v[228:231], v252 offset:12288\n"
"ds_read_b128 v[232:235], v254 offset:896\n"
"v_mfma_f32_16x16x32_bf16 v[176:179], v[216:219], v[68:71], v[176:179]\n"
"v_fma_f32 v172, v172, s65, v239\n"
"v_fma_f32 v173, v173, s65, v239\n"
"v_mfma_f32_16x16x32_bf16 v[180:183], v[216:219], v[76:79], v[180:183]\n"
"v_fma_f32 v174, v174, s65, v239\n"
"v_fma_f32 v175, v175, s65, v239\n"
"v_mfma_f32_16x16x32_bf16 v[184:187], v[216:219], v[84:87], v[184:187]\n"
"v_mfma_f32_16x16x32_bf16 v[188:191], v[216:219], v[92:95], v[188:191]\n"
"s_waitcnt lgkmcnt(4)\n"
"v_mfma_f32_16x16x32_bf16 v[204:207], v[216:219], v[212:215], v[204:207]\n"
"s_waitcnt lgkmcnt(3)\n"
"v_mfma_f32_16x16x32_bf16 v[208:211], v[224:227], v[212:215], v[208:211]\n"
"s_waitcnt lgkmcnt(2)\n"
"v_mfma_f32_16x16x32_bf16 v[176:179], v[220:223], v[100:103], v[176:179]\n"
"v_mfma_f32_16x16x32_bf16 v[180:183], v[220:223], v[108:111], v[180:183]\n"
"v_mfma_f32_16x16x32_bf16 v[184:187], v[220:223], v[116:119], v[184:187]\n"
"v_mfma_f32_16x16x32_bf16 v[188:191], v[220:223], v[124:127], v[188:191]\n"
"s_waitcnt lgkmcnt(1)\n"
"v_mfma_f32_16x16x32_bf16 v[204:207], v[220:223], v[228:231], v[204:207]\n"
"s_waitcnt lgkmcnt(0)\n"
"v_mfma_f32_16x16x32_bf16 v[208:211], v[232:235], v[228:231], v[208:211]\n"
"s_nop 5\n"
"v_max3_f32 v240, v204, v205, v206\n"
"v_max_f32 v240, v240, v207\n"
"v_mov_b32 v245, v240\n"
"s_nop 1\n"
"v_permlane32_swap_b32 v240, v245\n"
"v_max_f32 v240, v240, v245\n"
"v_mov_b32 v245, v240\n"
"s_nop 1\n"
"v_permlane16_swap_b32 v240, v245\n"
"v_max_f32 v240, v240, v245\n"
"v_mul_f32 v240, s95, v240\n"
"v_fma_f32 v211, v211, s65, v240\n"
"v_fma_f32 v208, v208, s65, v240\n"
"v_fma_f32 v209, v209, s65, v240\n"
"v_fma_f32 v210, v210, s65, v240\n"
"v_max3_f32 v250, v208, v209, v210\n"
"v_max_f32 v250, v250, v211\n"
"v_cmp_lt_f32_e64 s[78:79], s66, v250\n"
"s_or_b64 s[96:97], s[78:79], s[76:77]\n"
"s_cbranch_scc1 Lslow_0\n"
"Lret_0:\n"
"v_mfma_f32_16x16x32_bf16 a[80:83], a[0:3], a[0:3], 0\n"
"s_waitcnt vmcnt(12)\n"
"s_barrier\n"
"v_mfma_f32_16x16x32_bf16 a[84:87], a[0:3], a[0:3], 0\n"
"ds_read_b128 v[212:215], v246 offset:16640\n"
"ds_read_b128 v[216:219], v246 offset:16768\n"
"v_mfma_f32_16x16x32_bf16 a[88:91], a[0:3], a[0:3], 0\n"
"ds_read_b128 v[220:223], v246 offset:16896\n"
"ds_read_b128 v[224:227], v246 offset:17024\n"
"v_mfma_f32_16x16x32_bf16 a[92:95], a[0:3], a[0:3], 0\n"
"ds_read_b32 v250, v248 offset:0\n"
"v_fma_f32 v188, v188, s65, v239\n"
"v_mfma_f32_16x16x32_bf16 a[96:99], a[0:3], a[0:3], 0\n"
"s_add_u32 m0, s69, 0x1f600\n"
"v_fma_f32 v189, v189, s65, v239\n"
"v_fma_f32 v190, v190, s65, v239\n"
"v_mfma_f32_16x16x32_bf16 a[100:103], a[0:3], a[0:3], 0\n"
"buffer_load_dwordx4 v249, s[52:55], s68 offen nt lds\n"
"v_mfma_f32_16x16x32_bf16 a[104:107], a[0:3], a[0:3], 0\n"
"v_max3_f32 v251, v251, v163, v188\n"
"v_fma_f32 v191, v191, s65, v239\n"
"v_mfma_f32_16x16x32_bf16 a[108:111], a[0:3], a[0:3], 0\n"
"v_fma_f32 v184, v184, s65, v238\n"
"v_max3_f32 v251, v251, v189, v190\n"
"v_mfma_f32_16x16x32_bf16 a[112:115], a[0:3], a[0:3], 0\n"
"v_fma_f32 v185, v185, s65, v238\n"
"v_fma_f32 v186, v186, s65, v238\n"
"v_mfma_f32_16x16x32_bf16 a[116:119], a[0:3], a[0:3], 0\n"
"v_max3_f32 v251, v251, v191, v184\n"
"v_fma_f32 v187, v187, s65, v238\n"
"v_mfma_f32_16x16x32_bf16 a[120:123], a[0:3], a[0:3], 0\n"
"v_fma_f32 v180, v180, s65, v237\n"
"v_max3_f32 v251, v251, v185, v186\n"
"v_mfma_f32_16x16x32_bf16 a[124:127], a[0:3], a[0:3], 0\n"
"buffer_load_dwordx4 v249, s[52:55], s68 offen offset:1024 nt lds\n"
"v_mfma_f32_16x16x32_bf16 a[128:131], a[0:3], a[0:3], 0\n"
"v_fma_f32 v181, v181, s65, v237\n"
"v_fma_f32 v182, v182, s65, v237\n"
"v_mfma_f32_16x16x32_bf16 a[132:135], a[0:3], a[0:3], 0\n"
"v_max3_f32 v251, v251, v187, v180\n"
"v_fma_f32 v183, v183, s65, v237\n"
"v_mfma_f32_16x16x32_bf16 a[136:139], a[0:3], a[0:3], 0\n"
"v_fma_f32 v176, v176, s65, v236\n"
"v_max3_f32 v251, v251, v181, v182\n"
"v_mfma_f32_16x16x32_bf16 a[140:143], a[0:3], a[0:3], 0\n"
"v_fma_f32 v177, v177, s65, v236\n"
"v_fma_f32 v178, v178, s65, v236\n"
"v_mfma_f32_16x16x32_bf16 a[144:147], a[0:3], a[0:3], 0\n"
"v_max3_f32 v251, v251, v183, v176\n"
"v_fma_f32 v179, v179, s65, v236\n"
"v_mfma_f32_16x16x32_bf16 a[148:151], a[0:3], a[0:3], 0\n"
"buffer_load_dwordx4 v249, s[52:55], s68 offen offset:2048 nt lds\n"
"v_mfma_f32_16x16x32_bf16 a[152:155], a[0:3], a[0:3], 0\n"
"v_max3_f32 v251, v251, v177, v178\n"
"v_mov_b32 v245, 0\n"
"v_mfma_f32_16x16x32_bf16 a[156:159], a[0:3], a[0:3], 0\n"
"s_waitcnt lgkmcnt(0)\n"
"v_max3_f32 v251, v251, v179, v250\n"
"v_cmp_lt_f32 vcc, s66, v251\n"
"v_mfma_f32_16x16x32_bf16 a[160:163], a[0:3], a[0:3], 0\n"
"v_mfma_f32_16x16x32_bf16 a[164:167], a[0:3], a[0:3], 0\n"
"v_mfma_f32_16x16x32_bf16 a[168:171], a[0:3], a[0:3], 0\n"
"v_mfma_f32_16x16x32_bf16 a[172:175], a[0:3], a[0:3], 0\n"
"buffer_load_dwordx4 v249, s[52:55], s68 offen offset:3072 nt lds\n"
"v_mfma_f32_16x16x32_bf16 a[176:179], a[0:3], a[0:3], 0\n"
"s_add_u32 s68, s68, 0x4000\n"
"s_cbranch_vccnz Lslow_1\n"
"Lret_1:\n"
"v_mfma_f32_16x16x32_bf16 a[180:183], a[0:3], a[0:3], 0\n"
"v_exp_f32 v208, v208\n"
"v_mfma_f32_16x16x32_bf16 a[184:187], a[0:3], a[0:3], 0\n"
"v_exp_f32 v160, v160\n"
"v_mfma_f32_16x16x32_bf16 a[188:191], a[0:3], a[0:3], 0\n"
"v_exp_f32 v164, v164\n"
"v_mfma_f32_16x16x32_bf16 a[192:195], a[0:3], a[0:3], 0\n"
"v_exp_f32 v168, v168\n"
"v_mfma_f32_16x16x32_bf16 a[196:199], a[0:3], a[0:3], 0\n"
"v_exp_f32 v172, v172\n"
"v_mfma_f32_16x16x32_bf16 a[200:203], a[0:3], a[0:3], 0\n"
"v_exp_f32 v209, v209\n"
"v_mfma_f32_16x16x32_bf16 a[204:207], a[0:3], a[0:3], 0\n"
"v_exp_f32 v161, v161\n"
"v_mfma_f32_16x16x32_bf16 v[128:131], a[0:3], a[0:3], 0\n"
"v_exp_f32 v165, v165\n"
"v_mfma_f32_16x16x32_bf16 v[132:135], a[0:3], a[0:3], 0\n"
"v_exp_f32 v169, v169\n"
"v_mfma_f32_16x16x32_bf16 v[136:139], a[0:3], a[0:3], 0\n"
"v_exp_f32 v173, v173\n"
"v_mfma_f32_16x16x32_bf16 v[140:143], a[0:3], a[0:3], 0\n"
"v_add_f32 v245, v245, v208\n"
"v_add_f32 v241, v241, v160\n"
"v_mfma_f32_16x16x32_bf16 v[144:147], a[0:3], a[0:3], 0\n"
"v_exp_f32 v210, v210\n"
"v_mfma_f32_16x16x32_bf16 v[148:151], a[0:3], a[0:3], 0\n"
"v_exp_f32 v211, v211\n"
"v_mfma_f32_16x16x32_bf16 v[152:155], a[0:3], a[0:3], 0\n"
"v_exp_f32 v162, v162\n"
"v_mfma_f32_16x16x32_bf16 v[156:159], a[0:3], a[0:3], 0\n"
"v_add_f32 v242, v242, v164\n"
"v_add_f32 v243, v243, v168\n"
"v_add_f32 v245, v245, v209\n"
"v_add_f32 v245, v245, v210\n"
"v_add_f32 v245, v245, v211\n"
"v_cvt_pk_bf16_f32 v208, v208, v209\n"
"v_cvt_pk_bf16_f32 v209, v210, v211\n"
"ds_write_b64 v253, v[208:209] offset:1024\n"
"v_add_f32 v241, v241, v161\n"
"v_add_f32 v241, v241, v162\n"
"v_exp_f32 v163, v163\n"
"s_nop 0\n"
"v_add_f32 v241, v241, v163\n"
"v_add_f32 v242, v242, v165\n"
"v_exp_f32 v166, v166\n"
"s_nop 0\n"
"v_add_f32 v242, v242, v166\n"
"v_exp_f32 v167, v167\n"
"s_nop 0\n"
"v_add_f32 v242, v242, v167\n"
"v_add_f32 v243, v243, v169\n"
"v_exp_f32 v170, v170\n"
"s_nop 0\n"
"v_add_f32 v243, v243, v170\n"
"v_exp_f32 v171, v171\n"
"s_nop 0\n"
"v_add_f32 v243, v243, v171\n"
"v_add_f32 v244, v244, v172\n"
"v_add_f32 v244, v244, v173\n"
"v_exp_f32 v174, v174\n"
"s_nop 0\n"
"v_add_f32 v244, v244, v174\n"
"v_exp_f32 v175, v175\n"
"s_nop 0\n"
"v_add_f32 v244, v244, v175\n"
"v_cvt_pk_bf16_f32 v192, v160, v161\n"
"v_cvt_pk_bf16_f32 v193, v162, v163\n"
"v_cvt_pk_bf16_f32 v196, v164, v165\n"
"v_cvt_pk_bf16_f32 v197, v166, v167\n"
"v_cvt_pk_bf16_f32 v200, v168, v169\n"
"v_cvt_pk_bf16_f32 v201, v170, v171\n"
"v_cvt_pk_bf16_f32 v204, v172, v173\n"
"v_cvt_pk_bf16_f32 v205, v174, v175\n"
"s_cmp_eq_u32 s64, 1\n"
"s_cbranch_scc1 Lloop1\n"
"s_cmp_eq_u32 s64, 2\n"
"s_cbranch_scc1 Lloop2\n"
"s_cmp_eq_u32 s64, 3\n"
"s_cbranch_scc1 Lloop3\n"
"Lloop0:\n"
"s_waitcnt lgkmcnt(3)\n"
"v_mfma_f32_16x16x32_bf16 v[160:163], v[212:215], v[0:3], 0\n"
"s_waitcnt lgkmcnt(2)\n"
"ds_read_b128 v[228:231], v248 offset:5120\n"
"ds_read_b128 v[232:235], v248 offset:6144\n"
"v_mfma_f32_16x16x32_bf16 v[160:163], v[216:219], v[32:35], v[160:163]\n"
"ds_read_b128 v[252:255], v248 offset:7168\n"
"v_mfma_f32_16x16x32_bf16 v[164:167], v[216:219], v[40:43], 0\n"
"v_exp_f32 v176, v176\n"
"v_mfma_f32_16x16x32_bf16 v[164:167], v[212:215], v[8:11], v[164:167]\n"
"v_exp_f32 v177, v177\n"
"v_mfma_f32_16x16x32_bf16 v[168:171], v[212:215], v[16:19], 0\n"
"v_add_f32 v241, v241, v176\n"
"v_add_f32 v241, v241, v177\n"
"v_mfma_f32_16x16x32_bf16 v[168:171], v[216:219], v[48:51], v[168:171]\n"
"v_exp_f32 v178, v178\n"
"v_mfma_f32_16x16x32_bf16 v[172:175], v[216:219], v[56:59], 0\n"
"v_exp_f32 v179, v179\n"
"v_mfma_f32_16x16x32_bf16 v[172:175], v[212:215], v[24:27], v[172:175]\n"
"s_waitcnt lgkmcnt(2)\n"
"v_add_f32 v241, v241, v178\n"
"v_add_f32 v241, v241, v179\n"
"v_mfma_f32_16x16x32_bf16 v[208:211], v[212:215], v[228:231], 0\n"
"s_waitcnt lgkmcnt(1)\n"
"ds_read_b128 v[212:215], v248 offset:8192\n"
"ds_read_b128 v[228:231], v246 offset:17152\n"
"v_mfma_f32_16x16x32_bf16 v[208:211], v[216:219], v[232:235], v[208:211]\n"
"ds_read_b128 v[216:219], v246 offset:17280\n"
"ds_read_b128 v[232:235], v248 offset:9216\n"
"v_mfma_f32_16x16x32_bf16 v[172:175], v[220:223], v[88:91], v[172:175]\n"
"v_cvt_pk_bf16_f32 v194, v176, v177\n"
"v_cvt_pk_bf16_f32 v195, v178, v179\n"
"v_mfma_f32_16x16x32_bf16 v[172:175], v[224:227], v[120:123], v[172:175]\n"
"v_exp_f32 v180, v180\n"
"v_mfma_f32_16x16x32_bf16 v[168:171], v[224:227], v[112:115], v[168:171]\n"
"v_exp_f32 v181, v181\n"
"v_mfma_f32_16x16x32_bf16 v[168:171], v[220:223], v[80:83], v[168:171]\n"
"v_add_f32 v242, v242, v180\n"
"v_add_f32 v242, v242, v181\n"
"v_mfma_f32_16x16x32_bf16 v[164:167], v[220:223], v[72:75], v[164:167]\n"
"v_exp_f32 v182, v182\n"
"v_mfma_f32_16x16x32_bf16 v[164:167], v[224:227], v[104:107], v[164:167]\n"
"v_exp_f32 v183, v183\n"
"v_mfma_f32_16x16x32_bf16 v[160:163], v[224:227], v[96:99], v[160:163]\n"
"v_add_f32 v242, v242, v182\n"
"v_add_f32 v242, v242, v183\n"
"v_mfma_f32_16x16x32_bf16 v[160:163], v[220:223], v[64:67], v[160:163]\n"
"s_waitcnt lgkmcnt(4)\n"
"v_cvt_pk_bf16_f32 v198, v180, v181\n"
"v_cvt_pk_bf16_f32 v199, v182, v183\n"
"v_mfma_f32_16x16x32_bf16 v[208:211], v[220:223], v[252:255], v[208:211]\n"
"s_waitcnt lgkmcnt(3)\n"
"ds_read_b128 v[220:223], v248 offset:10240\n"
"ds_read_b128 v[252:255], v246 offset:17408\n"
"v_mfma_f32_16x16x32_bf16 v[208:211], v[224:227], v[212:215], v[208:211]\n"
"s_waitcnt lgkmcnt(4)\n"
"ds_read_b128 v[212:215], v246 offset:17536\n"
"ds_read_b128 v[224:227], v248 offset:11264\n"
"v_mfma_f32_16x16x32_bf16 v[160:163], v[228:231], v[4:7], v[160:163]\n"
"s_waitcnt lgkmcnt(5)\n"
"v_exp_f32 v184, v184\n"
"v_mfma_f32_16x16x32_bf16 v[160:163], v[216:219], v[36:39], v[160:163]\n"
"v_exp_f32 v185, v185\n"
"v_mfma_f32_16x16x32_bf16 v[164:167], v[216:219], v[44:47], v[164:167]\n"
"v_add_f32 v243, v243, v184\n"
"v_add_f32 v243, v243, v185\n"
"v_mfma_f32_16x16x32_bf16 v[164:167], v[228:231], v[12:15], v[164:167]\n"
"v_exp_f32 v186, v186\n"
"v_mfma_f32_16x16x32_bf16 v[168:171], v[228:231], v[20:23], v[168:171]\n"
"v_exp_f32 v187, v187\n"
"v_mfma_f32_16x16x32_bf16 v[168:171], v[216:219], v[52:55], v[168:171]\n"
"v_add_f32 v243, v243, v186\n"
"v_add_f32 v243, v243, v187\n"
"v_mfma_f32_16x16x32_bf16 v[172:175], v[216:219], v[60:63], v[172:175]\n"
"v_cvt_pk_bf16_f32 v202, v184, v185\n"
"v_cvt_pk_bf16_f32 v203, v186, v187\n"
"v_mfma_f32_16x16x32_bf16 v[172:175], v[228:231], v[28:31], v[172:175]\n"
"s_waitcnt lgkmcnt(4)\n"
"v_exp_f32 v188, v188\n"
"v_mfma_f32_16x16x32_bf16 v[208:211], v[228:231], v[232:235], v[208:211]\n"
"s_waitcnt lgkmcnt(3)\n"
"ds_read_b128 v[228:231], v248 offset:12288\n"
"ds_read_b128 v[232:235], v246 offset:18688\n"
"v_mfma_f32_16x16x32_bf16 v[208:211], v[216:219], v[220:223], v[208:211]\n"
"s_waitcnt lgkmcnt(4)\n"
"ds_read_b128 v[216:219], v246 offset:18816\n"
"ds_read_b128 v[220:223], v246 offset:18944\n"
"v_mfma_f32_16x16x32_bf16 v[172:175], v[252:255], v[92:95], v[172:175]\n"
"s_waitcnt lgkmcnt(5)\n"
"v_exp_f32 v189, v189\n"
"v_mfma_f32_16x16x32_bf16 v[172:175], v[212:215], v[124:127], v[172:175]\n"
"v_add_f32 v244, v244, v188\n"
"v_add_f32 v244, v244, v189\n"
"v_mfma_f32_16x16x32_bf16 v[168:171], v[212:215], v[116:119], v[168:171]\n"
"v_exp_f32 v190, v190\n"
"v_mfma_f32_16x16x32_bf16 v[168:171], v[252:255], v[84:87], v[168:171]\n"
"v_exp_f32 v191, v191\n"
"v_mfma_f32_16x16x32_bf16 v[164:167], v[252:255], v[76:79], v[164:167]\n"
"v_add_f32 v244, v244, v190\n"
"v_add_f32 v244, v244, v191\n"
"v_mfma_f32_16x16x32_bf16 v[164:167], v[212:215], v[108:111], v[164:167]\n"
"v_cvt_pk_bf16_f32 v206, v188, v189\n"
"v_cvt_pk_bf16_f32 v207, v190, v191\n"
"v_mfma_f32_16x16x32_bf16 v[160:163], v[212:215], v[100:103], v[160:163]\n"
"s_add_u32 m0, s31, 0\n"
"v_fma_f32 v172, v172, s65, v239\n"
"v_fma_f32 v173, v173, s65, v239\n"
"v_mfma_f32_16x16x32_bf16 v[160:163], v[252:255], v[68:71], v[160:163]\n"
"s_waitcnt lgkmcnt(4)\n"
"buffer_load_dwordx4 v249, s[48:51], s67 offen nt lds\n"
"v_mfma_f32_16x16x32_bf16 v[208:211], v[252:255], v[224:227], v[208:211]\n"
"s_waitcnt lgkmcnt(3)\n"
"ds_read_b128 v[224:227], v246 offset:19072\n"
"ds_read_b128 v[252:255], v246 offset:19200\n"
"v_mfma_f32_16x16x32_bf16 v[208:211], v[212:215], v[228:231], v[208:211]\n"
"s_waitcnt lgkmcnt(4)\n"
"ds_read_b128 v[212:215], v246 offset:19328\n"
"ds_read_b128 v[228:231], v246 offset:19456\n"
"v_mfma_f32_16x16x32_bf16 v[176:179], v[232:235], v[0:3], 0\n"
"s_waitcnt lgkmcnt(5)\n"
"v_fma_f32 v174, v174, s65, v239\n"
"v_fma_f32 v175, v175, s65, v239\n"
"v_mfma_f32_16x16x32_bf16 v[176:179], v[216:219], v[32:35], v[176:179]\n"
"v_fma_f32 v208, v208, s65, v240\n"
"v_fma_f32 v209, v209, s65, v240\n"
"v_mfma_f32_16x16x32_bf16 v[180:183], v[216:219], v[40:43], 0\n"
"v_fma_f32 v210, v210, s65, v240\n"
"v_fma_f32 v211, v211, s65, v240\n"
"v_mfma_f32_16x16x32_bf16 v[180:183], v[232:235], v[8:11], v[180:183]\n"
"buffer_load_dwordx4 v249, s[48:51], s67 offen offset:1024 nt lds\n"
"v_mfma_f32_16x16x32_bf16 v[184:187], v[232:235], v[16:19], 0\n"
"v_max3_f32 v250, v208, v209, v210\n"
"v_max_f32 v250, v250, v211\n"
"v_mfma_f32_16x16x32_bf16 v[184:187], v[216:219], v[48:51], v[184:187]\n"
"v_cmp_lt_f32_e64 s[78:79], s66, v250\n"
"v_fma_f32 v168, v168, s65, v238\n"
"v_mfma_f32_16x16x32_bf16 v[188:191], v[216:219], v[56:59], 0\n"
"ds_read_b128 v[216:219], v246 offset:19584\n"
"v_max3_f32 v251, v172, v173, v174\n"
"v_mfma_f32_16x16x32_bf16 v[188:191], v[232:235], v[24:27], v[188:191]\n"
"s_waitcnt lgkmcnt(5)\n"
"ds_read_b64_tr_b16 v[232:233], v247 offset:256\n"
"ds_read_b64_tr_b16 v[234:235], v247 offset:2304\n"
"v_mfma_f32_16x16x32_bf16 v[188:191], v[220:223], v[88:91], v[188:191]\n"
"s_waitcnt lgkmcnt(6)\n"
"s_or_b64 s[96:97], s[78:79], s[76:77]\n"
"s_cbranch_scc1 Lslow_2\n"
"Lret_2:\n"
"v_mfma_f32_16x16x32_bf16 v[188:191], v[224:227], v[120:123], v[188:191]\n"
"buffer_load_dwordx4 v249, s[48:51], s67 offen offset:2048 nt lds\n"
"v_mfma_f32_16x16x32_bf16 v[184:187], v[224:227], v[112:115], v[184:187]\n"
"v_fma_f32 v169, v169, s65, v238\n"
"v_fma_f32 v170, v170, s65, v238\n"
"v_mfma_f32_16x16x32_bf16 v[184:187], v[220:223], v[80:83], v[184:187]\n"
"v_max3_f32 v251, v251, v175, v168\n"
"v_add_u32 v246, s34, v246\n"
"v_mfma_f32_16x16x32_bf16 v[180:183], v[220:223], v[72:75], v[180:183]\n"
"s_sub_u32 s34, 0, s34\n"
"v_fma_f32 v171, v171, s65, v238\n"
"v_fma_f32 v164, v164, s65, v237\n"
"v_mfma_f32_16x16x32_bf16 v[180:183], v[224:227], v[104:107], v[180:183]\n"
"v_max3_f32 v251, v251, v169, v170\n"
"v_fma_f32 v165, v165, s65, v237\n"
"v_mfma_f32_16x16x32_bf16 v[176:179], v[224:227], v[96:99], v[176:179]\n"
"ds_read_b64_tr_b16 v[224:225], v247 offset:288\n"
"ds_read_b64_tr_b16 v[226:227], v247 offset:2336\n"
"v_mfma_f32_16x16x32_bf16 v[176:179], v[220:223], v[64:67], v[176:179]\n"
"s_waitcnt lgkmcnt(7)\n"
"buffer_load_dwordx4 v249, s[48:51], s67 offen offset:3072 nt lds\n"
"v_mfma_f32_16x16x32_bf16 v[176:179], v[252:255], v[4:7], v[176:179]\n"
"s_waitcnt lgkmcnt(6)\n"
"ds_read_b64_tr_b16 v[220:221], v247 offset:384\n"
"ds_read_b64_tr_b16 v[222:223], v247 offset:2432\n"
"v_mfma_f32_16x16x32_bf16 v[176:179], v[212:215], v[36:39], v[176:179]\n"
"s_add_u32 s67, s67, 0x4000\n"
"v_fma_f32 v166, v166, s65, v237\n"
"v_max3_f32 v251, v251, v171, v164\n"
"v_mfma_f32_16x16x32_bf16 v[180:183], v[212:215], v[44:47], v[180:183]\n"
"v_fma_f32 v167, v167, s65, v237\n"
"v_fma_f32 v160, v160, s65, v236\n"
"v_mfma_f32_16x16x32_bf16 v[180:183], v[252:255], v[12:15], v[180:183]\n"
"v_max3_f32 v251, v251, v165, v166\n"
"v_fma_f32 v161, v161, s65, v236\n"
"v_mfma_f32_16x16x32_bf16 v[184:187], v[252:255], v[20:23], v[184:187]\n"
"v_fma_f32 v162, v162, s65, v236\n"
"v_max3_f32 v251, v251, v167, v160\n"
"v_mfma_f32_16x16x32_bf16 v[184:187], v[212:215], v[52:55], v[184:187]\n"
"v_fma_f32 v163, v163, s65, v236\n"
"v_max3_f32 v251, v251, v161, v162\n"
"v_mfma_f32_16x16x32_bf16 v[188:191], v[212:215], v[60:63], v[188:191]\n"
"ds_read_b64_tr_b16 v[212:213], v247 offset:416\n"
"ds_read_b64_tr_b16 v[214:215], v247 offset:2464\n"
"v_mfma_f32_16x16x32_bf16 v[188:191], v[252:255], v[28:31], v[188:191]\n"
"s_waitcnt lgkmcnt(9)\n"
"ds_read_b64_tr_b16 v[252:253], v247 offset:512\n"
"ds_read_b64_tr_b16 v[254:255], v247 offset:2560\n"
"v_mfma_f32_16x16x32_bf16 v[188:191], v[228:231], v[92:95], v[188:191]\n"
"s_waitcnt lgkmcnt(10)\n"
"v_mfma_f32_16x16x32_bf16 v[188:191], v[216:219], v[124:127], v[188:191]\n"
"v_mfma_f32_16x16x32_bf16 v[184:187], v[216:219], v[116:119], v[184:187]\n"
"v_mfma_f32_16x16x32_bf16 v[184:187], v[228:231], v[84:87], v[184:187]\n"
"v_mfma_f32_16x16x32_bf16 v[180:183], v[228:231], v[76:79], v[180:183]\n"
"v_mfma_f32_16x16x32_bf16 v[180:183], v[216:219], v[108:111], v[180:183]\n"
"v_mfma_f32_16x16x32_bf16 v[176:179], v[216:219], v[100:103], v[176:179]\n"
"ds_read_b64_tr_b16 v[216:217], v247 offset:544\n"
"ds_read_b64_tr_b16 v[218:219], v247 offset:2592\n"
"v_mfma_f32_16x16x32_bf16 v[176:179], v[228:231], v[68:71], v[176:179]\n"
"s_waitcnt lgkmcnt(10)\n"
"ds_read_b64_tr_b16 v[228:229], v247 offset:0\n"
"ds_read_b64_tr_b16 v[230:231], v247 offset:2048\n"
"v_mfma_f32_16x16x32_bf16 a[76:79], v[232:235], v[204:207], a[76:79]\n"
"v_fma_f32 v188, v188, s65, v239\n"
"v_fma_f32 v189, v189, s65, v239\n"
"v_mfma_f32_16x16x32_bf16 a[72:75], v[232:235], v[200:203], a[72:75]\n"
"v_fma_f32 v190, v190, s65, v239\n"
"v_max3_f32 v251, v251, v163, v188\n"
"v_mfma_f32_16x16x32_bf16 a[68:71], v[232:235], v[196:199], a[68:71]\n"
"v_fma_f32 v191, v191, s65, v239\n"
"v_fma_f32 v184, v184, s65, v238\n"
"v_mfma_f32_16x16x32_bf16 a[64:67], v[232:235], v[192:195], a[64:67]\n"
"s_waitcnt lgkmcnt(10)\n"
"v_max3_f32 v251, v251, v189, v190\n"
"v_fma_f32 v185, v185, s65, v238\n"
"v_mfma_f32_16x16x32_bf16 a[80:83], v[224:227], v[192:195], a[80:83]\n"
"v_fma_f32 v186, v186, s65, v238\n"
"v_max3_f32 v251, v251, v191, v184\n"
"v_mfma_f32_16x16x32_bf16 a[84:87], v[224:227], v[196:199], a[84:87]\n"
"v_fma_f32 v187, v187, s65, v238\n"
"v_fma_f32 v180, v180, s65, v237\n"
"v_mfma_f32_16x16x32_bf16 a[88:91], v[224:227], v[200:203], a[88:91]\n"
"v_max3_f32 v251, v251, v185, v186\n"
"v_fma_f32 v181, v181, s65, v237\n"
"v_mfma_f32_16x16x32_bf16 a[92:95], v[224:227], v[204:207], a[92:95]\n"
"s_waitcnt lgkmcnt(8)\n"
"v_fma_f32 v182, v182, s65, v237\n"
"v_max3_f32 v251, v251, v187, v180\n"
"v_mfma_f32_16x16x32_bf16 a[108:111], v[220:223], v[204:207], a[108:111]\n"
"v_fma_f32 v183, v183, s65, v237\n"
"v_fma_f32 v176, v176, s65, v236\n"
"v_mfma_f32_16x16x32_bf16 a[104:107], v[220:223], v[200:203], a[104:107]\n"
"v_max3_f32 v251, v251, v181, v182\n"
"v_fma_f32 v177, v177, s65, v236\n"
"v_mfma_f32_16x16x32_bf16 a[100:103], v[220:223], v[196:199], a[100:103]\n"
"s_waitcnt vmcnt(12)\n"
"s_barrier\n"
"v_mfma_f32_16x16x32_bf16 a[96:99], v[220:223], v[192:195], a[96:99]\n"
"s_waitcnt lgkmcnt(6)\n"
"ds_read_b128 v[232:235], v248 offset:2048\n"
"ds_read_b128 v[224:227], v248 offset:1024\n"
"v_mfma_f32_16x16x32_bf16 a[112:115], v[212:215], v[192:195], a[112:115]\n"
"ds_read_b64_tr_b16 v[220:221], v247 offset:32\n"
"ds_read_b64_tr_b16 v[222:223], v247 offset:2080\n"
"v_mfma_f32_16x16x32_bf16 a[116:119], v[212:215], v[196:199], a[116:119]\n"
"ds_read_b32 v250, v248 offset:12\n"
"v_fma_f32 v178, v178, s65, v236\n"
"v_mfma_f32_16x16x32_bf16 a[120:123], v[212:215], v[200:203], a[120:123]\n"
"s_add_u32 m0, s33, 0x4100\n"
"v_max3_f32 v251, v251, v183, v176\n"
"v_fma_f32 v179, v179, s65, v236\n"
"v_mfma_f32_16x16x32_bf16 a[124:127], v[212:215], v[204:207], a[124:127]\n"
"s_waitcnt lgkmcnt(9)\n"
"buffer_load_dwordx4 v249, s[52:55], s68 offen nt lds\n"
"v_mfma_f32_16x16x32_bf16 a[140:143], v[252:255], v[204:207], a[140:143]\n"
"s_sub_u32 s33, s33, s30\n"
"ds_read_b64_tr_b16 v[212:213], v247 offset:128\n"
"ds_read_b64_tr_b16 v[214:215], v247 offset:2176\n"
"v_mfma_f32_16x16x32_bf16 a[136:139], v[252:255], v[200:203], a[136:139]\n"
"v_max3_f32 v251, v251, v177, v178\n"
"v_mfma_f32_16x16x32_bf16 a[132:135], v[252:255], v[196:199], a[132:135]\n"
"v_mfma_f32_16x16x32_bf16 a[128:131], v[252:255], v[192:195], a[128:131]\n"
"s_waitcnt lgkmcnt(9)\n"
"ds_read_b64_tr_b16 v[252:253], v247 offset:160\n"
"ds_read_b64_tr_b16 v[254:255], v247 offset:2208\n"
"v_mfma_f32_16x16x32_bf16 a[144:147], v[216:219], v[192:195], a[144:147]\n"
"v_mfma_f32_16x16x32_bf16 a[148:151], v[216:219], v[196:199], a[148:151]\n"
"buffer_load_dwordx4 v249, s[52:55], s68 offen offset:1024 nt lds\n"
"v_mfma_f32_16x16x32_bf16 a[152:155], v[216:219], v[200:203], a[152:155]\n"
"s_waitcnt lgkmcnt(4)\n"
"v_max3_f32 v251, v251, v179, v250\n"
"v_cmp_lt_f32 vcc, s66, v251\n"
"v_mfma_f32_16x16x32_bf16 a[156:159], v[216:219], v[204:207], a[156:159]\n"
"ds_read_b64_tr_b16 v[216:217], v247 offset:640\n"
"ds_read_b64_tr_b16 v[218:219], v247 offset:2688\n"
"v_mfma_f32_16x16x32_bf16 v[132:135], v[228:231], v[232:235], v[132:135]\n"
"v_mfma_f32_16x16x32_bf16 v[128:131], v[228:231], v[224:227], v[128:131]\n"
"s_cbranch_vccnz Lslow_3\n"
"Lret_3:\n"
"v_mfma_f32_16x16x32_bf16 a[12:15], v[228:231], v[204:207], a[12:15]\n"
"v_exp_f32 v160, v160\n"
"v_mfma_f32_16x16x32_bf16 a[8:11], v[228:231], v[200:203], a[8:11]\n"
"buffer_load_dwordx4 v249, s[52:55], s68 offen offset:2048 nt lds\n"
"v_mfma_f32_16x16x32_bf16 a[4:7], v[228:231], v[196:199], a[4:7]\n"
"v_exp_f32 v161, v161\n"
"v_mfma_f32_16x16x32_bf16 a[0:3], v[228:231], v[192:195], a[0:3]\n"
"ds_read_b64_tr_b16 v[228:229], v247 offset:672\n"
"ds_read_b64_tr_b16 v[230:231], v247 offset:2720\n"
"v_mfma_f32_16x16x32_bf16 a[16:19], v[220:223], v[192:195], a[16:19]\n"
"v_add_f32 v241, v241, v160\n"
"v_add_f32 v241, v241, v161\n"
"v_mfma_f32_16x16x32_bf16 a[20:23], v[220:223], v[196:199], a[20:23]\n"
"v_exp_f32 v162, v162\n"
"v_mfma_f32_16x16x32_bf16 a[24:27], v[220:223], v[200:203], a[24:27]\n"
"v_exp_f32 v163, v163\n"
"v_mfma_f32_16x16x32_bf16 a[28:31], v[220:223], v[204:207], a[28:31]\n"
"buffer_load_dwordx4 v249, s[52:55], s68 offen offset:3072 nt lds\n"
"v_mfma_f32_16x16x32_bf16 v[136:139], v[220:223], v[224:227], v[136:139]\n"
"s_add_u32 s68, s68, 0x4000\n"
"v_add_f32 v241, v241, v162\n"
"v_add_f32 v241, v241, v163\n"
"v_mfma_f32_16x16x32_bf16 v[140:143], v[220:223], v[232:235], v[140:143]\n"
"s_waitcnt lgkmcnt(6)\n"
"ds_read_b64_tr_b16 v[220:221], v247 offset:768\n"
"ds_read_b64_tr_b16 v[222:223], v247 offset:2816\n"
"v_mfma_f32_16x16x32_bf16 v[148:151], v[212:215], v[232:235], v[148:151]\n"
"v_exp_f32 v164, v164\n"
"v_mfma_f32_16x16x32_bf16 v[144:147], v[212:215], v[224:227], v[144:147]\n"
"v_exp_f32 v165, v165\n"
"v_mfma_f32_16x16x32_bf16 a[44:47], v[212:215], v[204:207], a[44:47]\n"
"v_add_f32 v242, v242, v164\n"
"v_add_f32 v242, v242, v165\n"
"v_mfma_f32_16x16x32_bf16 a[40:43], v[212:215], v[200:203], a[40:43]\n"
"v_exp_f32 v166, v166\n"
"v_mfma_f32_16x16x32_bf16 a[36:39], v[212:215], v[196:199], a[36:39]\n"
"v_exp_f32 v167, v167\n"
"v_mfma_f32_16x16x32_bf16 a[32:35], v[212:215], v[192:195], a[32:35]\n"
"s_waitcnt lgkmcnt(6)\n"
"ds_read_b64_tr_b16 v[212:213], v247 offset:800\n"
"ds_read_b64_tr_b16 v[214:215], v247 offset:2848\n"
"v_mfma_f32_16x16x32_bf16 a[48:51], v[252:255], v[192:195], a[48:51]\n"
"v_add_f32 v242, v242, v166\n"
"v_add_f32 v242, v242, v167\n"
"v_mfma_f32_16x16x32_bf16 a[52:55], v[252:255], v[196:199], a[52:55]\n"
"v_exp_f32 v168, v168\n"
"v_mfma_f32_16x16x32_bf16 a[56:59], v[252:255], v[200:203], a[56:59]\n"
"v_exp_f32 v169, v169\n"
"v_mfma_f32_16x16x32_bf16 a[60:63], v[252:255], v[204:207], a[60:63]\n"
"v_add_f32 v243, v243, v168\n"
"v_add_f32 v243, v243, v169\n"
"v_mfma_f32_16x16x32_bf16 v[152:155], v[252:255], v[224:227], v[152:155]\n"
"ds_read_b64_tr_b16 v[224:225], v247 offset:896\n"
"ds_read_b64_tr_b16 v[226:227], v247 offset:2944\n"
"v_mfma_f32_16x16x32_bf16 v[156:159], v[252:255], v[232:235], v[156:159]\n"
"s_waitcnt lgkmcnt(8)\n"
"ds_read_b64_tr_b16 v[232:233], v247 offset:928\n"
"ds_read_b64_tr_b16 v[234:235], v247 offset:2976\n"
"v_mfma_f32_16x16x32_bf16 a[172:175], v[216:219], v[204:207], a[172:175]\n"
"ds_read_b128 v[252:255], v246 offset:0\n"
"v_mfma_f32_16x16x32_bf16 a[168:171], v[216:219], v[200:203], a[168:171]\n"
"v_exp_f32 v170, v170\n"
"v_mfma_f32_16x16x32_bf16 a[164:167], v[216:219], v[196:199], a[164:167]\n"
"v_exp_f32 v171, v171\n"
"v_mfma_f32_16x16x32_bf16 a[160:163], v[216:219], v[192:195], a[160:163]\n"
"s_waitcnt lgkmcnt(9)\n"
"ds_read_b128 v[216:219], v246 offset:128\n"
"v_add_f32 v243, v243, v170\n"
"v_mfma_f32_16x16x32_bf16 a[176:179], v[228:231], v[192:195], a[176:179]\n"
"v_add_f32 v243, v243, v171\n"
"v_mfma_f32_16x16x32_bf16 a[180:183], v[228:231], v[196:199], a[180:183]\n"
"v_exp_f32 v172, v172\n"
"v_mfma_f32_16x16x32_bf16 a[184:187], v[228:231], v[200:203], a[184:187]\n"
"v_exp_f32 v173, v173\n"
"v_mfma_f32_16x16x32_bf16 a[188:191], v[228:231], v[204:207], a[188:191]\n"
"s_waitcnt lgkmcnt(8)\n"
"ds_read_b128 v[228:231], v248 offset:5120\n"
"v_add_f32 v244, v244, v172\n"
"v_mfma_f32_16x16x32_bf16 a[204:207], v[220:223], v[204:207], a[204:207]\n"
"v_exp_f32 v174, v174\n"
"v_mfma_f32_16x16x32_bf16 a[200:203], v[220:223], v[200:203], a[200:203]\n"
"v_add_f32 v244, v244, v173\n"
"v_add_f32 v244, v244, v174\n"
"v_mfma_f32_16x16x32_bf16 a[196:199], v[220:223], v[196:199], a[196:199]\n"
"v_exp_f32 v175, v175\n"
"v_mfma_f32_16x16x32_bf16 a[192:195], v[220:223], v[192:195], a[192:195]\n"
"s_waitcnt lgkmcnt(7)\n"
"ds_read_b128 v[220:223], v248 offset:6144\n"
"v_add_f32 v244, v244, v175\n"
"v_mfma_f32_16x16x32_bf16 a[208:211], v[212:215], v[192:195], a[208:211]\n"
"v_exp_f32 v208, v208\n"
"v_mfma_f32_16x16x32_bf16 a[212:215], v[212:215], v[196:199], a[212:215]\n"
"v_exp_f32 v209, v209\n"
"v_mfma_f32_16x16x32_bf16 a[216:219], v[212:215], v[200:203], a[216:219]\n"
"v_add_f32 v245, v245, v208\n"
"v_add_f32 v245, v245, v209\n"
"v_mfma_f32_16x16x32_bf16 a[220:223], v[212:215], v[204:207], a[220:223]\n"
"s_waitcnt lgkmcnt(6)\n"
"ds_read_b128 v[212:215], v246 offset:256\n"
"v_cvt_pk_bf16_f32 v208, v208, v209\n"
"v_mfma_f32_16x16x32_bf16 a[236:239], v[224:227], v[204:207], a[236:239]\n"
"v_exp_f32 v210, v210\n"
"v_mfma_f32_16x16x32_bf16 a[232:235], v[224:227], v[200:203], a[232:235]\n"
"v_exp_f32 v211, v211\n"
"v_mfma_f32_16x16x32_bf16 a[228:231], v[224:227], v[196:199], a[228:231]\n"
"v_cvt_pk_bf16_f32 v209, v210, v211\n"
"v_add_f32 v245, v245, v210\n"
"v_mfma_f32_16x16x32_bf16 a[224:227], v[224:227], v[192:195], a[224:227]\n"
"s_waitcnt lgkmcnt(5)\n"
"ds_read_b128 v[224:227], v246 offset:384\n"
"v_add_f32 v245, v245, v211\n"
"v_mfma_f32_16x16x32_bf16 a[240:243], v[232:235], v[192:195], a[240:243]\n"
"v_cvt_pk_bf16_f32 v192, v160, v161\n"
"v_cvt_pk_bf16_f32 v193, v162, v163\n"
"v_mfma_f32_16x16x32_bf16 a[244:247], v[232:235], v[196:199], a[244:247]\n"
"v_cvt_pk_bf16_f32 v196, v164, v165\n"
"v_cvt_pk_bf16_f32 v197, v166, v167\n"
"v_mfma_f32_16x16x32_bf16 a[248:251], v[232:235], v[200:203], a[248:251]\n"
"v_cvt_pk_bf16_f32 v200, v168, v169\n"
"v_cvt_pk_bf16_f32 v201, v170, v171\n"
"v_mfma_f32_16x16x32_bf16 a[252:255], v[232:235], v[204:207], a[252:255]\n"
"s_waitcnt lgkmcnt(5)\n"
"v_cvt_pk_bf16_f32 v204, v172, v173\n"
"v_cvt_pk_bf16_f32 v205, v174, v175\n"
"v_mfma_f32_16x16x32_bf16 v[160:163], v[252:255], v[0:3], 0\n"
"s_waitcnt lgkmcnt(4)\n"
"ds_write_b64 v248, v[208:209] offset:3072\n"
"v_mfma_f32_16x16x32_bf16 v[160:163], v[216:219], v[32:35], v[160:163]\n"
"ds_read_b128 v[232:235], v248 offset:7168\n"
"v_mfma_f32_16x16x32_bf16 v[164:167], v[216:219], v[40:43], 0\n"
"v_exp_f32 v176, v176\n"
"v_mfma_f32_16x16x32_bf16 v[164:167], v[252:255], v[8:11], v[164:167]\n"
"v_exp_f32 v177, v177\n"
"v_mfma_f32_16x16x32_bf16 v[168:171], v[252:255], v[16:19], 0\n"
"v_add_f32 v241, v241, v176\n"
"v_add_f32 v241, v241, v177\n"
"v_mfma_f32_16x16x32_bf16 v[168:171], v[216:219], v[48:51], v[168:171]\n"
"v_exp_f32 v178, v178\n"
"v_mfma_f32_16x16x32_bf16 v[172:175], v[216:219], v[56:59], 0\n"
"v_exp_f32 v179, v179\n"
"v_mfma_f32_16x16x32_bf16 v[172:175], v[252:255], v[24:27], v[172:175]\n"
"s_waitcnt lgkmcnt(5)\n"
"v_add_f32 v241, v241, v178\n"
"v_add_f32 v241, v241, v179\n"
"v_mfma_f32_16x16x32_bf16 v[208:211], v[252:255], v[228:231], 0\n"
"s_waitcnt lgkmcnt(4)\n"
"ds_read_b128 v[228:231], v248 offset:8192\n"
"ds_read_b128 v[252:255], v246 offset:512\n"
"v_mfma_f32_16x16x32_bf16 v[208:211], v[216:219], v[220:223], v[208:211]\n"
"s_waitcnt lgkmcnt(5)\n"
"ds_read_b128 v[216:219], v246 offset:640\n"
"ds_read_b128 v[220:223], v248 offset:9216\n"
"v_mfma_f32_16x16x32_bf16 v[172:175], v[212:215], v[88:91], v[172:175]\n"
"s_waitcnt lgkmcnt(6)\n"
"v_cvt_pk_bf16_f32 v194, v176, v177\n"
"v_cvt_pk_bf16_f32 v195, v178, v179\n"
"v_mfma_f32_16x16x32_bf16 v[172:175], v[224:227], v[120:123], v[172:175]\n"
"v_exp_f32 v180, v180\n"
"v_mfma_f32_16x16x32_bf16 v[168:171], v[224:227], v[112:115], v[168:171]\n"
"v_exp_f32 v181, v181\n"
"v_mfma_f32_16x16x32_bf16 v[168:171], v[212:215], v[80:83], v[168:171]\n"
"v_add_f32 v242, v242, v180\n"
"v_add_f32 v242, v242, v181\n"
"v_mfma_f32_16x16x32_bf16 v[164:167], v[212:215], v[72:75], v[164:167]\n"
"v_exp_f32 v182, v182\n"
"v_mfma_f32_16x16x32_bf16 v[164:167], v[224:227], v[104:107], v[164:167]\n"
"v_exp_f32 v183, v183\n"
"v_mfma_f32_16x16x32_bf16 v[160:163], v[224:227], v[96:99], v[160:163]\n"
"v_add_f32 v242, v242, v182\n"
"v_add_f32 v242, v242, v183\n"
"v_mfma_f32_16x16x32_bf16 v[160:163], v[212:215], v[64:67], v[160:163]\n"
"s_waitcnt lgkmcnt(4)\n"
"v_cvt_pk_bf16_f32 v198, v180, v181\n"
"v_cvt_pk_bf16_f32 v199, v182, v183\n"
"v_mfma_f32_16x16x32_bf16 v[208:211], v[212:215], v[232:235], v[208:211]\n"
"s_waitcnt lgkmcnt(3)\n"
"ds_read_b128 v[212:215], v248 offset:10240\n"
"ds_read_b128 v[232:235], v246 offset:768\n"
"v_mfma_f32_16x16x32_bf16 v[208:211], v[224:227], v[228:231], v[208:211]\n"
"s_waitcnt lgkmcnt(4)\n"
"ds_read_b128 v[224:227], v246 offset:896\n"
"ds_read_b128 v[228:231], v248 offset:11264\n"
"v_mfma_f32_16x16x32_bf16 v[160:163], v[252:255], v[4:7], v[160:163]\n"
"s_waitcnt lgkmcnt(5)\n"
"v_exp_f32 v184, v184\n"
"v_mfma_f32_16x16x32_bf16 v[160:163], v[216:219], v[36:39], v[160:163]\n"
"v_exp_f32 v185, v185\n"
"v_mfma_f32_16x16x32_bf16 v[164:167], v[216:219], v[44:47], v[164:167]\n"
"v_add_f32 v243, v243, v184\n"
"v_add_f32 v243, v243, v185\n"
"v_mfma_f32_16x16x32_bf16 v[164:167], v[252:255], v[12:15], v[164:167]\n"
"v_exp_f32 v186, v186\n"
"v_mfma_f32_16x16x32_bf16 v[168:171], v[252:255], v[20:23], v[168:171]\n"
"v_exp_f32 v187, v187\n"
"v_mfma_f32_16x16x32_bf16 v[168:171], v[216:219], v[52:55], v[168:171]\n"
"v_add_f32 v243, v243, v186\n"
"v_add_f32 v243, v243, v187\n"
"v_mfma_f32_16x16x32_bf16 v[172:175], v[216:219], v[60:63], v[172:175]\n"
"v_cvt_pk_bf16_f32 v202, v184, v185\n"
"v_cvt_pk_bf16_f32 v203, v186, v187\n"
"v_mfma_f32_16x16x32_bf16 v[172:175], v[252:255], v[28:31], v[172:175]\n"
"s_waitcnt lgkmcnt(4)\n"
"v_exp_f32 v188, v188\n"
"v_mfma_f32_16x16x32_bf16 v[208:211], v[252:255], v[220:223], v[208:211]\n"
"s_waitcnt lgkmcnt(3)\n"
"ds_read_b128 v[220:223], v248 offset:12288\n"
"ds_read_b128 v[252:255], v246 offset:2048\n"
"v_mfma_f32_16x16x32_bf16 v[208:211], v[216:219], v[212:215], v[208:211]\n"
"s_waitcnt lgkmcnt(4)\n"
"ds_read_b128 v[212:215], v246 offset:2176\n"
"ds_read_b128 v[216:219], v246 offset:2304\n"
"v_mfma_f32_16x16x32_bf16 v[172:175], v[232:235], v[92:95], v[172:175]\n"
"s_waitcnt lgkmcnt(5)\n"
"v_exp_f32 v189, v189\n"
"v_mfma_f32_16x16x32_bf16 v[172:175], v[224:227], v[124:127], v[172:175]\n"
"v_add_f32 v244, v244, v188\n"
"v_add_f32 v244, v244, v189\n"
"v_mfma_f32_16x16x32_bf16 v[168:171], v[224:227], v[116:119], v[168:171]\n"
"v_exp_f32 v190, v190\n"
"v_mfma_f32_16x16x32_bf16 v[168:171], v[232:235], v[84:87], v[168:171]\n"
"v_exp_f32 v191, v191\n"
"v_mfma_f32_16x16x32_bf16 v[164:167], v[232:235], v[76:79], v[164:167]\n"
"v_add_f32 v244, v244, v190\n"
"v_add_f32 v244, v244, v191\n"
"v_mfma_f32_16x16x32_bf16 v[164:167], v[224:227], v[108:111], v[164:167]\n"
"v_cvt_pk_bf16_f32 v206, v188, v189\n"
"v_cvt_pk_bf16_f32 v207, v190, v191\n"
"v_mfma_f32_16x16x32_bf16 v[160:163], v[224:227], v[100:103], v[160:163]\n"
"s_add_u32 m0, s31, 0x4100\n"
"v_fma_f32 v172, v172, s65, v239\n"
"v_fma_f32 v173, v173, s65, v239\n"
"v_mfma_f32_16x16x32_bf16 v[160:163], v[232:235], v[68:71], v[160:163]\n"
"s_waitcnt lgkmcnt(4)\n"
"buffer_load_dwordx4 v249, s[48:51], s67 offen nt lds\n"
"v_mfma_f32_16x16x32_bf16 v[208:211], v[232:235], v[228:231], v[208:211]\n"
"s_waitcnt lgkmcnt(3)\n"
"ds_read_b128 v[228:231], v246 offset:2432\n"
"ds_read_b128 v[232:235], v246 offset:2560\n"
"v_mfma_f32_16x16x32_bf16 v[208:211], v[224:227], v[220:223], v[208:211]\n"
"s_waitcnt lgkmcnt(4)\n"
"ds_read_b128 v[220:223], v246 offset:2688\n"
"ds_read_b128 v[224:227], v246 offset:2816\n"
"v_mfma_f32_16x16x32_bf16 v[176:179], v[252:255], v[0:3], 0\n"
"s_waitcnt lgkmcnt(5)\n"
"v_fma_f32 v174, v174, s65, v239\n"
"v_fma_f32 v175, v175, s65, v239\n"
"v_mfma_f32_16x16x32_bf16 v[176:179], v[212:215], v[32:35], v[176:179]\n"
"s_sub_u32 s31, s31, s34\n"
"v_fma_f32 v208, v208, s65, v240\n"
"v_fma_f32 v209, v209, s65, v240\n"
"v_mfma_f32_16x16x32_bf16 v[180:183], v[212:215], v[40:43], 0\n"
"v_fma_f32 v210, v210, s65, v240\n"
"v_fma_f32 v211, v211, s65, v240\n"
"v_mfma_f32_16x16x32_bf16 v[180:183], v[252:255], v[8:11], v[180:183]\n"
"buffer_load_dwordx4 v249, s[48:51], s67 offen offset:1024 nt lds\n"
"v_mfma_f32_16x16x32_bf16 v[184:187], v[252:255], v[16:19], 0\n"
"v_max3_f32 v250, v208, v209, v210\n"
"v_max_f32 v250, v250, v211\n"
"v_mfma_f32_16x16x32_bf16 v[184:187], v[212:215], v[48:51], v[184:187]\n"
"v_cmp_lt_f32_e64 s[78:79], s66, v250\n"
"v_fma_f32 v168, v168, s65, v238\n"
"v_mfma_f32_16x16x32_bf16 v[188:191], v[212:215], v[56:59], 0\n"
"ds_read_b128 v[212:215], v246 offset:2944\n"
"v_max3_f32 v251, v172, v173, v174\n"
"v_mfma_f32_16x16x32_bf16 v[188:191], v[252:255], v[24:27], v[188:191]\n"
"s_waitcnt lgkmcnt(5)\n"
"ds_read_b64_tr_b16 v[252:253], v247 offset:16896\n"
"ds_read_b64_tr_b16 v[254:255], v247 offset:18944\n"
"v_mfma_f32_16x16x32_bf16 v[188:191], v[216:219], v[88:91], v[188:191]\n"
"s_waitcnt lgkmcnt(6)\n"
"s_or_b64 s[96:97], s[78:79], s[76:77]\n"
"s_cbranch_scc1 Lslow_4\n"
"Lret_4:\n"
"v_mfma_f32_16x16x32_bf16 v[188:191], v[228:231], v[120:123], v[188:191]\n"
"buffer_load_dwordx4 v249, s[48:51], s67 offen offset:2048 nt lds\n"
"v_mfma_f32_16x16x32_bf16 v[184:187], v[228:231], v[112:115], v[184:187]\n"
"v_fma_f32 v169, v169, s65, v238\n"
"v_fma_f32 v170, v170, s65, v238\n"
"v_mfma_f32_16x16x32_bf16 v[184:187], v[216:219], v[80:83], v[184:187]\n"
"v_max3_f32 v251, v251, v175, v168\n"
"v_fma_f32 v171, v171, s65, v238\n"
"v_mfma_f32_16x16x32_bf16 v[180:183], v[216:219], v[72:75], v[180:183]\n"
"v_fma_f32 v164, v164, s65, v237\n"
"v_max3_f32 v251, v251, v169, v170\n"
"v_mfma_f32_16x16x32_bf16 v[180:183], v[228:231], v[104:107], v[180:183]\n"
"v_fma_f32 v165, v165, s65, v237\n"
"v_fma_f32 v166, v166, s65, v237\n"
"v_mfma_f32_16x16x32_bf16 v[176:179], v[228:231], v[96:99], v[176:179]\n"
"ds_read_b64_tr_b16 v[228:229], v247 offset:16928\n"
"ds_read_b64_tr_b16 v[230:231], v247 offset:18976\n"
"v_mfma_f32_16x16x32_bf16 v[176:179], v[216:219], v[64:67], v[176:179]\n"
"s_waitcnt lgkmcnt(7)\n"
"buffer_load_dwordx4 v249, s[48:51], s67 offen offset:3072 nt lds\n"
"v_mfma_f32_16x16x32_bf16 v[176:179], v[232:235], v[4:7], v[176:179]\n"
"s_waitcnt lgkmcnt(6)\n"
"ds_read_b64_tr_b16 v[216:217], v247 offset:17024\n"
"ds_read_b64_tr_b16 v[218:219], v247 offset:19072\n"
"v_mfma_f32_16x16x32_bf16 v[176:179], v[220:223], v[36:39], v[176:179]\n"
"s_add_u32 s67, s67, 0x4000\n"
"v_max3_f32 v251, v251, v171, v164\n"
"v_fma_f32 v167, v167, s65, v237\n"
"v_mfma_f32_16x16x32_bf16 v[180:183], v[220:223], v[44:47], v[180:183]\n"
"v_fma_f32 v160, v160, s65, v236\n"
"v_max3_f32 v251, v251, v165, v166\n"
"v_mfma_f32_16x16x32_bf16 v[180:183], v[232:235], v[12:15], v[180:183]\n"
"v_fma_f32 v161, v161, s65, v236\n"
"v_fma_f32 v162, v162, s65, v236\n"
"v_mfma_f32_16x16x32_bf16 v[184:187], v[232:235], v[20:23], v[184:187]\n"
"v_max3_f32 v251, v251, v167, v160\n"
"v_fma_f32 v163, v163, s65, v236\n"
"v_mfma_f32_16x16x32_bf16 v[184:187], v[220:223], v[52:55], v[184:187]\n"
"v_max3_f32 v251, v251, v161, v162\n"
"v_mfma_f32_16x16x32_bf16 v[188:191], v[220:223], v[60:63], v[188:191]\n"
"ds_read_b64_tr_b16 v[220:221], v247 offset:17056\n"
"ds_read_b64_tr_b16 v[222:223], v247 offset:19104\n"
"v_mfma_f32_16x16x32_bf16 v[188:191], v[232:235], v[28:31], v[188:191]\n"
"s_waitcnt lgkmcnt(9)\n"
"ds_read_b64_tr_b16 v[232:233], v247 offset:17152\n"
"ds_read_b64_tr_b16 v[234:235], v247 offset:19200\n"
"v_mfma_f32_16x16x32_bf16 v[188:191], v[224:227], v[92:95], v[188:191]\n"
"s_waitcnt lgkmcnt(10)\n"
"v_mfma_f32_16x16x32_bf16 v[188:191], v[212:215], v[124:127], v[188:191]\n"
"v_mfma_f32_16x16x32_bf16 v[184:187], v[212:215], v[116:119], v[184:187]\n"
"v_mfma_f32_16x16x32_bf16 v[184:187], v[224:227], v[84:87], v[184:187]\n"
"v_mfma_f32_16x16x32_bf16 v[180:183], v[224:227], v[76:79], v[180:183]\n"
"v_mfma_f32_16x16x32_bf16 v[180:183], v[212:215], v[108:111], v[180:183]\n"
"v_mfma_f32_16x16x32_bf16 v[176:179], v[212:215], v[100:103], v[176:179]\n"
"ds_read_b64_tr_b16 v[212:213], v247 offset:17184\n"
"ds_read_b64_tr_b16 v[214:215], v247 offset:19232\n"
"v_mfma_f32_16x16x32_bf16 v[176:179], v[224:227], v[68:71], v[176:179]\n"
"s_waitcnt lgkmcnt(10)\n"
"ds_read_b64_tr_b16 v[224:225], v247 offset:16640\n"
"ds_read_b64_tr_b16 v[226:227], v247 offset:18688\n"
"v_mfma_f32_16x16x32_bf16 a[76:79], v[252:255], v[204:207], a[76:79]\n"
"v_fma_f32 v188, v188, s65, v239\n"
"v_fma_f32 v189, v189, s65, v239\n"
"v_mfma_f32_16x16x32_bf16 a[72:75], v[252:255], v[200:203], a[72:75]\n"
"v_fma_f32 v190, v190, s65, v239\n"
"v_max3_f32 v251, v251, v163, v188\n"
"v_mfma_f32_16x16x32_bf16 a[68:71], v[252:255], v[196:199], a[68:71]\n"
"v_fma_f32 v191, v191, s65, v239\n"
"v_fma_f32 v184, v184, s65, v238\n"
"v_mfma_f32_16x16x32_bf16 a[64:67], v[252:255], v[192:195], a[64:67]\n"
"s_waitcnt lgkmcnt(10)\n"
"v_max3_f32 v251, v251, v189, v190\n"
"v_fma_f32 v185, v185, s65, v238\n"
"v_mfma_f32_16x16x32_bf16 a[80:83], v[228:231], v[192:195], a[80:83]\n"
"v_fma_f32 v186, v186, s65, v238\n"
"v_max3_f32 v251, v251, v191, v184\n"
"v_mfma_f32_16x16x32_bf16 a[84:87], v[228:231], v[196:199], a[84:87]\n"
"v_fma_f32 v187, v187, s65, v238\n"
"v_fma_f32 v180, v180, s65, v237\n"
"v_mfma_f32_16x16x32_bf16 a[88:91], v[228:231], v[200:203], a[88:91]\n"
"v_max3_f32 v251, v251, v185, v186\n"
"v_fma_f32 v181, v181, s65, v237\n"
"v_mfma_f32_16x16x32_bf16 a[92:95], v[228:231], v[204:207], a[92:95]\n"
"s_waitcnt lgkmcnt(8)\n"
"v_fma_f32 v182, v182, s65, v237\n"
"v_max3_f32 v251, v251, v187, v180\n"
"v_mfma_f32_16x16x32_bf16 a[108:111], v[216:219], v[204:207], a[108:111]\n"
"v_fma_f32 v183, v183, s65, v237\n"
"v_fma_f32 v176, v176, s65, v236\n"
"v_mfma_f32_16x16x32_bf16 a[104:107], v[216:219], v[200:203], a[104:107]\n"
"v_max3_f32 v251, v251, v181, v182\n"
"v_fma_f32 v177, v177, s65, v236\n"
"v_mfma_f32_16x16x32_bf16 a[100:103], v[216:219], v[196:199], a[100:103]\n"
"s_waitcnt vmcnt(12)\n"
"s_barrier\n"
"v_mfma_f32_16x16x32_bf16 a[96:99], v[216:219], v[192:195], a[96:99]\n"
"s_waitcnt lgkmcnt(6)\n"
"ds_read_b128 v[252:255], v248 offset:4096\n"
"ds_read_b128 v[228:231], v248 offset:3072\n"
"v_mfma_f32_16x16x32_bf16 a[112:115], v[220:223], v[192:195], a[112:115]\n"
"ds_read_b64_tr_b16 v[216:217], v247 offset:16672\n"
"ds_read_b64_tr_b16 v[218:219], v247 offset:18720\n"
"v_mfma_f32_16x16x32_bf16 a[116:119], v[220:223], v[196:199], a[116:119]\n"
"ds_read_b32 v250, v248 offset:0\n"
"v_fma_f32 v178, v178, s65, v236\n"
"v_mfma_f32_16x16x32_bf16 a[120:123], v[220:223], v[200:203], a[120:123]\n"
"s_add_u32 m0, s33, 0\n"
"v_max3_f32 v251, v251, v183, v176\n"
"v_fma_f32 v179, v179, s65, v236\n"
"v_mfma_f32_16x16x32_bf16 a[124:127], v[220:223], v[204:207], a[124:127]\n"
"s_waitcnt lgkmcnt(9)\n"
"buffer_load_dwordx4 v249, s[52:55], s68 offen nt lds\n"
"v_mfma_f32_16x16x32_bf16 a[140:143], v[232:235], v[204:207], a[140:143]\n"
"ds_read_b64_tr_b16 v[220:221], v247 offset:16768\n"
"ds_read_b64_tr_b16 v[222:223], v247 offset:18816\n"
"v_mfma_f32_16x16x32_bf16 a[136:139], v[232:235], v[200:203], a[136:139]\n"
"v_max3_f32 v251, v251, v177, v178\n"
"v_mfma_f32_16x16x32_bf16 a[132:135], v[232:235], v[196:199], a[132:135]\n"
"v_mfma_f32_16x16x32_bf16 a[128:131], v[232:235], v[192:195], a[128:131]\n"
"s_waitcnt lgkmcnt(9)\n"
"ds_read_b64_tr_b16 v[232:233], v247 offset:16800\n"
"ds_read_b64_tr_b16 v[234:235], v247 offset:18848\n"
"v_mfma_f32_16x16x32_bf16 a[144:147], v[212:215], v[192:195], a[144:147]\n"
"v_mfma_f32_16x16x32_bf16 a[148:151], v[212:215], v[196:199], a[148:151]\n"
"buffer_load_dwordx4 v249, s[52:55], s68 offen offset:1024 nt lds\n"
"v_mfma_f32_16x16x32_bf16 a[152:155], v[212:215], v[200:203], a[152:155]\n"
"v_mfma_f32_16x16x32_bf16 a[156:159], v[212:215], v[204:207], a[156:159]\n"
"s_waitcnt lgkmcnt(8)\n"
"ds_read_b64_tr_b16 v[212:213], v247 offset:17280\n"
"ds_read_b64_tr_b16 v[214:215], v247 offset:19328\n"
"v_mfma_f32_16x16x32_bf16 v[132:135], v[224:227], v[252:255], v[132:135]\n"
"s_waitcnt lgkmcnt(9)\n"
"s_waitcnt lgkmcnt(6)\n"
"v_max3_f32 v251, v251, v179, v250\n"
"v_mfma_f32_16x16x32_bf16 v[128:131], v[224:227], v[228:231], v[128:131]\n"
"v_cmp_lt_f32 vcc, s66, v251\n"
"v_mfma_f32_16x16x32_bf16 a[12:15], v[224:227], v[204:207], a[12:15]\n"
"v_mfma_f32_16x16x32_bf16 a[8:11], v[224:227], v[200:203], a[8:11]\n"
"buffer_load_dwordx4 v249, s[52:55], s68 offen offset:2048 nt lds\n"
"v_mfma_f32_16x16x32_bf16 a[4:7], v[224:227], v[196:199], a[4:7]\n"
"v_mfma_f32_16x16x32_bf16 a[0:3], v[224:227], v[192:195], a[0:3]\n"
"ds_read_b64_tr_b16 v[224:225], v247 offset:17312\n"
"ds_read_b64_tr_b16 v[226:227], v247 offset:19360\n"
"v_mfma_f32_16x16x32_bf16 a[16:19], v[216:219], v[192:195], a[16:19]\n"
"s_cbranch_vccnz Lslow_5\n"
"Lret_5:\n"
"v_mfma_f32_16x16x32_bf16 a[20:23], v[216:219], v[196:199], a[20:23]\n"
"v_exp_f32 v208, v208\n"
"v_mfma_f32_16x16x32_bf16 a[24:27], v[216:219], v[200:203], a[24:27]\n"
"v_exp_f32 v160, v160\n"
"v_mfma_f32_16x16x32_bf16 a[28:31], v[216:219], v[204:207], a[28:31]\n"
"buffer_load_dwordx4 v249, s[52:55], s68 offen offset:3072 nt lds\n"
"v_mfma_f32_16x16x32_bf16 v[136:139], v[216:219], v[228:231], v[136:139]\n"
"s_add_u32 s68, s68, 0x4000\n"
"v_exp_f32 v164, v164\n"
"v_mfma_f32_16x16x32_bf16 v[140:143], v[216:219], v[252:255], v[140:143]\n"
"s_waitcnt lgkmcnt(6)\n"
"ds_read_b64_tr_b16 v[216:217], v247 offset:17408\n"
"ds_read_b64_tr_b16 v[218:219], v247 offset:19456\n"
"v_mfma_f32_16x16x32_bf16 v[148:151], v[220:223], v[252:255], v[148:151]\n"
"v_exp_f32 v168, v168\n"
"v_mfma_f32_16x16x32_bf16 v[144:147], v[220:223], v[228:231], v[144:147]\n"
"v_exp_f32 v172, v172\n"
"v_mfma_f32_16x16x32_bf16 a[44:47], v[220:223], v[204:207], a[44:47]\n"
"v_exp_f32 v209, v209\n"
"v_mfma_f32_16x16x32_bf16 a[40:43], v[220:223], v[200:203], a[40:43]\n"
"v_exp_f32 v161, v161\n"
"v_mfma_f32_16x16x32_bf16 a[36:39], v[220:223], v[196:199], a[36:39]\n"
"v_exp_f32 v165, v165\n"
"v_mfma_f32_16x16x32_bf16 a[32:35], v[220:223], v[192:195], a[32:35]\n"
"s_waitcnt lgkmcnt(6)\n"
"ds_read_b64_tr_b16 v[220:221], v247 offset:17440\n"
"ds_read_b64_tr_b16 v[222:223], v247 offset:19488\n"
"v_mfma_f32_16x16x32_bf16 a[48:51], v[232:235], v[192:195], a[48:51]\n"
"v_exp_f32 v169, v169\n"
"v_mfma_f32_16x16x32_bf16 a[52:55], v[232:235], v[196:199], a[52:55]\n"
"v_exp_f32 v173, v173\n"
"v_mfma_f32_16x16x32_bf16 a[56:59], v[232:235], v[200:203], a[56:59]\n"
"v_add_f32 v245, v245, v208\n"
"v_add_f32 v241, v241, v160\n"
"v_mfma_f32_16x16x32_bf16 a[60:63], v[232:235], v[204:207], a[60:63]\n"
"v_exp_f32 v210, v210\n"
"v_mfma_f32_16x16x32_bf16 v[152:155], v[232:235], v[228:231], v[152:155]\n"
"ds_read_b64_tr_b16 v[228:229], v247 offset:17536\n"
"ds_read_b64_tr_b16 v[230:231], v247 offset:19584\n"
"v_mfma_f32_16x16x32_bf16 v[156:159], v[232:235], v[252:255], v[156:159]\n"
"s_waitcnt lgkmcnt(8)\n"
"ds_read_b64_tr_b16 v[232:233], v247 offset:17568\n"
"ds_read_b64_tr_b16 v[234:235], v247 offset:19616\n"
"v_mfma_f32_16x16x32_bf16 a[172:175], v[212:215], v[204:207], a[172:175]\n"
"v_exp_f32 v211, v211\n"
"v_mfma_f32_16x16x32_bf16 a[168:171], v[212:215], v[200:203], a[168:171]\n"
"v_exp_f32 v162, v162\n"
"v_mfma_f32_16x16x32_bf16 a[164:167], v[212:215], v[196:199], a[164:167]\n"
"v_add_f32 v242, v242, v164\n"
"v_add_f32 v243, v243, v168\n"
"v_mfma_f32_16x16x32_bf16 a[160:163], v[212:215], v[192:195], a[160:163]\n"
"s_waitcnt lgkmcnt(8)\n"
"ds_read_b128 v[212:215], v246 offset:16640\n"
"v_add_f32 v244, v244, v172\n"
"v_mfma_f32_16x16x32_bf16 a[176:179], v[224:227], v[192:195], a[176:179]\n"
"v_exp_f32 v166, v166\n"
"v_mfma_f32_16x16x32_bf16 a[180:183], v[224:227], v[196:199], a[180:183]\n"
"v_exp_f32 v170, v170\n"
"v_mfma_f32_16x16x32_bf16 a[184:187], v[224:227], v[200:203], a[184:187]\n"
"v_exp_f32 v174, v174\n"
"v_mfma_f32_16x16x32_bf16 a[188:191], v[224:227], v[204:207], a[188:191]\n"
"s_waitcnt lgkmcnt(7)\n"
"v_add_f32 v245, v245, v209\n"
"v_cvt_pk_bf16_f32 v208, v208, v209\n"
"v_mfma_f32_16x16x32_bf16 a[204:207], v[216:219], v[204:207], a[204:207]\n"
"v_add_f32 v241, v241, v161\n"
"v_add_f32 v242, v242, v165\n"
"v_mfma_f32_16x16x32_bf16 a[200:203], v[216:219], v[200:203], a[200:203]\n"
"v_exp_f32 v163, v163\n"
"v_mfma_f32_16x16x32_bf16 a[196:199], v[216:219], v[196:199], a[196:199]\n"
"v_exp_f32 v167, v167\n"
"v_mfma_f32_16x16x32_bf16 a[192:195], v[216:219], v[192:195], a[192:195]\n"
"s_waitcnt lgkmcnt(5)\n"
"ds_read_b128 v[216:219], v246 offset:16768\n"
"v_add_f32 v243, v243, v169\n"
"v_mfma_f32_16x16x32_bf16 a[208:211], v[220:223], v[192:195], a[208:211]\n"
"v_exp_f32 v171, v171\n"
"v_mfma_f32_16x16x32_bf16 a[212:215], v[220:223], v[196:199], a[212:215]\n"
"v_add_f32 v244, v244, v173\n"
"v_add_f32 v245, v245, v210\n"
"v_mfma_f32_16x16x32_bf16 a[216:219], v[220:223], v[200:203], a[216:219]\n"
"v_exp_f32 v175, v175\n"
"v_mfma_f32_16x16x32_bf16 a[220:223], v[220:223], v[204:207], a[220:223]\n"
"s_waitcnt lgkmcnt(4)\n"
"ds_read_b128 v[220:223], v246 offset:16896\n"
"ds_read_b128 v[224:227], v246 offset:17024\n"
"v_mfma_f32_16x16x32_bf16 a[236:239], v[228:231], v[204:207], a[236:239]\n"
"v_cvt_pk_bf16_f32 v209, v210, v211\n"
"v_add_f32 v241, v241, v162\n"
"v_mfma_f32_16x16x32_bf16 a[232:235], v[228:231], v[200:203], a[232:235]\n"
"v_add_f32 v242, v242, v166\n"
"v_add_f32 v243, v243, v170\n"
"v_mfma_f32_16x16x32_bf16 a[228:231], v[228:231], v[196:199], a[228:231]\n"
"v_add_f32 v244, v244, v174\n"
"v_add_u32 v247, s30, v247\n"
"v_mfma_f32_16x16x32_bf16 a[224:227], v[228:231], v[192:195], a[224:227]\n"
"s_waitcnt lgkmcnt(4)\n"
"v_add_f32 v245, v245, v211\n"
"v_add_f32 v241, v241, v163\n"
"v_mfma_f32_16x16x32_bf16 a[240:243], v[232:235], v[192:195], a[240:243]\n"
"s_sub_u32 s30, 0, s30\n"
"ds_write_b64 v248, v[208:209] offset:1024\n"
"v_mfma_f32_16x16x32_bf16 a[244:247], v[232:235], v[196:199], a[244:247]\n"
"s_sub_u32 s70, s70, 1\n"
"v_add_f32 v242, v242, v167\n"
"v_add_f32 v243, v243, v171\n"
"v_mfma_f32_16x16x32_bf16 a[248:251], v[232:235], v[200:203], a[248:251]\n"
"v_add_f32 v244, v244, v175\n"
"v_cvt_pk_bf16_f32 v192, v160, v161\n"
"v_mfma_f32_16x16x32_bf16 a[252:255], v[232:235], v[204:207], a[252:255]\n"
"v_cvt_pk_bf16_f32 v193, v162, v163\n"
"v_cvt_pk_bf16_f32 v196, v164, v165\n"
"v_cvt_pk_bf16_f32 v197, v166, v167\n"
"v_cvt_pk_bf16_f32 v200, v168, v169\n"
"v_cvt_pk_bf16_f32 v201, v170, v171\n"
"v_cvt_pk_bf16_f32 v204, v172, v173\n"
"v_cvt_pk_bf16_f32 v205, v174, v175\n"
"s_cmp_lg_u32 s70, 0\n"
"s_cbranch_scc1 Lloop0\n"
"s_branch Lloopx\n"
"Lloop1:\n"
"s_waitcnt lgkmcnt(3)\n"
"v_mfma_f32_16x16x32_bf16 v[160:163], v[212:215], v[0:3], 0\n"
"s_waitcnt lgkmcnt(2)\n"
"ds_read_b128 v[228:231], v248 offset:13312\n"
"ds_read_b128 v[232:235], v248 offset:14336\n"
"v_mfma_f32_16x16x32_bf16 v[160:163], v[216:219], v[32:35], v[160:163]\n"
"ds_read_b128 v[252:255], v248 offset:15360\n"
"v_mfma_f32_16x16x32_bf16 v[164:167], v[216:219], v[40:43], 0\n"
"v_exp_f32 v176, v176\n"
"v_mfma_f32_16x16x32_bf16 v[164:167], v[212:215], v[8:11], v[164:167]\n"
"v_exp_f32 v177, v177\n"
"v_mfma_f32_16x16x32_bf16 v[168:171], v[212:215], v[16:19], 0\n"
"v_add_f32 v241, v241, v176\n"
"v_add_f32 v241, v241, v177\n"
"v_mfma_f32_16x16x32_bf16 v[168:171], v[216:219], v[48:51], v[168:171]\n"
"v_exp_f32 v178, v178\n"
"v_mfma_f32_16x16x32_bf16 v[172:175], v[216:219], v[56:59], 0\n"
"v_exp_f32 v179, v179\n"
"v_mfma_f32_16x16x32_bf16 v[172:175], v[212:215], v[24:27], v[172:175]\n"
"s_waitcnt lgkmcnt(2)\n"
"v_add_f32 v241, v241, v178\n"
"v_add_f32 v241, v241, v179\n"
"v_mfma_f32_16x16x32_bf16 v[208:211], v[212:215], v[228:231], 0\n"
"s_waitcnt lgkmcnt(1)\n"
"ds_read_b128 v[212:215], v248 offset:16384\n"
"ds_read_b128 v[228:231], v246 offset:17152\n"
"v_mfma_f32_16x16x32_bf16 v[208:211], v[216:219], v[232:235], v[208:211]\n"
"ds_read_b128 v[216:219], v246 offset:17280\n"
"ds_read_b128 v[232:235], v248 offset:17408\n"
"v_mfma_f32_16x16x32_bf16 v[172:175], v[220:223], v[88:91], v[172:175]\n"
"v_cvt_pk_bf16_f32 v194, v176, v177\n"
"v_cvt_pk_bf16_f32 v195, v178, v179\n"
"v_mfma_f32_16x16x32_bf16 v[172:175], v[224:227], v[120:123], v[172:175]\n"
"v_exp_f32 v180, v180\n"
"v_mfma_f32_16x16x32_bf16 v[168:171], v[224:227], v[112:115], v[168:171]\n"
"v_exp_f32 v181, v181\n"
"v_mfma_f32_16x16x32_bf16 v[168:171], v[220:223], v[80:83], v[168:171]\n"
"v_add_f32 v242, v242, v180\n"
"v_add_f32 v242, v242, v181\n"
"v_mfma_f32_16x16x32_bf16 v[164:167], v[220:223], v[72:75], v[164:167]\n"
"v_exp_f32 v182, v182\n"
"v_mfma_f32_16x16x32_bf16 v[164:167], v[224:227], v[104:107], v[164:167]\n"
"v_exp_f32 v183, v183\n"
"v_mfma_f32_16x16x32_bf16 v[160:163], v[224:227], v[96:99], v[160:163]\n"
"v_add_f32 v242, v242, v182\n"
"v_add_f32 v242, v242, v183\n"
"v_mfma_f32_16x16x32_bf16 v[160:163], v[220:223], v[64:67], v[160:163]\n"
"s_waitcnt lgkmcnt(4)\n"
"v_cvt_pk_bf16_f32 v198, v180, v181\n"
"v_cvt_pk_bf16_f32 v199, v182, v183\n"
"v_mfma_f32_16x16x32_bf16 v[208:211], v[220:223], v[252:255], v[208:211]\n"
"s_waitcnt lgkmcnt(3)\n"
"ds_read_b128 v[220:223], v248 offset:18432\n"
"ds_read_b128 v[252:255], v246 offset:17408\n"
"v_mfma_f32_16x16x32_bf16 v[208:211], v[224:227], v[212:215], v[208:211]\n"
"s_waitcnt lgkmcnt(4)\n"
"ds_read_b128 v[212:215], v246 offset:17536\n"
"ds_read_b128 v[224:227], v248 offset:19456\n"
"v_mfma_f32_16x16x32_bf16 v[160:163], v[228:231], v[4:7], v[160:163]\n"
"s_waitcnt lgkmcnt(5)\n"
"v_exp_f32 v184, v184\n"
"v_mfma_f32_16x16x32_bf16 v[160:163], v[216:219], v[36:39], v[160:163]\n"
"v_exp_f32 v185, v185\n"
"v_mfma_f32_16x16x32_bf16 v[164:167], v[216:219], v[44:47], v[164:167]\n"
"v_add_f32 v243, v243, v184\n"
"v_add_f32 v243, v243, v185\n"
"v_mfma_f32_16x16x32_bf16 v[164:167], v[228:231], v[12:15], v[164:167]\n"
"v_exp_f32 v186, v186\n"
"v_mfma_f32_16x16x32_bf16 v[168:171], v[228:231], v[20:23], v[168:171]\n"
"v_exp_f32 v187, v187\n"
"v_mfma_f32_16x16x32_bf16 v[168:171], v[216:219], v[52:55], v[168:171]\n"
"v_add_f32 v243, v243, v186\n"
"v_add_f32 v243, v243, v187\n"
"v_mfma_f32_16x16x32_bf16 v[172:175], v[216:219], v[60:63], v[172:175]\n"
"v_cvt_pk_bf16_f32 v202, v184, v185\n"
"v_cvt_pk_bf16_f32 v203, v186, v187\n"
"v_mfma_f32_16x16x32_bf16 v[172:175], v[228:231], v[28:31], v[172:175]\n"
"s_waitcnt lgkmcnt(4)\n"
"v_exp_f32 v188, v188\n"
"v_mfma_f32_16x16x32_bf16 v[208:211], v[228:231], v[232:235], v[208:211]\n"
"s_waitcnt lgkmcnt(3)\n"
"ds_read_b128 v[228:231], v248 offset:20480\n"
"ds_read_b128 v[232:235], v246 offset:18688\n"
"v_mfma_f32_16x16x32_bf16 v[208:211], v[216:219], v[220:223], v[208:211]\n"
"s_waitcnt lgkmcnt(4)\n"
"ds_read_b128 v[216:219], v246 offset:18816\n"
"ds_read_b128 v[220:223], v246 offset:18944\n"
"v_mfma_f32_16x16x32_bf16 v[172:175], v[252:255], v[92:95], v[172:175]\n"
"s_waitcnt lgkmcnt(5)\n"
"v_exp_f32 v189, v189\n"
"v_mfma_f32_16x16x32_bf16 v[172:175], v[212:215], v[124:127], v[172:175]\n"
"v_add_f32 v244, v244, v188\n"
"v_add_f32 v244, v244, v189\n"
"v_mfma_f32_16x16x32_bf16 v[168:171], v[212:215], v[116:119], v[168:171]\n"
"v_exp_f32 v190, v190\n"
"v_mfma_f32_16x16x32_bf16 v[168:171], v[252:255], v[84:87], v[168:171]\n"
"v_exp_f32 v191, v191\n"
"v_mfma_f32_16x16x32_bf16 v[164:167], v[252:255], v[76:79], v[164:167]\n"
"v_add_f32 v244, v244, v190\n"
"v_add_f32 v244, v244, v191\n"
"v_mfma_f32_16x16x32_bf16 v[164:167], v[212:215], v[108:111], v[164:167]\n"
"v_cvt_pk_bf16_f32 v206, v188, v189\n"
"v_cvt_pk_bf16_f32 v207, v190, v191\n"
"v_mfma_f32_16x16x32_bf16 v[160:163], v[212:215], v[100:103], v[160:163]\n"
"s_add_u32 m0, s31, 0\n"
"v_fma_f32 v172, v172, s65, v239\n"
"v_fma_f32 v173, v173, s65, v239\n"
"v_mfma_f32_16x16x32_bf16 v[160:163], v[252:255], v[68:71], v[160:163]\n"
"s_waitcnt lgkmcnt(4)\n"
"buffer_load_dwordx4 v249, s[48:51], s67 offen nt lds\n"
"v_mfma_f32_16x16x32_bf16 v[208:211], v[252:255], v[224:227], v[208:211]\n"
"s_waitcnt lgkmcnt(3)\n"
"ds_read_b128 v[224:227], v246 offset:19072\n"
"ds_read_b128 v[252:255], v246 offset:19200\n"
"v_mfma_f32_16x16x32_bf16 v[208:211], v[212:215], v[228:231], v[208:211]\n"
"s_waitcnt lgkmcnt(4)\n"
"ds_read_b128 v[212:215], v246 offset:19328\n"
"ds_read_b128 v[228:231], v246 offset:19456\n"
"v_mfma_f32_16x16x32_bf16 v[176:179], v[232:235], v[0:3], 0\n"
"s_waitcnt lgkmcnt(5)\n"
"v_fma_f32 v174, v174, s65, v239\n"
"v_fma_f32 v175, v175, s65, v239\n"
"v_mfma_f32_16x16x32_bf16 v[176:179], v[216:219], v[32:35], v[176:179]\n"
"v_fma_f32 v208, v208, s65, v240\n"
"v_fma_f32 v209, v209, s65, v240\n"
"v_mfma_f32_16x16x32_bf16 v[180:183], v[216:219], v[40:43], 0\n"
"v_fma_f32 v210, v210, s65, v240\n"
"v_fma_f32 v211, v211, s65, v240\n"
"v_mfma_f32_16x16x32_bf16 v[180:183], v[232:235], v[8:11], v[180:183]\n"
"buffer_load_dwordx4 v249, s[48:51], s67 offen offset:1024 nt lds\n"
"v_mfma_f32_16x16x32_bf16 v[184:187], v[232:235], v[16:19], 0\n"
"v_max3_f32 v250, v208, v209, v210\n"
"v_max_f32 v250, v250, v211\n"
"v_mfma_f32_16x16x32_bf16 v[184:187], v[216:219], v[48:51], v[184:187]\n"
"v_cmp_lt_f32_e64 s[78:79], s66, v250\n"
"v_fma_f32 v168, v168, s65, v238\n"
"v_mfma_f32_16x16x32_bf16 v[188:191], v[216:219], v[56:59], 0\n"
"ds_read_b128 v[216:219], v246 offset:19584\n"
"v_max3_f32 v251, v172, v173, v174\n"
"v_mfma_f32_16x16x32_bf16 v[188:191], v[232:235], v[24:27], v[188:191]\n"
"s_waitcnt lgkmcnt(5)\n"
"ds_read_b64_tr_b16 v[232:233], v247 offset:0\n"
"ds_read_b64_tr_b16 v[234:235], v247 offset:2048\n"
"v_mfma_f32_16x16x32_bf16 v[188:191], v[220:223], v[88:91], v[188:191]\n"
"s_waitcnt lgkmcnt(6)\n"
"s_or_b64 s[96:97], s[78:79], s[76:77]\n"
"s_cbranch_scc1 Lslow_6\n"
"Lret_6:\n"
"v_mfma_f32_16x16x32_bf16 v[188:191], v[224:227], v[120:123], v[188:191]\n"
"buffer_load_dwordx4 v249, s[48:51], s67 offen offset:2048 nt lds\n"
"v_mfma_f32_16x16x32_bf16 v[184:187], v[224:227], v[112:115], v[184:187]\n"
"v_fma_f32 v169, v169, s65, v238\n"
"v_fma_f32 v170, v170, s65, v238\n"
"v_mfma_f32_16x16x32_bf16 v[184:187], v[220:223], v[80:83], v[184:187]\n"
"v_max3_f32 v251, v251, v175, v168\n"
"v_add_u32 v246, s34, v246\n"
"v_mfma_f32_16x16x32_bf16 v[180:183], v[220:223], v[72:75], v[180:183]\n"
"s_sub_u32 s34, 0, s34\n"
"v_fma_f32 v171, v171, s65, v238\n"
"v_fma_f32 v164, v164, s65, v237\n"
"v_mfma_f32_16x16x32_bf16 v[180:183], v[224:227], v[104:107], v[180:183]\n"
"v_max3_f32 v251, v251, v169, v170\n"
"v_fma_f32 v165, v165, s65, v237\n"
"v_mfma_f32_16x16x32_bf16 v[176:179], v[224:227], v[96:99], v[176:179]\n"
"ds_read_b64_tr_b16 v[224:225], v247 offset:32\n"
"ds_read_b64_tr_b16 v[226:227], v247 offset:2080\n"
"v_mfma_f32_16x16x32_bf16 v[176:179], v[220:223], v[64:67], v[176:179]\n"
"s_waitcnt lgkmcnt(7)\n"
"buffer_load_dwordx4 v249, s[48:51], s67 offen offset:3072 nt lds\n"
"v_mfma_f32_16x16x32_bf16 v[176:179], v[252:255], v[4:7], v[176:179]\n"
"s_waitcnt lgkmcnt(6)\n"
"ds_read_b64_tr_b16 v[220:221], v247 offset:128\n"
"ds_read_b64_tr_b16 v[222:223], v247 offset:2176\n"
"v_mfma_f32_16x16x32_bf16 v[176:179], v[212:215], v[36:39], v[176:179]\n"
"s_add_u32 s67, s67, 0x4000\n"
"v_fma_f32 v166, v166, s65, v237\n"
"v_max3_f32 v251, v251, v171, v164\n"
"v_mfma_f32_16x16x32_bf16 v[180:183], v[212:215], v[44:47], v[180:183]\n"
"v_fma_f32 v167, v167, s65, v237\n"
"v_fma_f32 v160, v160, s65, v236\n"
"v_mfma_f32_16x16x32_bf16 v[180:183], v[252:255], v[12:15], v[180:183]\n"
"v_max3_f32 v251, v251, v165, v166\n"
"v_fma_f32 v161, v161, s65, v236\n"
"v_mfma_f32_16x16x32_bf16 v[184:187], v[252:255], v[20:23], v[184:187]\n"
"v_fma_f32 v162, v162, s65, v236\n"
"v_max3_f32 v251, v251, v167, v160\n"
"v_mfma_f32_16x16x32_bf16 v[184:187], v[212:215], v[52:55], v[184:187]\n"
"v_fma_f32 v163, v163, s65, v236\n"
"v_max3_f32 v251, v251, v161, v162\n"
"v_mfma_f32_16x16x32_bf16 v[188:191], v[212:215], v[60:63], v[188:191]\n"
"ds_read_b64_tr_b16 v[212:213], v247 offset:160\n"
"ds_read_b64_tr_b16 v[214:215], v247 offset:2208\n"
"v_mfma_f32_16x16x32_bf16 v[188:191], v[252:255], v[28:31], v[188:191]\n"
"s_waitcnt lgkmcnt(9)\n"
"ds_read_b64_tr_b16 v[252:253], v247 offset:512\n"
"ds_read_b64_tr_b16 v[254:255], v247 offset:2560\n"
"v_mfma_f32_16x16x32_bf16 v[188:191], v[228:231], v[92:95], v[188:191]\n"
"s_waitcnt lgkmcnt(10)\n"
"v_mfma_f32_16x16x32_bf16 v[188:191], v[216:219], v[124:127], v[188:191]\n"
"v_mfma_f32_16x16x32_bf16 v[184:187], v[216:219], v[116:119], v[184:187]\n"
"v_mfma_f32_16x16x32_bf16 v[184:187], v[228:231], v[84:87], v[184:187]\n"
"v_mfma_f32_16x16x32_bf16 v[180:183], v[228:231], v[76:79], v[180:183]\n"
"v_mfma_f32_16x16x32_bf16 v[180:183], v[216:219], v[108:111], v[180:183]\n"
"v_mfma_f32_16x16x32_bf16 v[176:179], v[216:219], v[100:103], v[176:179]\n"
"ds_read_b64_tr_b16 v[216:217], v247 offset:544\n"
"ds_read_b64_tr_b16 v[218:219], v247 offset:2592\n"
"v_mfma_f32_16x16x32_bf16 v[176:179], v[228:231], v[68:71], v[176:179]\n"
"s_waitcnt lgkmcnt(10)\n"
"ds_read_b64_tr_b16 v[228:229], v247 offset:256\n"
"ds_read_b64_tr_b16 v[230:231], v247 offset:2304\n"
"v_mfma_f32_16x16x32_bf16 a[12:15], v[232:235], v[204:207], a[12:15]\n"
"v_fma_f32 v188, v188, s65, v239\n"
"v_fma_f32 v189, v189, s65, v239\n"
"v_mfma_f32_16x16x32_bf16 a[8:11], v[232:235], v[200:203], a[8:11]\n"
"v_fma_f32 v190, v190, s65, v239\n"
"v_max3_f32 v251, v251, v163, v188\n"
"v_mfma_f32_16x16x32_bf16 a[4:7], v[232:235], v[196:199], a[4:7]\n"
"v_fma_f32 v191, v191, s65, v239\n"
"v_fma_f32 v184, v184, s65, v238\n"
"v_mfma_f32_16x16x32_bf16 a[0:3], v[232:235], v[192:195], a[0:3]\n"
"s_waitcnt lgkmcnt(10)\n"
"v_max3_f32 v251, v251, v189, v190\n"
"v_fma_f32 v185, v185, s65, v238\n"
"v_mfma_f32_16x16x32_bf16 a[16:19], v[224:227], v[192:195], a[16:19]\n"
"v_fma_f32 v186, v186, s65, v238\n"
"v_max3_f32 v251, v251, v191, v184\n"
"v_mfma_f32_16x16x32_bf16 a[20:23], v[224:227], v[196:199], a[20:23]\n"
"v_fma_f32 v187, v187, s65, v238\n"
"v_fma_f32 v180, v180, s65, v237\n"
"v_mfma_f32_16x16x32_bf16 a[24:27], v[224:227], v[200:203], a[24:27]\n"
"v_max3_f32 v251, v251, v185, v186\n"
"v_fma_f32 v181, v181, s65, v237\n"
"v_mfma_f32_16x16x32_bf16 a[28:31], v[224:227], v[204:207], a[28:31]\n"
"s_waitcnt lgkmcnt(8)\n"
"v_fma_f32 v182, v182, s65, v237\n"
"v_max3_f32 v251, v251, v187, v180\n"
"v_mfma_f32_16x16x32_bf16 a[44:47], v[220:223], v[204:207], a[44:47]\n"
"v_fma_f32 v183, v183, s65, v237\n"
"v_fma_f32 v176, v176, s65, v236\n"
"v_mfma_f32_16x16x32_bf16 a[40:43], v[220:223], v[200:203], a[40:43]\n"
"v_max3_f32 v251, v251, v181, v182\n"
"v_fma_f32 v177, v177, s65, v236\n"
"v_mfma_f32_16x16x32_bf16 a[36:39], v[220:223], v[196:199], a[36:39]\n"
"s_waitcnt vmcnt(12)\n"
"s_barrier\n"
"v_mfma_f32_16x16x32_bf16 a[32:35], v[220:223], v[192:195], a[32:35]\n"
"s_waitcnt lgkmcnt(6)\n"
"ds_read_b128 v[232:235], v248 offset:2048\n"
"ds_read_b128 v[224:227], v248 offset:1024\n"
"v_mfma_f32_16x16x32_bf16 a[48:51], v[212:215], v[192:195], a[48:51]\n"
"ds_read_b64_tr_b16 v[220:221], v247 offset:288\n"
"ds_read_b64_tr_b16 v[222:223], v247 offset:2336\n"
"v_mfma_f32_16x16x32_bf16 a[52:55], v[212:215], v[196:199], a[52:55]\n"
"ds_read_b32 v250, v248 offset:12\n"
"v_fma_f32 v178, v178, s65, v236\n"
"v_mfma_f32_16x16x32_bf16 a[56:59], v[212:215], v[200:203], a[56:59]\n"
"s_add_u32 m0, s33, 0x4100\n"
"v_max3_f32 v251, v251, v183, v176\n"
"v_fma_f32 v179, v179, s65, v236\n"
"v_mfma_f32_16x16x32_bf16 a[60:63], v[212:215], v[204:207], a[60:63]\n"
"s_waitcnt lgkmcnt(9)\n"
"buffer_load_dwordx4 v249, s[52:55], s68 offen nt lds\n"
"v_mfma_f32_16x16x32_bf16 a[140:143], v[252:255], v[204:207], a[140:143]\n"
"s_sub_u32 s33, s33, s30\n"
"ds_read_b64_tr_b16 v[212:213], v247 offset:384\n"
"ds_read_b64_tr_b16 v[214:215], v247 offset:2432\n"
"v_mfma_f32_16x16x32_bf16 a[136:139], v[252:255], v[200:203], a[136:139]\n"
"v_max3_f32 v251, v251, v177, v178\n"
"v_mfma_f32_16x16x32_bf16 a[132:135], v[252:255], v[196:199], a[132:135]\n"
"v_mfma_f32_16x16x32_bf16 a[128:131], v[252:255], v[192:195], a[128:131]\n"
"s_waitcnt lgkmcnt(9)\n"
"ds_read_b64_tr_b16 v[252:253], v247 offset:416\n"
"ds_read_b64_tr_b16 v[254:255], v247 offset:2464\n"
"v_mfma_f32_16x16x32_bf16 a[144:147], v[216:219], v[192:195], a[144:147]\n"
"v_mfma_f32_16x16x32_bf16 a[148:151], v[216:219], v[196:199], a[148:151]\n"
"buffer_load_dwordx4 v249, s[52:55], s68 offen offset:1024 nt lds\n"
"v_mfma_f32_16x16x32_bf16 a[152:155], v[216:219], v[200:203], a[152:155]\n"
"s_waitcnt lgkmcnt(4)\n"
"v_max3_f32 v251, v251, v179, v250\n"
"v_cmp_lt_f32 vcc, s66, v251\n"
"v_mfma_f32_16x16x32_bf16 a[156:159], v[216:219], v[204:207], a[156:159]\n"
"ds_read_b64_tr_b16 v[216:217], v247 offset:640\n"
"ds_read_b64_tr_b16 v[218:219], v247 offset:2688\n"
"v_mfma_f32_16x16x32_bf16 v[132:135], v[228:231], v[232:235], v[132:135]\n"
"v_mfma_f32_16x16x32_bf16 v[128:131], v[228:231], v[224:227], v[128:131]\n"
"s_cbranch_vccnz Lslow_7\n"
"Lret_7:\n"
"v_mfma_f32_16x16x32_bf16 a[76:79], v[228:231], v[204:207], a[76:79]\n"
"v_exp_f32 v160, v160\n"
"v_mfma_f32_16x16x32_bf16 a[72:75], v[228:231], v[200:203], a[72:75]\n"
"buffer_load_dwordx4 v249, s[52:55], s68 offen offset:2048 nt lds\n"
"v_mfma_f32_16x16x32_bf16 a[68:71], v[228:231], v[196:199], a[68:71]\n"
"v_exp_f32 v161, v161\n"
"v_mfma_f32_16x16x32_bf16 a[64:67], v[228:231], v[192:195], a[64:67]\n"
"ds_read_b64_tr_b16 v[228:229], v247 offset:672\n"
"ds_read_b64_tr_b16 v[230:231], v247 offset:2720\n"
"v_mfma_f32_16x16x32_bf16 a[80:83], v[220:223], v[192:195], a[80:83]\n"
"v_add_f32 v241, v241, v160\n"
"v_add_f32 v241, v241, v161\n"
"v_mfma_f32_16x16x32_bf16 a[84:87], v[220:223], v[196:199], a[84:87]\n"
"v_exp_f32 v162, v162\n"
"v_mfma_f32_16x16x32_bf16 a[88:91], v[220:223], v[200:203], a[88:91]\n"
"v_exp_f32 v163, v163\n"
"v_mfma_f32_16x16x32_bf16 a[92:95], v[220:223], v[204:207], a[92:95]\n"
"buffer_load_dwordx4 v249, s[52:55], s68 offen offset:3072 nt lds\n"
"v_mfma_f32_16x16x32_bf16 v[136:139], v[220:223], v[224:227], v[136:139]\n"
"s_add_u32 s68, s68, 0x4000\n"
"v_add_f32 v241, v241, v162\n"
"v_add_f32 v241, v241, v163\n"
"v_mfma_f32_16x16x32_bf16 v[140:143], v[220:223], v[232:235], v[140:143]\n"
"s_waitcnt lgkmcnt(6)\n"
"ds_read_b64_tr_b16 v[220:221], v247 offset:768\n"
"ds_read_b64_tr_b16 v[222:223], v247 offset:2816\n"
"v_mfma_f32_16x16x32_bf16 v[148:151], v[212:215], v[232:235], v[148:151]\n"
"v_exp_f32 v164, v164\n"
"v_mfma_f32_16x16x32_bf16 v[144:147], v[212:215], v[224:227], v[144:147]\n"
"v_exp_f32 v165, v165\n"
"v_mfma_f32_16x16x32_bf16 a[108:111], v[212:215], v[204:207], a[108:111]\n"
"v_add_f32 v242, v242, v164\n"
"v_add_f32 v242, v242, v165\n"
"v_mfma_f32_16x16x32_bf16 a[104:107], v[212:215], v[200:203], a[104:107]\n"
"v_exp_f32 v166, v166\n"
"v_mfma_f32_16x16x32_bf16 a[100:103], v[212:215], v[196:199], a[100:103]\n"
"v_exp_f32 v167, v167\n"
"v_mfma_f32_16x16x32_bf16 a[96:99], v[212:215], v[192:195], a[96:99]\n"
"s_waitcnt lgkmcnt(6)\n"
"ds_read_b64_tr_b16 v[212:213], v247 offset:800\n"
"ds_read_b64_tr_b16 v[214:215], v247 offset:2848\n"
"v_mfma_f32_16x16x32_bf16 a[112:115], v[252:255], v[192:195], a[112:115]\n"
"v_add_f32 v242, v242, v166\n"
"v_add_f32 v242, v242, v167\n"
"v_mfma_f32_16x16x32_bf16 a[116:119], v[252:255], v[196:199], a[116:119]\n"
"v_exp_f32 v168, v168\n"
"v_mfma_f32_16x16x32_bf16 a[120:123], v[252:255], v[200:203], a[120:123]\n"
"v_exp_f32 v169, v169\n"
"v_mfma_f32_16x16x32_bf16 a[124:127], v[252:255], v[204:207], a[124:127]\n"
"v_add_f32 v243, v243, v168\n"
"v_add_f32 v243, v243, v169\n"
"v_mfma_f32_16x16x32_bf16 v[152:155], v[252:255], v[224:227], v[152:155]\n"
"ds_read_b64_tr_b16 v[224:225], v247 offset:896\n"
"ds_read_b64_tr_b16 v[226:227], v247 offset:2944\n"
"v_mfma_f32_16x16x32_bf16 v[156:159], v[252:255], v[232:235], v[156:159]\n"
"s_waitcnt lgkmcnt(8)\n"
"ds_read_b64_tr_b16 v[232:233], v247 offset:928\n"
"ds_read_b64_tr_b16 v[234:235], v247 offset:2976\n"
"v_mfma_f32_16x16x32_bf16 a[172:175], v[216:219], v[204:207], a[172:175]\n"
"ds_read_b128 v[252:255], v246 offset:0\n"
"v_mfma_f32_16x16x32_bf16 a[168:171], v[216:219], v[200:203], a[168:171]\n"
"v_exp_f32 v170, v170\n"
"v_mfma_f32_16x16x32_bf16 a[164:167], v[216:219], v[196:199], a[164:167]\n"
"v_exp_f32 v171, v171\n"
"v_mfma_f32_16x16x32_bf16 a[160:163], v[216:219], v[192:195], a[160:163]\n"
"s_waitcnt lgkmcnt(9)\n"
"ds_read_b128 v[216:219], v246 offset:128\n"
"v_add_f32 v243, v243, v170\n"
"v_mfma_f32_16x16x32_bf16 a[176:179], v[228:231], v[192:195], a[176:179]\n"
"v_add_f32 v243, v243, v171\n"
"v_mfma_f32_16x16x32_bf16 a[180:183], v[228:231], v[196:199], a[180:183]\n"
"v_exp_f32 v172, v172\n"
"v_mfma_f32_16x16x32_bf16 a[184:187], v[228:231], v[200:203], a[184:187]\n"
"v_exp_f32 v173, v173\n"
"v_mfma_f32_16x16x32_bf16 a[188:191], v[228:231], v[204:207], a[188:191]\n"
"s_waitcnt lgkmcnt(8)\n"
"ds_read_b128 v[228:231], v248 offset:13312\n"
"v_add_f32 v244, v244, v172\n"
"v_mfma_f32_16x16x32_bf16 a[204:207], v[220:223], v[204:207], a[204:207]\n"
"v_exp_f32 v174, v174\n"
"v_mfma_f32_16x16x32_bf16 a[200:203], v[220:223], v[200:203], a[200:203]\n"
"v_add_f32 v244, v244, v173\n"
"v_add_f32 v244, v244, v174\n"
"v_mfma_f32_16x16x32_bf16 a[196:199], v[220:223], v[196:199], a[196:199]\n"
"v_exp_f32 v175, v175\n"
"v_mfma_f32_16x16x32_bf16 a[192:195], v[220:223], v[192:195], a[192:195]\n"
"s_waitcnt lgkmcnt(7)\n"
"ds_read_b128 v[220:223], v248 offset:14336\n"
"v_add_f32 v244, v244, v175\n"
"v_mfma_f32_16x16x32_bf16 a[208:211], v[212:215], v[192:195], a[208:211]\n"
"v_exp_f32 v208, v208\n"
"v_mfma_f32_16x16x32_bf16 a[212:215], v[212:215], v[196:199], a[212:215]\n"
"v_exp_f32 v209, v209\n"
"v_mfma_f32_16x16x32_bf16 a[216:219], v[212:215], v[200:203], a[216:219]\n"
"v_add_f32 v245, v245, v208\n"
"v_add_f32 v245, v245, v209\n"
"v_mfma_f32_16x16x32_bf16 a[220:223], v[212:215], v[204:207], a[220:223]\n"
"s_waitcnt lgkmcnt(6)\n"
"ds_read_b128 v[212:215], v246 offset:256\n"
"v_cvt_pk_bf16_f32 v208, v208, v209\n"
"v_mfma_f32_16x16x32_bf16 a[236:239], v[224:227], v[204:207], a[236:239]\n"
"v_exp_f32 v210, v210\n"
"v_mfma_f32_16x16x32_bf16 a[232:235], v[224:227], v[200:203], a[232:235]\n"
"v_exp_f32 v211, v211\n"
"v_mfma_f32_16x16x32_bf16 a[228:231], v[224:227], v[196:199], a[228:231]\n"
"v_cvt_pk_bf16_f32 v209, v210, v211\n"
"v_add_f32 v245, v245, v210\n"
"v_mfma_f32_16x16x32_bf16 a[224:227], v[224:227], v[192:195], a[224:227]\n"
"s_waitcnt lgkmcnt(5)\n"
"ds_read_b128 v[224:227], v246 offset:384\n"
"v_add_f32 v245, v245, v211\n"
"v_mfma_f32_16x16x32_bf16 a[240:243], v[232:235], v[192:195], a[240:243]\n"
"v_cvt_pk_bf16_f32 v192, v160, v161\n"
"v_cvt_pk_bf16_f32 v193, v162, v163\n"
"v_mfma_f32_16x16x32_bf16 a[244:247], v[232:235], v[196:199], a[244:247]\n"
"v_cvt_pk_bf16_f32 v196, v164, v165\n"
"v_cvt_pk_bf16_f32 v197, v166, v167\n"
"v_mfma_f32_16x16x32_bf16 a[248:251], v[232:235], v[200:203], a[248:251]\n"
"v_cvt_pk_bf16_f32 v200, v168, v169\n"
"v_cvt_pk_bf16_f32 v201, v170, v171\n"
"v_mfma_f32_16x16x32_bf16 a[252:255], v[232:235], v[204:207], a[252:255]\n"
"s_waitcnt lgkmcnt(5)\n"
"v_cvt_pk_bf16_f32 v204, v172, v173\n"
"v_cvt_pk_bf16_f32 v205, v174, v175\n"
"v_mfma_f32_16x16x32_bf16 v[160:163], v[252:255], v[0:3], 0\n"
"s_waitcnt lgkmcnt(4)\n"
"ds_write_b64 v248, v[208:209] offset:4096\n"
"v_mfma_f32_16x16x32_bf16 v[160:163], v[216:219], v[32:35], v[160:163]\n"
"ds_read_b128 v[232:235], v248 offset:15360\n"
"v_mfma_f32_16x16x32_bf16 v[164:167], v[216:219], v[40:43], 0\n"
"v_exp_f32 v176, v176\n"
"v_mfma_f32_16x16x32_bf16 v[164:167], v[252:255], v[8:11], v[164:167]\n"
"v_exp_f32 v177, v177\n"
"v_mfma_f32_16x16x32_bf16 v[168:171], v[252:255], v[16:19], 0\n"
"v_add_f32 v241, v241, v176\n"
"v_add_f32 v241, v241, v177\n"
"v_mfma_f32_16x16x32_bf16 v[168:171], v[216:219], v[48:51], v[168:171]\n"
"v_exp_f32 v178, v178\n"
"v_mfma_f32_16x16x32_bf16 v[172:175], v[216:219], v[56:59], 0\n"
"v_exp_f32 v179, v179\n"
"v_mfma_f32_16x16x32_bf16 v[172:175], v[252:255], v[24:27], v[172:175]\n"
"s_waitcnt lgkmcnt(5)\n"
"v_add_f32 v241, v241, v178\n"
"v_add_f32 v241, v241, v179\n"
"v_mfma_f32_16x16x32_bf16 v[208:211], v[252:255], v[228:231], 0\n"
"s_waitcnt lgkmcnt(4)\n"
"ds_read_b128 v[228:231], v248 offset:16384\n"
"ds_read_b128 v[252:255], v246 offset:512\n"
"v_mfma_f32_16x16x32_bf16 v[208:211], v[216:219], v[220:223], v[208:211]\n"
"s_waitcnt lgkmcnt(5)\n"
"ds_read_b128 v[216:219], v246 offset:640\n"
"ds_read_b128 v[220:223], v248 offset:17408\n"
"v_mfma_f32_16x16x32_bf16 v[172:175], v[212:215], v[88:91], v[172:175]\n"
"s_waitcnt lgkmcnt(6)\n"
"v_cvt_pk_bf16_f32 v194, v176, v177\n"
"v_cvt_pk_bf16_f32 v195, v178, v179\n"
"v_mfma_f32_16x16x32_bf16 v[172:175], v[224:227], v[120:123], v[172:175]\n"
"v_exp_f32 v180, v180\n"
"v_mfma_f32_16x16x32_bf16 v[168:171], v[224:227], v[112:115], v[168:171]\n"
"v_exp_f32 v181, v181\n"
"v_mfma_f32_16x16x32_bf16 v[168:171], v[212:215], v[80:83], v[168:171]\n"
"v_add_f32 v242, v242, v180\n"
"v_add_f32 v242, v242, v181\n"
"v_mfma_f32_16x16x32_bf16 v[164:167], v[212:215], v[72:75], v[164:167]\n"
"v_exp_f32 v182, v182\n"
"v_mfma_f32_16x16x32_bf16 v[164:167], v[224:227], v[104:107], v[164:167]\n"
"v_exp_f32 v183, v183\n"
"v_mfma_f32_16x16x32_bf16 v[160:163], v[224:227], v[96:99], v[160:163]\n"
"v_add_f32 v242, v242, v182\n"
"v_add_f32 v242, v242, v183\n"
"v_mfma_f32_16x16x32_bf16 v[160:163], v[212:215], v[64:67], v[160:163]\n"
"s_waitcnt lgkmcnt(4)\n"
"v_cvt_pk_bf16_f32 v198, v180, v181\n"
"v_cvt_pk_bf16_f32 v199, v182, v183\n"
"v_mfma_f32_16x16x32_bf16 v[208:211], v[212:215], v[232:235], v[208:211]\n"
"s_waitcnt lgkmcnt(3)\n"
"ds_read_b128 v[212:215], v248 offset:18432\n"
"ds_read_b128 v[232:235], v246 offset:768\n"
"v_mfma_f32_16x16x32_bf16 v[208:211], v[224:227], v[228:231], v[208:211]\n"
"s_waitcnt lgkmcnt(4)\n"
"ds_read_b128 v[224:227], v246 offset:896\n"
"ds_read_b128 v[228:231], v248 offset:19456\n"
"v_mfma_f32_16x16x32_bf16 v[160:163], v[252:255], v[4:7], v[160:163]\n"
"s_waitcnt lgkmcnt(5)\n"
"v_exp_f32 v184, v184\n"
"v_mfma_f32_16x16x32_bf16 v[160:163], v[216:219], v[36:39], v[160:163]\n"
"v_exp_f32 v185, v185\n"
"v_mfma_f32_16x16x32_bf16 v[164:167], v[216:219], v[44:47], v[164:167]\n"
"v_add_f32 v243, v243, v184\n"
"v_add_f32 v243, v243, v185\n"
"v_mfma_f32_16x16x32_bf16 v[164:167], v[252:255], v[12:15], v[164:167]\n"
"v_exp_f32 v186, v186\n"
"v_mfma_f32_16x16x32_bf16 v[168:171], v[252:255], v[20:23], v[168:171]\n"
"v_exp_f32 v187, v187\n"
"v_mfma_f32_16x16x32_bf16 v[168:171], v[216:219], v[52:55], v[168:171]\n"
"v_add_f32 v243, v243, v186\n"
"v_add_f32 v243, v243, v187\n"
"v_mfma_f32_16x16x32_bf16 v[172:175], v[216:219], v[60:63], v[172:175]\n"
"v_cvt_pk_bf16_f32 v202, v184, v185\n"
"v_cvt_pk_bf16_f32 v203, v186, v187\n"
"v_mfma_f32_16x16x32_bf16 v[172:175], v[252:255], v[28:31], v[172:175]\n"
"s_waitcnt lgkmcnt(4)\n"
"v_exp_f32 v188, v188\n"
"v_mfma_f32_16x16x32_bf16 v[208:211], v[252:255], v[220:223], v[208:211]\n"
"s_waitcnt lgkmcnt(3)\n"
"ds_read_b128 v[220:223], v248 offset:20480\n"
"ds_read_b128 v[252:255], v246 offset:2048\n"
"v_mfma_f32_16x16x32_bf16 v[208:211], v[216:219], v[212:215], v[208:211]\n"
"s_waitcnt lgkmcnt(4)\n"
"ds_read_b128 v[212:215], v246 offset:2176\n"
"ds_read_b128 v[216:219], v246 offset:2304\n"
"v_mfma_f32_16x16x32_bf16 v[172:175], v[232:235], v[92:95], v[172:175]\n"
"s_waitcnt lgkmcnt(5)\n"
"v_exp_f32 v189, v189\n"
"v_mfma_f32_16x16x32_bf16 v[172:175], v[224:227], v[124:127], v[172:175]\n"
"v_add_f32 v244, v244, v188\n"
"v_add_f32 v244, v244, v189\n"
"v_mfma_f32_16x16x32_bf16 v[168:171], v[224:227], v[116:119], v[168:171]\n"
"v_exp_f32 v190, v190\n"
"v_mfma_f32_16x16x32_bf16 v[168:171], v[232:235], v[84:87], v[168:171]\n"
"v_exp_f32 v191, v191\n"
"v_mfma_f32_16x16x32_bf16 v[164:167], v[232:235], v[76:79], v[164:167]\n"
"v_add_f32 v244, v244, v190\n"
"v_add_f32 v244, v244, v191\n"
"v_mfma_f32_16x16x32_bf16 v[164:167], v[224:227], v[108:111], v[164:167]\n"
"v_cvt_pk_bf16_f32 v206, v188, v189\n"
"v_cvt_pk_bf16_f32 v207, v190, v191\n"
"v_mfma_f32_16x16x32_bf16 v[160:163], v[224:227], v[100:103], v[160:163]\n"
"s_add_u32 m0, s31, 0x4100\n"
"v_fma_f32 v172, v172, s65, v239\n"
"v_fma_f32 v173, v173, s65, v239\n"
"v_mfma_f32_16x16x32_bf16 v[160:163], v[232:235], v[68:71], v[160:163]\n"
"s_waitcnt lgkmcnt(4)\n"
"buffer_load_dwordx4 v249, s[48:51], s67 offen nt lds\n"
"v_mfma_f32_16x16x32_bf16 v[208:211], v[232:235], v[228:231], v[208:211]\n"
"s_waitcnt lgkmcnt(3)\n"
"ds_read_b128 v[228:231], v246 offset:2432\n"
"ds_read_b128 v[232:235], v246 offset:2560\n"
"v_mfma_f32_16x16x32_bf16 v[208:211], v[224:227], v[220:223], v[208:211]\n"
"s_waitcnt lgkmcnt(4)\n"
"ds_read_b128 v[220:223], v246 offset:2688\n"
"ds_read_b128 v[224:227], v246 offset:2816\n"
"v_mfma_f32_16x16x32_bf16 v[176:179], v[252:255], v[0:3], 0\n"
"s_waitcnt lgkmcnt(5)\n"
"v_fma_f32 v174, v174, s65, v239\n"
"v_fma_f32 v175, v175, s65, v239\n"
"v_mfma_f32_16x16x32_bf16 v[176:179], v[212:215], v[32:35], v[176:179]\n"
"s_sub_u32 s31, s31, s34\n"
"v_fma_f32 v208, v208, s65, v240\n"
"v_fma_f32 v209, v209, s65, v240\n"
"v_mfma_f32_16x16x32_bf16 v[180:183], v[212:215], v[40:43], 0\n"
"v_fma_f32 v210, v210, s65, v240\n"
"v_fma_f32 v211, v211, s65, v240\n"
"v_mfma_f32_16x16x32_bf16 v[180:183], v[252:255], v[8:11], v[180:183]\n"
"buffer_load_dwordx4 v249, s[48:51], s67 offen offset:1024 nt lds\n"
"v_mfma_f32_16x16x32_bf16 v[184:187], v[252:255], v[16:19], 0\n"
"v_max3_f32 v250, v208, v209, v210\n"
"v_max_f32 v250, v250, v211\n"
"v_mfma_f32_16x16x32_bf16 v[184:187], v[212:215], v[48:51], v[184:187]\n"
"v_cmp_lt_f32_e64 s[78:79], s66, v250\n"
"v_fma_f32 v168, v168, s65, v238\n"
"v_mfma_f32_16x16x32_bf16 v[188:191], v[212:215], v[56:59], 0\n"
"ds_read_b128 v[212:215], v246 offset:2944\n"
"v_max3_f32 v251, v172, v173, v174\n"
"v_mfma_f32_16x16x32_bf16 v[188:191], v[252:255], v[24:27], v[188:191]\n"
"s_waitcnt lgkmcnt(5)\n"
"ds_read_b64_tr_b16 v[252:253], v247 offset:16640\n"
"ds_read_b64_tr_b16 v[254:255], v247 offset:18688\n"
"v_mfma_f32_16x16x32_bf16 v[188:191], v[216:219], v[88:91], v[188:191]\n"
"s_waitcnt lgkmcnt(6)\n"
"s_or_b64 s[96:97], s[78:79], s[76:77]\n"
"s_cbranch_scc1 Lslow_8\n"
"Lret_8:\n"
"v_mfma_f32_16x16x32_bf16 v[188:191], v[228:231], v[120:123], v[188:191]\n"
"buffer_load_dwordx4 v249, s[48:51], s67 offen offset:2048 nt lds\n"
"v_mfma_f32_16x16x32_bf16 v[184:187], v[228:231], v[112:115], v[184:187]\n"
"v_fma_f32 v169, v169, s65, v238\n"
"v_fma_f32 v170, v170, s65, v238\n"
"v_mfma_f32_16x16x32_bf16 v[184:187], v[216:219], v[80:83], v[184:187]\n"
"v_max3_f32 v251, v251, v175, v168\n"
"v_fma_f32 v171, v171, s65, v238\n"
"v_mfma_f32_16x16x32_bf16 v[180:183], v[216:219], v[72:75], v[180:183]\n"
"v_fma_f32 v164, v164, s65, v237\n"
"v_max3_f32 v251, v251, v169, v170\n"
"v_mfma_f32_16x16x32_bf16 v[180:183], v[228:231], v[104:107], v[180:183]\n"
"v_fma_f32 v165, v165, s65, v237\n"
"v_fma_f32 v166, v166, s65, v237\n"
"v_mfma_f32_16x16x32_bf16 v[176:179], v[228:231], v[96:99], v[176:179]\n"
"ds_read_b64_tr_b16 v[228:229], v247 offset:16672\n"
"ds_read_b64_tr_b16 v[230:231], v247 offset:18720\n"
"v_mfma_f32_16x16x32_bf16 v[176:179], v[216:219], v[64:67], v[176:179]\n"
"s_waitcnt lgkmcnt(7)\n"
"buffer_load_dwordx4 v249, s[48:51], s67 offen offset:3072 nt lds\n"
"v_mfma_f32_16x16x32_bf16 v[176:179], v[232:235], v[4:7], v[176:179]\n"
"s_waitcnt lgkmcnt(6)\n"
"ds_read_b64_tr_b16 v[216:217], v247 offset:16768\n"
"ds_read_b64_tr_b16 v[218:219], v247 offset:18816\n"
"v_mfma_f32_16x16x32_bf16 v[176:179], v[220:223], v[36:39], v[176:179]\n"
"s_add_u32 s67, s67, 0x4000\n"
"v_max3_f32 v251, v251, v171, v164\n"
"v_fma_f32 v167, v167, s65, v237\n"
"v_mfma_f32_16x16x32_bf16 v[180:183], v[220:223], v[44:47], v[180:183]\n"
"v_fma_f32 v160, v160, s65, v236\n"
"v_max3_f32 v251, v251, v165, v166\n"
"v_mfma_f32_16x16x32_bf16 v[180:183], v[232:235], v[12:15], v[180:183]\n"
"v_fma_f32 v161, v161, s65, v236\n"
"v_fma_f32 v162, v162, s65, v236\n"
"v_mfma_f32_16x16x32_bf16 v[184:187], v[232:235], v[20:23], v[184:187]\n"
"v_max3_f32 v251, v251, v167, v160\n"
"v_fma_f32 v163, v163, s65, v236\n"
"v_mfma_f32_16x16x32_bf16 v[184:187], v[220:223], v[52:55], v[184:187]\n"
"v_max3_f32 v251, v251, v161, v162\n"
"v_mfma_f32_16x16x32_bf16 v[188:191], v[220:223], v[60:63], v[188:191]\n"
"ds_read_b64_tr_b16 v[220:221], v247 offset:16800\n"
"ds_read_b64_tr_b16 v[222:223], v247 offset:18848\n"
"v_mfma_f32_16x16x32_bf16 v[188:191], v[232:235], v[28:31], v[188:191]\n"
"s_waitcnt lgkmcnt(9)\n"
"ds_read_b64_tr_b16 v[232:233], v247 offset:17152\n"
"ds_read_b64_tr_b16 v[234:235], v247 offset:19200\n"
"v_mfma_f32_16x16x32_bf16 v[188:191], v[224:227], v[92:95], v[188:191]\n"
"s_waitcnt lgkmcnt(10)\n"
"v_mfma_f32_16x16x32_bf16 v[188:191], v[212:215], v[124:127], v[188:191]\n"
"v_mfma_f32_16x16x32_bf16 v[184:187], v[212:215], v[116:119], v[184:187]\n"
"v_mfma_f32_16x16x32_bf16 v[184:187], v[224:227], v[84:87], v[184:187]\n"
"v_mfma_f32_16x16x32_bf16 v[180:183], v[224:227], v[76:79], v[180:183]\n"
"v_mfma_f32_16x16x32_bf16 v[180:183], v[212:215], v[108:111], v[180:183]\n"
"v_mfma_f32_16x16x32_bf16 v[176:179], v[212:215], v[100:103], v[176:179]\n"
"ds_read_b64_tr_b16 v[212:213], v247 offset:17184\n"
"ds_read_b64_tr_b16 v[214:215], v247 offset:19232\n"
"v_mfma_f32_16x16x32_bf16 v[176:179], v[224:227], v[68:71], v[176:179]\n"
"s_waitcnt lgkmcnt(10)\n"
"ds_read_b64_tr_b16 v[224:225], v247 offset:16896\n"
"ds_read_b64_tr_b16 v[226:227], v247 offset:18944\n"
"v_mfma_f32_16x16x32_bf16 a[12:15], v[252:255], v[204:207], a[12:15]\n"
"v_fma_f32 v188, v188, s65, v239\n"
"v_fma_f32 v189, v189, s65, v239\n"
"v_mfma_f32_16x16x32_bf16 a[8:11], v[252:255], v[200:203], a[8:11]\n"
"v_fma_f32 v190, v190, s65, v239\n"
"v_max3_f32 v251, v251, v163, v188\n"
"v_mfma_f32_16x16x32_bf16 a[4:7], v[252:255], v[196:199], a[4:7]\n"
"v_fma_f32 v191, v191, s65, v239\n"
"v_fma_f32 v184, v184, s65, v238\n"
"v_mfma_f32_16x16x32_bf16 a[0:3], v[252:255], v[192:195], a[0:3]\n"
"s_waitcnt lgkmcnt(10)\n"
"v_max3_f32 v251, v251, v189, v190\n"
"v_fma_f32 v185, v185, s65, v238\n"
"v_mfma_f32_16x16x32_bf16 a[16:19], v[228:231], v[192:195], a[16:19]\n"
"v_fma_f32 v186, v186, s65, v238\n"
"v_max3_f32 v251, v251, v191, v184\n"
"v_mfma_f32_16x16x32_bf16 a[20:23], v[228:231], v[196:199], a[20:23]\n"
"v_fma_f32 v187, v187, s65, v238\n"
"v_fma_f32 v180, v180, s65, v237\n"
"v_mfma_f32_16x16x32_bf16 a[24:27], v[228:231], v[200:203], a[24:27]\n"
"v_max3_f32 v251, v251, v185, v186\n"
"v_fma_f32 v181, v181, s65, v237\n"
"v_mfma_f32_16x16x32_bf16 a[28:31], v[228:231], v[204:207], a[28:31]\n"
"s_waitcnt lgkmcnt(8)\n"
"v_fma_f32 v182, v182, s65, v237\n"
"v_max3_f32 v251, v251, v187, v180\n"
"v_mfma_f32_16x16x32_bf16 a[44:47], v[216:219], v[204:207], a[44:47]\n"
"v_fma_f32 v183, v183, s65, v237\n"
"v_fma_f32 v176, v176, s65, v236\n"
"v_mfma_f32_16x16x32_bf16 a[40:43], v[216:219], v[200:203], a[40:43]\n"
"v_max3_f32 v251, v251, v181, v182\n"
"v_fma_f32 v177, v177, s65, v236\n"
"v_mfma_f32_16x16x32_bf16 a[36:39], v[216:219], v[196:199], a[36:39]\n"
"s_waitcnt vmcnt(12)\n"
"s_barrier\n"
"v_mfma_f32_16x16x32_bf16 a[32:35], v[216:219], v[192:195], a[32:35]\n"
"s_waitcnt lgkmcnt(6)\n"
"ds_read_b128 v[252:255], v248 offset:4096\n"
"ds_read_b128 v[228:231], v248 offset:3072\n"
"v_mfma_f32_16x16x32_bf16 a[48:51], v[220:223], v[192:195], a[48:51]\n"
"ds_read_b64_tr_b16 v[216:217], v247 offset:16928\n"
"ds_read_b64_tr_b16 v[218:219], v247 offset:18976\n"
"v_mfma_f32_16x16x32_bf16 a[52:55], v[220:223], v[196:199], a[52:55]\n"
"ds_read_b32 v250, v248 offset:0\n"
"v_fma_f32 v178, v178, s65, v236\n"
"v_mfma_f32_16x16x32_bf16 a[56:59], v[220:223], v[200:203], a[56:59]\n"
"s_add_u32 m0, s33, 0\n"
"v_max3_f32 v251, v251, v183, v176\n"
"v_fma_f32 v179, v179, s65, v236\n"
"v_mfma_f32_16x16x32_bf16 a[60:63], v[220:223], v[204:207], a[60:63]\n"
"s_waitcnt lgkmcnt(9)\n"
"buffer_load_dwordx4 v249, s[52:55], s68 offen nt lds\n"
"v_mfma_f32_16x16x32_bf16 a[140:143], v[232:235], v[204:207], a[140:143]\n"
"ds_read_b64_tr_b16 v[220:221], v247 offset:17024\n"
"ds_read_b64_tr_b16 v[222:223], v247 offset:19072\n"
"v_mfma_f32_16x16x32_bf16 a[136:139], v[232:235], v[200:203], a[136:139]\n"
"v_max3_f32 v251, v251, v177, v178\n"
"v_mfma_f32_16x16x32_bf16 a[132:135], v[232:235], v[196:199], a[132:135]\n"
"v_mfma_f32_16x16x32_bf16 a[128:131], v[232:235], v[192:195], a[128:131]\n"
"s_waitcnt lgkmcnt(9)\n"
"ds_read_b64_tr_b16 v[232:233], v247 offset:17056\n"
"ds_read_b64_tr_b16 v[234:235], v247 offset:19104\n"
"v_mfma_f32_16x16x32_bf16 a[144:147], v[212:215], v[192:195], a[144:147]\n"
"v_mfma_f32_16x16x32_bf16 a[148:151], v[212:215], v[196:199], a[148:151]\n"
"buffer_load_dwordx4 v249, s[52:55], s68 offen offset:1024 nt lds\n"
"v_mfma_f32_16x16x32_bf16 a[152:155], v[212:215], v[200:203], a[152:155]\n"
"v_mfma_f32_16x16x32_bf16 a[156:159], v[212:215], v[204:207], a[156:159]\n"
"s_waitcnt lgkmcnt(8)\n"
"ds_read_b64_tr_b16 v[212:213], v247 offset:17280\n"
"ds_read_b64_tr_b16 v[214:215], v247 offset:19328\n"
"v_mfma_f32_16x16x32_bf16 v[132:135], v[224:227], v[252:255], v[132:135]\n"
"s_waitcnt lgkmcnt(9)\n"
"s_waitcnt lgkmcnt(6)\n"
"v_max3_f32 v251, v251, v179, v250\n"
"v_mfma_f32_16x16x32_bf16 v[128:131], v[224:227], v[228:231], v[128:131]\n"
"v_cmp_lt_f32 vcc, s66, v251\n"
"v_mfma_f32_16x16x32_bf16 a[76:79], v[224:227], v[204:207], a[76:79]\n"
"v_mfma_f32_16x16x32_bf16 a[72:75], v[224:227], v[200:203], a[72:75]\n"
"buffer_load_dwordx4 v249, s[52:55], s68 offen offset:2048 nt lds\n"
"v_mfma_f32_16x16x32_bf16 a[68:71], v[224:227], v[196:199], a[68:71]\n"
"v_mfma_f32_16x16x32_bf16 a[64:67], v[224:227], v[192:195], a[64:67]\n"
"ds_read_b64_tr_b16 v[224:225], v247 offset:17312\n"
"ds_read_b64_tr_b16 v[226:227], v247 offset:19360\n"
"v_mfma_f32_16x16x32_bf16 a[80:83], v[216:219], v[192:195], a[80:83]\n"
"s_cbranch_vccnz Lslow_9\n"
"Lret_9:\n"
"v_mfma_f32_16x16x32_bf16 a[84:87], v[216:219], v[196:199], a[84:87]\n"
"v_exp_f32 v208, v208\n"
"v_mfma_f32_16x16x32_bf16 a[88:91], v[216:219], v[200:203], a[88:91]\n"
"v_exp_f32 v160, v160\n"
"v_mfma_f32_16x16x32_bf16 a[92:95], v[216:219], v[204:207], a[92:95]\n"
"buffer_load_dwordx4 v249, s[52:55], s68 offen offset:3072 nt lds\n"
"v_mfma_f32_16x16x32_bf16 v[136:139], v[216:219], v[228:231], v[136:139]\n"
"s_add_u32 s68, s68, 0x4000\n"
"v_exp_f32 v164, v164\n"
"v_mfma_f32_16x16x32_bf16 v[140:143], v[216:219], v[252:255], v[140:143]\n"
"s_waitcnt lgkmcnt(6)\n"
"ds_read_b64_tr_b16 v[216:217], v247 offset:17408\n"
"ds_read_b64_tr_b16 v[218:219], v247 offset:19456\n"
"v_mfma_f32_16x16x32_bf16 v[148:151], v[220:223], v[252:255], v[148:151]\n"
"v_exp_f32 v168, v168\n"
"v_mfma_f32_16x16x32_bf16 v[144:147], v[220:223], v[228:231], v[144:147]\n"
"v_exp_f32 v172, v172\n"
"v_mfma_f32_16x16x32_bf16 a[108:111], v[220:223], v[204:207], a[108:111]\n"
"v_exp_f32 v209, v209\n"
"v_mfma_f32_16x16x32_bf16 a[104:107], v[220:223], v[200:203], a[104:107]\n"
"v_exp_f32 v161, v161\n"
"v_mfma_f32_16x16x32_bf16 a[100:103], v[220:223], v[196:199], a[100:103]\n"
"v_exp_f32 v165, v165\n"
"v_mfma_f32_16x16x32_bf16 a[96:99], v[220:223], v[192:195], a[96:99]\n"
"s_waitcnt lgkmcnt(6)\n"
"ds_read_b64_tr_b16 v[220:221], v247 offset:17440\n"
"ds_read_b64_tr_b16 v[222:223], v247 offset:19488\n"
"v_mfma_f32_16x16x32_bf16 a[112:115], v[232:235], v[192:195], a[112:115]\n"
"v_exp_f32 v169, v169\n"
"v_mfma_f32_16x16x32_bf16 a[116:119], v[232:235], v[196:199], a[116:119]\n"
"v_exp_f32 v173, v173\n"
"v_mfma_f32_16x16x32_bf16 a[120:123], v[232:235], v[200:203], a[120:123]\n"
"v_add_f32 v245, v245, v208\n"
"v_add_f32 v241, v241, v160\n"
"v_mfma_f32_16x16x32_bf16 a[124:127], v[232:235], v[204:207], a[124:127]\n"
"v_exp_f32 v210, v210\n"
"v_mfma_f32_16x16x32_bf16 v[152:155], v[232:235], v[228:231], v[152:155]\n"
"ds_read_b64_tr_b16 v[228:229], v247 offset:17536\n"
"ds_read_b64_tr_b16 v[230:231], v247 offset:19584\n"
"v_mfma_f32_16x16x32_bf16 v[156:159], v[232:235], v[252:255], v[156:159]\n"
"s_waitcnt lgkmcnt(8)\n"
"ds_read_b64_tr_b16 v[232:233], v247 offset:17568\n"
"ds_read_b64_tr_b16 v[234:235], v247 offset:19616\n"
"v_mfma_f32_16x16x32_bf16 a[172:175], v[212:215], v[204:207], a[172:175]\n"
"v_exp_f32 v211, v211\n"
"v_mfma_f32_16x16x32_bf16 a[168:171], v[212:215], v[200:203], a[168:171]\n"
"v_exp_f32 v162, v162\n"
"v_mfma_f32_16x16x32_bf16 a[164:167], v[212:215], v[196:199], a[164:167]\n"
"v_add_f32 v242, v242, v164\n"
"v_add_f32 v243, v243, v168\n"
"v_mfma_f32_16x16x32_bf16 a[160:163], v[212:215], v[192:195], a[160:163]\n"
"s_waitcnt lgkmcnt(8)\n"
"ds_read_b128 v[212:215], v246 offset:16640\n"
"v_add_f32 v244, v244, v172\n"
"v_mfma_f32_16x16x32_bf16 a[176:179], v[224:227], v[192:195], a[176:179]\n"
"v_exp_f32 v166, v166\n"
"v_mfma_f32_16x16x32_bf16 a[180:183], v[224:227], v[196:199], a[180:183]\n"
"v_exp_f32 v170, v170\n"
"v_mfma_f32_16x16x32_bf16 a[184:187], v[224:227], v[200:203], a[184:187]\n"
"v_exp_f32 v174, v174\n"
"v_mfma_f32_16x16x32_bf16 a[188:191], v[224:227], v[204:207], a[188:191]\n"
"s_waitcnt lgkmcnt(7)\n"
"v_add_f32 v245, v245, v209\n"
"v_cvt_pk_bf16_f32 v208, v208, v209\n"
"v_mfma_f32_16x16x32_bf16 a[204:207], v[216:219], v[204:207], a[204:207]\n"
"v_add_f32 v241, v241, v161\n"
"v_add_f32 v242, v242, v165\n"
"v_mfma_f32_16x16x32_bf16 a[200:203], v[216:219], v[200:203], a[200:203]\n"
"v_exp_f32 v163, v163\n"
"v_mfma_f32_16x16x32_bf16 a[196:199], v[216:219], v[196:199], a[196:199]\n"
"v_exp_f32 v167, v167\n"
"v_mfma_f32_16x16x32_bf16 a[192:195], v[216:219], v[192:195], a[192:195]\n"
"s_waitcnt lgkmcnt(5)\n"
"ds_read_b128 v[216:219], v246 offset:16768\n"
"v_add_f32 v243, v243, v169\n"
"v_mfma_f32_16x16x32_bf16 a[208:211], v[220:223], v[192:195], a[208:211]\n"
"v_exp_f32 v171, v171\n"
"v_mfma_f32_16x16x32_bf16 a[212:215], v[220:223], v[196:199], a[212:215]\n"
"v_add_f32 v244, v244, v173\n"
"v_add_f32 v245, v245, v210\n"
"v_mfma_f32_16x16x32_bf16 a[216:219], v[220:223], v[200:203], a[216:219]\n"
"v_exp_f32 v175, v175\n"
"v_mfma_f32_16x16x32_bf16 a[220:223], v[220:223], v[204:207], a[220:223]\n"
"s_waitcnt lgkmcnt(4)\n"
"ds_read_b128 v[220:223], v246 offset:16896\n"
"ds_read_b128 v[224:227], v246 offset:17024\n"
"v_mfma_f32_16x16x32_bf16 a[236:239], v[228:231], v[204:207], a[236:239]\n"
"v_cvt_pk_bf16_f32 v209, v210, v211\n"
"v_add_f32 v241, v241, v162\n"
"v_mfma_f32_16x16x32_bf16 a[232:235], v[228:231], v[200:203], a[232:235]\n"
"v_add_f32 v242, v242, v166\n"
"v_add_f32 v243, v243, v170\n"
"v_mfma_f32_16x16x32_bf16 a[228:231], v[228:231], v[196:199], a[228:231]\n"
"v_add_f32 v244, v244, v174\n"
"v_add_u32 v247, s30, v247\n"
"v_mfma_f32_16x16x32_bf16 a[224:227], v[228:231], v[192:195], a[224:227]\n"
"s_waitcnt lgkmcnt(4)\n"
"v_add_f32 v245, v245, v211\n"
"v_add_f32 v241, v241, v163\n"
"v_mfma_f32_16x16x32_bf16 a[240:243], v[232:235], v[192:195], a[240:243]\n"
"s_sub_u32 s30, 0, s30\n"
"ds_write_b64 v248, v[208:209] offset:2048\n"
"v_mfma_f32_16x16x32_bf16 a[244:247], v[232:235], v[196:199], a[244:247]\n"
"s_sub_u32 s70, s70, 1\n"
"v_add_f32 v242, v242, v167\n"
"v_add_f32 v243, v243, v171\n"
"v_mfma_f32_16x16x32_bf16 a[248:251], v[232:235], v[200:203], a[248:251]\n"
"v_add_f32 v244, v244, v175\n"
"v_cvt_pk_bf16_f32 v192, v160, v161\n"
"v_mfma_f32_16x16x32_bf16 a[252:255], v[232:235], v[204:207], a[252:255]\n"
"v_cvt_pk_bf16_f32 v193, v162, v163\n"
"v_cvt_pk_bf16_f32 v196, v164, v165\n"
"v_cvt_pk_bf16_f32 v197, v166, v167\n"
"v_cvt_pk_bf16_f32 v200, v168, v169\n"
"v_cvt_pk_bf16_f32 v201, v170, v171\n"
"v_cvt_pk_bf16_f32 v204, v172, v173\n"
"v_cvt_pk_bf16_f32 v205, v174, v175\n"
"s_cmp_lg_u32 s70, 0\n"
"s_cbranch_scc1 Lloop1\n"
"s_branch Lloopx\n"
"Lloop2:\n"
"s_waitcnt lgkmcnt(3)\n"
"v_mfma_f32_16x16x32_bf16 v[160:163], v[212:215], v[0:3], 0\n"
"s_waitcnt lgkmcnt(2)\n"
"ds_read_b128 v[228:231], v246 offset:17152\n"
"ds_read_b128 v[232:235], v246 offset:17280\n"
"v_mfma_f32_16x16x32_bf16 v[160:163], v[216:219], v[32:35], v[160:163]\n"
"ds_read_b128 v[252:255], v246 offset:17408\n"
"v_mfma_f32_16x16x32_bf16 v[164:167], v[216:219], v[40:43], 0\n"
"v_exp_f32 v176, v176\n"
"v_mfma_f32_16x16x32_bf16 v[164:167], v[212:215], v[8:11], v[164:167]\n"
"v_exp_f32 v177, v177\n"
"v_mfma_f32_16x16x32_bf16 v[168:171], v[212:215], v[16:19], 0\n"
"v_add_f32 v241, v241, v176\n"
"v_add_f32 v241, v241, v177\n"
"v_mfma_f32_16x16x32_bf16 v[168:171], v[216:219], v[48:51], v[168:171]\n"
"v_exp_f32 v178, v178\n"
"v_mfma_f32_16x16x32_bf16 v[172:175], v[216:219], v[56:59], 0\n"
"ds_read_b128 v[216:219], v246 offset:17536\n"
"v_add_f32 v241, v241, v178\n"
"v_mfma_f32_16x16x32_bf16 v[172:175], v[212:215], v[24:27], v[172:175]\n"
"s_waitcnt lgkmcnt(5)\n"
"ds_read_b128 v[212:215], v246 offset:18688\n"
"v_cvt_pk_bf16_f32 v194, v176, v177\n"
"v_mfma_f32_16x16x32_bf16 v[172:175], v[220:223], v[88:91], v[172:175]\n"
"s_waitcnt lgkmcnt(5)\n"
"v_exp_f32 v179, v179\n"
"v_mfma_f32_16x16x32_bf16 v[172:175], v[224:227], v[120:123], v[172:175]\n"
"v_add_f32 v241, v241, v179\n"
"v_cvt_pk_bf16_f32 v195, v178, v179\n"
"v_mfma_f32_16x16x32_bf16 v[168:171], v[224:227], v[112:115], v[168:171]\n"
"v_exp_f32 v180, v180\n"
"v_mfma_f32_16x16x32_bf16 v[168:171], v[220:223], v[80:83], v[168:171]\n"
"v_exp_f32 v181, v181\n"
"v_mfma_f32_16x16x32_bf16 v[164:167], v[220:223], v[72:75], v[164:167]\n"
"v_add_f32 v242, v242, v180\n"
"v_add_f32 v242, v242, v181\n"
"v_mfma_f32_16x16x32_bf16 v[164:167], v[224:227], v[104:107], v[164:167]\n"
"v_exp_f32 v182, v182\n"
"v_mfma_f32_16x16x32_bf16 v[160:163], v[224:227], v[96:99], v[160:163]\n"
"ds_read_b128 v[224:227], v246 offset:18816\n"
"v_add_f32 v242, v242, v182\n"
"v_mfma_f32_16x16x32_bf16 v[160:163], v[220:223], v[64:67], v[160:163]\n"
"s_waitcnt lgkmcnt(5)\n"
"ds_read_b128 v[220:223], v248 offset:5120\n"
"v_cvt_pk_bf16_f32 v198, v180, v181\n"
"v_mfma_f32_16x16x32_bf16 v[160:163], v[228:231], v[4:7], v[160:163]\n"
"s_waitcnt lgkmcnt(5)\n"
"v_exp_f32 v183, v183\n"
"v_mfma_f32_16x16x32_bf16 v[160:163], v[232:235], v[36:39], v[160:163]\n"
"v_add_f32 v242, v242, v183\n"
"v_cvt_pk_bf16_f32 v199, v182, v183\n"
"v_mfma_f32_16x16x32_bf16 v[164:167], v[232:235], v[44:47], v[164:167]\n"
"v_exp_f32 v184, v184\n"
"v_mfma_f32_16x16x32_bf16 v[164:167], v[228:231], v[12:15], v[164:167]\n"
"v_exp_f32 v185, v185\n"
"v_mfma_f32_16x16x32_bf16 v[168:171], v[228:231], v[20:23], v[168:171]\n"
"v_add_f32 v243, v243, v184\n"
"v_add_f32 v243, v243, v185\n"
"v_mfma_f32_16x16x32_bf16 v[168:171], v[232:235], v[52:55], v[168:171]\n"
"v_exp_f32 v186, v186\n"
"v_mfma_f32_16x16x32_bf16 v[172:175], v[232:235], v[60:63], v[172:175]\n"
"ds_read_b128 v[232:235], v248 offset:6144\n"
"v_add_f32 v243, v243, v186\n"
"v_mfma_f32_16x16x32_bf16 v[172:175], v[228:231], v[28:31], v[172:175]\n"
"s_waitcnt lgkmcnt(5)\n"
"ds_read_b128 v[228:231], v246 offset:18944\n"
"v_cvt_pk_bf16_f32 v202, v184, v185\n"
"v_mfma_f32_16x16x32_bf16 v[172:175], v[252:255], v[92:95], v[172:175]\n"
"s_waitcnt lgkmcnt(5)\n"
"v_exp_f32 v187, v187\n"
"v_mfma_f32_16x16x32_bf16 v[172:175], v[216:219], v[124:127], v[172:175]\n"
"v_add_f32 v243, v243, v187\n"
"v_cvt_pk_bf16_f32 v203, v186, v187\n"
"v_mfma_f32_16x16x32_bf16 v[168:171], v[216:219], v[116:119], v[168:171]\n"
"v_exp_f32 v188, v188\n"
"v_mfma_f32_16x16x32_bf16 v[168:171], v[252:255], v[84:87], v[168:171]\n"
"v_exp_f32 v189, v189\n"
"v_mfma_f32_16x16x32_bf16 v[164:167], v[252:255], v[76:79], v[164:167]\n"
"v_add_f32 v244, v244, v188\n"
"v_add_f32 v244, v244, v189\n"
"v_mfma_f32_16x16x32_bf16 v[164:167], v[216:219], v[108:111], v[164:167]\n"
"v_exp_f32 v190, v190\n"
"v_mfma_f32_16x16x32_bf16 v[160:163], v[216:219], v[100:103], v[160:163]\n"
"ds_read_b128 v[216:219], v246 offset:19072\n"
"v_add_f32 v244, v244, v190\n"
"v_mfma_f32_16x16x32_bf16 v[160:163], v[252:255], v[68:71], v[160:163]\n"
"s_waitcnt lgkmcnt(5)\n"
"v_exp_f32 v191, v191\n"
"v_mfma_f32_16x16x32_bf16 v[176:179], v[212:215], v[0:3], 0\n"
"s_waitcnt lgkmcnt(4)\n"
"v_add_f32 v244, v244, v191\n"
"v_cvt_pk_bf16_f32 v206, v188, v189\n"
"v_mfma_f32_16x16x32_bf16 v[176:179], v[224:227], v[32:35], v[176:179]\n"
"ds_read_b128 v[252:255], v248 offset:7168\n"
"v_cvt_pk_bf16_f32 v207, v190, v191\n"
"v_mfma_f32_16x16x32_bf16 v[180:183], v[224:227], v[40:43], 0\n"
"v_fma_f32 v172, v172, s65, v239\n"
"v_fma_f32 v173, v173, s65, v239\n"
"v_mfma_f32_16x16x32_bf16 v[180:183], v[212:215], v[8:11], v[180:183]\n"
"v_fma_f32 v174, v174, s65, v239\n"
"v_fma_f32 v175, v175, s65, v239\n"
"v_mfma_f32_16x16x32_bf16 v[184:187], v[212:215], v[16:19], 0\n"
"s_add_u32 m0, s31, 0\n"
"v_fma_f32 v168, v168, s65, v238\n"
"v_max3_f32 v251, v172, v173, v174\n"
"v_mfma_f32_16x16x32_bf16 v[184:187], v[224:227], v[48:51], v[184:187]\n"
"buffer_load_dwordx4 v249, s[48:51], s67 offen nt lds\n"
"v_mfma_f32_16x16x32_bf16 v[188:191], v[224:227], v[56:59], 0\n"
"v_fma_f32 v169, v169, s65, v238\n"
"v_fma_f32 v170, v170, s65, v238\n"
"v_mfma_f32_16x16x32_bf16 v[188:191], v[212:215], v[24:27], v[188:191]\n"
"s_waitcnt lgkmcnt(4)\n"
"v_max3_f32 v251, v251, v175, v168\n"
"v_fma_f32 v171, v171, s65, v238\n"
"v_mfma_f32_16x16x32_bf16 v[208:211], v[212:215], v[220:223], 0\n"
"s_waitcnt lgkmcnt(3)\n"
"ds_read_b128 v[212:215], v248 offset:8192\n"
"ds_read_b128 v[220:223], v246 offset:19200\n"
"v_mfma_f32_16x16x32_bf16 v[208:211], v[224:227], v[232:235], v[208:211]\n"
"s_waitcnt lgkmcnt(4)\n"
"ds_read_b128 v[224:227], v246 offset:19328\n"
"ds_read_b128 v[232:235], v248 offset:9216\n"
"v_mfma_f32_16x16x32_bf16 v[188:191], v[228:231], v[88:91], v[188:191]\n"
"s_waitcnt lgkmcnt(5)\n"
"v_fma_f32 v164, v164, s65, v237\n"
"v_max3_f32 v251, v251, v169, v170\n"
"v_mfma_f32_16x16x32_bf16 v[188:191], v[216:219], v[120:123], v[188:191]\n"
"buffer_load_dwordx4 v249, s[48:51], s67 offen offset:1024 nt lds\n"
"v_mfma_f32_16x16x32_bf16 v[184:187], v[216:219], v[112:115], v[184:187]\n"
"s_cmp_lg_u64 s[76:77], 0\n"
"s_cbranch_scc1 Lslow_10\n"
"Lret_10:\n"
"v_mfma_f32_16x16x32_bf16 v[184:187], v[228:231], v[80:83], v[184:187]\n"
"v_fma_f32 v165, v165, s65, v237\n"
"v_fma_f32 v166, v166, s65, v237\n"
"v_mfma_f32_16x16x32_bf16 v[180:183], v[228:231], v[72:75], v[180:183]\n"
"v_max3_f32 v251, v251, v171, v164\n"
"v_fma_f32 v167, v167, s65, v237\n"
"v_mfma_f32_16x16x32_bf16 v[180:183], v[216:219], v[104:107], v[180:183]\n"
"v_fma_f32 v160, v160, s65, v236\n"
"v_max3_f32 v251, v251, v165, v166\n"
"v_mfma_f32_16x16x32_bf16 v[176:179], v[216:219], v[96:99], v[176:179]\n"
"v_fma_f32 v161, v161, s65, v236\n"
"v_fma_f32 v162, v162, s65, v236\n"
"v_mfma_f32_16x16x32_bf16 v[176:179], v[228:231], v[64:67], v[176:179]\n"
"s_waitcnt lgkmcnt(4)\n"
"buffer_load_dwordx4 v249, s[48:51], s67 offen offset:2048 nt lds\n"
"v_mfma_f32_16x16x32_bf16 v[208:211], v[228:231], v[252:255], v[208:211]\n"
"s_waitcnt lgkmcnt(3)\n"
"ds_read_b128 v[228:231], v248 offset:10240\n"
"ds_read_b128 v[252:255], v246 offset:19456\n"
"v_mfma_f32_16x16x32_bf16 v[208:211], v[216:219], v[212:215], v[208:211]\n"
"s_waitcnt lgkmcnt(4)\n"
"ds_read_b128 v[212:215], v246 offset:19584\n"
"ds_read_b128 v[216:219], v248 offset:11264\n"
"v_mfma_f32_16x16x32_bf16 v[176:179], v[220:223], v[4:7], v[176:179]\n"
"s_waitcnt lgkmcnt(5)\n"
"v_add_u32 v246, s34, v246\n"
"v_max3_f32 v251, v251, v167, v160\n"
"v_mfma_f32_16x16x32_bf16 v[176:179], v[224:227], v[36:39], v[176:179]\n"
"s_sub_u32 s34, 0, s34\n"
"v_fma_f32 v163, v163, s65, v236\n"
"v_max3_f32 v251, v251, v161, v162\n"
"v_mfma_f32_16x16x32_bf16 v[180:183], v[224:227], v[44:47], v[180:183]\n"
"v_mfma_f32_16x16x32_bf16 v[180:183], v[220:223], v[12:15], v[180:183]\n"
"buffer_load_dwordx4 v249, s[48:51], s67 offen offset:3072 nt lds\n"
"v_mfma_f32_16x16x32_bf16 v[184:187], v[220:223], v[20:23], v[184:187]\n"
"s_add_u32 s67, s67, 0x4000\n"
"v_mfma_f32_16x16x32_bf16 v[184:187], v[224:227], v[52:55], v[184:187]\n"
"v_mfma_f32_16x16x32_bf16 v[188:191], v[224:227], v[60:63], v[188:191]\n"
"v_mfma_f32_16x16x32_bf16 v[188:191], v[220:223], v[28:31], v[188:191]\n"
"s_waitcnt lgkmcnt(4)\n"
"v_mfma_f32_16x16x32_bf16 v[208:211], v[220:223], v[232:235], v[208:211]\n"
"s_waitcnt lgkmcnt(3)\n"
"ds_read_b128 v[220:223], v248 offset:12288\n"
"ds_read_b64_tr_b16 v[232:233], v247 offset:0\n"
"v_mfma_f32_16x16x32_bf16 v[208:211], v[224:227], v[228:231], v[208:211]\n"
"s_waitcnt lgkmcnt(4)\n"
"ds_read_b64_tr_b16 v[234:235], v247 offset:2048\n"
"ds_read_b64_tr_b16 v[224:225], v247 offset:32\n"
"v_mfma_f32_16x16x32_bf16 v[188:191], v[252:255], v[92:95], v[188:191]\n"
"s_waitcnt lgkmcnt(5)\n"
"ds_read_b64_tr_b16 v[226:227], v247 offset:2080\n"
"ds_read_b64_tr_b16 v[228:229], v247 offset:128\n"
"v_mfma_f32_16x16x32_bf16 v[188:191], v[212:215], v[124:127], v[188:191]\n"
"ds_read_b64_tr_b16 v[230:231], v247 offset:2176\n"
"v_mfma_f32_16x16x32_bf16 v[184:187], v[212:215], v[116:119], v[184:187]\n"
"v_mfma_f32_16x16x32_bf16 v[184:187], v[252:255], v[84:87], v[184:187]\n"
"v_mfma_f32_16x16x32_bf16 v[180:183], v[252:255], v[76:79], v[180:183]\n"
"v_mfma_f32_16x16x32_bf16 v[180:183], v[212:215], v[108:111], v[180:183]\n"
"v_mfma_f32_16x16x32_bf16 v[176:179], v[212:215], v[100:103], v[176:179]\n"
"v_mfma_f32_16x16x32_bf16 v[176:179], v[252:255], v[68:71], v[176:179]\n"
"s_waitcnt lgkmcnt(7)\n"
"v_fma_f32 v188, v188, s65, v239\n"
"v_fma_f32 v189, v189, s65, v239\n"
"v_mfma_f32_16x16x32_bf16 v[208:211], v[252:255], v[216:219], v[208:211]\n"
"s_waitcnt lgkmcnt(6)\n"
"ds_read_b64_tr_b16 v[216:217], v247 offset:160\n"
"ds_read_b64_tr_b16 v[218:219], v247 offset:2208\n"
"v_mfma_f32_16x16x32_bf16 v[208:211], v[212:215], v[220:223], v[208:211]\n"
"s_waitcnt lgkmcnt(6)\n"
"ds_read_b64_tr_b16 v[252:253], v247 offset:256\n"
"ds_read_b64_tr_b16 v[254:255], v247 offset:2304\n"
"v_mfma_f32_16x16x32_bf16 a[12:15], v[232:235], v[204:207], a[12:15]\n"
"ds_read_b64_tr_b16 v[212:213], v247 offset:288\n"
"ds_read_b64_tr_b16 v[214:215], v247 offset:2336\n"
"v_mfma_f32_16x16x32_bf16 a[8:11], v[232:235], v[200:203], a[8:11]\n"
"ds_read_b64_tr_b16 v[220:221], v247 offset:512\n"
"v_fma_f32 v208, v208, s65, v240\n"
"v_mfma_f32_16x16x32_bf16 a[4:7], v[232:235], v[196:199], a[4:7]\n"
"v_fma_f32 v209, v209, s65, v240\n"
"v_fma_f32 v210, v210, s65, v240\n"
"v_mfma_f32_16x16x32_bf16 a[0:3], v[232:235], v[192:195], a[0:3]\n"
"s_waitcnt lgkmcnt(9)\n"
"v_fma_f32 v211, v211, s65, v240\n"
"v_max3_f32 v250, v208, v209, v210\n"
"v_mfma_f32_16x16x32_bf16 a[16:19], v[224:227], v[192:195], a[16:19]\n"
"ds_read_b64_tr_b16 v[222:223], v247 offset:2560\n"
"v_max_f32 v250, v250, v211\n"
"v_mfma_f32_16x16x32_bf16 a[20:23], v[224:227], v[196:199], a[20:23]\n"
"v_cmp_lt_f32_e64 s[78:79], s66, v250\n"
"v_fma_f32 v190, v190, s65, v239\n"
"v_mfma_f32_16x16x32_bf16 a[24:27], v[224:227], v[200:203], a[24:27]\n"
"v_max3_f32 v251, v251, v163, v188\n"
"v_fma_f32 v191, v191, s65, v239\n"
"v_mfma_f32_16x16x32_bf16 a[28:31], v[224:227], v[204:207], a[28:31]\n"
"s_waitcnt lgkmcnt(8)\n"
"s_cmp_lg_u64 s[78:79], 0\n"
"s_cbranch_scc1 Lslow_11\n"
"Lret_11:\n"
"v_mfma_f32_16x16x32_bf16 a[44:47], v[228:231], v[204:207], a[44:47]\n"
"v_fma_f32 v184, v184, s65, v238\n"
"v_max3_f32 v251, v251, v189, v190\n"
"v_mfma_f32_16x16x32_bf16 a[40:43], v[228:231], v[200:203], a[40:43]\n"
"v_fma_f32 v185, v185, s65, v238\n"
"v_fma_f32 v186, v186, s65, v238\n"
"v_mfma_f32_16x16x32_bf16 a[36:39], v[228:231], v[196:199], a[36:39]\n"
"s_waitcnt vmcnt(12)\n"
"s_barrier\n"
"v_mfma_f32_16x16x32_bf16 a[32:35], v[228:231], v[192:195], a[32:35]\n"
"s_waitcnt lgkmcnt(6)\n"
"ds_read_b128 v[232:235], v248 offset:2048\n"
"ds_read_b128 v[224:227], v248 offset:1024\n"
"v_mfma_f32_16x16x32_bf16 a[48:51], v[216:219], v[192:195], a[48:51]\n"
"ds_read_b64_tr_b16 v[228:229], v247 offset:544\n"
"ds_read_b64_tr_b16 v[230:231], v247 offset:2592\n"
"v_mfma_f32_16x16x32_bf16 a[52:55], v[216:219], v[196:199], a[52:55]\n"
"ds_read_b32 v250, v248 offset:12\n"
"v_max3_f32 v251, v251, v191, v184\n"
"v_mfma_f32_16x16x32_bf16 a[56:59], v[216:219], v[200:203], a[56:59]\n"
"s_add_u32 m0, s33, 0x4100\n"
"v_fma_f32 v187, v187, s65, v238\n"
"v_fma_f32 v180, v180, s65, v237\n"
"v_mfma_f32_16x16x32_bf16 a[60:63], v[216:219], v[204:207], a[60:63]\n"
"s_waitcnt lgkmcnt(9)\n"
"buffer_load_dwordx4 v249, s[52:55], s68 offen nt lds\n"
"v_mfma_f32_16x16x32_bf16 a[76:79], v[252:255], v[204:207], a[76:79]\n"
"s_sub_u32 s33, s33, s30\n"
"ds_read_b64_tr_b16 v[216:217], v247 offset:640\n"
"ds_read_b64_tr_b16 v[218:219], v247 offset:2688\n"
"v_mfma_f32_16x16x32_bf16 a[72:75], v[252:255], v[200:203], a[72:75]\n"
"v_max3_f32 v251, v251, v185, v186\n"
"v_fma_f32 v181, v181, s65, v237\n"
"v_mfma_f32_16x16x32_bf16 a[68:71], v[252:255], v[196:199], a[68:71]\n"
"v_fma_f32 v182, v182, s65, v237\n"
"v_max3_f32 v251, v251, v187, v180\n"
"v_mfma_f32_16x16x32_bf16 a[64:67], v[252:255], v[192:195], a[64:67]\n"
"s_waitcnt lgkmcnt(9)\n"
"ds_read_b64_tr_b16 v[252:253], v247 offset:672\n"
"ds_read_b64_tr_b16 v[254:255], v247 offset:2720\n"
"v_mfma_f32_16x16x32_bf16 a[80:83], v[212:215], v[192:195], a[80:83]\n"
"v_fma_f32 v183, v183, s65, v237\n"
"v_fma_f32 v176, v176, s65, v236\n"
"v_mfma_f32_16x16x32_bf16 a[84:87], v[212:215], v[196:199], a[84:87]\n"
"buffer_load_dwordx4 v249, s[52:55], s68 offen offset:1024 nt lds\n"
"v_mfma_f32_16x16x32_bf16 a[88:91], v[212:215], v[200:203], a[88:91]\n"
"v_max3_f32 v251, v251, v181, v182\n"
"v_fma_f32 v177, v177, s65, v236\n"
"v_mfma_f32_16x16x32_bf16 a[92:95], v[212:215], v[204:207], a[92:95]\n"
"s_waitcnt lgkmcnt(8)\n"
"ds_read_b64_tr_b16 v[212:213], v247 offset:384\n"
"ds_read_b64_tr_b16 v[214:215], v247 offset:2432\n"
"v_mfma_f32_16x16x32_bf16 v[132:135], v[220:223], v[232:235], v[132:135]\n"
"s_waitcnt lgkmcnt(9)\n"
"v_fma_f32 v178, v178, s65, v236\n"
"v_max3_f32 v251, v251, v183, v176\n"
"v_mfma_f32_16x16x32_bf16 v[128:131], v[220:223], v[224:227], v[128:131]\n"
"v_fma_f32 v179, v179, s65, v236\n"
"v_max3_f32 v251, v251, v177, v178\n"
"v_mfma_f32_16x16x32_bf16 a[140:143], v[220:223], v[204:207], a[140:143]\n"
"s_waitcnt lgkmcnt(6)\n"
"v_max3_f32 v251, v251, v179, v250\n"
"v_cmp_lt_f32 vcc, s66, v251\n"
"v_mfma_f32_16x16x32_bf16 a[136:139], v[220:223], v[200:203], a[136:139]\n"
"buffer_load_dwordx4 v249, s[52:55], s68 offen offset:2048 nt lds\n"
"v_mfma_f32_16x16x32_bf16 a[132:135], v[220:223], v[196:199], a[132:135]\n"
"v_mfma_f32_16x16x32_bf16 a[128:131], v[220:223], v[192:195], a[128:131]\n"
"ds_read_b64_tr_b16 v[220:221], v247 offset:416\n"
"ds_read_b64_tr_b16 v[222:223], v247 offset:2464\n"
"v_mfma_f32_16x16x32_bf16 a[144:147], v[228:231], v[192:195], a[144:147]\n"
"s_cbranch_vccnz Lslow_12\n"
"Lret_12:\n"
"v_mfma_f32_16x16x32_bf16 a[148:151], v[228:231], v[196:199], a[148:151]\n"
"v_exp_f32 v160, v160\n"
"v_mfma_f32_16x16x32_bf16 a[152:155], v[228:231], v[200:203], a[152:155]\n"
"v_exp_f32 v161, v161\n"
"v_mfma_f32_16x16x32_bf16 a[156:159], v[228:231], v[204:207], a[156:159]\n"
"buffer_load_dwordx4 v249, s[52:55], s68 offen offset:3072 nt lds\n"
"v_mfma_f32_16x16x32_bf16 v[136:139], v[228:231], v[224:227], v[136:139]\n"
"s_add_u32 s68, s68, 0x4000\n"
"v_add_f32 v241, v241, v160\n"
"v_add_f32 v241, v241, v161\n"
"v_mfma_f32_16x16x32_bf16 v[140:143], v[228:231], v[232:235], v[140:143]\n"
"s_waitcnt lgkmcnt(6)\n"
"ds_read_b64_tr_b16 v[228:229], v247 offset:768\n"
"ds_read_b64_tr_b16 v[230:231], v247 offset:2816\n"
"v_mfma_f32_16x16x32_bf16 v[148:151], v[216:219], v[232:235], v[148:151]\n"
"v_exp_f32 v162, v162\n"
"v_mfma_f32_16x16x32_bf16 v[144:147], v[216:219], v[224:227], v[144:147]\n"
"v_exp_f32 v163, v163\n"
"v_mfma_f32_16x16x32_bf16 a[172:175], v[216:219], v[204:207], a[172:175]\n"
"v_add_f32 v241, v241, v162\n"
"v_add_f32 v241, v241, v163\n"
"v_mfma_f32_16x16x32_bf16 a[168:171], v[216:219], v[200:203], a[168:171]\n"
"v_exp_f32 v164, v164\n"
"v_mfma_f32_16x16x32_bf16 a[164:167], v[216:219], v[196:199], a[164:167]\n"
"v_exp_f32 v165, v165\n"
"v_mfma_f32_16x16x32_bf16 a[160:163], v[216:219], v[192:195], a[160:163]\n"
"s_waitcnt lgkmcnt(6)\n"
"ds_read_b64_tr_b16 v[216:217], v247 offset:800\n"
"ds_read_b64_tr_b16 v[218:219], v247 offset:2848\n"
"v_mfma_f32_16x16x32_bf16 a[176:179], v[252:255], v[192:195], a[176:179]\n"
"v_add_f32 v242, v242, v164\n"
"v_add_f32 v242, v242, v165\n"
"v_mfma_f32_16x16x32_bf16 a[180:183], v[252:255], v[196:199], a[180:183]\n"
"v_exp_f32 v166, v166\n"
"v_mfma_f32_16x16x32_bf16 a[184:187], v[252:255], v[200:203], a[184:187]\n"
"v_exp_f32 v167, v167\n"
"v_mfma_f32_16x16x32_bf16 a[188:191], v[252:255], v[204:207], a[188:191]\n"
"v_add_f32 v242, v242, v166\n"
"v_add_f32 v242, v242, v167\n"
"v_mfma_f32_16x16x32_bf16 v[152:155], v[252:255], v[224:227], v[152:155]\n"
"ds_read_b64_tr_b16 v[224:225], v247 offset:896\n"
"ds_read_b64_tr_b16 v[226:227], v247 offset:2944\n"
"v_mfma_f32_16x16x32_bf16 v[156:159], v[252:255], v[232:235], v[156:159]\n"
"s_waitcnt lgkmcnt(8)\n"
"ds_read_b64_tr_b16 v[232:233], v247 offset:928\n"
"ds_read_b64_tr_b16 v[234:235], v247 offset:2976\n"
"v_mfma_f32_16x16x32_bf16 a[108:111], v[212:215], v[204:207], a[108:111]\n"
"ds_read_b128 v[252:255], v246 offset:0\n"
"v_mfma_f32_16x16x32_bf16 a[104:107], v[212:215], v[200:203], a[104:107]\n"
"v_exp_f32 v168, v168\n"
"v_mfma_f32_16x16x32_bf16 a[100:103], v[212:215], v[196:199], a[100:103]\n"
"v_exp_f32 v169, v169\n"
"v_mfma_f32_16x16x32_bf16 a[96:99], v[212:215], v[192:195], a[96:99]\n"
"s_waitcnt lgkmcnt(9)\n"
"ds_read_b128 v[212:215], v246 offset:128\n"
"v_add_f32 v243, v243, v168\n"
"v_mfma_f32_16x16x32_bf16 a[112:115], v[220:223], v[192:195], a[112:115]\n"
"v_exp_f32 v170, v170\n"
"v_mfma_f32_16x16x32_bf16 a[116:119], v[220:223], v[196:199], a[116:119]\n"
"v_add_f32 v243, v243, v169\n"
"v_add_f32 v243, v243, v170\n"
"v_mfma_f32_16x16x32_bf16 a[120:123], v[220:223], v[200:203], a[120:123]\n"
"v_exp_f32 v171, v171\n"
"v_mfma_f32_16x16x32_bf16 a[124:127], v[220:223], v[204:207], a[124:127]\n"
"s_waitcnt lgkmcnt(8)\n"
"ds_read_b128 v[220:223], v246 offset:256\n"
"v_add_f32 v243, v243, v171\n"
"v_mfma_f32_16x16x32_bf16 a[204:207], v[228:231], v[204:207], a[204:207]\n"
"v_exp_f32 v172, v172\n"
"v_mfma_f32_16x16x32_bf16 a[200:203], v[228:231], v[200:203], a[200:203]\n"
"v_exp_f32 v173, v173\n"
"v_mfma_f32_16x16x32_bf16 a[196:199], v[228:231], v[196:199], a[196:199]\n"
"v_add_f32 v244, v244, v172\n"
"v_add_f32 v244, v244, v173\n"
"v_mfma_f32_16x16x32_bf16 a[192:195], v[228:231], v[192:195], a[192:195]\n"
"s_waitcnt lgkmcnt(7)\n"
"ds_read_b128 v[228:231], v246 offset:384\n"
"v_mfma_f32_16x16x32_bf16 a[208:211], v[216:219], v[192:195], a[208:211]\n"
"v_exp_f32 v174, v174\n"
"v_mfma_f32_16x16x32_bf16 a[212:215], v[216:219], v[196:199], a[212:215]\n"
"v_exp_f32 v175, v175\n"
"v_mfma_f32_16x16x32_bf16 a[216:219], v[216:219], v[200:203], a[216:219]\n"
"v_add_f32 v244, v244, v174\n"
"v_add_f32 v244, v244, v175\n"
"v_mfma_f32_16x16x32_bf16 a[220:223], v[216:219], v[204:207], a[220:223]\n"
"s_waitcnt lgkmcnt(6)\n"
"ds_read_b128 v[216:219], v246 offset:512\n"
"v_mfma_f32_16x16x32_bf16 a[236:239], v[224:227], v[204:207], a[236:239]\n"
"v_exp_f32 v176, v176\n"
"v_mfma_f32_16x16x32_bf16 a[232:235], v[224:227], v[200:203], a[232:235]\n"
"v_exp_f32 v177, v177\n"
"v_mfma_f32_16x16x32_bf16 a[228:231], v[224:227], v[196:199], a[228:231]\n"
"v_add_f32 v241, v241, v176\n"
"v_add_f32 v241, v241, v177\n"
"v_mfma_f32_16x16x32_bf16 a[224:227], v[224:227], v[192:195], a[224:227]\n"
"s_waitcnt lgkmcnt(5)\n"
"ds_read_b128 v[224:227], v246 offset:640\n"
"v_mfma_f32_16x16x32_bf16 a[240:243], v[232:235], v[192:195], a[240:243]\n"
"v_cvt_pk_bf16_f32 v192, v160, v161\n"
"v_cvt_pk_bf16_f32 v193, v162, v163\n"
"v_mfma_f32_16x16x32_bf16 a[244:247], v[232:235], v[196:199], a[244:247]\n"
"v_cvt_pk_bf16_f32 v196, v164, v165\n"
"v_cvt_pk_bf16_f32 v197, v166, v167\n"
"v_mfma_f32_16x16x32_bf16 a[248:251], v[232:235], v[200:203], a[248:251]\n"
"v_cvt_pk_bf16_f32 v200, v168, v169\n"
"v_cvt_pk_bf16_f32 v201, v170, v171\n"
"v_mfma_f32_16x16x32_bf16 a[252:255], v[232:235], v[204:207], a[252:255]\n"
"s_waitcnt lgkmcnt(5)\n"
"v_cvt_pk_bf16_f32 v204, v172, v173\n"
"v_cvt_pk_bf16_f32 v205, v174, v175\n"
"v_mfma_f32_16x16x32_bf16 v[160:163], v[252:255], v[0:3], 0\n"
"s_waitcnt lgkmcnt(4)\n"
"ds_read_b128 v[232:235], v246 offset:768\n"
"v_cvt_pk_bf16_f32 v194, v176, v177\n"
"v_mfma_f32_16x16x32_bf16 v[160:163], v[212:215], v[32:35], v[160:163]\n"
"v_exp_f32 v178, v178\n"
"v_mfma_f32_16x16x32_bf16 v[164:167], v[212:215], v[40:43], 0\n"
"v_exp_f32 v179, v179\n"
"v_mfma_f32_16x16x32_bf16 v[164:167], v[252:255], v[8:11], v[164:167]\n"
"v_add_f32 v241, v241, v178\n"
"v_add_f32 v241, v241, v179\n"
"v_mfma_f32_16x16x32_bf16 v[168:171], v[252:255], v[16:19], 0\n"
"v_cvt_pk_bf16_f32 v195, v178, v179\n"
"v_mfma_f32_16x16x32_bf16 v[168:171], v[212:215], v[48:51], v[168:171]\n"
"v_exp_f32 v180, v180\n"
"v_mfma_f32_16x16x32_bf16 v[172:175], v[212:215], v[56:59], 0\n"
"ds_read_b128 v[212:215], v246 offset:896\n"
"v_add_f32 v242, v242, v180\n"
"v_mfma_f32_16x16x32_bf16 v[172:175], v[252:255], v[24:27], v[172:175]\n"
"s_waitcnt lgkmcnt(5)\n"
"ds_read_b128 v[252:255], v246 offset:2048\n"
"v_mfma_f32_16x16x32_bf16 v[172:175], v[220:223], v[88:91], v[172:175]\n"
"s_waitcnt lgkmcnt(5)\n"
"v_exp_f32 v181, v181\n"
"v_mfma_f32_16x16x32_bf16 v[172:175], v[228:231], v[120:123], v[172:175]\n"
"v_exp_f32 v182, v182\n"
"v_mfma_f32_16x16x32_bf16 v[168:171], v[228:231], v[112:115], v[168:171]\n"
"v_add_f32 v242, v242, v181\n"
"v_add_f32 v242, v242, v182\n"
"v_mfma_f32_16x16x32_bf16 v[168:171], v[220:223], v[80:83], v[168:171]\n"
"v_exp_f32 v183, v183\n"
"v_mfma_f32_16x16x32_bf16 v[164:167], v[220:223], v[72:75], v[164:167]\n"
"v_add_f32 v242, v242, v183\n"
"v_cvt_pk_bf16_f32 v198, v180, v181\n"
"v_mfma_f32_16x16x32_bf16 v[164:167], v[228:231], v[104:107], v[164:167]\n"
"v_cvt_pk_bf16_f32 v199, v182, v183\n"
"v_mfma_f32_16x16x32_bf16 v[160:163], v[228:231], v[96:99], v[160:163]\n"
"ds_read_b128 v[228:231], v246 offset:2176\n"
"v_mfma_f32_16x16x32_bf16 v[160:163], v[220:223], v[64:67], v[160:163]\n"
"s_waitcnt lgkmcnt(5)\n"
"ds_read_b128 v[220:223], v248 offset:5120\n"
"v_mfma_f32_16x16x32_bf16 v[160:163], v[216:219], v[4:7], v[160:163]\n"
"s_waitcnt lgkmcnt(5)\n"
"v_exp_f32 v184, v184\n"
"v_mfma_f32_16x16x32_bf16 v[160:163], v[224:227], v[36:39], v[160:163]\n"
"v_exp_f32 v185, v185\n"
"v_mfma_f32_16x16x32_bf16 v[164:167], v[224:227], v[44:47], v[164:167]\n"
"v_add_f32 v243, v243, v184\n"
"v_add_f32 v243, v243, v185\n"
"v_mfma_f32_16x16x32_bf16 v[164:167], v[216:219], v[12:15], v[164:167]\n"
"v_exp_f32 v186, v186\n"
"v_mfma_f32_16x16x32_bf16 v[168:171], v[216:219], v[20:23], v[168:171]\n"
"v_exp_f32 v187, v187\n"
"v_mfma_f32_16x16x32_bf16 v[168:171], v[224:227], v[52:55], v[168:171]\n"
"v_add_f32 v243, v243, v186\n"
"v_add_f32 v243, v243, v187\n"
"v_mfma_f32_16x16x32_bf16 v[172:175], v[224:227], v[60:63], v[172:175]\n"
"ds_read_b128 v[224:227], v248 offset:6144\n"
"v_cvt_pk_bf16_f32 v202, v184, v185\n"
"v_mfma_f32_16x16x32_bf16 v[172:175], v[216:219], v[28:31], v[172:175]\n"
"s_waitcnt lgkmcnt(5)\n"
"ds_read_b128 v[216:219], v246 offset:2304\n"
"v_cvt_pk_bf16_f32 v203, v186, v187\n"
"v_mfma_f32_16x16x32_bf16 v[172:175], v[232:235], v[92:95], v[172:175]\n"
"s_waitcnt lgkmcnt(5)\n"
"v_exp_f32 v188, v188\n"
"v_mfma_f32_16x16x32_bf16 v[172:175], v[212:215], v[124:127], v[172:175]\n"
"v_exp_f32 v189, v189\n"
"v_mfma_f32_16x16x32_bf16 v[168:171], v[212:215], v[116:119], v[168:171]\n"
"v_add_f32 v244, v244, v188\n"
"v_add_f32 v244, v244, v189\n"
"v_mfma_f32_16x16x32_bf16 v[168:171], v[232:235], v[84:87], v[168:171]\n"
"v_exp_f32 v190, v190\n"
"v_mfma_f32_16x16x32_bf16 v[164:167], v[232:235], v[76:79], v[164:167]\n"
"v_exp_f32 v191, v191\n"
"v_mfma_f32_16x16x32_bf16 v[164:167], v[212:215], v[108:111], v[164:167]\n"
"v_add_f32 v244, v244, v190\n"
"v_add_f32 v244, v244, v191\n"
"v_mfma_f32_16x16x32_bf16 v[160:163], v[212:215], v[100:103], v[160:163]\n"
"ds_read_b128 v[212:215], v246 offset:2432\n"
"v_cvt_pk_bf16_f32 v206, v188, v189\n"
"v_mfma_f32_16x16x32_bf16 v[160:163], v[232:235], v[68:71], v[160:163]\n"
"s_waitcnt lgkmcnt(5)\n"
"v_cvt_pk_bf16_f32 v207, v190, v191\n"
"v_fma_f32 v172, v172, s65, v239\n"
"v_mfma_f32_16x16x32_bf16 v[176:179], v[252:255], v[0:3], 0\n"
"s_waitcnt lgkmcnt(4)\n"
"v_exp_f32 v208, v208\n"
"v_mfma_f32_16x16x32_bf16 v[176:179], v[228:231], v[32:35], v[176:179]\n"
"v_exp_f32 v209, v209\n"
"v_add_f32 v245, v245, v208\n"
"v_mfma_f32_16x16x32_bf16 v[180:183], v[228:231], v[40:43], 0\n"
"v_exp_f32 v210, v210\n"
"v_mfma_f32_16x16x32_bf16 v[180:183], v[252:255], v[8:11], v[180:183]\n"
"v_exp_f32 v211, v211\n"
"v_mfma_f32_16x16x32_bf16 v[184:187], v[252:255], v[16:19], 0\n"
"s_add_u32 m0, s31, 0x4100\n"
"v_add_f32 v245, v245, v209\n"
"v_cvt_pk_bf16_f32 v208, v208, v209\n"
"v_mfma_f32_16x16x32_bf16 v[184:187], v[228:231], v[48:51], v[184:187]\n"
"s_sub_u32 s31, s31, s34\n"
"v_cvt_pk_bf16_f32 v209, v210, v211\n"
"v_add_f32 v245, v245, v210\n"
"v_mfma_f32_16x16x32_bf16 v[188:191], v[228:231], v[56:59], 0\n"
"ds_read_b128 v[232:235], v248 offset:7168\n"
"v_add_f32 v245, v245, v211\n"
"v_mfma_f32_16x16x32_bf16 v[188:191], v[252:255], v[24:27], v[188:191]\n"
"ds_write_b64 v248, v[208:209] offset:3080\n"
"s_waitcnt lgkmcnt(5)\n"
"v_mfma_f32_16x16x32_bf16 v[208:211], v[252:255], v[220:223], 0\n"
"s_waitcnt lgkmcnt(4)\n"
"ds_read_b128 v[220:223], v248 offset:8192\n"
"ds_read_b128 v[252:255], v246 offset:2560\n"
"v_mfma_f32_16x16x32_bf16 v[208:211], v[228:231], v[224:227], v[208:211]\n"
"s_waitcnt lgkmcnt(5)\n"
"ds_read_b128 v[224:227], v246 offset:2688\n"
"ds_read_b128 v[228:231], v248 offset:9216\n"
"v_mfma_f32_16x16x32_bf16 v[188:191], v[216:219], v[88:91], v[188:191]\n"
"s_waitcnt lgkmcnt(6)\n"
"buffer_load_dwordx4 v249, s[48:51], s67 offen nt lds\n"
"v_mfma_f32_16x16x32_bf16 v[188:191], v[212:215], v[120:123], v[188:191]\n"
"v_fma_f32 v173, v173, s65, v239\n"
"v_fma_f32 v174, v174, s65, v239\n"
"v_mfma_f32_16x16x32_bf16 v[184:187], v[212:215], v[112:115], v[184:187]\n"
"s_cmp_lg_u64 s[76:77], 0\n"
"s_cbranch_scc1 Lslow_13\n"
"Lret_13:\n"
"v_mfma_f32_16x16x32_bf16 v[184:187], v[216:219], v[80:83], v[184:187]\n"
"buffer_load_dwordx4 v249, s[48:51], s67 offen offset:1024 nt lds\n"
"v_mfma_f32_16x16x32_bf16 v[180:183], v[216:219], v[72:75], v[180:183]\n"
"v_fma_f32 v175, v175, s65, v239\n"
"v_fma_f32 v168, v168, s65, v238\n"
"v_mfma_f32_16x16x32_bf16 v[180:183], v[212:215], v[104:107], v[180:183]\n"
"v_max3_f32 v251, v172, v173, v174\n"
"v_fma_f32 v169, v169, s65, v238\n"
"v_mfma_f32_16x16x32_bf16 v[176:179], v[212:215], v[96:99], v[176:179]\n"
"v_fma_f32 v170, v170, s65, v238\n"
"v_max3_f32 v251, v251, v175, v168\n"
"v_mfma_f32_16x16x32_bf16 v[176:179], v[216:219], v[64:67], v[176:179]\n"
"s_waitcnt lgkmcnt(5)\n"
"buffer_load_dwordx4 v249, s[48:51], s67 offen offset:2048 nt lds\n"
"v_mfma_f32_16x16x32_bf16 v[208:211], v[216:219], v[232:235], v[208:211]\n"
"s_waitcnt lgkmcnt(3)\n"
"ds_read_b128 v[216:219], v248 offset:10240\n"
"ds_read_b128 v[232:235], v246 offset:2816\n"
"v_mfma_f32_16x16x32_bf16 v[208:211], v[212:215], v[220:223], v[208:211]\n"
"s_waitcnt lgkmcnt(4)\n"
"ds_read_b128 v[212:215], v246 offset:2944\n"
"ds_read_b128 v[220:223], v248 offset:11264\n"
"v_mfma_f32_16x16x32_bf16 v[176:179], v[252:255], v[4:7], v[176:179]\n"
"s_waitcnt lgkmcnt(5)\n"
"v_fma_f32 v171, v171, s65, v238\n"
"v_fma_f32 v164, v164, s65, v237\n"
"v_mfma_f32_16x16x32_bf16 v[176:179], v[224:227], v[36:39], v[176:179]\n"
"v_max3_f32 v251, v251, v169, v170\n"
"v_fma_f32 v165, v165, s65, v237\n"
"v_mfma_f32_16x16x32_bf16 v[180:183], v[224:227], v[44:47], v[180:183]\n"
"v_fma_f32 v166, v166, s65, v237\n"
"v_max3_f32 v251, v251, v171, v164\n"
"v_mfma_f32_16x16x32_bf16 v[180:183], v[252:255], v[12:15], v[180:183]\n"
"buffer_load_dwordx4 v249, s[48:51], s67 offen offset:3072 nt lds\n"
"v_mfma_f32_16x16x32_bf16 v[184:187], v[252:255], v[20:23], v[184:187]\n"
"s_add_u32 s67, s67, 0x4000\n"
"v_fma_f32 v167, v167, s65, v237\n"
"v_fma_f32 v160, v160, s65, v236\n"
"v_mfma_f32_16x16x32_bf16 v[184:187], v[224:227], v[52:55], v[184:187]\n"
"v_max3_f32 v251, v251, v165, v166\n"
"v_fma_f32 v161, v161, s65, v236\n"
"v_mfma_f32_16x16x32_bf16 v[188:191], v[224:227], v[60:63], v[188:191]\n"
"v_fma_f32 v162, v162, s65, v236\n"
"v_max3_f32 v251, v251, v167, v160\n"
"v_mfma_f32_16x16x32_bf16 v[188:191], v[252:255], v[28:31], v[188:191]\n"
"s_waitcnt lgkmcnt(4)\n"
"v_fma_f32 v163, v163, s65, v236\n"
"v_max3_f32 v251, v251, v161, v162\n"
"v_mfma_f32_16x16x32_bf16 v[208:211], v[252:255], v[228:231], v[208:211]\n"
"s_waitcnt lgkmcnt(3)\n"
"ds_read_b128 v[228:231], v248 offset:12288\n"
"ds_read_b64_tr_b16 v[252:253], v247 offset:16640\n"
"v_mfma_f32_16x16x32_bf16 v[208:211], v[224:227], v[216:219], v[208:211]\n"
"s_waitcnt lgkmcnt(4)\n"
"ds_read_b64_tr_b16 v[254:255], v247 offset:18688\n"
"ds_read_b64_tr_b16 v[216:217], v247 offset:16672\n"
"v_mfma_f32_16x16x32_bf16 v[188:191], v[232:235], v[92:95], v[188:191]\n"
"s_waitcnt lgkmcnt(5)\n"
"ds_read_b64_tr_b16 v[218:219], v247 offset:18720\n"
"ds_read_b64_tr_b16 v[224:225], v247 offset:16768\n"
"v_mfma_f32_16x16x32_bf16 v[188:191], v[212:215], v[124:127], v[188:191]\n"
"ds_read_b64_tr_b16 v[226:227], v247 offset:18816\n"
"v_mfma_f32_16x16x32_bf16 v[184:187], v[212:215], v[116:119], v[184:187]\n"
"v_mfma_f32_16x16x32_bf16 v[184:187], v[232:235], v[84:87], v[184:187]\n"
"v_mfma_f32_16x16x32_bf16 v[180:183], v[232:235], v[76:79], v[180:183]\n"
"v_mfma_f32_16x16x32_bf16 v[180:183], v[212:215], v[108:111], v[180:183]\n"
"v_mfma_f32_16x16x32_bf16 v[176:179], v[212:215], v[100:103], v[176:179]\n"
"v_mfma_f32_16x16x32_bf16 v[176:179], v[232:235], v[68:71], v[176:179]\n"
"s_waitcnt lgkmcnt(7)\n"
"v_fma_f32 v188, v188, s65, v239\n"
"v_fma_f32 v189, v189, s65, v239\n"
"v_mfma_f32_16x16x32_bf16 v[208:211], v[232:235], v[220:223], v[208:211]\n"
"s_waitcnt lgkmcnt(6)\n"
"ds_read_b64_tr_b16 v[220:221], v247 offset:16800\n"
"ds_read_b64_tr_b16 v[222:223], v247 offset:18848\n"
"v_mfma_f32_16x16x32_bf16 v[208:211], v[212:215], v[228:231], v[208:211]\n"
"s_waitcnt lgkmcnt(6)\n"
"ds_read_b64_tr_b16 v[232:233], v247 offset:16896\n"
"ds_read_b64_tr_b16 v[234:235], v247 offset:18944\n"
"v_mfma_f32_16x16x32_bf16 a[12:15], v[252:255], v[204:207], a[12:15]\n"
"ds_read_b64_tr_b16 v[212:213], v247 offset:16928\n"
"ds_read_b64_tr_b16 v[214:215], v247 offset:18976\n"
"v_mfma_f32_16x16x32_bf16 a[8:11], v[252:255], v[200:203], a[8:11]\n"
"ds_read_b64_tr_b16 v[228:229], v247 offset:17152\n"
"v_fma_f32 v208, v208, s65, v240\n"
"v_mfma_f32_16x16x32_bf16 a[4:7], v[252:255], v[196:199], a[4:7]\n"
"v_fma_f32 v209, v209, s65, v240\n"
"v_fma_f32 v210, v210, s65, v240\n"
"v_mfma_f32_16x16x32_bf16 a[0:3], v[252:255], v[192:195], a[0:3]\n"
"s_waitcnt lgkmcnt(9)\n"
"v_fma_f32 v211, v211, s65, v240\n"
"v_max3_f32 v250, v208, v209, v210\n"
"v_mfma_f32_16x16x32_bf16 a[16:19], v[216:219], v[192:195], a[16:19]\n"
"ds_read_b64_tr_b16 v[230:231], v247 offset:19200\n"
"v_max_f32 v250, v250, v211\n"
"v_mfma_f32_16x16x32_bf16 a[20:23], v[216:219], v[196:199], a[20:23]\n"
"v_cmp_lt_f32_e64 s[78:79], s66, v250\n"
"v_fma_f32 v190, v190, s65, v239\n"
"v_mfma_f32_16x16x32_bf16 a[24:27], v[216:219], v[200:203], a[24:27]\n"
"v_max3_f32 v251, v251, v163, v188\n"
"v_fma_f32 v191, v191, s65, v239\n"
"v_mfma_f32_16x16x32_bf16 a[28:31], v[216:219], v[204:207], a[28:31]\n"
"s_waitcnt lgkmcnt(8)\n"
"s_cmp_lg_u64 s[78:79], 0\n"
"s_cbranch_scc1 Lslow_14\n"
"Lret_14:\n"
"v_mfma_f32_16x16x32_bf16 a[44:47], v[224:227], v[204:207], a[44:47]\n"
"v_fma_f32 v184, v184, s65, v238\n"
"v_max3_f32 v251, v251, v189, v190\n"
"v_mfma_f32_16x16x32_bf16 a[40:43], v[224:227], v[200:203], a[40:43]\n"
"v_fma_f32 v185, v185, s65, v238\n"
"v_fma_f32 v186, v186, s65, v238\n"
"v_mfma_f32_16x16x32_bf16 a[36:39], v[224:227], v[196:199], a[36:39]\n"
"s_waitcnt vmcnt(12)\n"
"s_barrier\n"
"v_mfma_f32_16x16x32_bf16 a[32:35], v[224:227], v[192:195], a[32:35]\n"
"s_waitcnt lgkmcnt(6)\n"
"ds_read_b128 v[252:255], v248 offset:4096\n"
"ds_read_b128 v[216:219], v248 offset:3072\n"
"v_mfma_f32_16x16x32_bf16 a[48:51], v[220:223], v[192:195], a[48:51]\n"
"ds_read_b64_tr_b16 v[224:225], v247 offset:17184\n"
"ds_read_b64_tr_b16 v[226:227], v247 offset:19232\n"
"v_mfma_f32_16x16x32_bf16 a[52:55], v[220:223], v[196:199], a[52:55]\n"
"ds_read_b32 v250, v248 offset:0\n"
"v_max3_f32 v251, v251, v191, v184\n"
"v_mfma_f32_16x16x32_bf16 a[56:59], v[220:223], v[200:203], a[56:59]\n"
"s_add_u32 m0, s33, 0\n"
"v_fma_f32 v187, v187, s65, v238\n"
"v_fma_f32 v180, v180, s65, v237\n"
"v_mfma_f32_16x16x32_bf16 a[60:63], v[220:223], v[204:207], a[60:63]\n"
"s_waitcnt lgkmcnt(9)\n"
"buffer_load_dwordx4 v249, s[52:55], s68 offen nt lds\n"
"v_mfma_f32_16x16x32_bf16 a[76:79], v[232:235], v[204:207], a[76:79]\n"
"ds_read_b64_tr_b16 v[220:221], v247 offset:17280\n"
"ds_read_b64_tr_b16 v[222:223], v247 offset:19328\n"
"v_mfma_f32_16x16x32_bf16 a[72:75], v[232:235], v[200:203], a[72:75]\n"
"v_max3_f32 v251, v251, v185, v186\n"
"v_fma_f32 v181, v181, s65, v237\n"
"v_mfma_f32_16x16x32_bf16 a[68:71], v[232:235], v[196:199], a[68:71]\n"
"v_fma_f32 v182, v182, s65, v237\n"
"v_max3_f32 v251, v251, v187, v180\n"
"v_mfma_f32_16x16x32_bf16 a[64:67], v[232:235], v[192:195], a[64:67]\n"
"s_waitcnt lgkmcnt(9)\n"
"ds_read_b64_tr_b16 v[232:233], v247 offset:17312\n"
"ds_read_b64_tr_b16 v[234:235], v247 offset:19360\n"
"v_mfma_f32_16x16x32_bf16 a[80:83], v[212:215], v[192:195], a[80:83]\n"
"v_fma_f32 v183, v183, s65, v237\n"
"v_fma_f32 v176, v176, s65, v236\n"
"v_mfma_f32_16x16x32_bf16 a[84:87], v[212:215], v[196:199], a[84:87]\n"
"buffer_load_dwordx4 v249, s[52:55], s68 offen offset:1024 nt lds\n"
"v_mfma_f32_16x16x32_bf16 a[88:91], v[212:215], v[200:203], a[88:91]\n"
"v_max3_f32 v251, v251, v181, v182\n"
"v_fma_f32 v177, v177, s65, v236\n"
"v_mfma_f32_16x16x32_bf16 a[92:95], v[212:215], v[204:207], a[92:95]\n"
"s_waitcnt lgkmcnt(8)\n"
"ds_read_b64_tr_b16 v[212:213], v247 offset:17024\n"
"ds_read_b64_tr_b16 v[214:215], v247 offset:19072\n"
"v_mfma_f32_16x16x32_bf16 v[132:135], v[228:231], v[252:255], v[132:135]\n"
"s_waitcnt lgkmcnt(9)\n"
"v_fma_f32 v178, v178, s65, v236\n"
"v_max3_f32 v251, v251, v183, v176\n"
"v_mfma_f32_16x16x32_bf16 v[128:131], v[228:231], v[216:219], v[128:131]\n"
"v_fma_f32 v179, v179, s65, v236\n"
"v_max3_f32 v251, v251, v177, v178\n"
"v_mfma_f32_16x16x32_bf16 a[140:143], v[228:231], v[204:207], a[140:143]\n"
"s_waitcnt lgkmcnt(6)\n"
"v_max3_f32 v251, v251, v179, v250\n"
"v_cmp_lt_f32 vcc, s66, v251\n"
"v_mfma_f32_16x16x32_bf16 a[136:139], v[228:231], v[200:203], a[136:139]\n"
"buffer_load_dwordx4 v249, s[52:55], s68 offen offset:2048 nt lds\n"
"v_mfma_f32_16x16x32_bf16 a[132:135], v[228:231], v[196:199], a[132:135]\n"
"v_mfma_f32_16x16x32_bf16 a[128:131], v[228:231], v[192:195], a[128:131]\n"
"ds_read_b64_tr_b16 v[228:229], v247 offset:17056\n"
"ds_read_b64_tr_b16 v[230:231], v247 offset:19104\n"
"v_mfma_f32_16x16x32_bf16 a[144:147], v[224:227], v[192:195], a[144:147]\n"
"s_cbranch_vccnz Lslow_15\n"
"Lret_15:\n"
"v_mfma_f32_16x16x32_bf16 a[148:151], v[224:227], v[196:199], a[148:151]\n"
"v_exp_f32 v208, v208\n"
"v_mfma_f32_16x16x32_bf16 a[152:155], v[224:227], v[200:203], a[152:155]\n"
"v_exp_f32 v160, v160\n"
"v_mfma_f32_16x16x32_bf16 a[156:159], v[224:227], v[204:207], a[156:159]\n"
"buffer_load_dwordx4 v249, s[52:55], s68 offen offset:3072 nt lds\n"
"v_mfma_f32_16x16x32_bf16 v[136:139], v[224:227], v[216:219], v[136:139]\n"
"s_add_u32 s68, s68, 0x4000\n"
"v_exp_f32 v164, v164\n"
"v_mfma_f32_16x16x32_bf16 v[140:143], v[224:227], v[252:255], v[140:143]\n"
"s_waitcnt lgkmcnt(6)\n"
"ds_read_b64_tr_b16 v[224:225], v247 offset:17408\n"
"ds_read_b64_tr_b16 v[226:227], v247 offset:19456\n"
"v_mfma_f32_16x16x32_bf16 v[148:151], v[220:223], v[252:255], v[148:151]\n"
"v_exp_f32 v168, v168\n"
"v_mfma_f32_16x16x32_bf16 v[144:147], v[220:223], v[216:219], v[144:147]\n"
"v_exp_f32 v172, v172\n"
"v_mfma_f32_16x16x32_bf16 a[172:175], v[220:223], v[204:207], a[172:175]\n"
"v_exp_f32 v209, v209\n"
"v_mfma_f32_16x16x32_bf16 a[168:171], v[220:223], v[200:203], a[168:171]\n"
"v_exp_f32 v161, v161\n"
"v_mfma_f32_16x16x32_bf16 a[164:167], v[220:223], v[196:199], a[164:167]\n"
"v_exp_f32 v165, v165\n"
"v_mfma_f32_16x16x32_bf16 a[160:163], v[220:223], v[192:195], a[160:163]\n"
"s_waitcnt lgkmcnt(6)\n"
"ds_read_b64_tr_b16 v[220:221], v247 offset:17440\n"
"ds_read_b64_tr_b16 v[222:223], v247 offset:19488\n"
"v_mfma_f32_16x16x32_bf16 a[176:179], v[232:235], v[192:195], a[176:179]\n"
"v_exp_f32 v169, v169\n"
"v_mfma_f32_16x16x32_bf16 a[180:183], v[232:235], v[196:199], a[180:183]\n"
"v_exp_f32 v173, v173\n"
"v_mfma_f32_16x16x32_bf16 a[184:187], v[232:235], v[200:203], a[184:187]\n"
"v_add_f32 v245, v245, v208\n"
"v_add_f32 v241, v241, v160\n"
"v_mfma_f32_16x16x32_bf16 a[188:191], v[232:235], v[204:207], a[188:191]\n"
"v_exp_f32 v210, v210\n"
"v_mfma_f32_16x16x32_bf16 v[152:155], v[232:235], v[216:219], v[152:155]\n"
"ds_read_b64_tr_b16 v[216:217], v247 offset:17536\n"
"ds_read_b64_tr_b16 v[218:219], v247 offset:19584\n"
"v_mfma_f32_16x16x32_bf16 v[156:159], v[232:235], v[252:255], v[156:159]\n"
"s_waitcnt lgkmcnt(8)\n"
"ds_read_b64_tr_b16 v[232:233], v247 offset:17568\n"
"ds_read_b64_tr_b16 v[234:235], v247 offset:19616\n"
"v_mfma_f32_16x16x32_bf16 a[108:111], v[212:215], v[204:207], a[108:111]\n"
"v_exp_f32 v211, v211\n"
"v_mfma_f32_16x16x32_bf16 a[104:107], v[212:215], v[200:203], a[104:107]\n"
"v_exp_f32 v162, v162\n"
"v_mfma_f32_16x16x32_bf16 a[100:103], v[212:215], v[196:199], a[100:103]\n"
"v_add_f32 v242, v242, v164\n"
"v_add_f32 v243, v243, v168\n"
"v_mfma_f32_16x16x32_bf16 a[96:99], v[212:215], v[192:195], a[96:99]\n"
"s_waitcnt lgkmcnt(8)\n"
"ds_read_b128 v[212:215], v246 offset:16640\n"
"v_add_f32 v244, v244, v172\n"
"v_mfma_f32_16x16x32_bf16 a[112:115], v[228:231], v[192:195], a[112:115]\n"
"v_exp_f32 v166, v166\n"
"v_mfma_f32_16x16x32_bf16 a[116:119], v[228:231], v[196:199], a[116:119]\n"
"v_exp_f32 v170, v170\n"
"v_mfma_f32_16x16x32_bf16 a[120:123], v[228:231], v[200:203], a[120:123]\n"
"v_exp_f32 v174, v174\n"
"v_mfma_f32_16x16x32_bf16 a[124:127], v[228:231], v[204:207], a[124:127]\n"
"s_waitcnt lgkmcnt(7)\n"
"v_add_f32 v245, v245, v209\n"
"v_cvt_pk_bf16_f32 v208, v208, v209\n"
"v_mfma_f32_16x16x32_bf16 a[204:207], v[224:227], v[204:207], a[204:207]\n"
"v_add_f32 v241, v241, v161\n"
"v_add_f32 v242, v242, v165\n"
"v_mfma_f32_16x16x32_bf16 a[200:203], v[224:227], v[200:203], a[200:203]\n"
"v_exp_f32 v163, v163\n"
"v_mfma_f32_16x16x32_bf16 a[196:199], v[224:227], v[196:199], a[196:199]\n"
"v_exp_f32 v167, v167\n"
"v_mfma_f32_16x16x32_bf16 a[192:195], v[224:227], v[192:195], a[192:195]\n"
"s_waitcnt lgkmcnt(5)\n"
"v_add_f32 v243, v243, v169\n"
"v_add_f32 v244, v244, v173\n"
"v_mfma_f32_16x16x32_bf16 a[208:211], v[220:223], v[192:195], a[208:211]\n"
"v_exp_f32 v171, v171\n"
"v_mfma_f32_16x16x32_bf16 a[212:215], v[220:223], v[196:199], a[212:215]\n"
"v_exp_f32 v175, v175\n"
"v_mfma_f32_16x16x32_bf16 a[216:219], v[220:223], v[200:203], a[216:219]\n"
"v_add_f32 v245, v245, v210\n"
"v_cvt_pk_bf16_f32 v209, v210, v211\n"
"v_mfma_f32_16x16x32_bf16 a[220:223], v[220:223], v[204:207], a[220:223]\n"
"s_waitcnt lgkmcnt(3)\n"
"v_add_f32 v241, v241, v162\n"
"v_add_f32 v242, v242, v166\n"
"v_mfma_f32_16x16x32_bf16 a[236:239], v[216:219], v[204:207], a[236:239]\n"
"v_add_f32 v243, v243, v170\n"
"v_add_f32 v244, v244, v174\n"
"v_mfma_f32_16x16x32_bf16 a[232:235], v[216:219], v[200:203], a[232:235]\n"
"v_add_u32 v247, s30, v247\n"
"v_add_f32 v245, v245, v211\n"
"v_mfma_f32_16x16x32_bf16 a[228:231], v[216:219], v[196:199], a[228:231]\n"
"s_sub_u32 s30, 0, s30\n"
"ds_write_b64 v248, v[208:209] offset:1032\n"
"v_mfma_f32_16x16x32_bf16 a[224:227], v[216:219], v[192:195], a[224:227]\n"
"s_waitcnt lgkmcnt(2)\n"
"ds_read_b128 v[216:219], v246 offset:16768\n"
"ds_read_b128 v[220:223], v246 offset:16896\n"
"v_mfma_f32_16x16x32_bf16 a[240:243], v[232:235], v[192:195], a[240:243]\n"
"ds_read_b128 v[224:227], v246 offset:17024\n"
"v_add_f32 v241, v241, v163\n"
"v_mfma_f32_16x16x32_bf16 a[244:247], v[232:235], v[196:199], a[244:247]\n"
"s_sub_u32 s70, s70, 1\n"
"v_add_f32 v242, v242, v167\n"
"v_add_f32 v243, v243, v171\n"
"v_mfma_f32_16x16x32_bf16 a[248:251], v[232:235], v[200:203], a[248:251]\n"
"v_add_f32 v244, v244, v175\n"
"v_cvt_pk_bf16_f32 v192, v160, v161\n"
"v_mfma_f32_16x16x32_bf16 a[252:255], v[232:235], v[204:207], a[252:255]\n"
"v_cvt_pk_bf16_f32 v193, v162, v163\n"
"v_cvt_pk_bf16_f32 v196, v164, v165\n"
"v_cvt_pk_bf16_f32 v197, v166, v167\n"
"v_cvt_pk_bf16_f32 v200, v168, v169\n"
"v_cvt_pk_bf16_f32 v201, v170, v171\n"
"v_cvt_pk_bf16_f32 v204, v172, v173\n"
"v_cvt_pk_bf16_f32 v205, v174, v175\n"
"s_cmp_lg_u32 s70, 0\n"
"s_cbranch_scc1 Lloop2\n"
"s_branch Lloopx\n"
"Lloop3:\n"
"s_waitcnt lgkmcnt(3)\n"
"v_mfma_f32_16x16x32_bf16 v[160:163], v[212:215], v[0:3], 0\n"
"s_waitcnt lgkmcnt(2)\n"
"ds_read_b128 v[228:231], v246 offset:17152\n"
"ds_read_b128 v[232:235], v246 offset:17280\n"
"v_mfma_f32_16x16x32_bf16 v[160:163], v[216:219], v[32:35], v[160:163]\n"
"ds_read_b128 v[252:255], v246 offset:17408\n"
"v_mfma_f32_16x16x32_bf16 v[164:167], v[216:219], v[40:43], 0\n"
"v_exp_f32 v176, v176\n"
"v_mfma_f32_16x16x32_bf16 v[164:167], v[212:215], v[8:11], v[164:167]\n"
"v_exp_f32 v177, v177\n"
"v_mfma_f32_16x16x32_bf16 v[168:171], v[212:215], v[16:19], 0\n"
"v_add_f32 v241, v241, v176\n"
"v_add_f32 v241, v241, v177\n"
"v_mfma_f32_16x16x32_bf16 v[168:171], v[216:219], v[48:51], v[168:171]\n"
"v_exp_f32 v178, v178\n"
"v_mfma_f32_16x16x32_bf16 v[172:175], v[216:219], v[56:59], 0\n"
"ds_read_b128 v[216:219], v246 offset:17536\n"
"v_add_f32 v241, v241, v178\n"
"v_mfma_f32_16x16x32_bf16 v[172:175], v[212:215], v[24:27], v[172:175]\n"
"s_waitcnt lgkmcnt(5)\n"
"ds_read_b128 v[212:215], v246 offset:18688\n"
"v_cvt_pk_bf16_f32 v194, v176, v177\n"
"v_mfma_f32_16x16x32_bf16 v[172:175], v[220:223], v[88:91], v[172:175]\n"
"s_waitcnt lgkmcnt(5)\n"
"v_exp_f32 v179, v179\n"
"v_mfma_f32_16x16x32_bf16 v[172:175], v[224:227], v[120:123], v[172:175]\n"
"v_add_f32 v241, v241, v179\n"
"v_cvt_pk_bf16_f32 v195, v178, v179\n"
"v_mfma_f32_16x16x32_bf16 v[168:171], v[224:227], v[112:115], v[168:171]\n"
"v_exp_f32 v180, v180\n"
"v_mfma_f32_16x16x32_bf16 v[168:171], v[220:223], v[80:83], v[168:171]\n"
"v_exp_f32 v181, v181\n"
"v_mfma_f32_16x16x32_bf16 v[164:167], v[220:223], v[72:75], v[164:167]\n"
"v_add_f32 v242, v242, v180\n"
"v_add_f32 v242, v242, v181\n"
"v_mfma_f32_16x16x32_bf16 v[164:167], v[224:227], v[104:107], v[164:167]\n"
"v_exp_f32 v182, v182\n"
"v_mfma_f32_16x16x32_bf16 v[160:163], v[224:227], v[96:99], v[160:163]\n"
"ds_read_b128 v[224:227], v246 offset:18816\n"
"v_add_f32 v242, v242, v182\n"
"v_mfma_f32_16x16x32_bf16 v[160:163], v[220:223], v[64:67], v[160:163]\n"
"s_waitcnt lgkmcnt(5)\n"
"ds_read_b128 v[220:223], v248 offset:13312\n"
"v_cvt_pk_bf16_f32 v198, v180, v181\n"
"v_mfma_f32_16x16x32_bf16 v[160:163], v[228:231], v[4:7], v[160:163]\n"
"s_waitcnt lgkmcnt(5)\n"
"v_exp_f32 v183, v183\n"
"v_mfma_f32_16x16x32_bf16 v[160:163], v[232:235], v[36:39], v[160:163]\n"
"v_add_f32 v242, v242, v183\n"
"v_cvt_pk_bf16_f32 v199, v182, v183\n"
"v_mfma_f32_16x16x32_bf16 v[164:167], v[232:235], v[44:47], v[164:167]\n"
"v_exp_f32 v184, v184\n"
"v_mfma_f32_16x16x32_bf16 v[164:167], v[228:231], v[12:15], v[164:167]\n"
"v_exp_f32 v185, v185\n"
"v_mfma_f32_16x16x32_bf16 v[168:171], v[228:231], v[20:23], v[168:171]\n"
"v_add_f32 v243, v243, v184\n"
"v_add_f32 v243, v243, v185\n"
"v_mfma_f32_16x16x32_bf16 v[168:171], v[232:235], v[52:55], v[168:171]\n"
"v_exp_f32 v186, v186\n"
"v_mfma_f32_16x16x32_bf16 v[172:175], v[232:235], v[60:63], v[172:175]\n"
"ds_read_b128 v[232:235], v248 offset:14336\n"
"v_add_f32 v243, v243, v186\n"
"v_mfma_f32_16x16x32_bf16 v[172:175], v[228:231], v[28:31], v[172:175]\n"
"s_waitcnt lgkmcnt(5)\n"
"ds_read_b128 v[228:231], v246 offset:18944\n"
"v_cvt_pk_bf16_f32 v202, v184, v185\n"
"v_mfma_f32_16x16x32_bf16 v[172:175], v[252:255], v[92:95], v[172:175]\n"
"s_waitcnt lgkmcnt(5)\n"
"v_exp_f32 v187, v187\n"
"v_mfma_f32_16x16x32_bf16 v[172:175], v[216:219], v[124:127], v[172:175]\n"
"v_add_f32 v243, v243, v187\n"
"v_cvt_pk_bf16_f32 v203, v186, v187\n"
"v_mfma_f32_16x16x32_bf16 v[168:171], v[216:219], v[116:119], v[168:171]\n"
"v_exp_f32 v188, v188\n"
"v_mfma_f32_16x16x32_bf16 v[168:171], v[252:255], v[84:87], v[168:171]\n"
"v_exp_f32 v189, v189\n"
"v_mfma_f32_16x16x32_bf16 v[164:167], v[252:255], v[76:79], v[164:167]\n"
"v_add_f32 v244, v244, v188\n"
"v_add_f32 v244, v244, v189\n"
"v_mfma_f32_16x16x32_bf16 v[164:167], v[216:219], v[108:111], v[164:167]\n"
"v_exp_f32 v190, v190\n"
"v_mfma_f32_16x16x32_bf16 v[160:163], v[216:219], v[100:103], v[160:163]\n"
"ds_read_b128 v[216:219], v246 offset:19072\n"
"v_add_f32 v244, v244, v190\n"
"v_mfma_f32_16x16x32_bf16 v[160:163], v[252:255], v[68:71], v[160:163]\n"
"s_waitcnt lgkmcnt(5)\n"
"v_exp_f32 v191, v191\n"
"v_mfma_f32_16x16x32_bf16 v[176:179], v[212:215], v[0:3], 0\n"
"s_waitcnt lgkmcnt(4)\n"
"v_add_f32 v244, v244, v191\n"
"v_cvt_pk_bf16_f32 v206, v188, v189\n"
"v_mfma_f32_16x16x32_bf16 v[176:179], v[224:227], v[32:35], v[176:179]\n"
"ds_read_b128 v[252:255], v248 offset:15360\n"
"v_cvt_pk_bf16_f32 v207, v190, v191\n"
"v_mfma_f32_16x16x32_bf16 v[180:183], v[224:227], v[40:43], 0\n"
"v_fma_f32 v172, v172, s65, v239\n"
"v_fma_f32 v173, v173, s65, v239\n"
"v_mfma_f32_16x16x32_bf16 v[180:183], v[212:215], v[8:11], v[180:183]\n"
"v_fma_f32 v174, v174, s65, v239\n"
"v_fma_f32 v175, v175, s65, v239\n"
"v_mfma_f32_16x16x32_bf16 v[184:187], v[212:215], v[16:19], 0\n"
"s_add_u32 m0, s31, 0\n"
"v_fma_f32 v168, v168, s65, v238\n"
"v_max3_f32 v251, v172, v173, v174\n"
"v_mfma_f32_16x16x32_bf16 v[184:187], v[224:227], v[48:51], v[184:187]\n"
"buffer_load_dwordx4 v249, s[48:51], s67 offen nt lds\n"
"v_mfma_f32_16x16x32_bf16 v[188:191], v[224:227], v[56:59], 0\n"
"v_fma_f32 v169, v169, s65, v238\n"
"v_fma_f32 v170, v170, s65, v238\n"
"v_mfma_f32_16x16x32_bf16 v[188:191], v[212:215], v[24:27], v[188:191]\n"
"s_waitcnt lgkmcnt(4)\n"
"v_max3_f32 v251, v251, v175, v168\n"
"v_fma_f32 v171, v171, s65, v238\n"
"v_mfma_f32_16x16x32_bf16 v[208:211], v[212:215], v[220:223], 0\n"
"s_waitcnt lgkmcnt(3)\n"
"ds_read_b128 v[212:215], v248 offset:16384\n"
"ds_read_b128 v[220:223], v246 offset:19200\n"
"v_mfma_f32_16x16x32_bf16 v[208:211], v[224:227], v[232:235], v[208:211]\n"
"s_waitcnt lgkmcnt(4)\n"
"ds_read_b128 v[224:227], v246 offset:19328\n"
"ds_read_b128 v[232:235], v248 offset:17408\n"
"v_mfma_f32_16x16x32_bf16 v[188:191], v[228:231], v[88:91], v[188:191]\n"
"s_waitcnt lgkmcnt(5)\n"
"v_fma_f32 v164, v164, s65, v237\n"
"v_max3_f32 v251, v251, v169, v170\n"
"v_mfma_f32_16x16x32_bf16 v[188:191], v[216:219], v[120:123], v[188:191]\n"
"buffer_load_dwordx4 v249, s[48:51], s67 offen offset:1024 nt lds\n"
"v_mfma_f32_16x16x32_bf16 v[184:187], v[216:219], v[112:115], v[184:187]\n"
"s_cmp_lg_u64 s[76:77], 0\n"
"s_cbranch_scc1 Lslow_16\n"
"Lret_16:\n"
"v_mfma_f32_16x16x32_bf16 v[184:187], v[228:231], v[80:83], v[184:187]\n"
"v_fma_f32 v165, v165, s65, v237\n"
"v_fma_f32 v166, v166, s65, v237\n"
"v_mfma_f32_16x16x32_bf16 v[180:183], v[228:231], v[72:75], v[180:183]\n"
"v_max3_f32 v251, v251, v171, v164\n"
"v_fma_f32 v167, v167, s65, v237\n"
"v_mfma_f32_16x16x32_bf16 v[180:183], v[216:219], v[104:107], v[180:183]\n"
"v_fma_f32 v160, v160, s65, v236\n"
"v_max3_f32 v251, v251, v165, v166\n"
"v_mfma_f32_16x16x32_bf16 v[176:179], v[216:219], v[96:99], v[176:179]\n"
"v_fma_f32 v161, v161, s65, v236\n"
"v_fma_f32 v162, v162, s65, v236\n"
"v_mfma_f32_16x16x32_bf16 v[176:179], v[228:231], v[64:67], v[176:179]\n"
"s_waitcnt lgkmcnt(4)\n"
"buffer_load_dwordx4 v249, s[48:51], s67 offen offset:2048 nt lds\n"
"v_mfma_f32_16x16x32_bf16 v[208:211], v[228:231], v[252:255], v[208:211]\n"
"s_waitcnt lgkmcnt(3)\n"
"ds_read_b128 v[228:231], v248 offset:18432\n"
"ds_read_b128 v[252:255], v246 offset:19456\n"
"v_mfma_f32_16x16x32_bf16 v[208:211], v[216:219], v[212:215], v[208:211]\n"
"s_waitcnt lgkmcnt(4)\n"
"ds_read_b128 v[212:215], v246 offset:19584\n"
"ds_read_b128 v[216:219], v248 offset:19456\n"
"v_mfma_f32_16x16x32_bf16 v[176:179], v[220:223], v[4:7], v[176:179]\n"
"s_waitcnt lgkmcnt(5)\n"
"v_add_u32 v246, s34, v246\n"
"v_max3_f32 v251, v251, v167, v160\n"
"v_mfma_f32_16x16x32_bf16 v[176:179], v[224:227], v[36:39], v[176:179]\n"
"s_sub_u32 s34, 0, s34\n"
"v_fma_f32 v163, v163, s65, v236\n"
"v_max3_f32 v251, v251, v161, v162\n"
"v_mfma_f32_16x16x32_bf16 v[180:183], v[224:227], v[44:47], v[180:183]\n"
"v_mfma_f32_16x16x32_bf16 v[180:183], v[220:223], v[12:15], v[180:183]\n"
"buffer_load_dwordx4 v249, s[48:51], s67 offen offset:3072 nt lds\n"
"v_mfma_f32_16x16x32_bf16 v[184:187], v[220:223], v[20:23], v[184:187]\n"
"s_add_u32 s67, s67, 0x4000\n"
"v_mfma_f32_16x16x32_bf16 v[184:187], v[224:227], v[52:55], v[184:187]\n"
"v_mfma_f32_16x16x32_bf16 v[188:191], v[224:227], v[60:63], v[188:191]\n"
"v_mfma_f32_16x16x32_bf16 v[188:191], v[220:223], v[28:31], v[188:191]\n"
"s_waitcnt lgkmcnt(4)\n"
"v_mfma_f32_16x16x32_bf16 v[208:211], v[220:223], v[232:235], v[208:211]\n"
"s_waitcnt lgkmcnt(3)\n"
"ds_read_b128 v[220:223], v248 offset:20480\n"
"ds_read_b64_tr_b16 v[232:233], v247 offset:0\n"
"v_mfma_f32_16x16x32_bf16 v[208:211], v[224:227], v[228:231], v[208:211]\n"
"s_waitcnt lgkmcnt(4)\n"
"ds_read_b64_tr_b16 v[234:235], v247 offset:2048\n"
"ds_read_b64_tr_b16 v[224:225], v247 offset:32\n"
"v_mfma_f32_16x16x32_bf16 v[188:191], v[252:255], v[92:95], v[188:191]\n"
"s_waitcnt lgkmcnt(5)\n"
"ds_read_b64_tr_b16 v[226:227], v247 offset:2080\n"
"ds_read_b64_tr_b16 v[228:229], v247 offset:128\n"
"v_mfma_f32_16x16x32_bf16 v[188:191], v[212:215], v[124:127], v[188:191]\n"
"ds_read_b64_tr_b16 v[230:231], v247 offset:2176\n"
"v_mfma_f32_16x16x32_bf16 v[184:187], v[212:215], v[116:119], v[184:187]\n"
"v_mfma_f32_16x16x32_bf16 v[184:187], v[252:255], v[84:87], v[184:187]\n"
"v_mfma_f32_16x16x32_bf16 v[180:183], v[252:255], v[76:79], v[180:183]\n"
"v_mfma_f32_16x16x32_bf16 v[180:183], v[212:215], v[108:111], v[180:183]\n"
"v_mfma_f32_16x16x32_bf16 v[176:179], v[212:215], v[100:103], v[176:179]\n"
"v_mfma_f32_16x16x32_bf16 v[176:179], v[252:255], v[68:71], v[176:179]\n"
"s_waitcnt lgkmcnt(7)\n"
"v_fma_f32 v188, v188, s65, v239\n"
"v_fma_f32 v189, v189, s65, v239\n"
"v_mfma_f32_16x16x32_bf16 v[208:211], v[252:255], v[216:219], v[208:211]\n"
"s_waitcnt lgkmcnt(6)\n"
"ds_read_b64_tr_b16 v[216:217], v247 offset:160\n"
"ds_read_b64_tr_b16 v[218:219], v247 offset:2208\n"
"v_mfma_f32_16x16x32_bf16 v[208:211], v[212:215], v[220:223], v[208:211]\n"
"s_waitcnt lgkmcnt(6)\n"
"ds_read_b64_tr_b16 v[252:253], v247 offset:256\n"
"ds_read_b64_tr_b16 v[254:255], v247 offset:2304\n"
"v_mfma_f32_16x16x32_bf16 a[12:15], v[232:235], v[204:207], a[12:15]\n"
"ds_read_b64_tr_b16 v[212:213], v247 offset:288\n"
"ds_read_b64_tr_b16 v[214:215], v247 offset:2336\n"
"v_mfma_f32_16x16x32_bf16 a[8:11], v[232:235], v[200:203], a[8:11]\n"
"ds_read_b64_tr_b16 v[220:221], v247 offset:768\n"
"v_fma_f32 v208, v208, s65, v240\n"
"v_mfma_f32_16x16x32_bf16 a[4:7], v[232:235], v[196:199], a[4:7]\n"
"v_fma_f32 v209, v209, s65, v240\n"
"v_fma_f32 v210, v210, s65, v240\n"
"v_mfma_f32_16x16x32_bf16 a[0:3], v[232:235], v[192:195], a[0:3]\n"
"s_waitcnt lgkmcnt(9)\n"
"v_fma_f32 v211, v211, s65, v240\n"
"v_max3_f32 v250, v208, v209, v210\n"
"v_mfma_f32_16x16x32_bf16 a[16:19], v[224:227], v[192:195], a[16:19]\n"
"ds_read_b64_tr_b16 v[222:223], v247 offset:2816\n"
"v_max_f32 v250, v250, v211\n"
"v_mfma_f32_16x16x32_bf16 a[20:23], v[224:227], v[196:199], a[20:23]\n"
"v_cmp_lt_f32_e64 s[78:79], s66, v250\n"
"v_fma_f32 v190, v190, s65, v239\n"
"v_mfma_f32_16x16x32_bf16 a[24:27], v[224:227], v[200:203], a[24:27]\n"
"v_max3_f32 v251, v251, v163, v188\n"
"v_fma_f32 v191, v191, s65, v239\n"
"v_mfma_f32_16x16x32_bf16 a[28:31], v[224:227], v[204:207], a[28:31]\n"
"s_waitcnt lgkmcnt(8)\n"
"s_cmp_lg_u64 s[78:79], 0\n"
"s_cbranch_scc1 Lslow_17\n"
"Lret_17:\n"
"v_mfma_f32_16x16x32_bf16 a[44:47], v[228:231], v[204:207], a[44:47]\n"
"v_fma_f32 v184, v184, s65, v238\n"
"v_max3_f32 v251, v251, v189, v190\n"
"v_mfma_f32_16x16x32_bf16 a[40:43], v[228:231], v[200:203], a[40:43]\n"
"v_fma_f32 v185, v185, s65, v238\n"
"v_fma_f32 v186, v186, s65, v238\n"
"v_mfma_f32_16x16x32_bf16 a[36:39], v[228:231], v[196:199], a[36:39]\n"
"s_waitcnt vmcnt(12)\n"
"s_barrier\n"
"v_mfma_f32_16x16x32_bf16 a[32:35], v[228:231], v[192:195], a[32:35]\n"
"s_waitcnt lgkmcnt(6)\n"
"ds_read_b128 v[232:235], v248 offset:2048\n"
"ds_read_b128 v[224:227], v248 offset:1024\n"
"v_mfma_f32_16x16x32_bf16 a[48:51], v[216:219], v[192:195], a[48:51]\n"
"ds_read_b64_tr_b16 v[228:229], v247 offset:800\n"
"ds_read_b64_tr_b16 v[230:231], v247 offset:2848\n"
"v_mfma_f32_16x16x32_bf16 a[52:55], v[216:219], v[196:199], a[52:55]\n"
"ds_read_b32 v250, v248 offset:12\n"
"v_max3_f32 v251, v251, v191, v184\n"
"v_mfma_f32_16x16x32_bf16 a[56:59], v[216:219], v[200:203], a[56:59]\n"
"s_add_u32 m0, s33, 0x4100\n"
"v_fma_f32 v187, v187, s65, v238\n"
"v_fma_f32 v180, v180, s65, v237\n"
"v_mfma_f32_16x16x32_bf16 a[60:63], v[216:219], v[204:207], a[60:63]\n"
"s_waitcnt lgkmcnt(9)\n"
"buffer_load_dwordx4 v249, s[52:55], s68 offen nt lds\n"
"v_mfma_f32_16x16x32_bf16 a[76:79], v[252:255], v[204:207], a[76:79]\n"
"s_sub_u32 s33, s33, s30\n"
"ds_read_b64_tr_b16 v[216:217], v247 offset:896\n"
"ds_read_b64_tr_b16 v[218:219], v247 offset:2944\n"
"v_mfma_f32_16x16x32_bf16 a[72:75], v[252:255], v[200:203], a[72:75]\n"
"v_max3_f32 v251, v251, v185, v186\n"
"v_fma_f32 v181, v181, s65, v237\n"
"v_mfma_f32_16x16x32_bf16 a[68:71], v[252:255], v[196:199], a[68:71]\n"
"v_fma_f32 v182, v182, s65, v237\n"
"v_max3_f32 v251, v251, v187, v180\n"
"v_mfma_f32_16x16x32_bf16 a[64:67], v[252:255], v[192:195], a[64:67]\n"
"s_waitcnt lgkmcnt(9)\n"
"ds_read_b64_tr_b16 v[252:253], v247 offset:928\n"
"ds_read_b64_tr_b16 v[254:255], v247 offset:2976\n"
"v_mfma_f32_16x16x32_bf16 a[80:83], v[212:215], v[192:195], a[80:83]\n"
"v_fma_f32 v183, v183, s65, v237\n"
"v_fma_f32 v176, v176, s65, v236\n"
"v_mfma_f32_16x16x32_bf16 a[84:87], v[212:215], v[196:199], a[84:87]\n"
"buffer_load_dwordx4 v249, s[52:55], s68 offen offset:1024 nt lds\n"
"v_mfma_f32_16x16x32_bf16 a[88:91], v[212:215], v[200:203], a[88:91]\n"
"v_max3_f32 v251, v251, v181, v182\n"
"v_fma_f32 v177, v177, s65, v236\n"
"v_mfma_f32_16x16x32_bf16 a[92:95], v[212:215], v[204:207], a[92:95]\n"
"s_waitcnt lgkmcnt(8)\n"
"ds_read_b64_tr_b16 v[212:213], v247 offset:384\n"
"ds_read_b64_tr_b16 v[214:215], v247 offset:2432\n"
"v_mfma_f32_16x16x32_bf16 v[132:135], v[220:223], v[232:235], v[132:135]\n"
"s_waitcnt lgkmcnt(9)\n"
"v_fma_f32 v178, v178, s65, v236\n"
"v_max3_f32 v251, v251, v183, v176\n"
"v_mfma_f32_16x16x32_bf16 v[128:131], v[220:223], v[224:227], v[128:131]\n"
"v_fma_f32 v179, v179, s65, v236\n"
"v_max3_f32 v251, v251, v177, v178\n"
"v_mfma_f32_16x16x32_bf16 a[204:207], v[220:223], v[204:207], a[204:207]\n"
"s_waitcnt lgkmcnt(6)\n"
"v_max3_f32 v251, v251, v179, v250\n"
"v_cmp_lt_f32 vcc, s66, v251\n"
"v_mfma_f32_16x16x32_bf16 a[200:203], v[220:223], v[200:203], a[200:203]\n"
"buffer_load_dwordx4 v249, s[52:55], s68 offen offset:2048 nt lds\n"
"v_mfma_f32_16x16x32_bf16 a[196:199], v[220:223], v[196:199], a[196:199]\n"
"v_mfma_f32_16x16x32_bf16 a[192:195], v[220:223], v[192:195], a[192:195]\n"
"ds_read_b64_tr_b16 v[220:221], v247 offset:416\n"
"ds_read_b64_tr_b16 v[222:223], v247 offset:2464\n"
"v_mfma_f32_16x16x32_bf16 a[208:211], v[228:231], v[192:195], a[208:211]\n"
"s_cbranch_vccnz Lslow_18\n"
"Lret_18:\n"
"v_mfma_f32_16x16x32_bf16 a[212:215], v[228:231], v[196:199], a[212:215]\n"
"v_exp_f32 v160, v160\n"
"v_mfma_f32_16x16x32_bf16 a[216:219], v[228:231], v[200:203], a[216:219]\n"
"v_exp_f32 v161, v161\n"
"v_mfma_f32_16x16x32_bf16 a[220:223], v[228:231], v[204:207], a[220:223]\n"
"buffer_load_dwordx4 v249, s[52:55], s68 offen offset:3072 nt lds\n"
"v_mfma_f32_16x16x32_bf16 v[136:139], v[228:231], v[224:227], v[136:139]\n"
"s_add_u32 s68, s68, 0x4000\n"
"v_add_f32 v241, v241, v160\n"
"v_add_f32 v241, v241, v161\n"
"v_mfma_f32_16x16x32_bf16 v[140:143], v[228:231], v[232:235], v[140:143]\n"
"s_waitcnt lgkmcnt(6)\n"
"ds_read_b64_tr_b16 v[228:229], v247 offset:512\n"
"ds_read_b64_tr_b16 v[230:231], v247 offset:2560\n"
"v_mfma_f32_16x16x32_bf16 v[148:151], v[216:219], v[232:235], v[148:151]\n"
"v_exp_f32 v162, v162\n"
"v_mfma_f32_16x16x32_bf16 v[144:147], v[216:219], v[224:227], v[144:147]\n"
"v_exp_f32 v163, v163\n"
"v_mfma_f32_16x16x32_bf16 a[236:239], v[216:219], v[204:207], a[236:239]\n"
"v_add_f32 v241, v241, v162\n"
"v_add_f32 v241, v241, v163\n"
"v_mfma_f32_16x16x32_bf16 a[232:235], v[216:219], v[200:203], a[232:235]\n"
"v_exp_f32 v164, v164\n"
"v_mfma_f32_16x16x32_bf16 a[228:231], v[216:219], v[196:199], a[228:231]\n"
"v_exp_f32 v165, v165\n"
"v_mfma_f32_16x16x32_bf16 a[224:227], v[216:219], v[192:195], a[224:227]\n"
"s_waitcnt lgkmcnt(6)\n"
"ds_read_b64_tr_b16 v[216:217], v247 offset:544\n"
"ds_read_b64_tr_b16 v[218:219], v247 offset:2592\n"
"v_mfma_f32_16x16x32_bf16 a[240:243], v[252:255], v[192:195], a[240:243]\n"
"v_add_f32 v242, v242, v164\n"
"v_add_f32 v242, v242, v165\n"
"v_mfma_f32_16x16x32_bf16 a[244:247], v[252:255], v[196:199], a[244:247]\n"
"v_exp_f32 v166, v166\n"
"v_mfma_f32_16x16x32_bf16 a[248:251], v[252:255], v[200:203], a[248:251]\n"
"v_exp_f32 v167, v167\n"
"v_mfma_f32_16x16x32_bf16 a[252:255], v[252:255], v[204:207], a[252:255]\n"
"v_add_f32 v242, v242, v166\n"
"v_add_f32 v242, v242, v167\n"
"v_mfma_f32_16x16x32_bf16 v[152:155], v[252:255], v[224:227], v[152:155]\n"
"ds_read_b64_tr_b16 v[224:225], v247 offset:640\n"
"ds_read_b64_tr_b16 v[226:227], v247 offset:2688\n"
"v_mfma_f32_16x16x32_bf16 v[156:159], v[252:255], v[232:235], v[156:159]\n"
"s_waitcnt lgkmcnt(8)\n"
"ds_read_b64_tr_b16 v[232:233], v247 offset:672\n"
"ds_read_b64_tr_b16 v[234:235], v247 offset:2720\n"
"v_mfma_f32_16x16x32_bf16 a[108:111], v[212:215], v[204:207], a[108:111]\n"
"ds_read_b128 v[252:255], v246 offset:0\n"
"v_mfma_f32_16x16x32_bf16 a[104:107], v[212:215], v[200:203], a[104:107]\n"
"v_exp_f32 v168, v168\n"
"v_mfma_f32_16x16x32_bf16 a[100:103], v[212:215], v[196:199], a[100:103]\n"
"v_exp_f32 v169, v169\n"
"v_mfma_f32_16x16x32_bf16 a[96:99], v[212:215], v[192:195], a[96:99]\n"
"s_waitcnt lgkmcnt(9)\n"
"ds_read_b128 v[212:215], v246 offset:128\n"
"v_add_f32 v243, v243, v168\n"
"v_mfma_f32_16x16x32_bf16 a[112:115], v[220:223], v[192:195], a[112:115]\n"
"v_exp_f32 v170, v170\n"
"v_mfma_f32_16x16x32_bf16 a[116:119], v[220:223], v[196:199], a[116:119]\n"
"v_add_f32 v243, v243, v169\n"
"v_add_f32 v243, v243, v170\n"
"v_mfma_f32_16x16x32_bf16 a[120:123], v[220:223], v[200:203], a[120:123]\n"
"v_exp_f32 v171, v171\n"
"v_mfma_f32_16x16x32_bf16 a[124:127], v[220:223], v[204:207], a[124:127]\n"
"s_waitcnt lgkmcnt(8)\n"
"ds_read_b128 v[220:223], v246 offset:256\n"
"v_add_f32 v243, v243, v171\n"
"v_mfma_f32_16x16x32_bf16 a[140:143], v[228:231], v[204:207], a[140:143]\n"
"v_exp_f32 v172, v172\n"
"v_mfma_f32_16x16x32_bf16 a[136:139], v[228:231], v[200:203], a[136:139]\n"
"v_exp_f32 v173, v173\n"
"v_mfma_f32_16x16x32_bf16 a[132:135], v[228:231], v[196:199], a[132:135]\n"
"v_add_f32 v244, v244, v172\n"
"v_add_f32 v244, v244, v173\n"
"v_mfma_f32_16x16x32_bf16 a[128:131], v[228:231], v[192:195], a[128:131]\n"
"s_waitcnt lgkmcnt(7)\n"
"ds_read_b128 v[228:231], v246 offset:384\n"
"v_mfma_f32_16x16x32_bf16 a[144:147], v[216:219], v[192:195], a[144:147]\n"
"v_exp_f32 v174, v174\n"
"v_mfma_f32_16x16x32_bf16 a[148:151], v[216:219], v[196:199], a[148:151]\n"
"v_exp_f32 v175, v175\n"
"v_mfma_f32_16x16x32_bf16 a[152:155], v[216:219], v[200:203], a[152:155]\n"
"v_add_f32 v244, v244, v174\n"
"v_add_f32 v244, v244, v175\n"
"v_mfma_f32_16x16x32_bf16 a[156:159], v[216:219], v[204:207], a[156:159]\n"
"s_waitcnt lgkmcnt(6)\n"
"ds_read_b128 v[216:219], v246 offset:512\n"
"v_mfma_f32_16x16x32_bf16 a[172:175], v[224:227], v[204:207], a[172:175]\n"
"v_exp_f32 v176, v176\n"
"v_mfma_f32_16x16x32_bf16 a[168:171], v[224:227], v[200:203], a[168:171]\n"
"v_exp_f32 v177, v177\n"
"v_mfma_f32_16x16x32_bf16 a[164:167], v[224:227], v[196:199], a[164:167]\n"
"v_add_f32 v241, v241, v176\n"
"v_add_f32 v241, v241, v177\n"
"v_mfma_f32_16x16x32_bf16 a[160:163], v[224:227], v[192:195], a[160:163]\n"
"s_waitcnt lgkmcnt(5)\n"
"ds_read_b128 v[224:227], v246 offset:640\n"
"v_mfma_f32_16x16x32_bf16 a[176:179], v[232:235], v[192:195], a[176:179]\n"
"v_cvt_pk_bf16_f32 v192, v160, v161\n"
"v_cvt_pk_bf16_f32 v193, v162, v163\n"
"v_mfma_f32_16x16x32_bf16 a[180:183], v[232:235], v[196:199], a[180:183]\n"
"v_cvt_pk_bf16_f32 v196, v164, v165\n"
"v_cvt_pk_bf16_f32 v197, v166, v167\n"
"v_mfma_f32_16x16x32_bf16 a[184:187], v[232:235], v[200:203], a[184:187]\n"
"v_cvt_pk_bf16_f32 v200, v168, v169\n"
"v_cvt_pk_bf16_f32 v201, v170, v171\n"
"v_mfma_f32_16x16x32_bf16 a[188:191], v[232:235], v[204:207], a[188:191]\n"
"s_waitcnt lgkmcnt(5)\n"
"v_cvt_pk_bf16_f32 v204, v172, v173\n"
"v_cvt_pk_bf16_f32 v205, v174, v175\n"
"v_mfma_f32_16x16x32_bf16 v[160:163], v[252:255], v[0:3], 0\n"
"s_waitcnt lgkmcnt(4)\n"
"ds_read_b128 v[232:235], v246 offset:768\n"
"v_cvt_pk_bf16_f32 v194, v176, v177\n"
"v_mfma_f32_16x16x32_bf16 v[160:163], v[212:215], v[32:35], v[160:163]\n"
"v_exp_f32 v178, v178\n"
"v_mfma_f32_16x16x32_bf16 v[164:167], v[212:215], v[40:43], 0\n"
"v_exp_f32 v179, v179\n"
"v_mfma_f32_16x16x32_bf16 v[164:167], v[252:255], v[8:11], v[164:167]\n"
"v_add_f32 v241, v241, v178\n"
"v_add_f32 v241, v241, v179\n"
"v_mfma_f32_16x16x32_bf16 v[168:171], v[252:255], v[16:19], 0\n"
"v_cvt_pk_bf16_f32 v195, v178, v179\n"
"v_mfma_f32_16x16x32_bf16 v[168:171], v[212:215], v[48:51], v[168:171]\n"
"v_exp_f32 v180, v180\n"
"v_mfma_f32_16x16x32_bf16 v[172:175], v[212:215], v[56:59], 0\n"
"ds_read_b128 v[212:215], v246 offset:896\n"
"v_add_f32 v242, v242, v180\n"
"v_mfma_f32_16x16x32_bf16 v[172:175], v[252:255], v[24:27], v[172:175]\n"
"s_waitcnt lgkmcnt(5)\n"
"ds_read_b128 v[252:255], v246 offset:2048\n"
"v_mfma_f32_16x16x32_bf16 v[172:175], v[220:223], v[88:91], v[172:175]\n"
"s_waitcnt lgkmcnt(5)\n"
"v_exp_f32 v181, v181\n"
"v_mfma_f32_16x16x32_bf16 v[172:175], v[228:231], v[120:123], v[172:175]\n"
"v_exp_f32 v182, v182\n"
"v_mfma_f32_16x16x32_bf16 v[168:171], v[228:231], v[112:115], v[168:171]\n"
"v_add_f32 v242, v242, v181\n"
"v_add_f32 v242, v242, v182\n"
"v_mfma_f32_16x16x32_bf16 v[168:171], v[220:223], v[80:83], v[168:171]\n"
"v_exp_f32 v183, v183\n"
"v_mfma_f32_16x16x32_bf16 v[164:167], v[220:223], v[72:75], v[164:167]\n"
"v_add_f32 v242, v242, v183\n"
"v_cvt_pk_bf16_f32 v198, v180, v181\n"
"v_mfma_f32_16x16x32_bf16 v[164:167], v[228:231], v[104:107], v[164:167]\n"
"v_cvt_pk_bf16_f32 v199, v182, v183\n"
"v_mfma_f32_16x16x32_bf16 v[160:163], v[228:231], v[96:99], v[160:163]\n"
"ds_read_b128 v[228:231], v246 offset:2176\n"
"v_mfma_f32_16x16x32_bf16 v[160:163], v[220:223], v[64:67], v[160:163]\n"
"s_waitcnt lgkmcnt(5)\n"
"ds_read_b128 v[220:223], v248 offset:13312\n"
"v_mfma_f32_16x16x32_bf16 v[160:163], v[216:219], v[4:7], v[160:163]\n"
"s_waitcnt lgkmcnt(5)\n"
"v_exp_f32 v184, v184\n"
"v_mfma_f32_16x16x32_bf16 v[160:163], v[224:227], v[36:39], v[160:163]\n"
"v_exp_f32 v185, v185\n"
"v_mfma_f32_16x16x32_bf16 v[164:167], v[224:227], v[44:47], v[164:167]\n"
"v_add_f32 v243, v243, v184\n"
"v_add_f32 v243, v243, v185\n"
"v_mfma_f32_16x16x32_bf16 v[164:167], v[216:219], v[12:15], v[164:167]\n"
"v_exp_f32 v186, v186\n"
"v_mfma_f32_16x16x32_bf16 v[168:171], v[216:219], v[20:23], v[168:171]\n"
"v_exp_f32 v187, v187\n"
"v_mfma_f32_16x16x32_bf16 v[168:171], v[224:227], v[52:55], v[168:171]\n"
"v_add_f32 v243, v243, v186\n"
"v_add_f32 v243, v243, v187\n"
"v_mfma_f32_16x16x32_bf16 v[172:175], v[224:227], v[60:63], v[172:175]\n"
"ds_read_b128 v[224:227], v248 offset:14336\n"
"v_cvt_pk_bf16_f32 v202, v184, v185\n"
"v_mfma_f32_16x16x32_bf16 v[172:175], v[216:219], v[28:31], v[172:175]\n"
"s_waitcnt lgkmcnt(5)\n"
"ds_read_b128 v[216:219], v246 offset:2304\n"
"v_cvt_pk_bf16_f32 v203, v186, v187\n"
"v_mfma_f32_16x16x32_bf16 v[172:175], v[232:235], v[92:95], v[172:175]\n"
"s_waitcnt lgkmcnt(5)\n"
"v_exp_f32 v188, v188\n"
"v_mfma_f32_16x16x32_bf16 v[172:175], v[212:215], v[124:127], v[172:175]\n"
"v_exp_f32 v189, v189\n"
"v_mfma_f32_16x16x32_bf16 v[168:171], v[212:215], v[116:119], v[168:171]\n"
"v_add_f32 v244, v244, v188\n"
"v_add_f32 v244, v244, v189\n"
"v_mfma_f32_16x16x32_bf16 v[168:171], v[232:235], v[84:87], v[168:171]\n"
"v_exp_f32 v190, v190\n"
"v_mfma_f32_16x16x32_bf16 v[164:167], v[232:235], v[76:79], v[164:167]\n"
"v_exp_f32 v191, v191\n"
"v_mfma_f32_16x16x32_bf16 v[164:167], v[212:215], v[108:111], v[164:167]\n"
"v_add_f32 v244, v244, v190\n"
"v_add_f32 v244, v244, v191\n"
"v_mfma_f32_16x16x32_bf16 v[160:163], v[212:215], v[100:103], v[160:163]\n"
"ds_read_b128 v[212:215], v246 offset:2432\n"
"v_cvt_pk_bf16_f32 v206, v188, v189\n"
"v_mfma_f32_16x16x32_bf16 v[160:163], v[232:235], v[68:71], v[160:163]\n"
"s_waitcnt lgkmcnt(5)\n"
"v_cvt_pk_bf16_f32 v207, v190, v191\n"
"v_fma_f32 v172, v172, s65, v239\n"
"v_mfma_f32_16x16x32_bf16 v[176:179], v[252:255], v[0:3], 0\n"
"s_waitcnt lgkmcnt(4)\n"
"v_exp_f32 v208, v208\n"
"v_mfma_f32_16x16x32_bf16 v[176:179], v[228:231], v[32:35], v[176:179]\n"
"v_exp_f32 v209, v209\n"
"v_add_f32 v245, v245, v208\n"
"v_mfma_f32_16x16x32_bf16 v[180:183], v[228:231], v[40:43], 0\n"
"v_exp_f32 v210, v210\n"
"v_mfma_f32_16x16x32_bf16 v[180:183], v[252:255], v[8:11], v[180:183]\n"
"v_exp_f32 v211, v211\n"
"v_mfma_f32_16x16x32_bf16 v[184:187], v[252:255], v[16:19], 0\n"
"s_add_u32 m0, s31, 0x4100\n"
"v_add_f32 v245, v245, v209\n"
"v_cvt_pk_bf16_f32 v208, v208, v209\n"
"v_mfma_f32_16x16x32_bf16 v[184:187], v[228:231], v[48:51], v[184:187]\n"
"s_sub_u32 s31, s31, s34\n"
"v_cvt_pk_bf16_f32 v209, v210, v211\n"
"v_add_f32 v245, v245, v210\n"
"v_mfma_f32_16x16x32_bf16 v[188:191], v[228:231], v[56:59], 0\n"
"ds_read_b128 v[232:235], v248 offset:15360\n"
"v_add_f32 v245, v245, v211\n"
"v_mfma_f32_16x16x32_bf16 v[188:191], v[252:255], v[24:27], v[188:191]\n"
"ds_write_b64 v248, v[208:209] offset:4104\n"
"s_waitcnt lgkmcnt(5)\n"
"v_mfma_f32_16x16x32_bf16 v[208:211], v[252:255], v[220:223], 0\n"
"s_waitcnt lgkmcnt(4)\n"
"ds_read_b128 v[220:223], v248 offset:16384\n"
"ds_read_b128 v[252:255], v246 offset:2560\n"
"v_mfma_f32_16x16x32_bf16 v[208:211], v[228:231], v[224:227], v[208:211]\n"
"s_waitcnt lgkmcnt(5)\n"
"ds_read_b128 v[224:227], v246 offset:2688\n"
"ds_read_b128 v[228:231], v248 offset:17408\n"
"v_mfma_f32_16x16x32_bf16 v[188:191], v[216:219], v[88:91], v[188:191]\n"
"s_waitcnt lgkmcnt(6)\n"
"buffer_load_dwordx4 v249, s[48:51], s67 offen nt lds\n"
"v_mfma_f32_16x16x32_bf16 v[188:191], v[212:215], v[120:123], v[188:191]\n"
"v_fma_f32 v173, v173, s65, v239\n"
"v_fma_f32 v174, v174, s65, v239\n"
"v_mfma_f32_16x16x32_bf16 v[184:187], v[212:215], v[112:115], v[184:187]\n"
"s_cmp_lg_u64 s[76:77], 0\n"
"s_cbranch_scc1 Lslow_19\n"
"Lret_19:\n"
"v_mfma_f32_16x16x32_bf16 v[184:187], v[216:219], v[80:83], v[184:187]\n"
"buffer_load_dwordx4 v249, s[48:51], s67 offen offset:1024 nt lds\n"
"v_mfma_f32_16x16x32_bf16 v[180:183], v[216:219], v[72:75], v[180:183]\n"
"v_fma_f32 v175, v175, s65, v239\n"
"v_fma_f32 v168, v168, s65, v238\n"
"v_mfma_f32_16x16x32_bf16 v[180:183], v[212:215], v[104:107], v[180:183]\n"
"v_max3_f32 v251, v172, v173, v174\n"
"v_fma_f32 v169, v169, s65, v238\n"
"v_mfma_f32_16x16x32_bf16 v[176:179], v[212:215], v[96:99], v[176:179]\n"
"v_fma_f32 v170, v170, s65, v238\n"
"v_max3_f32 v251, v251, v175, v168\n"
"v_mfma_f32_16x16x32_bf16 v[176:179], v[216:219], v[64:67], v[176:179]\n"
"s_waitcnt lgkmcnt(5)\n"
"buffer_load_dwordx4 v249, s[48:51], s67 offen offset:2048 nt lds\n"
"v_mfma_f32_16x16x32_bf16 v[208:211], v[216:219], v[232:235], v[208:211]\n"
"s_waitcnt lgkmcnt(3)\n"
"ds_read_b128 v[216:219], v248 offset:18432\n"
"ds_read_b128 v[232:235], v246 offset:2816\n"
"v_mfma_f32_16x16x32_bf16 v[208:211], v[212:215], v[220:223], v[208:211]\n"
"s_waitcnt lgkmcnt(4)\n"
"ds_read_b128 v[212:215], v246 offset:2944\n"
"ds_read_b128 v[220:223], v248 offset:19456\n"
"v_mfma_f32_16x16x32_bf16 v[176:179], v[252:255], v[4:7], v[176:179]\n"
"s_waitcnt lgkmcnt(5)\n"
"v_fma_f32 v171, v171, s65, v238\n"
"v_fma_f32 v164, v164, s65, v237\n"
"v_mfma_f32_16x16x32_bf16 v[176:179], v[224:227], v[36:39], v[176:179]\n"
"v_max3_f32 v251, v251, v169, v170\n"
"v_fma_f32 v165, v165, s65, v237\n"
"v_mfma_f32_16x16x32_bf16 v[180:183], v[224:227], v[44:47], v[180:183]\n"
"v_fma_f32 v166, v166, s65, v237\n"
"v_max3_f32 v251, v251, v171, v164\n"
"v_mfma_f32_16x16x32_bf16 v[180:183], v[252:255], v[12:15], v[180:183]\n"
"buffer_load_dwordx4 v249, s[48:51], s67 offen offset:3072 nt lds\n"
"v_mfma_f32_16x16x32_bf16 v[184:187], v[252:255], v[20:23], v[184:187]\n"
"s_add_u32 s67, s67, 0x4000\n"
"v_fma_f32 v167, v167, s65, v237\n"
"v_fma_f32 v160, v160, s65, v236\n"
"v_mfma_f32_16x16x32_bf16 v[184:187], v[224:227], v[52:55], v[184:187]\n"
"v_max3_f32 v251, v251, v165, v166\n"
"v_fma_f32 v161, v161, s65, v236\n"
"v_mfma_f32_16x16x32_bf16 v[188:191], v[224:227], v[60:63], v[188:191]\n"
"v_fma_f32 v162, v162, s65, v236\n"
"v_max3_f32 v251, v251, v167, v160\n"
"v_mfma_f32_16x16x32_bf16 v[188:191], v[252:255], v[28:31], v[188:191]\n"
"s_waitcnt lgkmcnt(4)\n"
"v_fma_f32 v163, v163, s65, v236\n"
"v_max3_f32 v251, v251, v161, v162\n"
"v_mfma_f32_16x16x32_bf16 v[208:211], v[252:255], v[228:231], v[208:211]\n"
"s_waitcnt lgkmcnt(3)\n"
"ds_read_b128 v[228:231], v248 offset:20480\n"
"ds_read_b64_tr_b16 v[252:253], v247 offset:16640\n"
"v_mfma_f32_16x16x32_bf16 v[208:211], v[224:227], v[216:219], v[208:211]\n"
"s_waitcnt lgkmcnt(4)\n"
"ds_read_b64_tr_b16 v[254:255], v247 offset:18688\n"
"ds_read_b64_tr_b16 v[216:217], v247 offset:16672\n"
"v_mfma_f32_16x16x32_bf16 v[188:191], v[232:235], v[92:95], v[188:191]\n"
"s_waitcnt lgkmcnt(5)\n"
"ds_read_b64_tr_b16 v[218:219], v247 offset:18720\n"
"ds_read_b64_tr_b16 v[224:225], v247 offset:16768\n"
"v_mfma_f32_16x16x32_bf16 v[188:191], v[212:215], v[124:127], v[188:191]\n"
"ds_read_b64_tr_b16 v[226:227], v247 offset:18816\n"
"v_mfma_f32_16x16x32_bf16 v[184:187], v[212:215], v[116:119], v[184:187]\n"
"v_mfma_f32_16x16x32_bf16 v[184:187], v[232:235], v[84:87], v[184:187]\n"
"v_mfma_f32_16x16x32_bf16 v[180:183], v[232:235], v[76:79], v[180:183]\n"
"v_mfma_f32_16x16x32_bf16 v[180:183], v[212:215], v[108:111], v[180:183]\n"
"v_mfma_f32_16x16x32_bf16 v[176:179], v[212:215], v[100:103], v[176:179]\n"
"v_mfma_f32_16x16x32_bf16 v[176:179], v[232:235], v[68:71], v[176:179]\n"
"s_waitcnt lgkmcnt(7)\n"
"v_fma_f32 v188, v188, s65, v239\n"
"v_fma_f32 v189, v189, s65, v239\n"
"v_mfma_f32_16x16x32_bf16 v[208:211], v[232:235], v[220:223], v[208:211]\n"
"s_waitcnt lgkmcnt(6)\n"
"ds_read_b64_tr_b16 v[220:221], v247 offset:16800\n"
"ds_read_b64_tr_b16 v[222:223], v247 offset:18848\n"
"v_mfma_f32_16x16x32_bf16 v[208:211], v[212:215], v[228:231], v[208:211]\n"
"s_waitcnt lgkmcnt(6)\n"
"ds_read_b64_tr_b16 v[232:233], v247 offset:16896\n"
"ds_read_b64_tr_b16 v[234:235], v247 offset:18944\n"
"v_mfma_f32_16x16x32_bf16 a[12:15], v[252:255], v[204:207], a[12:15]\n"
"ds_read_b64_tr_b16 v[212:213], v247 offset:16928\n"
"ds_read_b64_tr_b16 v[214:215], v247 offset:18976\n"
"v_mfma_f32_16x16x32_bf16 a[8:11], v[252:255], v[200:203], a[8:11]\n"
"ds_read_b64_tr_b16 v[228:229], v247 offset:17408\n"
"v_fma_f32 v208, v208, s65, v240\n"
"v_mfma_f32_16x16x32_bf16 a[4:7], v[252:255], v[196:199], a[4:7]\n"
"v_fma_f32 v209, v209, s65, v240\n"
"v_fma_f32 v210, v210, s65, v240\n"
"v_mfma_f32_16x16x32_bf16 a[0:3], v[252:255], v[192:195], a[0:3]\n"
"s_waitcnt lgkmcnt(9)\n"
"v_fma_f32 v211, v211, s65, v240\n"
"v_max3_f32 v250, v208, v209, v210\n"
"v_mfma_f32_16x16x32_bf16 a[16:19], v[216:219], v[192:195], a[16:19]\n"
"ds_read_b64_tr_b16 v[230:231], v247 offset:19456\n"
"v_max_f32 v250, v250, v211\n"
"v_mfma_f32_16x16x32_bf16 a[20:23], v[216:219], v[196:199], a[20:23]\n"
"v_cmp_lt_f32_e64 s[78:79], s66, v250\n"
"v_fma_f32 v190, v190, s65, v239\n"
"v_mfma_f32_16x16x32_bf16 a[24:27], v[216:219], v[200:203], a[24:27]\n"
"v_max3_f32 v251, v251, v163, v188\n"
"v_fma_f32 v191, v191, s65, v239\n"
"v_mfma_f32_16x16x32_bf16 a[28:31], v[216:219], v[204:207], a[28:31]\n"
"s_waitcnt lgkmcnt(8)\n"
"s_cmp_lg_u64 s[78:79], 0\n"
"s_cbranch_scc1 Lslow_20\n"
"Lret_20:\n"
"v_mfma_f32_16x16x32_bf16 a[44:47], v[224:227], v[204:207], a[44:47]\n"
"v_fma_f32 v184, v184, s65, v238\n"
"v_max3_f32 v251, v251, v189, v190\n"
"v_mfma_f32_16x16x32_bf16 a[40:43], v[224:227], v[200:203], a[40:43]\n"
"v_fma_f32 v185, v185, s65, v238\n"
"v_fma_f32 v186, v186, s65, v238\n"
"v_mfma_f32_16x16x32_bf16 a[36:39], v[224:227], v[196:199], a[36:39]\n"
"s_waitcnt vmcnt(12)\n"
"s_barrier\n"
"v_mfma_f32_16x16x32_bf16 a[32:35], v[224:227], v[192:195], a[32:35]\n"
"s_waitcnt lgkmcnt(6)\n"
"ds_read_b128 v[252:255], v248 offset:4096\n"
"ds_read_b128 v[216:219], v248 offset:3072\n"
"v_mfma_f32_16x16x32_bf16 a[48:51], v[220:223], v[192:195], a[48:51]\n"
"ds_read_b64_tr_b16 v[224:225], v247 offset:17440\n"
"ds_read_b64_tr_b16 v[226:227], v247 offset:19488\n"
"v_mfma_f32_16x16x32_bf16 a[52:55], v[220:223], v[196:199], a[52:55]\n"
"ds_read_b32 v250, v248 offset:0\n"
"v_max3_f32 v251, v251, v191, v184\n"
"v_mfma_f32_16x16x32_bf16 a[56:59], v[220:223], v[200:203], a[56:59]\n"
"s_add_u32 m0, s33, 0\n"
"v_fma_f32 v187, v187, s65, v238\n"
"v_fma_f32 v180, v180, s65, v237\n"
"v_mfma_f32_16x16x32_bf16 a[60:63], v[220:223], v[204:207], a[60:63]\n"
"s_waitcnt lgkmcnt(9)\n"
"buffer_load_dwordx4 v249, s[52:55], s68 offen nt lds\n"
"v_mfma_f32_16x16x32_bf16 a[76:79], v[232:235], v[204:207], a[76:79]\n"
"ds_read_b64_tr_b16 v[220:221], v247 offset:17536\n"
"ds_read_b64_tr_b16 v[222:223], v247 offset:19584\n"
"v_mfma_f32_16x16x32_bf16 a[72:75], v[232:235], v[200:203], a[72:75]\n"
"v_max3_f32 v251, v251, v185, v186\n"
"v_fma_f32 v181, v181, s65, v237\n"
"v_mfma_f32_16x16x32_bf16 a[68:71], v[232:235], v[196:199], a[68:71]\n"
"v_fma_f32 v182, v182, s65, v237\n"
"v_max3_f32 v251, v251, v187, v180\n"
"v_mfma_f32_16x16x32_bf16 a[64:67], v[232:235], v[192:195], a[64:67]\n"
"s_waitcnt lgkmcnt(9)\n"
"ds_read_b64_tr_b16 v[232:233], v247 offset:17568\n"
"ds_read_b64_tr_b16 v[234:235], v247 offset:19616\n"
"v_mfma_f32_16x16x32_bf16 a[80:83], v[212:215], v[192:195], a[80:83]\n"
"v_fma_f32 v183, v183, s65, v237\n"
"v_fma_f32 v176, v176, s65, v236\n"
"v_mfma_f32_16x16x32_bf16 a[84:87], v[212:215], v[196:199], a[84:87]\n"
"buffer_load_dwordx4 v249, s[52:55], s68 offen offset:1024 nt lds\n"
"v_mfma_f32_16x16x32_bf16 a[88:91], v[212:215], v[200:203], a[88:91]\n"
"v_max3_f32 v251, v251, v181, v182\n"
"v_fma_f32 v177, v177, s65, v236\n"
"v_mfma_f32_16x16x32_bf16 a[92:95], v[212:215], v[204:207], a[92:95]\n"
"s_waitcnt lgkmcnt(8)\n"
"ds_read_b64_tr_b16 v[212:213], v247 offset:17024\n"
"ds_read_b64_tr_b16 v[214:215], v247 offset:19072\n"
"v_mfma_f32_16x16x32_bf16 v[132:135], v[228:231], v[252:255], v[132:135]\n"
"s_waitcnt lgkmcnt(9)\n"
"v_fma_f32 v178, v178, s65, v236\n"
"v_max3_f32 v251, v251, v183, v176\n"
"v_mfma_f32_16x16x32_bf16 v[128:131], v[228:231], v[216:219], v[128:131]\n"
"v_fma_f32 v179, v179, s65, v236\n"
"v_max3_f32 v251, v251, v177, v178\n"
"v_mfma_f32_16x16x32_bf16 a[204:207], v[228:231], v[204:207], a[204:207]\n"
"s_waitcnt lgkmcnt(6)\n"
"v_max3_f32 v251, v251, v179, v250\n"
"v_cmp_lt_f32 vcc, s66, v251\n"
"v_mfma_f32_16x16x32_bf16 a[200:203], v[228:231], v[200:203], a[200:203]\n"
"buffer_load_dwordx4 v249, s[52:55], s68 offen offset:2048 nt lds\n"
"v_mfma_f32_16x16x32_bf16 a[196:199], v[228:231], v[196:199], a[196:199]\n"
"v_mfma_f32_16x16x32_bf16 a[192:195], v[228:231], v[192:195], a[192:195]\n"
"ds_read_b64_tr_b16 v[228:229], v247 offset:17056\n"
"ds_read_b64_tr_b16 v[230:231], v247 offset:19104\n"
"v_mfma_f32_16x16x32_bf16 a[208:211], v[224:227], v[192:195], a[208:211]\n"
"s_cbranch_vccnz Lslow_21\n"
"Lret_21:\n"
"v_mfma_f32_16x16x32_bf16 a[212:215], v[224:227], v[196:199], a[212:215]\n"
"v_exp_f32 v208, v208\n"
"v_mfma_f32_16x16x32_bf16 a[216:219], v[224:227], v[200:203], a[216:219]\n"
"v_exp_f32 v160, v160\n"
"v_mfma_f32_16x16x32_bf16 a[220:223], v[224:227], v[204:207], a[220:223]\n"
"buffer_load_dwordx4 v249, s[52:55], s68 offen offset:3072 nt lds\n"
"v_mfma_f32_16x16x32_bf16 v[136:139], v[224:227], v[216:219], v[136:139]\n"
"s_add_u32 s68, s68, 0x4000\n"
"v_exp_f32 v164, v164\n"
"v_mfma_f32_16x16x32_bf16 v[140:143], v[224:227], v[252:255], v[140:143]\n"
"s_waitcnt lgkmcnt(6)\n"
"ds_read_b64_tr_b16 v[224:225], v247 offset:17152\n"
"ds_read_b64_tr_b16 v[226:227], v247 offset:19200\n"
"v_mfma_f32_16x16x32_bf16 v[148:151], v[220:223], v[252:255], v[148:151]\n"
"v_exp_f32 v168, v168\n"
"v_mfma_f32_16x16x32_bf16 v[144:147], v[220:223], v[216:219], v[144:147]\n"
"v_exp_f32 v172, v172\n"
"v_mfma_f32_16x16x32_bf16 a[236:239], v[220:223], v[204:207], a[236:239]\n"
"v_exp_f32 v209, v209\n"
"v_mfma_f32_16x16x32_bf16 a[232:235], v[220:223], v[200:203], a[232:235]\n"
"v_exp_f32 v161, v161\n"
"v_mfma_f32_16x16x32_bf16 a[228:231], v[220:223], v[196:199], a[228:231]\n"
"v_exp_f32 v165, v165\n"
"v_mfma_f32_16x16x32_bf16 a[224:227], v[220:223], v[192:195], a[224:227]\n"
"s_waitcnt lgkmcnt(6)\n"
"ds_read_b64_tr_b16 v[220:221], v247 offset:17184\n"
"ds_read_b64_tr_b16 v[222:223], v247 offset:19232\n"
"v_mfma_f32_16x16x32_bf16 a[240:243], v[232:235], v[192:195], a[240:243]\n"
"v_exp_f32 v169, v169\n"
"v_mfma_f32_16x16x32_bf16 a[244:247], v[232:235], v[196:199], a[244:247]\n"
"v_exp_f32 v173, v173\n"
"v_mfma_f32_16x16x32_bf16 a[248:251], v[232:235], v[200:203], a[248:251]\n"
"v_add_f32 v245, v245, v208\n"
"v_add_f32 v241, v241, v160\n"
"v_mfma_f32_16x16x32_bf16 a[252:255], v[232:235], v[204:207], a[252:255]\n"
"v_exp_f32 v210, v210\n"
"v_mfma_f32_16x16x32_bf16 v[152:155], v[232:235], v[216:219], v[152:155]\n"
"ds_read_b64_tr_b16 v[216:217], v247 offset:17280\n"
"ds_read_b64_tr_b16 v[218:219], v247 offset:19328\n"
"v_mfma_f32_16x16x32_bf16 v[156:159], v[232:235], v[252:255], v[156:159]\n"
"s_waitcnt lgkmcnt(8)\n"
"ds_read_b64_tr_b16 v[232:233], v247 offset:17312\n"
"ds_read_b64_tr_b16 v[234:235], v247 offset:19360\n"
"v_mfma_f32_16x16x32_bf16 a[108:111], v[212:215], v[204:207], a[108:111]\n"
"v_exp_f32 v211, v211\n"
"v_mfma_f32_16x16x32_bf16 a[104:107], v[212:215], v[200:203], a[104:107]\n"
"v_exp_f32 v162, v162\n"
"v_mfma_f32_16x16x32_bf16 a[100:103], v[212:215], v[196:199], a[100:103]\n"
"v_add_f32 v242, v242, v164\n"
"v_add_f32 v243, v243, v168\n"
"v_mfma_f32_16x16x32_bf16 a[96:99], v[212:215], v[192:195], a[96:99]\n"
"s_waitcnt lgkmcnt(8)\n"
"ds_read_b128 v[212:215], v246 offset:16640\n"
"v_add_f32 v244, v244, v172\n"
"v_mfma_f32_16x16x32_bf16 a[112:115], v[228:231], v[192:195], a[112:115]\n"
"v_exp_f32 v166, v166\n"
"v_mfma_f32_16x16x32_bf16 a[116:119], v[228:231], v[196:199], a[116:119]\n"
"v_exp_f32 v170, v170\n"
"v_mfma_f32_16x16x32_bf16 a[120:123], v[228:231], v[200:203], a[120:123]\n"
"v_exp_f32 v174, v174\n"
"v_mfma_f32_16x16x32_bf16 a[124:127], v[228:231], v[204:207], a[124:127]\n"
"s_waitcnt lgkmcnt(7)\n"
"v_add_f32 v245, v245, v209\n"
"v_cvt_pk_bf16_f32 v208, v208, v209\n"
"v_mfma_f32_16x16x32_bf16 a[140:143], v[224:227], v[204:207], a[140:143]\n"
"v_add_f32 v241, v241, v161\n"
"v_add_f32 v242, v242, v165\n"
"v_mfma_f32_16x16x32_bf16 a[136:139], v[224:227], v[200:203], a[136:139]\n"
"v_exp_f32 v163, v163\n"
"v_mfma_f32_16x16x32_bf16 a[132:135], v[224:227], v[196:199], a[132:135]\n"
"v_exp_f32 v167, v167\n"
"v_mfma_f32_16x16x32_bf16 a[128:131], v[224:227], v[192:195], a[128:131]\n"
"s_waitcnt lgkmcnt(5)\n"
"v_add_f32 v243, v243, v169\n"
"v_add_f32 v244, v244, v173\n"
"v_mfma_f32_16x16x32_bf16 a[144:147], v[220:223], v[192:195], a[144:147]\n"
"v_exp_f32 v171, v171\n"
"v_mfma_f32_16x16x32_bf16 a[148:151], v[220:223], v[196:199], a[148:151]\n"
"v_exp_f32 v175, v175\n"
"v_mfma_f32_16x16x32_bf16 a[152:155], v[220:223], v[200:203], a[152:155]\n"
"v_add_f32 v245, v245, v210\n"
"v_cvt_pk_bf16_f32 v209, v210, v211\n"
"v_mfma_f32_16x16x32_bf16 a[156:159], v[220:223], v[204:207], a[156:159]\n"
"s_waitcnt lgkmcnt(3)\n"
"v_add_f32 v241, v241, v162\n"
"v_add_f32 v242, v242, v166\n"
"v_mfma_f32_16x16x32_bf16 a[172:175], v[216:219], v[204:207], a[172:175]\n"
"v_add_f32 v243, v243, v170\n"
"v_add_f32 v244, v244, v174\n"
"v_mfma_f32_16x16x32_bf16 a[168:171], v[216:219], v[200:203], a[168:171]\n"
"v_add_u32 v247, s30, v247\n"
"v_add_f32 v245, v245, v211\n"
"v_mfma_f32_16x16x32_bf16 a[164:167], v[216:219], v[196:199], a[164:167]\n"
"s_sub_u32 s30, 0, s30\n"
"ds_write_b64 v248, v[208:209] offset:2056\n"
"v_mfma_f32_16x16x32_bf16 a[160:163], v[216:219], v[192:195], a[160:163]\n"
"s_waitcnt lgkmcnt(2)\n"
"ds_read_b128 v[216:219], v246 offset:16768\n"
"ds_read_b128 v[220:223], v246 offset:16896\n"
"v_mfma_f32_16x16x32_bf16 a[176:179], v[232:235], v[192:195], a[176:179]\n"
"ds_read_b128 v[224:227], v246 offset:17024\n"
"v_add_f32 v241, v241, v163\n"
"v_mfma_f32_16x16x32_bf16 a[180:183], v[232:235], v[196:199], a[180:183]\n"
"s_sub_u32 s70, s70, 1\n"
"v_add_f32 v242, v242, v167\n"
"v_add_f32 v243, v243, v171\n"
"v_mfma_f32_16x16x32_bf16 a[184:187], v[232:235], v[200:203], a[184:187]\n"
"v_add_f32 v244, v244, v175\n"
"v_cvt_pk_bf16_f32 v192, v160, v161\n"
"v_mfma_f32_16x16x32_bf16 a[188:191], v[232:235], v[204:207], a[188:191]\n"
"v_cvt_pk_bf16_f32 v193, v162, v163\n"
"v_cvt_pk_bf16_f32 v196, v164, v165\n"
"v_cvt_pk_bf16_f32 v197, v166, v167\n"
"v_cvt_pk_bf16_f32 v200, v168, v169\n"
"v_cvt_pk_bf16_f32 v201, v170, v171\n"
"v_cvt_pk_bf16_f32 v204, v172, v173\n"
"v_cvt_pk_bf16_f32 v205, v174, v175\n"
"s_cmp_lg_u32 s70, 0\n"
"s_cbranch_scc1 Lloop3\n"
"Lloopx:\n"
"s_nop 1\n"
"s_waitcnt lgkmcnt(3)\n"
"v_mfma_f32_16x16x32_bf16 v[160:163], v[212:215], v[0:3], 0\n"
"s_waitcnt lgkmcnt(2)\n"
"s_lshr_b32 s72, s64, 1\n"
"v_add_u32 v246, s34, v246\n"
"s_lshl_b32 s72, s72, 11\n"
"s_lshl_b32 s74, s86, 5\n"
"v_mfma_f32_16x16x32_bf16 v[160:163], v[216:219], v[32:35], v[160:163]\n"
"s_lshl_b32 s73, s64, 8\n"
"v_add_u32 v254, s72, v246\n"
"v_add_u32 v252, s74, v248\n"
"v_add_u32 v247, s30, v247\n"
"v_add_u32 v255, s73, v247\n"
"v_mfma_f32_16x16x32_bf16 v[164:167], v[216:219], v[40:43], 0\n"
"s_lshr_b32 s72, s64, 1\n"
"ds_read_b128 v[228:231], v254 offset:49920\n"
"ds_read_b128 v[232:235], v252 offset:5120\n"
"v_lshlrev_b32 v249, 3, v248\n"
"v_mfma_f32_16x16x32_bf16 v[164:167], v[212:215], v[8:11], v[164:167]\n"
"s_lshl_b32 s73, s86, 2\n"
"v_exp_f32 v176, v176\n"
"v_exp_f32 v177, v177\n"
"v_mfma_f32_16x16x32_bf16 v[168:171], v[212:215], v[16:19], 0\n"
"s_lshl_b32 s72, s72, 3\n"
"v_add_f32 v241, v241, v176\n"
"v_add_f32 v241, v241, v177\n"
"v_mfma_f32_16x16x32_bf16 v[168:171], v[216:219], v[48:51], v[168:171]\n"
"s_add_u32 s73, s73, s72\n"
"v_add_u32 v253, s73, v248\n"
"v_cvt_pk_bf16_f32 v194, v176, v177\n"
"v_mfma_f32_16x16x32_bf16 v[172:175], v[216:219], v[56:59], 0\n"
"s_lshl_b32 s73, s88, 1\n"
"ds_read_b128 v[216:219], v254 offset:50048\n"
"s_lshr_b32 s74, s89, 2\n"
"v_mfma_f32_16x16x32_bf16 v[172:175], v[212:215], v[24:27], v[172:175]\n"
"s_waitcnt lgkmcnt(1)\n"
"ds_read_b128 v[212:215], v252 offset:6144\n"
"s_add_u32 s73, s73, s74\n"
"v_mfma_f32_16x16x32_bf16 v[208:211], v[228:231], v[232:235], 0\n"
"s_lshl_b32 s73, s73, 2\n"
"ds_read_b128 v[228:231], v246 offset:50432\n"
"ds_read_b128 v[232:235], v246 offset:50560\n"
"s_add_u32 s73, s73, s64\n"
"s_mul_i32 s73, s73, 0x1c00\n"
"v_mfma_f32_16x16x32_bf16 v[172:175], v[220:223], v[88:91], v[172:175]\n"
"s_add_u32 s73, s73, s26\n"
"s_mov_b32 m0, 0x27800\n"
"v_exp_f32 v178, v178\n"
"v_mfma_f32_16x16x32_bf16 v[172:175], v[224:227], v[120:123], v[172:175]\n"
"s_add_u32 s91, s26, 0\n"
"buffer_load_dword v249, s[56:59], s73 offen lds\n"
"v_exp_f32 v179, v179\n"
"v_mfma_f32_16x16x32_bf16 v[168:171], v[224:227], v[112:115], v[168:171]\n"
"s_mul_i32 s73, s64, 0xe000\n"
"v_add_f32 v241, v241, v178\n"
"v_add_f32 v241, v241, v179\n"
"v_mfma_f32_16x16x32_bf16 v[168:171], v[220:223], v[80:83], v[168:171]\n"
"s_add_u32 s92, s26, 0x1800\n"
"v_cvt_pk_bf16_f32 v195, v178, v179\n"
"v_mfma_f32_16x16x32_bf16 v[164:167], v[220:223], v[72:75], v[164:167]\n"
"s_add_u32 s93, s26, 0x3000\n"
"v_exp_f32 v180, v180\n"
"v_mfma_f32_16x16x32_bf16 v[164:167], v[224:227], v[104:107], v[164:167]\n"
"s_add_u32 s94, s26, 0x4800\n"
"v_exp_f32 v181, v181\n"
"v_mfma_f32_16x16x32_bf16 v[160:163], v[224:227], v[96:99], v[160:163]\n"
"s_add_u32 s84, s35, 0\n"
"ds_read_b128 v[224:227], v254 offset:50176\n"
"v_add_f32 v242, v242, v180\n"
"v_mfma_f32_16x16x32_bf16 v[160:163], v[220:223], v[64:67], v[160:163]\n"
"s_waitcnt lgkmcnt(3)\n"
"ds_read_b128 v[220:223], v252 offset:7168\n"
"v_add_f32 v242, v242, v181\n"
"v_mfma_f32_16x16x32_bf16 v[208:211], v[216:219], v[212:215], v[208:211]\n"
"s_waitcnt lgkmcnt(3)\n"
"ds_read_b128 v[212:215], v246 offset:50688\n"
"ds_read_b128 v[216:219], v246 offset:50816\n"
"v_mfma_f32_16x16x32_bf16 v[160:163], v[228:231], v[4:7], v[160:163]\n"
"s_waitcnt lgkmcnt(4)\n"
"v_exp_f32 v182, v182\n"
"v_mfma_f32_16x16x32_bf16 v[160:163], v[232:235], v[36:39], v[160:163]\n"
"s_lshl_b32 s72, s64, 5\n"
"v_exp_f32 v183, v183\n"
"v_mfma_f32_16x16x32_bf16 v[164:167], v[232:235], v[44:47], v[164:167]\n"
"s_xor_b32 s72, s72, 32\n"
"v_add_f32 v242, v242, v182\n"
"v_add_f32 v242, v242, v183\n"
"v_mfma_f32_16x16x32_bf16 v[164:167], v[228:231], v[12:15], v[164:167]\n"
"s_add_u32 s67, s35, 0x2000\n"
"v_cvt_pk_bf16_f32 v198, v180, v181\n"
"v_cvt_pk_bf16_f32 v199, v182, v183\n"
"v_mfma_f32_16x16x32_bf16 v[168:171], v[228:231], v[20:23], v[168:171]\n"
"s_add_u32 s68, s35, 0x20000\n"
"v_exp_f32 v184, v184\n"
"v_mfma_f32_16x16x32_bf16 v[168:171], v[232:235], v[52:55], v[168:171]\n"
"s_lshl_b32 s74, s64, 11\n"
"v_exp_f32 v185, v185\n"
"v_mfma_f32_16x16x32_bf16 v[172:175], v[232:235], v[60:63], v[172:175]\n"
"s_add_u32 s83, s35, 0x22000\n"
"ds_read_b128 v[232:235], v254 offset:50304\n"
"v_add_f32 v243, v243, v184\n"
"v_mfma_f32_16x16x32_bf16 v[172:175], v[228:231], v[28:31], v[172:175]\n"
"s_waitcnt lgkmcnt(3)\n"
"ds_read_b128 v[228:231], v252 offset:8192\n"
"v_add_f32 v243, v243, v185\n"
"v_mfma_f32_16x16x32_bf16 v[208:211], v[224:227], v[220:223], v[208:211]\n"
"s_waitcnt lgkmcnt(3)\n"
"ds_read_b128 v[220:223], v246 offset:51968\n"
"ds_read_b128 v[224:227], v246 offset:52096\n"
"v_mfma_f32_16x16x32_bf16 v[172:175], v[212:215], v[92:95], v[172:175]\n"
"s_waitcnt lgkmcnt(4)\n"
"v_exp_f32 v186, v186\n"
"v_mfma_f32_16x16x32_bf16 v[172:175], v[216:219], v[124:127], v[172:175]\n"
"v_exp_f32 v187, v187\n"
"v_mfma_f32_16x16x32_bf16 v[168:171], v[216:219], v[116:119], v[168:171]\n"
"v_add_f32 v243, v243, v186\n"
"v_add_f32 v243, v243, v187\n"
"v_mfma_f32_16x16x32_bf16 v[168:171], v[212:215], v[84:87], v[168:171]\n"
"v_cvt_pk_bf16_f32 v202, v184, v185\n"
"v_cvt_pk_bf16_f32 v203, v186, v187\n"
"v_mfma_f32_16x16x32_bf16 v[164:167], v[212:215], v[76:79], v[164:167]\n"
"v_exp_f32 v188, v188\n"
"v_mfma_f32_16x16x32_bf16 v[164:167], v[216:219], v[108:111], v[164:167]\n"
"v_exp_f32 v189, v189\n"
"v_mfma_f32_16x16x32_bf16 v[160:163], v[216:219], v[100:103], v[160:163]\n"
"ds_read_b128 v[216:219], v254 offset:50432\n"
"v_add_f32 v244, v244, v188\n"
"v_mfma_f32_16x16x32_bf16 v[160:163], v[212:215], v[68:71], v[160:163]\n"
"s_waitcnt lgkmcnt(3)\n"
"ds_read_b128 v[212:215], v252 offset:9216\n"
"v_add_f32 v244, v244, v189\n"
"v_mfma_f32_16x16x32_bf16 v[208:211], v[232:235], v[228:231], v[208:211]\n"
"s_waitcnt lgkmcnt(3)\n"
"ds_read_b128 v[228:231], v246 offset:52224\n"
"ds_read_b128 v[232:235], v246 offset:52352\n"
"v_mfma_f32_16x16x32_bf16 v[176:179], v[220:223], v[0:3], 0\n"
"s_waitcnt lgkmcnt(4)\n"
"v_exp_f32 v190, v190\n"
"v_mfma_f32_16x16x32_bf16 v[176:179], v[224:227], v[32:35], v[176:179]\n"
"v_exp_f32 v191, v191\n"
"v_mfma_f32_16x16x32_bf16 v[180:183], v[224:227], v[40:43], 0\n"
"v_add_f32 v244, v244, v190\n"
"v_add_f32 v244, v244, v191\n"
"v_mfma_f32_16x16x32_bf16 v[180:183], v[220:223], v[8:11], v[180:183]\n"
"v_cvt_pk_bf16_f32 v206, v188, v189\n"
"v_cvt_pk_bf16_f32 v207, v190, v191\n"
"v_mfma_f32_16x16x32_bf16 v[184:187], v[220:223], v[16:19], 0\n"
"v_fma_f32 v172, v172, s65, v239\n"
"v_fma_f32 v173, v173, s65, v239\n"
"v_mfma_f32_16x16x32_bf16 v[184:187], v[224:227], v[48:51], v[184:187]\n"
"v_fma_f32 v174, v174, s65, v239\n"
"v_fma_f32 v175, v175, s65, v239\n"
"v_mfma_f32_16x16x32_bf16 v[188:191], v[224:227], v[56:59], 0\n"
"ds_read_b128 v[224:227], v254 offset:50560\n"
"v_fma_f32 v168, v168, s65, v238\n"
"v_mfma_f32_16x16x32_bf16 v[188:191], v[220:223], v[24:27], v[188:191]\n"
"s_waitcnt lgkmcnt(3)\n"
"ds_read_b128 v[220:223], v252 offset:10240\n"
"v_max3_f32 v251, v172, v173, v174\n"
"v_mfma_f32_16x16x32_bf16 v[208:211], v[216:219], v[212:215], v[208:211]\n"
"s_waitcnt lgkmcnt(3)\n"
"ds_read_b128 v[212:215], v246 offset:52480\n"
"ds_read_b128 v[216:219], v246 offset:52608\n"
"v_mfma_f32_16x16x32_bf16 v[188:191], v[228:231], v[88:91], v[188:191]\n"
"s_waitcnt lgkmcnt(4)\n"
"v_fma_f32 v169, v169, s65, v238\n"
"v_fma_f32 v170, v170, s65, v238\n"
"v_mfma_f32_16x16x32_bf16 v[188:191], v[232:235], v[120:123], v[188:191]\n"
"v_max3_f32 v251, v251, v175, v168\n"
"v_fma_f32 v171, v171, s65, v238\n"
"v_mfma_f32_16x16x32_bf16 v[184:187], v[232:235], v[112:115], v[184:187]\n"
"v_fma_f32 v164, v164, s65, v237\n"
"v_max3_f32 v251, v251, v169, v170\n"
"v_mfma_f32_16x16x32_bf16 v[184:187], v[228:231], v[80:83], v[184:187]\n"
"v_fma_f32 v165, v165, s65, v237\n"
"v_fma_f32 v166, v166, s65, v237\n"
"v_mfma_f32_16x16x32_bf16 v[180:183], v[228:231], v[72:75], v[180:183]\n"
"v_max3_f32 v251, v251, v171, v164\n"
"v_fma_f32 v167, v167, s65, v237\n"
"v_mfma_f32_16x16x32_bf16 v[180:183], v[232:235], v[104:107], v[180:183]\n"
"v_fma_f32 v160, v160, s65, v236\n"
"v_max3_f32 v251, v251, v165, v166\n"
"v_mfma_f32_16x16x32_bf16 v[176:179], v[232:235], v[96:99], v[176:179]\n"
"ds_read_b128 v[232:235], v254 offset:50688\n"
"v_fma_f32 v161, v161, s65, v236\n"
"v_mfma_f32_16x16x32_bf16 v[176:179], v[228:231], v[64:67], v[176:179]\n"
"s_waitcnt lgkmcnt(3)\n"
"ds_read_b128 v[228:231], v252 offset:11264\n"
"v_fma_f32 v162, v162, s65, v236\n"
"v_mfma_f32_16x16x32_bf16 v[208:211], v[224:227], v[220:223], v[208:211]\n"
"s_waitcnt lgkmcnt(3)\n"
"ds_read_b128 v[220:223], v254 offset:50816\n"
"ds_read_b128 v[224:227], v252 offset:12288\n"
"v_mfma_f32_16x16x32_bf16 v[176:179], v[212:215], v[4:7], v[176:179]\n"
"s_waitcnt lgkmcnt(4)\n"
"v_max3_f32 v251, v251, v167, v160\n"
"v_fma_f32 v163, v163, s65, v236\n"
"v_mfma_f32_16x16x32_bf16 v[176:179], v[216:219], v[36:39], v[176:179]\n"
"v_max3_f32 v251, v251, v161, v162\n"
"v_mfma_f32_16x16x32_bf16 v[180:183], v[216:219], v[44:47], v[180:183]\n"
"v_mfma_f32_16x16x32_bf16 v[180:183], v[212:215], v[12:15], v[180:183]\n"
"v_mfma_f32_16x16x32_bf16 v[184:187], v[212:215], v[20:23], v[184:187]\n"
"v_mfma_f32_16x16x32_bf16 v[184:187], v[216:219], v[52:55], v[184:187]\n"
"v_mfma_f32_16x16x32_bf16 v[188:191], v[216:219], v[60:63], v[188:191]\n"
"ds_read_b128 v[216:219], v246 offset:52736\n"
"v_mfma_f32_16x16x32_bf16 v[188:191], v[212:215], v[28:31], v[188:191]\n"
"s_waitcnt lgkmcnt(3)\n"
"ds_read_b128 v[212:215], v246 offset:52864\n"
"v_mfma_f32_16x16x32_bf16 v[208:211], v[232:235], v[228:231], v[208:211]\n"
"s_waitcnt lgkmcnt(2)\n"
"ds_read_b64_tr_b16 v[228:229], v247 offset:33280\n"
"ds_read_b64_tr_b16 v[230:231], v247 offset:35328\n"
"ds_read_b64_tr_b16 v[232:233], v247 offset:33312\n"
"ds_read_b64_tr_b16 v[234:235], v247 offset:35360\n"
"v_mfma_f32_16x16x32_bf16 v[208:211], v[220:223], v[224:227], v[208:211]\n"
"s_waitcnt lgkmcnt(5)\n"
"ds_read_b64_tr_b16 v[220:221], v247 offset:33408\n"
"ds_read_b64_tr_b16 v[222:223], v247 offset:35456\n"
"ds_read_b64_tr_b16 v[224:225], v247 offset:33440\n"
"ds_read_b64_tr_b16 v[226:227], v247 offset:35488\n"
"v_mfma_f32_16x16x32_bf16 v[188:191], v[216:219], v[92:95], v[188:191]\n"
"s_waitcnt lgkmcnt(8)\n"
"v_mfma_f32_16x16x32_bf16 v[188:191], v[212:215], v[124:127], v[188:191]\n"
"v_fma_f32 v208, v208, s65, v240\n"
"v_fma_f32 v209, v209, s65, v240\n"
"v_fma_f32 v210, v210, s65, v240\n"
"v_fma_f32 v211, v211, s65, v240\n"
"v_max3_f32 v250, v208, v209, v210\n"
"v_mfma_f32_16x16x32_bf16 v[184:187], v[212:215], v[116:119], v[184:187]\n"
"v_max_f32 v250, v250, v211\n"
"v_cmp_lt_f32_e64 s[78:79], s66, v250\n"
"v_mbcnt_lo_u32_b32 v126, -1, 0\n"
"v_mbcnt_hi_u32_b32 v126, -1, v126\n"
"v_lshrrev_b32 v127, 3, v126\n"
"v_mfma_f32_16x16x32_bf16 v[184:187], v[216:219], v[84:87], v[184:187]\n"
"v_xor_b32 v127, v127, v126\n"
"s_or_b64 s[96:97], s[78:79], s[76:77]\n"
"s_cbranch_scc1 Lslow_22\n"
"Lret_22:\n"
"v_and_b32 v127, 6, v127\n"
"v_lshlrev_b32 v252, 4, v127\n"
"v_mfma_f32_16x16x32_bf16 v[180:183], v[216:219], v[76:79], v[180:183]\n"
"v_and_b32 v127, 1, v126\n"
"v_lshlrev_b32 v127, 4, v127\n"
"v_add_u32 v252, v252, v127\n"
"v_lshrrev_b32 v127, 3, v126\n"
"v_mul_u32_u24 v127, 0x380, v127\n"
"v_mfma_f32_16x16x32_bf16 v[180:183], v[212:215], v[108:111], v[180:183]\n"
"s_waitcnt vmcnt(1)\n"
"s_barrier\n"
"ds_read_b32 v250, v248 offset:12\n"
"v_add_u32 v252, v252, v127\n"
"v_add_u32 v252, s73, v252\n"
"v_mfma_f32_16x16x32_bf16 v[176:179], v[212:215], v[100:103], v[176:179]\n"
"s_nop 0\n"
"ds_read_b64_tr_b16 v[212:213], v247 offset:33536\n"
"ds_read_b64_tr_b16 v[214:215], v247 offset:35584\n"
"v_fma_f32 v188, v188, s65, v239\n"
"v_fma_f32 v189, v189, s65, v239\n"
"v_fma_f32 v190, v190, s65, v239\n"
"v_mfma_f32_16x16x32_bf16 v[176:179], v[216:219], v[68:71], v[176:179]\n"
"s_waitcnt lgkmcnt(9)\n"
"ds_read_b64_tr_b16 v[216:217], v247 offset:33568\n"
"ds_read_b64_tr_b16 v[218:219], v247 offset:35616\n"
"v_max3_f32 v251, v251, v163, v188\n"
"v_fma_f32 v191, v191, s65, v239\n"
"v_fma_f32 v184, v184, s65, v238\n"
"v_mfma_f32_16x16x32_bf16 a[12:15], v[228:231], v[204:207], a[12:15]\n"
"s_lshl_b32 s73, s64, 8\n"
"v_max3_f32 v251, v251, v189, v190\n"
"v_fma_f32 v185, v185, s65, v238\n"
"v_fma_f32 v186, v186, s65, v238\n"
"v_max3_f32 v251, v251, v191, v184\n"
"v_fma_f32 v187, v187, s65, v238\n"
"v_mfma_f32_16x16x32_bf16 a[8:11], v[228:231], v[200:203], a[8:11]\n"
"v_fma_f32 v180, v180, s65, v237\n"
"v_max3_f32 v251, v251, v185, v186\n"
"v_fma_f32 v181, v181, s65, v237\n"
"v_fma_f32 v182, v182, s65, v237\n"
"v_max3_f32 v251, v251, v187, v180\n"
"v_mfma_f32_16x16x32_bf16 a[4:7], v[228:231], v[196:199], a[4:7]\n"
"v_fma_f32 v183, v183, s65, v237\n"
"v_fma_f32 v176, v176, s65, v236\n"
"v_max3_f32 v251, v251, v181, v182\n"
"v_fma_f32 v177, v177, s65, v236\n"
"v_fma_f32 v178, v178, s65, v236\n"
"v_mfma_f32_16x16x32_bf16 a[0:3], v[228:231], v[192:195], a[0:3]\n"
"s_waitcnt lgkmcnt(9)\n"
"ds_read_b64_tr_b16 v[228:229], v247 offset:33664\n"
"ds_read_b64_tr_b16 v[230:231], v247 offset:35712\n"
"v_max3_f32 v251, v251, v183, v176\n"
"v_fma_f32 v179, v179, s65, v236\n"
"v_max3_f32 v251, v251, v177, v178\n"
"v_mfma_f32_16x16x32_bf16 a[16:19], v[232:235], v[192:195], a[16:19]\n"
"s_waitcnt lgkmcnt(6)\n"
"v_max3_f32 v251, v251, v179, v250\n"
"v_cmp_lt_f32 vcc, s66, v251\n"
"v_mbcnt_lo_u32_b32 v124, -1, 0\n"
"v_mbcnt_hi_u32_b32 v124, -1, v124\n"
"v_bfe_u32 v125, v124, 2, 4\n"
"v_mfma_f32_16x16x32_bf16 a[20:23], v[232:235], v[196:199], a[20:23]\n"
"v_mul_u32_u24 v246, 0x80, v125\n"
"s_cbranch_vccnz Lslow_23\n"
"Lret_23:\n"
"v_exp_f32 v172, v172\n"
"v_mfma_f32_16x16x32_bf16 a[24:27], v[232:235], v[200:203], a[24:27]\n"
"s_add_u32 m0, s90, 0x5400\n"
"v_exp_f32 v173, v173\n"
"v_exp_f32 v174, v174\n"
"v_bfe_u32 v125, v124, 0, 2\n"
"v_mfma_f32_16x16x32_bf16 a[28:31], v[232:235], v[204:207], a[28:31]\n"
"buffer_load_dwordx4 v252, s[56:59], s91 offen lds\n"
"ds_read_b64_tr_b16 v[232:233], v255 offset:33280\n"
"ds_read_b64_tr_b16 v[234:235], v255 offset:35328\n"
"v_mul_u32_u24 v125, 8, v125\n"
"v_mfma_f32_16x16x32_bf16 a[44:47], v[220:223], v[204:207], a[44:47]\n"
"s_add_u32 s91, s26, 0x7000\n"
"v_exp_f32 v175, v175\n"
"v_exp_f32 v188, v188\n"
"v_add_u32 v246, v246, v125\n"
"v_mfma_f32_16x16x32_bf16 a[40:43], v[220:223], v[200:203], a[40:43]\n"
"v_exp_f32 v189, v189\n"
"v_exp_f32 v190, v190\n"
"v_bfe_u32 v125, v124, 3, 2\n"
"v_mfma_f32_16x16x32_bf16 a[36:39], v[220:223], v[196:199], a[36:39]\n"
"v_exp_f32 v191, v191\n"
"v_exp_f32 v168, v168\n"
"v_mul_u32_u24 v125, 32, v125\n"
"v_mfma_f32_16x16x32_bf16 a[32:35], v[220:223], v[192:195], a[32:35]\n"
"buffer_load_dwordx4 v252, s[56:59], s92 offen offset:1024 lds\n"
"ds_read_b128 v[220:223], v248 offset:1024\n"
"v_exp_f32 v169, v169\n"
"v_mfma_f32_16x16x32_bf16 a[48:51], v[224:227], v[192:195], a[48:51]\n"
"s_add_u32 s92, s26, 0x8800\n"
"v_exp_f32 v170, v170\n"
"v_exp_f32 v171, v171\n"
"v_add_f32 v243, v243, v168\n"
"v_mfma_f32_16x16x32_bf16 a[52:55], v[224:227], v[196:199], a[52:55]\n"
"v_exp_f32 v184, v184\n"
"v_exp_f32 v185, v185\n"
"v_add_f32 v243, v243, v169\n"
"v_mfma_f32_16x16x32_bf16 a[56:59], v[224:227], v[200:203], a[56:59]\n"
"v_exp_f32 v186, v186\n"
"v_exp_f32 v187, v187\n"
"v_add_f32 v243, v243, v170\n"
"v_mfma_f32_16x16x32_bf16 a[60:63], v[224:227], v[204:207], a[60:63]\n"
"s_waitcnt lgkmcnt(7)\n"
"buffer_load_dwordx4 v252, s[56:59], s93 offen offset:2048 lds\n"
"ds_read_b128 v[224:227], v248 offset:2048\n"
"v_exp_f32 v164, v164\n"
"v_mfma_f32_16x16x32_bf16 a[76:79], v[212:215], v[204:207], a[76:79]\n"
"s_add_u32 s93, s26, 0xa000\n"
"v_exp_f32 v165, v165\n"
"v_exp_f32 v166, v166\n"
"v_add_f32 v243, v243, v171\n"
"v_mfma_f32_16x16x32_bf16 a[72:75], v[212:215], v[200:203], a[72:75]\n"
"v_exp_f32 v167, v167\n"
"v_exp_f32 v180, v180\n"
"v_add_f32 v243, v243, v184\n"
"v_mfma_f32_16x16x32_bf16 a[68:71], v[212:215], v[196:199], a[68:71]\n"
"v_exp_f32 v181, v181\n"
"v_exp_f32 v182, v182\n"
"v_add_f32 v243, v243, v185\n"
"v_mfma_f32_16x16x32_bf16 a[64:67], v[212:215], v[192:195], a[64:67]\n"
"s_waitcnt lgkmcnt(6)\n"
"ds_read_b64_tr_b16 v[212:213], v247 offset:33696\n"
"ds_read_b64_tr_b16 v[214:215], v247 offset:35744\n"
"buffer_load_dwordx4 v252, s[56:59], s94 offen offset:3072 lds\n"
"v_add_f32 v243, v243, v186\n"
"v_mfma_f32_16x16x32_bf16 a[80:83], v[216:219], v[192:195], a[80:83]\n"
"s_add_u32 m0, s90, 0x6400\n"
"v_exp_f32 v183, v183\n"
"v_exp_f32 v160, v160\n"
"v_add_f32 v243, v243, v187\n"
"v_mfma_f32_16x16x32_bf16 a[84:87], v[216:219], v[196:199], a[84:87]\n"
"s_add_u32 s94, s26, 0xb800\n"
"v_exp_f32 v161, v161\n"
"v_exp_f32 v162, v162\n"
"v_mov_b32 v238, v243\n"
"v_mfma_f32_16x16x32_bf16 a[88:91], v[216:219], v[200:203], a[88:91]\n"
"v_exp_f32 v163, v163\n"
"v_exp_f32 v176, v176\n"
"v_permlane32_swap_b32 v243, v238\n"
"v_mfma_f32_16x16x32_bf16 a[92:95], v[216:219], v[204:207], a[92:95]\n"
"s_waitcnt lgkmcnt(6)\n"
"ds_read_b64_tr_b16 v[216:217], v255 offset:33312\n"
"ds_read_b64_tr_b16 v[218:219], v255 offset:35360\n"
"buffer_load_dwordx4 v252, s[56:59], s91 offen lds\n"
"v_add_f32 v243, v243, v238\n"
"v_mfma_f32_16x16x32_bf16 a[108:111], v[228:231], v[204:207], a[108:111]\n"
"s_add_u32 s91, s26, 0x80\n"
"v_exp_f32 v177, v177\n"
"v_exp_f32 v178, v178\n"
"v_mov_b32 v238, v243\n"
"v_mfma_f32_16x16x32_bf16 a[104:107], v[228:231], v[200:203], a[104:107]\n"
"v_exp_f32 v179, v179\n"
"v_exp_f32 v208, v208\n"
"v_permlane16_swap_b32 v243, v238\n"
"v_mfma_f32_16x16x32_bf16 a[100:103], v[228:231], v[196:199], a[100:103]\n"
"v_exp_f32 v209, v209\n"
"v_add_f32 v245, v245, v208\n"
"v_exp_f32 v210, v210\n"
"v_mfma_f32_16x16x32_bf16 a[96:99], v[228:231], v[192:195], a[96:99]\n"
"s_waitcnt lgkmcnt(5)\n"
"ds_read_b64_tr_b16 v[228:229], v247 offset:33792\n"
"ds_read_b64_tr_b16 v[230:231], v247 offset:35840\n"
"buffer_load_dwordx4 v252, s[56:59], s92 offen offset:1024 lds\n"
"v_add_f32 v245, v245, v209\n"
"v_mfma_f32_16x16x32_bf16 v[128:131], v[232:235], v[220:223], v[128:131]\n"
"s_waitcnt lgkmcnt(6)\n"
"v_exp_f32 v211, v211\n"
"v_add_f32 v245, v245, v210\n"
"v_add_f32 v245, v245, v211\n"
"v_mov_b32 v240, v245\n"
"v_mfma_f32_16x16x32_bf16 v[132:135], v[232:235], v[224:227], v[132:135]\n"
"s_waitcnt lgkmcnt(4)\n"
"ds_read_b64_tr_b16 v[232:233], v255 offset:33408\n"
"ds_read_b64_tr_b16 v[234:235], v255 offset:35456\n"
"v_permlane32_swap_b32 v245, v240\n"
"v_add_f32 v245, v245, v240\n"
"v_mov_b32 v240, v245\n"
"v_mfma_f32_16x16x32_bf16 a[112:115], v[212:215], v[192:195], a[112:115]\n"
"Lpada_5:\n"
"s_cbranch_execz Lpada_9\n"
"s_add_u32 s92, s26, 0x1880\n"
"v_permlane16_swap_b32 v245, v240\n"
"v_add_f32 v245, v245, v240\n"
"v_cvt_pk_bf16_f32 v208, v208, v209\n"
"v_and_b32 v240, 0xf0, v248\n"
"v_cvt_pk_bf16_f32 v209, v210, v211\n"
"v_mfma_f32_16x16x32_bf16 a[116:119], v[212:215], v[196:199], a[116:119]\n"
"buffer_load_dwordx4 v252, s[56:59], s93 offen offset:2048 lds\n"
"v_add_u32 v251, s73, v240\n"
"v_add_f32 v243, v243, v238\n"
"v_add_u32 v246, v246, v125\n"
"v_mfma_f32_16x16x32_bf16 a[120:123], v[212:215], v[200:203], a[120:123]\n"
"s_add_u32 s93, s26, 0x3080\n"
"ds_write_b64 v253, v[208:209] offset:3072\n"
"ds_write_b32 v251, v245 offset:8\n"
"v_xor_b32 v246, s72, v246\n"
"v_mfma_f32_16x16x32_bf16 a[124:127], v[212:215], v[204:207], a[124:127]\n"
"s_waitcnt lgkmcnt(6)\n"
"ds_read_b64_tr_b16 v[212:213], v247 offset:33824\n"
"ds_read_b64_tr_b16 v[214:215], v247 offset:35872\n"
"v_rcp_f32 v243, v243\n"
"v_xor_b32 v254, 0x60, v246\n"
"v_mfma_f32_16x16x32_bf16 v[136:139], v[216:219], v[220:223], v[136:139]\n"
"v_xor_b32 v249, 64, v246\n"
"v_xor_b32 v253, 32, v246\n"
"v_add_u32 v246, 0x5400, v246\n"
"v_add_f32 v241, v241, v160\n"
"v_add_f32 v241, v241, v161\n"
"v_mfma_f32_16x16x32_bf16 v[140:143], v[216:219], v[224:227], v[140:143]\n"
"s_waitcnt lgkmcnt(6)\n"
"ds_read_b64_tr_b16 v[216:217], v255 offset:33440\n"
"ds_read_b64_tr_b16 v[218:219], v255 offset:35488\n"
"buffer_load_dwordx4 v252, s[56:59], s94 offen offset:3072 lds\n"
"v_add_f32 v241, v241, v162\n"
"v_mfma_f32_16x16x32_bf16 a[140:143], v[228:231], v[204:207], a[140:143]\n"
"s_add_u32 s94, s26, 0x4880\n"
"s_waitcnt vmcnt(0) lgkmcnt(4)\n"
"s_barrier\n"
"v_add_f32 v241, v241, v163\n"
"v_add_f32 v242, v242, v164\n"
"v_mfma_f32_16x16x32_bf16 a[136:139], v[228:231], v[200:203], a[136:139]\n"
"v_add_f32 v242, v242, v165\n"
"v_add_f32 v241, v241, v176\n"
"v_add_f32 v242, v242, v166\n"
"v_add_f32 v241, v241, v177\n"
"v_add_f32 v242, v242, v167\n"
"v_mfma_f32_16x16x32_bf16 a[132:135], v[228:231], v[196:199], a[132:135]\n"
"v_add_f32 v241, v241, v178\n"
"v_add_f32 v244, v244, v172\n"
"v_add_f32 v241, v241, v179\n"
"v_add_f32 v242, v242, v180\n"
"v_add_f32 v244, v244, v173\n"
"v_mfma_f32_16x16x32_bf16 a[128:131], v[228:231], v[192:195], a[128:131]\n"
"s_add_u32 m0, s90, 0xd400\n"
"ds_read_b64_tr_b16 v[228:229], v247 offset:33920\n"
"ds_read_b64_tr_b16 v[230:231], v247 offset:35968\n"
"v_add_f32 v242, v242, v181\n"
"v_mov_b32 v236, v241\n"
"v_add_f32 v244, v244, v174\n"
"v_mfma_f32_16x16x32_bf16 v[144:147], v[232:235], v[220:223], v[144:147]\n"
"buffer_load_dwordx4 v252, s[56:59], s91 offen lds\n"
"v_add_f32 v242, v242, v182\n"
"v_permlane32_swap_b32 v241, v236\n"
"v_add_f32 v244, v244, v175\n"
"v_mfma_f32_16x16x32_bf16 v[148:151], v[232:235], v[224:227], v[148:151]\n"
"s_waitcnt lgkmcnt(4)\n"
"ds_read_b64_tr_b16 v[232:233], v247 offset:33952\n"
"ds_read_b64_tr_b16 v[234:235], v247 offset:36000\n"
"v_add_f32 v242, v242, v183\n"
"v_add_f32 v241, v241, v236\n"
"v_add_f32 v244, v244, v188\n"
"v_mfma_f32_16x16x32_bf16 a[144:147], v[212:215], v[192:195], a[144:147]\n"
"s_add_u32 s91, s26, 0x7080\n"
"v_mov_b32 v236, v241\n"
"v_mov_b32 v237, v242\n"
"v_add_f32 v244, v244, v189\n"
"v_permlane16_swap_b32 v241, v236\n"
"v_permlane32_swap_b32 v242, v237\n"
"v_mfma_f32_16x16x32_bf16 a[148:151], v[212:215], v[196:199], a[148:151]\n"
"v_add_f32 v244, v244, v190\n"
"v_add_u32 v254, 0x5400, v254\n"
"v_add_f32 v241, v241, v236\n"
"v_add_f32 v242, v242, v237\n"
"v_add_f32 v244, v244, v191\n"
"v_mfma_f32_16x16x32_bf16 a[152:155], v[212:215], v[200:203], a[152:155]\n"
"buffer_load_dwordx4 v252, s[56:59], s92 offen offset:1024 lds\n"
"v_rcp_f32 v241, v241\n"
"v_mov_b32 v237, v242\n"
"v_mfma_f32_16x16x32_bf16 a[156:159], v[212:215], v[204:207], a[156:159]\n"
"s_waitcnt lgkmcnt(4)\n"
"ds_read_b64_tr_b16 v[212:213], v247 offset:34048\n"
"ds_read_b64_tr_b16 v[214:215], v247 offset:36096\n"
"v_permlane16_swap_b32 v242, v237\n"
"v_mov_b32 v239, v244\n"
"v_add_f32 v242, v242, v237\n"
"v_mfma_f32_16x16x32_bf16 v[152:155], v[216:219], v[220:223], v[152:155]\n"
"s_add_u32 s92, s26, 0x8880\n"
"ds_read_b64_tr_b16 v[220:221], v247 offset:34080\n"
"ds_read_b64_tr_b16 v[222:223], v247 offset:36128\n"
"v_permlane32_swap_b32 v244, v239\n"
"v_rcp_f32 v242, v242\n"
"v_mfma_f32_16x16x32_bf16 v[156:159], v[216:219], v[224:227], v[156:159]\n"
"s_waitcnt lgkmcnt(6)\n"
"ds_read_b64_tr_b16 v[216:217], v247 offset:34176\n"
"ds_read_b64_tr_b16 v[218:219], v247 offset:36224\n"
"ds_read_b64_tr_b16 v[224:225], v247 offset:34208\n"
"ds_read_b64_tr_b16 v[226:227], v247 offset:36256\n"
"v_add_f32 v244, v244, v239\n"
"v_mfma_f32_16x16x32_bf16 a[172:175], v[228:231], v[204:207], a[172:175]\n"
"buffer_load_dwordx4 v252, s[56:59], s93 offen offset:2048 lds\n"
"v_mfma_f32_16x16x32_bf16 a[168:171], v[228:231], v[200:203], a[168:171]\n"
"s_add_u32 s93, s26, 0xa080\n"
"v_mov_b32 v239, v244\n"
"v_add_u32 v249, 0x5400, v249\n"
"v_mfma_f32_16x16x32_bf16 a[164:167], v[228:231], v[196:199], a[164:167]\n"
"ds_read_b32 v251, v240 offset:264\n"
"v_permlane16_swap_b32 v244, v239\n"
"v_mfma_f32_16x16x32_bf16 a[160:163], v[228:231], v[192:195], a[160:163]\n"
"s_waitcnt lgkmcnt(9)\n"
"ds_read_b64_tr_b16 v[228:229], v247 offset:49920\n"
"ds_read_b64_tr_b16 v[230:231], v247 offset:51968\n"
"v_mfma_f32_16x16x32_bf16 a[176:179], v[232:235], v[192:195], a[176:179]\n"
"buffer_load_dwordx4 v252, s[56:59], s94 offen offset:3072 lds\n"
"v_mfma_f32_16x16x32_bf16 a[180:183], v[232:235], v[196:199], a[180:183]\n"
"s_add_u32 m0, s90, 0xe400\n"
"ds_read_b32 v238, v240 offset:520\n"
"v_add_f32 v244, v244, v239\n"
"v_mfma_f32_16x16x32_bf16 a[184:187], v[232:235], v[200:203], a[184:187]\n"
"s_add_u32 s94, s26, 0xb880\n"
"v_rcp_f32 v244, v244\n"
"v_mfma_f32_16x16x32_bf16 a[188:191], v[232:235], v[204:207], a[188:191]\n"
"s_waitcnt lgkmcnt(10)\n"
"ds_read_b64_tr_b16 v[232:233], v247 offset:49952\n"
"ds_read_b64_tr_b16 v[234:235], v247 offset:52000\n"
"v_mfma_f32_16x16x32_bf16 a[204:207], v[212:215], v[204:207], a[204:207]\n"
"buffer_load_dwordx4 v252, s[56:59], s91 offen lds\n"
"v_mfma_f32_16x16x32_bf16 a[200:203], v[212:215], v[200:203], a[200:203]\n"
"s_add_u32 s91, s26, 0x100\n"
"ds_read_b32 v239, v240 offset:776\n"
"v_add_u32 v245, s74, v248\n"
"v_mfma_f32_16x16x32_bf16 a[196:199], v[212:215], v[196:199], a[196:199]\n"
"v_add_u32 v253, 0x5400, v253\n"
"v_mfma_f32_16x16x32_bf16 a[192:195], v[212:215], v[192:195], a[192:195]\n"
"s_waitcnt lgkmcnt(11)\n"
"ds_read_b64_tr_b16 v[212:213], v247 offset:50048\n"
"ds_read_b64_tr_b16 v[214:215], v247 offset:52096\n"
"v_mfma_f32_16x16x32_bf16 a[208:211], v[220:223], v[192:195], a[208:211]\n"
"buffer_load_dwordx4 v252, s[56:59], s92 offen offset:1024 lds\n"
"v_mfma_f32_16x16x32_bf16 a[212:215], v[220:223], v[196:199], a[212:215]\n"
"s_add_u32 s92, s26, 0x1900\n"
"v_mfma_f32_16x16x32_bf16 a[216:219], v[220:223], v[200:203], a[216:219]\n"
"v_mfma_f32_16x16x32_bf16 a[220:223], v[220:223], v[204:207], a[220:223]\n"
"s_waitcnt lgkmcnt(11)\n"
"ds_read_b64_tr_b16 v[220:221], v247 offset:50080\n"
"ds_read_b64_tr_b16 v[222:223], v247 offset:52128\n"
"v_mfma_f32_16x16x32_bf16 a[236:239], v[216:219], v[204:207], a[236:239]\n"
"buffer_load_dwordx4 v252, s[56:59], s93 offen offset:2048 lds\n"
"v_mfma_f32_16x16x32_bf16 a[232:235], v[216:219], v[200:203], a[232:235]\n"
"s_add_u32 s93, s26, 0x3100\n"
"v_mfma_f32_16x16x32_bf16 a[228:231], v[216:219], v[196:199], a[228:231]\n"
"v_mfma_f32_16x16x32_bf16 a[224:227], v[216:219], v[192:195], a[224:227]\n"
"s_waitcnt lgkmcnt(11)\n"
"ds_read_b64_tr_b16 v[216:217], v247 offset:50176\n"
"ds_read_b64_tr_b16 v[218:219], v247 offset:52224\n"
"s_waitcnt lgkmcnt(6)\n"
"v_add_f32 v251, v251, v239\n"
"v_mfma_f32_16x16x32_bf16 a[240:243], v[224:227], v[192:195], a[240:243]\n"
"buffer_load_dwordx4 v252, s[56:59], s94 offen offset:3072 lds\n"
"v_cvt_pk_bf16_f32 v192, v160, v161\n"
"v_cvt_pk_bf16_f32 v193, v162, v163\n"
"v_cvt_pk_bf16_f32 v194, v176, v177\n"
"v_mfma_f32_16x16x32_bf16 a[244:247], v[224:227], v[196:199], a[244:247]\n"
"s_add_u32 s94, s26, 0x4900\n"
"v_cvt_pk_bf16_f32 v196, v164, v165\n"
"v_cvt_pk_bf16_f32 v197, v166, v167\n"
"v_cvt_pk_bf16_f32 v198, v180, v181\n"
"v_cvt_pk_bf16_f32 v199, v182, v183\n"
"v_cvt_pk_bf16_f32 v195, v178, v179\n"
"v_mfma_f32_16x16x32_bf16 a[248:251], v[224:227], v[200:203], a[248:251]\n"
"v_cvt_pk_bf16_f32 v200, v168, v169\n"
"v_cvt_pk_bf16_f32 v201, v170, v171\n"
"v_cvt_pk_bf16_f32 v202, v184, v185\n"
"v_cvt_pk_bf16_f32 v203, v186, v187\n"
"v_mfma_f32_16x16x32_bf16 a[252:255], v[224:227], v[204:207], a[252:255]\n"
"v_cvt_pk_bf16_f32 v204, v172, v173\n"
"v_cvt_pk_bf16_f32 v205, v174, v175\n"
"v_cvt_pk_bf16_f32 v206, v188, v189\n"
"v_cvt_pk_bf16_f32 v207, v190, v191\n"
"v_rcp_f32 v251, v251\n"
"s_nop 2\n"
"s_cmp_lg_u64 s[76:77], 0\n"
"s_cbranch_scc1 Lslow_24\n"
"Lret_24:\n"
"v_mfma_f32_16x16x32_bf16 a[12:15], v[228:231], v[204:207], a[12:15]\n"
"ds_read_b64_tr_b16 v[224:225], v247 offset:50208\n"
"ds_read_b64_tr_b16 v[226:227], v247 offset:52256\n"
"ds_read_b32 v250, v240 offset:8\n"
"v_mfma_f32_16x16x32_bf16 a[8:11], v[228:231], v[200:203], a[8:11]\n"
"s_add_u32 m0, s90, 0x17400\n"
"v_mfma_f32_16x16x32_bf16 a[4:7], v[228:231], v[196:199], a[4:7]\n"
"buffer_load_dwordx4 v252, s[56:59], s91 offen lds\n"
"v_mfma_f32_16x16x32_bf16 a[0:3], v[228:231], v[192:195], a[0:3]\n"
"s_add_u32 s91, s26, 0x7100\n"
"ds_read_b64_tr_b16 v[228:229], v247 offset:50304\n"
"ds_read_b64_tr_b16 v[230:231], v247 offset:52352\n"
"v_accvgpr_read_b32 v24, a12\n"
"v_accvgpr_read_b32 v16, a8\n"
"v_accvgpr_read_b32 v17, a9\n"
"v_mul_f32 v16, v16, v243\n"
"v_mfma_f32_16x16x32_bf16 a[16:19], v[232:235], v[192:195], a[16:19]\n"
"v_mul_f32 v17, v17, v243\n"
"v_accvgpr_read_b32 v18, a10\n"
"v_accvgpr_read_b32 v19, a11\n"
"v_mul_f32 v18, v18, v243\n"
"v_mul_f32 v19, v19, v243\n"
"v_cvt_pk_bf16_f32 v16, v16, v17\n"
"v_mfma_f32_16x16x32_bf16 a[20:23], v[232:235], v[196:199], a[20:23]\n"
"v_cvt_pk_bf16_f32 v17, v18, v19\n"
"v_accvgpr_read_b32 v0, a0\n"
"v_accvgpr_read_b32 v1, a1\n"
"v_mul_f32 v0, v0, v241\n"
"v_mul_f32 v1, v1, v241\n"
"v_accvgpr_read_b32 v2, a2\n"
"v_mfma_f32_16x16x32_bf16 a[24:27], v[232:235], v[200:203], a[24:27]\n"
"buffer_load_dwordx4 v252, s[56:59], s92 offen offset:1024 lds\n"
"v_accvgpr_read_b32 v3, a3\n"
"v_mul_f32 v2, v2, v241\n"
"v_mul_f32 v3, v3, v241\n"
"v_cvt_pk_bf16_f32 v0, v0, v1\n"
"v_mfma_f32_16x16x32_bf16 a[28:31], v[232:235], v[204:207], a[28:31]\n"
"s_waitcnt lgkmcnt(9)\n"
"ds_read_b64_tr_b16 v[232:233], v255 offset:49920\n"
"ds_read_b64_tr_b16 v[234:235], v255 offset:51968\n"
"v_accvgpr_read_b32 v18, a24\n"
"v_accvgpr_read_b32 v19, a25\n"
"v_mul_f32 v18, v18, v243\n"
"v_mul_f32 v19, v19, v243\n"
"v_mfma_f32_16x16x32_bf16 a[44:47], v[212:215], v[204:207], a[44:47]\n"
"s_add_u32 s92, s26, 0x8900\n"
"v_cvt_pk_bf16_f32 v18, v18, v19\n"
"v_accvgpr_read_b32 v210, a27\n"
"v_accvgpr_read_b32 v19, a26\n"
"v_mul_f32 v210, v210, v243\n"
"v_mul_f32 v19, v19, v243\n"
"v_cvt_pk_bf16_f32 v19, v19, v210\n"
"v_mfma_f32_16x16x32_bf16 a[40:43], v[212:215], v[200:203], a[40:43]\n"
"buffer_store_dwordx4 v[16:19], v248, s[60:63], s84 offen sc1\n"
"v_cvt_pk_bf16_f32 v1, v2, v3\n"
"v_accvgpr_read_b32 v8, a4\n"
"v_accvgpr_read_b32 v9, a5\n"
"v_accvgpr_read_b32 v2, a16\n"
"v_mfma_f32_16x16x32_bf16 a[36:39], v[212:215], v[196:199], a[36:39]\n"
"s_add_u32 s84, s35, 0x8000\n"
"v_accvgpr_read_b32 v3, a17\n"
"v_mul_f32 v8, v8, v242\n"
"v_mul_f32 v9, v9, v242\n"
"v_accvgpr_read_b32 v10, a6\n"
"v_accvgpr_read_b32 v11, a7\n"
"v_mul_f32 v2, v2, v241\n"
"v_mfma_f32_16x16x32_bf16 a[32:35], v[212:215], v[192:195], a[32:35]\n"
"s_waitcnt lgkmcnt(9)\n"
"ds_read_b128 v[212:215], v248 offset:3072\n"
"v_mul_f32 v3, v3, v241\n"
"v_mul_f32 v10, v10, v242\n"
"v_mul_f32 v11, v11, v242\n"
"v_cvt_pk_bf16_f32 v8, v8, v9\n"
"v_cvt_pk_bf16_f32 v2, v2, v3\n"
"v_mfma_f32_16x16x32_bf16 a[48:51], v[220:223], v[192:195], a[48:51]\n"
"buffer_load_dwordx4 v252, s[56:59], s93 offen offset:2048 lds\n"
"v_cvt_pk_bf16_f32 v9, v10, v11\n"
"v_accvgpr_read_b32 v32, a32\n"
"v_accvgpr_read_b32 v33, a33\n"
"v_accvgpr_read_b32 v25, a13\n"
"v_mfma_f32_16x16x32_bf16 a[52:55], v[220:223], v[196:199], a[52:55]\n"
"s_add_u32 s93, s26, 0xa100\n"
"v_accvgpr_read_b32 v208, a19\n"
"v_accvgpr_read_b32 v3, a18\n"
"v_accvgpr_read_b32 v10, a20\n"
"v_accvgpr_read_b32 v11, a21\n"
"v_mul_f32 v32, v32, v241\n"
"v_mul_f32 v33, v33, v241\n"
"v_mfma_f32_16x16x32_bf16 a[56:59], v[220:223], v[200:203], a[56:59]\n"
"v_accvgpr_read_b32 v34, a34\n"
"v_accvgpr_read_b32 v35, a35\n"
"v_mul_f32 v24, v24, v244\n"
"v_mul_f32 v25, v25, v244\n"
"v_accvgpr_read_b32 v26, a14\n"
"v_accvgpr_read_b32 v27, a15\n"
"v_mfma_f32_16x16x32_bf16 a[60:63], v[220:223], v[204:207], a[60:63]\n"
"s_waitcnt lgkmcnt(8)\n"
"buffer_load_dwordx4 v252, s[56:59], s94 offen offset:3072 lds\n"
"ds_read_b128 v[220:223], v248 offset:4096\n"
"v_mul_f32 v208, v208, v241\n"
"v_mul_f32 v3, v3, v241\n"
"v_mul_f32 v10, v10, v242\n"
"v_mfma_f32_16x16x32_bf16 a[76:79], v[216:219], v[204:207], a[76:79]\n"
"s_add_u32 m0, s90, 0x18400\n"
"v_mul_f32 v11, v11, v242\n"
"v_mul_f32 v34, v34, v241\n"
"v_mul_f32 v35, v35, v241\n"
"v_cvt_pk_bf16_f32 v32, v32, v33\n"
"v_mul_f32 v26, v26, v244\n"
"v_mul_f32 v27, v27, v244\n"
"v_mfma_f32_16x16x32_bf16 a[72:75], v[216:219], v[200:203], a[72:75]\n"
"s_add_u32 s94, s26, 0xb900\n"
"v_cvt_pk_bf16_f32 v24, v24, v25\n"
"v_cvt_pk_bf16_f32 v3, v3, v208\n"
"v_cvt_pk_bf16_f32 v10, v10, v11\n"
"v_cvt_pk_bf16_f32 v33, v34, v35\n"
"v_cvt_pk_bf16_f32 v25, v26, v27\n"
"v_accvgpr_read_b32 v208, a23\n"
"v_mfma_f32_16x16x32_bf16 a[68:71], v[216:219], v[196:199], a[68:71]\n"
"buffer_load_dwordx4 v252, s[56:59], s91 offen lds\n"
"v_accvgpr_read_b32 v11, a22\n"
"v_accvgpr_read_b32 v34, a48\n"
"v_accvgpr_read_b32 v35, a49\n"
"v_accvgpr_read_b32 v40, a36\n"
"v_mfma_f32_16x16x32_bf16 a[64:67], v[216:219], v[192:195], a[64:67]\n"
"s_waitcnt lgkmcnt(7)\n"
"ds_read_b64_tr_b16 v[216:217], v247 offset:50336\n"
"ds_read_b64_tr_b16 v[218:219], v247 offset:52384\n"
"v_accvgpr_read_b32 v41, a37\n"
"v_accvgpr_read_b32 v26, a28\n"
"v_accvgpr_read_b32 v27, a29\n"
"v_mul_f32 v208, v208, v242\n"
"v_mfma_f32_16x16x32_bf16 a[80:83], v[224:227], v[192:195], a[80:83]\n"
"s_add_u32 s91, s26, 0x180\n"
"v_mul_f32 v11, v11, v242\n"
"v_mul_f32 v34, v34, v241\n"
"v_mul_f32 v35, v35, v241\n"
"v_mul_f32 v40, v40, v242\n"
"v_mul_f32 v41, v41, v242\n"
"v_accvgpr_read_b32 v42, a38\n"
"v_mfma_f32_16x16x32_bf16 a[84:87], v[224:227], v[196:199], a[84:87]\n"
"v_accvgpr_read_b32 v43, a39\n"
"v_mul_f32 v26, v26, v244\n"
"v_mul_f32 v27, v27, v244\n"
"v_cvt_pk_bf16_f32 v11, v11, v208\n"
"v_cvt_pk_bf16_f32 v34, v34, v35\n"
"v_mfma_f32_16x16x32_bf16 a[88:91], v[224:227], v[200:203], a[88:91]\n"
"buffer_load_dwordx4 v252, s[56:59], s92 offen offset:1024 lds\n"
"v_mul_f32 v42, v42, v242\n"
"v_mul_f32 v43, v43, v242\n"
"v_cvt_pk_bf16_f32 v40, v40, v41\n"
"v_mfma_f32_16x16x32_bf16 a[92:95], v[224:227], v[204:207], a[92:95]\n"
"s_waitcnt lgkmcnt(6)\n"
"ds_read_b64_tr_b16 v[224:225], v255 offset:49952\n"
"ds_read_b64_tr_b16 v[226:227], v255 offset:52000\n"
"v_cvt_pk_bf16_f32 v26, v26, v27\n"
"v_accvgpr_read_b32 v208, a51\n"
"v_accvgpr_read_b32 v35, a50\n"
"v_cvt_pk_bf16_f32 v41, v42, v43\n"
"v_mfma_f32_16x16x32_bf16 a[108:111], v[228:231], v[204:207], a[108:111]\n"
"s_add_u32 s92, s26, 0x1980\n"
"v_accvgpr_read_b32 v210, a31\n"
"v_accvgpr_read_b32 v27, a30\n"
"v_mul_f32 v208, v208, v241\n"
"v_mul_f32 v35, v35, v241\n"
"v_accvgpr_read_b32 v42, a52\n"
"v_accvgpr_read_b32 v43, a53\n"
"v_mfma_f32_16x16x32_bf16 a[104:107], v[228:231], v[200:203], a[104:107]\n"
"v_mul_f32 v210, v210, v244\n"
"v_mul_f32 v27, v27, v244\n"
"v_cvt_pk_bf16_f32 v35, v35, v208\n"
"v_mul_f32 v42, v42, v242\n"
"v_mul_f32 v43, v43, v242\n"
"v_cvt_pk_bf16_f32 v27, v27, v210\n"
"v_mfma_f32_16x16x32_bf16 a[100:103], v[228:231], v[196:199], a[100:103]\n"
"buffer_load_dwordx4 v252, s[56:59], s93 offen offset:2048 lds\n"
"v_cvt_pk_bf16_f32 v42, v42, v43\n"
"v_accvgpr_read_b32 v208, a55\n"
"v_accvgpr_read_b32 v43, a54\n"
"v_mfma_f32_16x16x32_bf16 a[96:99], v[228:231], v[192:195], a[96:99]\n"
"s_waitcnt lgkmcnt(5)\n"
"ds_read_b64_tr_b16 v[228:229], v247 offset:50432\n"
"ds_read_b64_tr_b16 v[230:231], v247 offset:52480\n"
"v_mul_f32 v208, v208, v242\n"
"v_mul_f32 v43, v43, v242\n"
"v_cvt_pk_bf16_f32 v43, v43, v208\n"
"v_accvgpr_read_b32 v48, a40\n"
"v_mfma_f32_16x16x32_bf16 v[128:131], v[232:235], v[212:215], v[128:131]\n"
"s_waitcnt lgkmcnt(6)\n"
"v_accvgpr_read_b32 v49, a41\n"
"v_accvgpr_read_b32 v50, a42\n"
"v_accvgpr_read_b32 v51, a43\n"
"v_accvgpr_read_b32 v56, a44\n"
"v_accvgpr_read_b32 v57, a45\n"
"v_accvgpr_read_b32 v58, a46\n"
"v_mfma_f32_16x16x32_bf16 v[132:135], v[232:235], v[220:223], v[132:135]\n"
"s_waitcnt lgkmcnt(4)\n"
"ds_read_b64_tr_b16 v[232:233], v255 offset:50048\n"
"ds_read_b64_tr_b16 v[234:235], v255 offset:52096\n"
"buffer_store_dwordx4 v[24:27], v248, s[60:63], s67 offen sc1\n"
"v_accvgpr_read_b32 v59, a47\n"
"v_mfma_f32_16x16x32_bf16 a[112:115], v[216:219], v[192:195], a[112:115]\n"
"s_add_u32 s93, s26, 0x3180\n"
"v_mul_f32 v48, v48, v243\n"
"v_mul_f32 v49, v49, v243\n"
"v_mul_f32 v50, v50, v243\n"
"v_mul_f32 v51, v51, v243\n"
"v_cvt_pk_bf16_f32 v48, v48, v49\n"
"v_cvt_pk_bf16_f32 v49, v50, v51\n"
"v_mfma_f32_16x16x32_bf16 a[116:119], v[216:219], v[196:199], a[116:119]\n"
"s_add_u32 s67, s35, 0xa000\n"
"v_accvgpr_read_b32 v50, a56\n"
"v_accvgpr_read_b32 v51, a57\n"
"v_mul_f32 v50, v50, v243\n"
"v_mul_f32 v51, v51, v243\n"
"v_cvt_pk_bf16_f32 v50, v50, v51\n"
"v_mfma_f32_16x16x32_bf16 a[120:123], v[216:219], v[200:203], a[120:123]\n"
"buffer_load_dwordx4 v252, s[56:59], s94 offen offset:3072 lds\n"
"v_accvgpr_read_b32 v64, a64\n"
"v_accvgpr_read_b32 v65, a65\n"
"v_accvgpr_read_b32 v210, a59\n"
"v_mfma_f32_16x16x32_bf16 a[124:127], v[216:219], v[204:207], a[124:127]\n"
"s_waitcnt lgkmcnt(4)\n"
"ds_read_b64_tr_b16 v[216:217], v247 offset:50464\n"
"ds_read_b64_tr_b16 v[218:219], v247 offset:52512\n"
"v_accvgpr_read_b32 v51, a58\n"
"v_mul_f32 v64, v64, v241\n"
"v_mul_f32 v65, v65, v241\n"
"v_accvgpr_read_b32 v66, a66\n"
"v_mfma_f32_16x16x32_bf16 v[136:139], v[224:227], v[212:215], v[136:139]\n"
"s_add_u32 s94, s26, 0x4980\n"
"v_accvgpr_read_b32 v67, a67\n"
"v_mul_f32 v66, v66, v241\n"
"v_mul_f32 v67, v67, v241\n"
"v_cvt_pk_bf16_f32 v64, v64, v65\n"
"v_mul_f32 v56, v56, v244\n"
"v_mul_f32 v57, v57, v244\n"
"v_mfma_f32_16x16x32_bf16 v[140:143], v[224:227], v[220:223], v[140:143]\n"
"s_waitcnt lgkmcnt(4)\n"
"ds_read_b64_tr_b16 v[224:225], v255 offset:50080\n"
"ds_read_b64_tr_b16 v[226:227], v255 offset:52128\n"
"v_cvt_pk_bf16_f32 v65, v66, v67\n"
"v_accvgpr_read_b32 v72, a68\n"
"v_accvgpr_read_b32 v73, a69\n"
"v_mfma_f32_16x16x32_bf16 a[140:143], v[228:231], v[204:207], a[140:143]\n"
"v_mul_f32 v58, v58, v244\n"
"v_mul_f32 v59, v59, v244\n"
"v_cvt_pk_bf16_f32 v56, v56, v57\n"
"v_accvgpr_read_b32 v66, a80\n"
"v_accvgpr_read_b32 v67, a81\n"
"v_mfma_f32_16x16x32_bf16 a[136:139], v[228:231], v[200:203], a[136:139]\n"
"v_mul_f32 v72, v72, v242\n"
"v_mul_f32 v73, v73, v242\n"
"v_accvgpr_read_b32 v74, a70\n"
"v_accvgpr_read_b32 v75, a71\n"
"v_mul_f32 v132, v132, v251\n"
"v_mul_f32 v133, v133, v251\n"
"v_mfma_f32_16x16x32_bf16 a[132:135], v[228:231], v[196:199], a[132:135]\n"
"v_cvt_pk_bf16_f32 v57, v58, v59\n"
"v_mul_f32 v66, v66, v241\n"
"v_mul_f32 v67, v67, v241\n"
"v_mul_f32 v74, v74, v242\n"
"v_mul_f32 v75, v75, v242\n"
"v_mfma_f32_16x16x32_bf16 a[128:131], v[228:231], v[192:195], a[128:131]\n"
"s_waitcnt lgkmcnt(4)\n"
"ds_read_b64_tr_b16 v[228:229], v247 offset:50560\n"
"ds_read_b64_tr_b16 v[230:231], v247 offset:52608\n"
"v_cvt_pk_bf16_f32 v72, v72, v73\n"
"v_mul_f32 v134, v134, v251\n"
"v_mul_f32 v135, v135, v251\n"
"v_mfma_f32_16x16x32_bf16 v[144:147], v[232:235], v[212:215], v[144:147]\n"
"v_cvt_pk_bf16_f32 v132, v132, v133\n"
"v_accvgpr_read_b32 v58, a60\n"
"v_accvgpr_read_b32 v59, a61\n"
"v_cvt_pk_bf16_f32 v66, v66, v67\n"
"v_cvt_pk_bf16_f32 v73, v74, v75\n"
"v_mfma_f32_16x16x32_bf16 v[148:151], v[232:235], v[220:223], v[148:151]\n"
"s_waitcnt lgkmcnt(4)\n"
"ds_read_b64_tr_b16 v[232:233], v247 offset:50592\n"
"ds_read_b64_tr_b16 v[234:235], v247 offset:52640\n"
"v_accvgpr_read_b32 v96, a96\n"
"v_accvgpr_read_b32 v97, a97\n"
"v_mul_f32 v140, v140, v251\n"
"v_mfma_f32_16x16x32_bf16 a[144:147], v[216:219], v[192:195], a[144:147]\n"
"v_mul_f32 v141, v141, v251\n"
"v_mul_f32 v142, v142, v251\n"
"v_mul_f32 v143, v143, v251\n"
"v_cvt_pk_bf16_f32 v133, v134, v135\n"
"v_mul_f32 v210, v210, v243\n"
"v_mfma_f32_16x16x32_bf16 a[148:151], v[216:219], v[196:199], a[148:151]\n"
"v_mul_f32 v51, v51, v243\n"
"v_mul_f32 v58, v58, v244\n"
"v_mul_f32 v59, v59, v244\n"
"v_accvgpr_read_b32 v208, a83\n"
"v_accvgpr_read_b32 v67, a82\n"
"v_accvgpr_read_b32 v74, a84\n"
"v_mfma_f32_16x16x32_bf16 a[152:155], v[216:219], v[200:203], a[152:155]\n"
"v_accvgpr_read_b32 v75, a85\n"
"v_mul_f32 v96, v96, v241\n"
"v_mul_f32 v97, v97, v241\n"
"v_accvgpr_read_b32 v98, a98\n"
"v_accvgpr_read_b32 v99, a99\n"
"v_mfma_f32_16x16x32_bf16 a[156:159], v[216:219], v[204:207], a[156:159]\n"
"s_waitcnt lgkmcnt(4)\n"
"ds_read_b64_tr_b16 v[216:217], v247 offset:50688\n"
"ds_read_b64_tr_b16 v[218:219], v247 offset:52736\n"
"v_cvt_pk_bf16_f32 v134, v140, v141\n"
"v_cvt_pk_bf16_f32 v135, v142, v143\n"
"v_cvt_pk_bf16_f32 v51, v51, v210\n"
"v_mfma_f32_16x16x32_bf16 v[152:155], v[224:227], v[212:215], v[152:155]\n"
"ds_read_b64_tr_b16 v[212:213], v247 offset:50720\n"
"ds_read_b64_tr_b16 v[214:215], v247 offset:52768\n"
"v_cvt_pk_bf16_f32 v58, v58, v59\n"
"v_mul_f32 v208, v208, v241\n"
"v_mul_f32 v67, v67, v241\n"
"v_mfma_f32_16x16x32_bf16 v[156:159], v[224:227], v[220:223], v[156:159]\n"
"s_waitcnt lgkmcnt(6)\n"
"ds_read_b64_tr_b16 v[220:221], v247 offset:50816\n"
"ds_read_b64_tr_b16 v[222:223], v247 offset:52864\n"
"ds_read_b64_tr_b16 v[224:225], v247 offset:50848\n"
"ds_read_b64_tr_b16 v[226:227], v247 offset:52896\n"
"v_mul_f32 v74, v74, v242\n"
"v_mfma_f32_16x16x32_bf16 a[172:175], v[228:231], v[204:207], a[172:175]\n"
"v_mul_f32 v75, v75, v242\n"
"v_mul_f32 v98, v98, v241\n"
"v_mul_f32 v99, v99, v241\n"
"v_cvt_pk_bf16_f32 v96, v96, v97\n"
"v_accvgpr_read_b32 v210, a63\n"
"v_mfma_f32_16x16x32_bf16 a[168:171], v[228:231], v[200:203], a[168:171]\n"
"buffer_store_dwordx4 v[132:135], v248, s[60:63], s68 offen sc1\n"
"v_accvgpr_read_b32 v59, a62\n"
"v_cvt_pk_bf16_f32 v67, v67, v208\n"
"v_cvt_pk_bf16_f32 v74, v74, v75\n"
"v_mfma_f32_16x16x32_bf16 a[164:167], v[228:231], v[196:199], a[164:167]\n"
"s_add_u32 s68, s35, 0x4000\n"
"v_cvt_pk_bf16_f32 v97, v98, v99\n"
"v_accvgpr_read_b32 v104, a100\n"
"v_accvgpr_read_b32 v105, a101\n"
"v_accvgpr_read_b32 v208, a87\n"
"v_accvgpr_read_b32 v75, a86\n"
"v_accvgpr_read_b32 v98, a112\n"
"v_mfma_f32_16x16x32_bf16 a[160:163], v[228:231], v[192:195], a[160:163]\n"
"s_waitcnt lgkmcnt(8)\n"
"ds_read_b64_tr_b16 v[228:229], v246 offset:0\n"
"ds_read_b64_tr_b16 v[230:231], v246 offset:2048\n"
"v_accvgpr_read_b32 v99, a113\n"
"v_mul_f32 v104, v104, v242\n"
"v_mul_f32 v105, v105, v242\n"
"v_mfma_f32_16x16x32_bf16 a[176:179], v[232:235], v[192:195], a[176:179]\n"
"v_accvgpr_read_b32 v106, a102\n"
"v_accvgpr_read_b32 v107, a103\n"
"v_mul_f32 v208, v208, v242\n"
"v_mul_f32 v75, v75, v242\n"
"v_mul_f32 v98, v98, v241\n"
"v_mfma_f32_16x16x32_bf16 a[180:183], v[232:235], v[196:199], a[180:183]\n"
"v_mul_f32 v99, v99, v241\n"
"v_mul_f32 v106, v106, v242\n"
"v_mul_f32 v107, v107, v242\n"
"v_cvt_pk_bf16_f32 v104, v104, v105\n"
"v_cvt_pk_bf16_f32 v75, v75, v208\n"
"v_mfma_f32_16x16x32_bf16 a[184:187], v[232:235], v[200:203], a[184:187]\n"
"v_cvt_pk_bf16_f32 v98, v98, v99\n"
"v_cvt_pk_bf16_f32 v105, v106, v107\n"
"v_accvgpr_read_b32 v208, a115\n"
"v_accvgpr_read_b32 v99, a114\n"
"v_accvgpr_read_b32 v106, a116\n"
"v_mfma_f32_16x16x32_bf16 a[188:191], v[232:235], v[204:207], a[188:191]\n"
"s_waitcnt lgkmcnt(8)\n"
"ds_read_b64_tr_b16 v[232:233], v246 offset:4096\n"
"ds_read_b64_tr_b16 v[234:235], v246 offset:6144\n"
"v_accvgpr_read_b32 v107, a117\n"
"v_mul_f32 v208, v208, v241\n"
"v_mul_f32 v99, v99, v241\n"
"v_mfma_f32_16x16x32_bf16 a[204:207], v[216:219], v[204:207], a[204:207]\n"
"v_mul_f32 v106, v106, v242\n"
"v_mul_f32 v107, v107, v242\n"
"v_cvt_pk_bf16_f32 v99, v99, v208\n"
"v_cvt_pk_bf16_f32 v106, v106, v107\n"
"v_accvgpr_read_b32 v208, a119\n"
"v_mfma_f32_16x16x32_bf16 a[200:203], v[216:219], v[200:203], a[200:203]\n"
"buffer_store_dwordx4 v[48:51], v248, s[60:63], s68 offen sc1\n"
"v_accvgpr_read_b32 v107, a118\n"
"v_mul_f32 v208, v208, v242\n"
"v_mul_f32 v107, v107, v242\n"
"v_cvt_pk_bf16_f32 v107, v107, v208\n"
"v_mfma_f32_16x16x32_bf16 a[196:199], v[216:219], v[196:199], a[196:199]\n"
"s_add_u32 s68, s35, 0xc000\n"
"v_add_f32 v250, v250, v238\n"
"v_rcp_f32 v250, v250\n"
"v_mul_f32 v148, v148, v251\n"
"v_mul_f32 v128, v128, v250\n"
"v_mfma_f32_16x16x32_bf16 a[192:195], v[216:219], v[192:195], a[192:195]\n"
"s_waitcnt lgkmcnt(8)\n"
"ds_read_b64_tr_b16 v[216:217], v254 offset:0\n"
"ds_read_b64_tr_b16 v[218:219], v254 offset:2048\n"
"v_mul_f32 v129, v129, v250\n"
"v_mul_f32 v144, v144, v250\n"
"v_mul_f32 v145, v145, v250\n"
"v_mfma_f32_16x16x32_bf16 a[208:211], v[212:215], v[192:195], a[208:211]\n"
"v_mul_f32 v130, v130, v250\n"
"v_mul_f32 v131, v131, v250\n"
"v_cvt_pk_bf16_f32 v128, v128, v129\n"
"v_mul_f32 v146, v146, v250\n"
"v_mul_f32 v147, v147, v250\n"
"v_mfma_f32_16x16x32_bf16 a[212:215], v[212:215], v[196:199], a[212:215]\n"
"v_cvt_pk_bf16_f32 v144, v144, v145\n"
"v_mul_f32 v136, v136, v250\n"
"v_mul_f32 v137, v137, v250\n"
"v_mul_f32 v138, v138, v250\n"
"v_mul_f32 v139, v139, v250\n"
"v_mfma_f32_16x16x32_bf16 a[216:219], v[212:215], v[200:203], a[216:219]\n"
"v_cvt_pk_bf16_f32 v129, v130, v131\n"
"v_mul_f32 v152, v152, v250\n"
"v_mul_f32 v153, v153, v250\n"
"v_mul_f32 v154, v154, v250\n"
"v_mul_f32 v155, v155, v250\n"
"v_cvt_pk_bf16_f32 v145, v146, v147\n"
"v_mfma_f32_16x16x32_bf16 a[220:223], v[212:215], v[204:207], a[220:223]\n"
"s_waitcnt lgkmcnt(8)\n"
"ds_read_b64_tr_b16 v[212:213], v254 offset:4096\n"
"ds_read_b64_tr_b16 v[214:215], v254 offset:6144\n"
"v_cvt_pk_bf16_f32 v130, v136, v137\n"
"v_cvt_pk_bf16_f32 v131, v138, v139\n"
"v_cvt_pk_bf16_f32 v146, v152, v153\n"
"v_cvt_pk_bf16_f32 v147, v154, v155\n"
"v_mfma_f32_16x16x32_bf16 a[236:239], v[220:223], v[204:207], a[236:239]\n"
"Lpada_6:\n"
"s_cbranch_execz Lpada_10\n"
"ds_write_b128 v245, v[128:131] offset:5120\n"
"ds_write_b128 v245, v[144:147] offset:6144\n"
"v_mul_f32 v149, v149, v251\n"
"v_mul_f32 v150, v150, v251\n"
"v_mfma_f32_16x16x32_bf16 a[232:235], v[220:223], v[200:203], a[232:235]\n"
"v_mul_f32 v151, v151, v251\n"
"v_cvt_pk_bf16_f32 v148, v148, v149\n"
"v_mul_f32 v156, v156, v251\n"
"v_mul_f32 v157, v157, v251\n"
"v_mul_f32 v158, v158, v251\n"
"v_mul_f32 v159, v159, v251\n"
"v_mfma_f32_16x16x32_bf16 a[228:231], v[220:223], v[196:199], a[228:231]\n"
"v_cvt_pk_bf16_f32 v149, v150, v151\n"
"v_cvt_pk_bf16_f32 v150, v156, v157\n"
"v_cvt_pk_bf16_f32 v151, v158, v159\n"
"v_mul_f32 v210, v210, v244\n"
"v_mul_f32 v59, v59, v244\n"
"v_cvt_pk_bf16_f32 v59, v59, v210\n"
"v_mfma_f32_16x16x32_bf16 a[224:227], v[220:223], v[192:195], a[224:227]\n"
"s_waitcnt lgkmcnt(10)\n"
"ds_read_b64_tr_b16 v[220:221], v249 offset:0\n"
"ds_read_b64_tr_b16 v[222:223], v249 offset:2048\n"
"buffer_store_dwordx4 v[148:151], v248, s[60:63], s83 offen sc1\n"
"v_accvgpr_read_b32 v4, a128\n"
"v_accvgpr_read_b32 v5, a129\n"
"v_mfma_f32_16x16x32_bf16 a[240:243], v[224:227], v[192:195], a[240:243]\n"
"s_add_u32 s83, s35, 0x6000\n"
"v_mul_f32 v4, v4, v241\n"
"v_mul_f32 v5, v5, v241\n"
"v_accvgpr_read_b32 v6, a130\n"
"v_accvgpr_read_b32 v7, a131\n"
"v_mul_f32 v6, v6, v241\n"
"v_mul_f32 v7, v7, v241\n"
"v_mfma_f32_16x16x32_bf16 a[244:247], v[224:227], v[196:199], a[244:247]\n"
"v_cvt_pk_bf16_f32 v4, v4, v5\n"
"v_cvt_pk_bf16_f32 v5, v6, v7\n"
"v_accvgpr_read_b32 v12, a132\n"
"v_accvgpr_read_b32 v13, a133\n"
"v_accvgpr_read_b32 v6, a144\n"
"v_accvgpr_read_b32 v7, a145\n"
"v_mfma_f32_16x16x32_bf16 a[248:251], v[224:227], v[200:203], a[248:251]\n"
"v_mul_f32 v12, v12, v242\n"
"v_mul_f32 v13, v13, v242\n"
"v_accvgpr_read_b32 v14, a134\n"
"v_accvgpr_read_b32 v15, a135\n"
"v_mul_f32 v6, v6, v241\n"
"v_mul_f32 v7, v7, v241\n"
"v_mfma_f32_16x16x32_bf16 a[252:255], v[224:227], v[204:207], a[252:255]\n"
"s_waitcnt lgkmcnt(10)\n"
"ds_read_b64_tr_b16 v[224:225], v249 offset:4096\n"
"ds_read_b64_tr_b16 v[226:227], v249 offset:6144\n"
"v_mul_f32 v14, v14, v242\n"
"v_mul_f32 v15, v15, v242\n"
"v_cvt_pk_bf16_f32 v12, v12, v13\n"
"v_cvt_pk_bf16_f32 v6, v6, v7\n"
"v_mfma_f32_16x16x32_bf16 a[24:27], v[228:231], v[0:3], 0\n"
"s_waitcnt lgkmcnt(10)\n"
"v_cvt_pk_bf16_f32 v13, v14, v15\n"
"v_accvgpr_read_b32 v36, a160\n"
"v_accvgpr_read_b32 v37, a161\n"
"v_accvgpr_read_b32 v208, a147\n"
"v_accvgpr_read_b32 v7, a146\n"
"v_accvgpr_read_b32 v14, a148\n"
"v_mfma_f32_16x16x32_bf16 a[24:27], v[232:235], v[32:35], a[24:27]\n"
"v_accvgpr_read_b32 v15, a149\n"
"v_mul_f32 v36, v36, v241\n"
"v_mul_f32 v37, v37, v241\n"
"v_accvgpr_read_b32 v38, a162\n"
"v_accvgpr_read_b32 v39, a163\n"
"v_mul_f32 v208, v208, v241\n"
"v_mfma_f32_16x16x32_bf16 a[28:31], v[232:235], v[40:43], 0\n"
"ds_read_b64_tr_b16 v[232:233], v246 offset:8192\n"
"ds_read_b64_tr_b16 v[234:235], v246 offset:10240\n"
"v_mul_f32 v7, v7, v241\n"
"v_mul_f32 v14, v14, v242\n"
"v_mul_f32 v15, v15, v242\n"
"v_mul_f32 v38, v38, v241\n"
"v_mfma_f32_16x16x32_bf16 a[28:31], v[228:231], v[8:11], a[28:31]\n"
"s_waitcnt lgkmcnt(10)\n"
"ds_read_b64_tr_b16 v[228:229], v246 offset:12288\n"
"ds_read_b64_tr_b16 v[230:231], v246 offset:14336\n"
"buffer_store_dwordx4 v[56:59], v248, s[60:63], s83 offen sc1\n"
"v_mul_f32 v39, v39, v241\n"
"v_cvt_pk_bf16_f32 v36, v36, v37\n"
"v_mfma_f32_16x16x32_bf16 a[40:43], v[216:219], v[0:3], 0\n"
"s_waitcnt lgkmcnt(10)\n"
"v_cvt_pk_bf16_f32 v7, v7, v208\n"
"v_cvt_pk_bf16_f32 v14, v14, v15\n"
"v_cvt_pk_bf16_f32 v37, v38, v39\n"
"v_accvgpr_read_b32 v208, a151\n"
"v_accvgpr_read_b32 v15, a150\n"
"v_accvgpr_read_b32 v38, a176\n"
"v_mfma_f32_16x16x32_bf16 a[40:43], v[212:215], v[32:35], a[40:43]\n"
"s_add_u32 s83, s35, 0xe000\n"
"v_accvgpr_read_b32 v39, a177\n"
"v_accvgpr_read_b32 v44, a164\n"
"v_accvgpr_read_b32 v45, a165\n"
"v_mul_f32 v208, v208, v242\n"
"v_mul_f32 v15, v15, v242\n"
"v_mul_f32 v38, v38, v241\n"
"v_mfma_f32_16x16x32_bf16 a[44:47], v[212:215], v[40:43], 0\n"
"ds_read_b64_tr_b16 v[212:213], v254 offset:8192\n"
"ds_read_b64_tr_b16 v[214:215], v254 offset:10240\n"
"v_mul_f32 v39, v39, v241\n"
"v_mul_f32 v44, v44, v242\n"
"v_mul_f32 v45, v45, v242\n"
"v_accvgpr_read_b32 v46, a166\n"
"v_mfma_f32_16x16x32_bf16 a[44:47], v[216:219], v[8:11], a[44:47]\n"
"s_waitcnt lgkmcnt(8)\n"
"ds_read_b64_tr_b16 v[216:217], v254 offset:12288\n"
"ds_read_b64_tr_b16 v[218:219], v254 offset:14336\n"
"v_accvgpr_read_b32 v47, a167\n"
"v_cvt_pk_bf16_f32 v15, v15, v208\n"
"v_cvt_pk_bf16_f32 v38, v38, v39\n"
"v_mul_f32 v46, v46, v242\n"
"v_mfma_f32_16x16x32_bf16 a[56:59], v[220:223], v[0:3], 0\n"
"s_waitcnt lgkmcnt(8)\n"
"s_barrier\n"
"v_mul_f32 v47, v47, v242\n"
"v_cvt_pk_bf16_f32 v44, v44, v45\n"
"v_accvgpr_read_b32 v208, a179\n"
"v_accvgpr_read_b32 v39, a178\n"
"v_mfma_f32_16x16x32_bf16 a[56:59], v[224:227], v[32:35], a[56:59]\n"
"v_cvt_pk_bf16_f32 v45, v46, v47\n"
"v_mul_f32 v208, v208, v241\n"
"v_mul_f32 v39, v39, v241\n"
"v_accvgpr_read_b32 v46, a180\n"
"v_accvgpr_read_b32 v47, a181\n"
"v_cvt_pk_bf16_f32 v39, v39, v208\n"
"v_mfma_f32_16x16x32_bf16 a[60:63], v[224:227], v[40:43], 0\n"
"s_add_u32 m0, s90, 0x1f400\n"
"ds_read_b64_tr_b16 v[224:225], v249 offset:8192\n"
"ds_read_b64_tr_b16 v[226:227], v249 offset:10240\n"
"v_mul_f32 v46, v46, v242\n"
"v_mul_f32 v47, v47, v242\n"
"v_cvt_pk_bf16_f32 v46, v46, v47\n"
"v_accvgpr_read_b32 v208, a183\n"
"v_mfma_f32_16x16x32_bf16 a[60:63], v[220:223], v[8:11], a[60:63]\n"
"s_waitcnt lgkmcnt(8)\n"
"ds_read_b64_tr_b16 v[220:221], v249 offset:12288\n"
"ds_read_b64_tr_b16 v[222:223], v249 offset:14336\n"
"buffer_load_dwordx4 v252, s[56:59], s91 offen lds\n"
"v_accvgpr_read_b32 v47, a182\n"
"v_mfma_f32_16x16x32_bf16 a[28:31], v[232:235], v[72:75], a[28:31]\n"
"s_waitcnt lgkmcnt(8)\n"
"v_mul_f32 v208, v208, v242\n"
"v_mul_f32 v47, v47, v242\n"
"v_cvt_pk_bf16_f32 v47, v47, v208\n"
"v_accvgpr_read_b32 v80, a72\n"
"v_accvgpr_read_b32 v81, a73\n"
"v_mul_f32 v80, v80, v243\n"
"v_mfma_f32_16x16x32_bf16 a[28:31], v[228:231], v[104:107], a[28:31]\n"
"s_add_u32 s91, s26, 0x7180\n"
"v_mul_f32 v81, v81, v243\n"
"v_accvgpr_read_b32 v82, a74\n"
"v_accvgpr_read_b32 v83, a75\n"
"v_mul_f32 v82, v82, v243\n"
"v_mul_f32 v83, v83, v243\n"
"v_cvt_pk_bf16_f32 v80, v80, v81\n"
"v_mfma_f32_16x16x32_bf16 a[24:27], v[228:231], v[96:99], a[24:27]\n"
"ds_read_b64_tr_b16 v[228:229], v246 offset:16384\n"
"ds_read_b64_tr_b16 v[230:231], v246 offset:18432\n"
"buffer_load_dwordx4 v252, s[56:59], s92 offen offset:1024 lds\n"
"v_cvt_pk_bf16_f32 v81, v82, v83\n"
"v_mfma_f32_16x16x32_bf16 a[24:27], v[232:235], v[64:67], a[24:27]\n"
"s_waitcnt lgkmcnt(8)\n"
"ds_read_b64_tr_b16 v[232:233], v246 offset:20480\n"
"ds_read_b64_tr_b16 v[234:235], v246 offset:22528\n"
"v_accvgpr_read_b32 v82, a88\n"
"v_accvgpr_read_b32 v83, a89\n"
"v_mul_f32 v82, v82, v243\n"
"v_mul_f32 v83, v83, v243\n"
"v_mfma_f32_16x16x32_bf16 a[44:47], v[212:215], v[72:75], a[44:47]\n"
"s_waitcnt lgkmcnt(8)\n"
"v_cvt_pk_bf16_f32 v82, v82, v83\n"
"v_accvgpr_read_b32 v210, a91\n"
"v_accvgpr_read_b32 v83, a90\n"
"v_mul_f32 v210, v210, v243\n"
"v_mul_f32 v83, v83, v243\n"
"v_cvt_pk_bf16_f32 v83, v83, v210\n"
"v_mfma_f32_16x16x32_bf16 a[44:47], v[216:219], v[104:107], a[44:47]\n"
"s_add_u32 s92, s26, 0x8980\n"
"buffer_load_dwordx4 v252, s[56:59], s93 offen offset:2048 lds\n"
"v_accvgpr_read_b32 v68, a192\n"
"v_accvgpr_read_b32 v69, a193\n"
"v_mul_f32 v68, v68, v241\n"
"v_mul_f32 v69, v69, v241\n"
"v_mfma_f32_16x16x32_bf16 a[40:43], v[216:219], v[96:99], a[40:43]\n"
"s_add_u32 s93, s26, 0xa180\n"
"ds_read_b64_tr_b16 v[216:217], v254 offset:16384\n"
"ds_read_b64_tr_b16 v[218:219], v254 offset:18432\n"
"v_accvgpr_read_b32 v70, a194\n"
"v_accvgpr_read_b32 v71, a195\n"
"v_mul_f32 v70, v70, v241\n"
"v_mul_f32 v71, v71, v241\n"
"v_mfma_f32_16x16x32_bf16 a[40:43], v[212:215], v[64:67], a[40:43]\n"
"s_waitcnt lgkmcnt(8)\n"
"ds_read_b64_tr_b16 v[212:213], v254 offset:20480\n"
"ds_read_b64_tr_b16 v[214:215], v254 offset:22528\n"
"v_cvt_pk_bf16_f32 v68, v68, v69\n"
"v_cvt_pk_bf16_f32 v69, v70, v71\n"
"v_accvgpr_read_b32 v76, a196\n"
"v_accvgpr_read_b32 v77, a197\n"
"v_mfma_f32_16x16x32_bf16 a[60:63], v[224:227], v[72:75], a[60:63]\n"
"s_waitcnt lgkmcnt(8)\n"
"buffer_load_dwordx4 v252, s[56:59], s94 offen offset:3072 lds\n"
"v_accvgpr_read_b32 v70, a208\n"
"v_accvgpr_read_b32 v71, a209\n"
"v_mul_f32 v76, v76, v242\n"
"v_mfma_f32_16x16x32_bf16 a[60:63], v[220:223], v[104:107], a[60:63]\n"
"s_add_u32 m0, s90, 0x20400\n"
"v_mul_f32 v77, v77, v242\n"
"v_accvgpr_read_b32 v78, a198\n"
"v_accvgpr_read_b32 v79, a199\n"
"v_mul_f32 v70, v70, v241\n"
"v_mul_f32 v71, v71, v241\n"
"v_mul_f32 v78, v78, v242\n"
"v_mfma_f32_16x16x32_bf16 a[56:59], v[220:223], v[96:99], a[56:59]\n"
"s_add_u32 s94, s26, 0xb980\n"
"ds_read_b64_tr_b16 v[220:221], v249 offset:16384\n"
"ds_read_b64_tr_b16 v[222:223], v249 offset:18432\n"
"v_mul_f32 v79, v79, v242\n"
"v_cvt_pk_bf16_f32 v76, v76, v77\n"
"v_cvt_pk_bf16_f32 v70, v70, v71\n"
"v_cvt_pk_bf16_f32 v77, v78, v79\n"
"v_mfma_f32_16x16x32_bf16 a[56:59], v[224:227], v[64:67], a[56:59]\n"
"s_waitcnt lgkmcnt(8)\n"
"ds_read_b64_tr_b16 v[224:225], v249 offset:20480\n"
"ds_read_b64_tr_b16 v[226:227], v249 offset:22528\n"
"buffer_load_dwordx4 v252, s[56:59], s91 offen lds\n"
"v_accvgpr_read_b32 v100, a224\n"
"v_accvgpr_read_b32 v101, a225\n"
"v_mfma_f32_16x16x32_bf16 a[24:27], v[228:231], v[4:7], a[24:27]\n"
"s_waitcnt lgkmcnt(8)\n"
"v_accvgpr_read_b32 v208, a211\n"
"v_accvgpr_read_b32 v71, a210\n"
"v_accvgpr_read_b32 v78, a212\n"
"v_accvgpr_read_b32 v79, a213\n"
"v_mul_f32 v100, v100, v241\n"
"v_mul_f32 v101, v101, v241\n"
"v_mfma_f32_16x16x32_bf16 a[24:27], v[232:235], v[36:39], a[24:27]\n"
"s_add_u32 s91, s26, 0x200\n"
"v_accvgpr_read_b32 v102, a226\n"
"v_accvgpr_read_b32 v103, a227\n"
"v_mul_f32 v208, v208, v241\n"
"v_mul_f32 v71, v71, v241\n"
"v_mul_f32 v78, v78, v242\n"
"v_mul_f32 v79, v79, v242\n"
"v_mfma_f32_16x16x32_bf16 a[28:31], v[232:235], v[44:47], a[28:31]\n"
"ds_read_b64_tr_b16 v[232:233], v246 offset:24576\n"
"ds_read_b64_tr_b16 v[234:235], v246 offset:26624\n"
"buffer_store_dwordx4 v[80:83], v248, s[60:63], s84 offen sc1\n"
"v_mul_f32 v102, v102, v241\n"
"v_mfma_f32_16x16x32_bf16 a[28:31], v[228:231], v[12:15], a[28:31]\n"
"s_waitcnt lgkmcnt(8)\n"
"ds_read_b64_tr_b16 v[228:229], v246 offset:28672\n"
"ds_read_b64_tr_b16 v[230:231], v246 offset:30720\n"
"v_mul_f32 v103, v103, v241\n"
"v_cvt_pk_bf16_f32 v100, v100, v101\n"
"v_cvt_pk_bf16_f32 v71, v71, v208\n"
"v_mfma_f32_16x16x32_bf16 a[40:43], v[216:219], v[4:7], a[40:43]\n"
"s_waitcnt lgkmcnt(8)\n"
"v_cvt_pk_bf16_f32 v78, v78, v79\n"
"v_cvt_pk_bf16_f32 v101, v102, v103\n"
"v_accvgpr_read_b32 v108, a228\n"
"v_accvgpr_read_b32 v109, a229\n"
"v_accvgpr_read_b32 v208, a215\n"
"v_accvgpr_read_b32 v79, a214\n"
"v_mfma_f32_16x16x32_bf16 a[40:43], v[212:215], v[36:39], a[40:43]\n"
"s_add_u32 s84, s35, 0x10000\n"
"buffer_load_dwordx4 v252, s[56:59], s92 offen offset:1024 lds\n"
"v_accvgpr_read_b32 v102, a240\n"
"v_accvgpr_read_b32 v103, a241\n"
"v_mul_f32 v108, v108, v242\n"
"v_mfma_f32_16x16x32_bf16 a[44:47], v[212:215], v[44:47], a[44:47]\n"
"s_add_u32 s92, s26, 0x1a00\n"
"ds_read_b64_tr_b16 v[212:213], v254 offset:24576\n"
"ds_read_b64_tr_b16 v[214:215], v254 offset:26624\n"
"v_mul_f32 v109, v109, v242\n"
"v_accvgpr_read_b32 v110, a230\n"
"v_accvgpr_read_b32 v111, a231\n"
"v_mul_f32 v208, v208, v242\n"
"v_mfma_f32_16x16x32_bf16 a[44:47], v[216:219], v[12:15], a[44:47]\n"
"s_waitcnt lgkmcnt(8)\n"
"ds_read_b64_tr_b16 v[216:217], v254 offset:28672\n"
"ds_read_b64_tr_b16 v[218:219], v254 offset:30720\n"
"v_mul_f32 v79, v79, v242\n"
"v_mul_f32 v102, v102, v241\n"
"v_mul_f32 v103, v103, v241\n"
"v_mul_f32 v110, v110, v242\n"
"v_mfma_f32_16x16x32_bf16 a[56:59], v[220:223], v[4:7], a[56:59]\n"
"s_waitcnt lgkmcnt(8)\n"
"v_mul_f32 v111, v111, v242\n"
"v_cvt_pk_bf16_f32 v108, v108, v109\n"
"v_cvt_pk_bf16_f32 v79, v79, v208\n"
"v_cvt_pk_bf16_f32 v102, v102, v103\n"
"v_cvt_pk_bf16_f32 v109, v110, v111\n"
"v_mfma_f32_16x16x32_bf16 a[56:59], v[224:227], v[36:39], a[56:59]\n"
"v_accvgpr_read_b32 v208, a243\n"
"v_accvgpr_read_b32 v103, a242\n"
"v_accvgpr_read_b32 v110, a244\n"
"v_accvgpr_read_b32 v111, a245\n"
"v_mul_f32 v208, v208, v241\n"
"v_mfma_f32_16x16x32_bf16 a[60:63], v[224:227], v[44:47], a[60:63]\n"
"ds_read_b64_tr_b16 v[224:225], v249 offset:24576\n"
"ds_read_b64_tr_b16 v[226:227], v249 offset:26624\n"
"v_mul_f32 v103, v103, v241\n"
"v_mul_f32 v110, v110, v242\n"
"v_mul_f32 v111, v111, v242\n"
"v_mfma_f32_16x16x32_bf16 a[60:63], v[220:223], v[12:15], a[60:63]\n"
"s_waitcnt lgkmcnt(8)\n"
"ds_read_b64_tr_b16 v[220:221], v249 offset:28672\n"
"ds_read_b64_tr_b16 v[222:223], v249 offset:30720\n"
"v_cvt_pk_bf16_f32 v103, v103, v208\n"
"v_cvt_pk_bf16_f32 v110, v110, v111\n"
"v_accvgpr_read_b32 v208, a247\n"
"v_accvgpr_read_b32 v111, a246\n"
"v_mfma_f32_16x16x32_bf16 a[28:31], v[232:235], v[76:79], a[28:31]\n"
"buffer_load_dwordx4 v252, s[56:59], s93 offen offset:2048 lds\n"
"v_mul_f32 v208, v208, v242\n"
"v_mul_f32 v111, v111, v242\n"
"v_cvt_pk_bf16_f32 v111, v111, v208\n"
"s_waitcnt lgkmcnt(8)\n"
"s_nop 0\n"
"v_mfma_f32_16x16x32_bf16 a[28:31], v[228:231], v[108:111], a[28:31]\n"
"s_add_u32 s93, s26, 0x3200\n"
"v_accvgpr_read_b32 v88, a76\n"
"v_accvgpr_read_b32 v89, a77\n"
"v_mul_f32 v88, v88, v244\n"
"v_mul_f32 v89, v89, v244\n"
"v_accvgpr_read_b32 v90, a78\n"
"v_accvgpr_read_b32 v91, a79\n"
"v_mfma_f32_16x16x32_bf16 a[24:27], v[228:231], v[100:103], a[24:27]\n"
"ds_read_b64_tr_b16 v[228:229], v253 offset:0\n"
"ds_read_b64_tr_b16 v[230:231], v253 offset:2048\n"
"v_mul_f32 v90, v90, v244\n"
"v_mul_f32 v91, v91, v244\n"
"v_cvt_pk_bf16_f32 v88, v88, v89\n"
"v_cvt_pk_bf16_f32 v89, v90, v91\n"
"v_mfma_f32_16x16x32_bf16 a[24:27], v[232:235], v[68:71], a[24:27]\n"
"s_waitcnt lgkmcnt(8)\n"
"ds_read_b64_tr_b16 v[232:233], v253 offset:4096\n"
"ds_read_b64_tr_b16 v[234:235], v253 offset:6144\n"
"buffer_load_dwordx4 v252, s[56:59], s94 offen offset:3072 lds\n"
"v_accvgpr_read_b32 v90, a92\n"
"v_accvgpr_read_b32 v91, a93\n"
"v_mfma_f32_16x16x32_bf16 a[44:47], v[212:215], v[76:79], a[44:47]\n"
"s_waitcnt lgkmcnt(8)\n"
"ds_read_b128 v[160:163], v248 offset:5120\n"
"v_mul_f32 v90, v90, v244\n"
"v_mul_f32 v91, v91, v244\n"
"v_cvt_pk_bf16_f32 v90, v90, v91\n"
"v_accvgpr_read_b32 v210, a95\n"
"v_accvgpr_read_b32 v91, a94\n"
"v_mul_f32 v210, v210, v244\n"
"v_mul_f32 v91, v91, v244\n"
"v_cvt_pk_bf16_f32 v91, v91, v210\n"
"v_accvgpr_read_b32 v112, a104\n"
"v_mfma_f32_16x16x32_bf16 a[44:47], v[216:219], v[108:111], a[44:47]\n"
"s_add_u32 s94, s26, 0x4a00\n"
"ds_read_b128 v[164:167], v248 offset:6144\n"
"buffer_store_dwordx4 v[88:91], v248, s[60:63], s67 offen sc1\n"
"ds_read_b128 v[168:171], v248 offset:7168\n"
"v_mfma_f32_16x16x32_bf16 a[40:43], v[216:219], v[100:103], a[40:43]\n"
"s_add_u32 s67, s35, 0x12000\n"
"ds_read_b64_tr_b16 v[216:217], v253 offset:8192\n"
"ds_read_b64_tr_b16 v[218:219], v253 offset:10240\n"
"ds_read_b128 v[172:175], v248 offset:8192\n"
"v_accvgpr_read_b32 v113, a105\n"
"v_mfma_f32_16x16x32_bf16 a[40:43], v[212:215], v[68:71], a[40:43]\n"
"s_waitcnt lgkmcnt(12)\n"
"ds_read_b64_tr_b16 v[212:213], v253 offset:12288\n"
"ds_read_b64_tr_b16 v[214:215], v253 offset:14336\n"
"v_mul_f32 v112, v112, v243\n"
"v_mfma_f32_16x16x32_bf16 a[60:63], v[224:227], v[76:79], a[60:63]\n"
"s_waitcnt lgkmcnt(12)\n"
"v_mul_f32 v113, v113, v243\n"
"v_accvgpr_read_b32 v114, a106\n"
"v_accvgpr_read_b32 v115, a107\n"
"v_mfma_f32_16x16x32_bf16 a[60:63], v[220:223], v[108:111], a[60:63]\n"
"v_mul_f32 v114, v114, v243\n"
"v_mul_f32 v115, v115, v243\n"
"v_cvt_pk_bf16_f32 v112, v112, v113\n"
"v_mfma_f32_16x16x32_bf16 a[56:59], v[220:223], v[100:103], a[56:59]\n"
"v_cvt_pk_bf16_f32 v113, v114, v115\n"
"v_accvgpr_read_b32 v114, a120\n"
"v_accvgpr_read_b32 v115, a121\n"
"v_mfma_f32_16x16x32_bf16 a[56:59], v[224:227], v[68:71], a[56:59]\n"
"s_waitcnt lgkmcnt(10)\n"
"v_mul_f32 v114, v114, v243\n"
"v_mul_f32 v115, v115, v243\n"
"v_cvt_pk_bf16_f32 v114, v114, v115\n"
"v_mfma_f32_16x16x32_bf16 a[8:11], v[228:231], v[0:3], 0\n"
"s_waitcnt lgkmcnt(8)\n"
"ds_read_b64_tr_b16 v[220:221], v253 offset:16384\n"
"ds_read_b64_tr_b16 v[222:223], v253 offset:18432\n"
"v_accvgpr_read_b32 v210, a123\n"
"v_accvgpr_read_b32 v115, a122\n"
"v_mfma_f32_16x16x32_bf16 a[8:11], v[232:235], v[32:35], a[8:11]\n"
"ds_read_b64_tr_b16 v[224:225], v253 offset:20480\n"
"ds_read_b64_tr_b16 v[226:227], v253 offset:22528\n"
"v_mul_f32 v210, v210, v243\n"
"v_mfma_f32_16x16x32_bf16 a[12:15], v[232:235], v[40:43], 0\n"
"v_mul_f32 v115, v115, v243\n"
"v_cvt_pk_bf16_f32 v115, v115, v210\n"
"v_accvgpr_read_b32 v120, a108\n"
"v_accvgpr_read_b32 v121, a109\n"
"v_mfma_f32_16x16x32_bf16 a[12:15], v[228:231], v[8:11], a[12:15]\n"
"s_waitcnt lgkmcnt(11)\n"
"buffer_store_dwordx4 v[112:115], v248, s[60:63], s68 offen sc1\n"
"ds_read_b128 v[176:179], v248 offset:9216\n"
"v_mfma_f32_16x16x32_bf16 a[72:75], v[228:231], v[160:163], 0\n"
"s_waitcnt lgkmcnt(11)\n"
"s_waitcnt vmcnt(25)\n"
"s_barrier\n"
"v_mfma_f32_16x16x32_bf16 a[72:75], v[232:235], v[164:167], a[72:75]\n"
"s_waitcnt lgkmcnt(8)\n"
"ds_read_b128 v[180:183], v248 offset:10240\n"
"ds_read_b128 v[184:187], v248 offset:11264\n"
"ds_read_b64_tr_b16 v[228:229], v253 offset:24576\n"
"v_mfma_f32_16x16x32_bf16 a[72:75], v[216:219], v[168:171], a[72:75]\n"
"s_waitcnt lgkmcnt(8)\n"
"ds_read_b64_tr_b16 v[230:231], v253 offset:26624\n"
"v_mul_f32 v120, v120, v244\n"
"v_mul_f32 v121, v121, v244\n"
"v_mfma_f32_16x16x32_bf16 a[72:75], v[212:215], v[172:175], a[72:75]\n"
"s_add_u32 s68, s35, 0x14000\n"
"ds_read_b128 v[188:191], v248 offset:12288\n"
"ds_read_b64_tr_b16 v[232:233], v253 offset:28672\n"
"ds_read_b64_tr_b16 v[234:235], v253 offset:30720\n"
"v_accvgpr_read_b32 v122, a110\n"
"v_mfma_f32_16x16x32_bf16 a[12:15], v[212:215], v[104:107], a[12:15]\n"
"v_accvgpr_read_b32 v123, a111\n"
"v_mul_f32 v122, v122, v244\n"
"v_mul_f32 v123, v123, v244\n"
"v_cvt_pk_bf16_f32 v120, v120, v121\n"
"v_mfma_f32_16x16x32_bf16 a[12:15], v[216:219], v[72:75], a[12:15]\n"
"v_cvt_pk_bf16_f32 v121, v122, v123\n"
"v_accvgpr_read_b32 v122, a124\n"
"v_accvgpr_read_b32 v123, a125\n"
"v_mul_f32 v122, v122, v244\n"
"v_mfma_f32_16x16x32_bf16 a[8:11], v[216:219], v[64:67], a[8:11]\n"
"v_mul_f32 v123, v123, v244\n"
"v_cvt_pk_bf16_f32 v122, v122, v123\n"
"v_accvgpr_read_b32 v210, a127\n"
"v_mfma_f32_16x16x32_bf16 a[8:11], v[212:215], v[96:99], a[8:11]\n"
"s_waitcnt lgkmcnt(10)\n"
"v_accvgpr_read_b32 v123, a126\n"
"v_mul_f32 v210, v210, v244\n"
"v_mul_f32 v123, v123, v244\n"
"v_mfma_f32_16x16x32_bf16 a[8:11], v[220:223], v[4:7], a[8:11]\n"
"s_waitcnt lgkmcnt(8)\n"
"ds_read_b64_tr_b16 v[216:217], v254 offset:32768\n"
"ds_read_b64_tr_b16 v[218:219], v254 offset:34816\n"
"v_cvt_pk_bf16_f32 v123, v123, v210\n"
"v_mfma_f32_16x16x32_bf16 a[8:11], v[224:227], v[36:39], a[8:11]\n"
"ds_read_b64_tr_b16 v[212:213], v254 offset:36864\n"
"ds_read_b64_tr_b16 v[214:215], v254 offset:38912\n"
"v_accvgpr_read_b32 v20, a136\n"
"v_mfma_f32_16x16x32_bf16 a[12:15], v[224:227], v[44:47], a[12:15]\n"
"buffer_store_dwordx4 v[120:123], v248, s[60:63], s83 offen sc1\n"
"v_accvgpr_read_b32 v21, a137\n"
"v_mfma_f32_16x16x32_bf16 a[12:15], v[220:223], v[12:15], a[12:15]\n"
"s_waitcnt lgkmcnt(11)\n"
"v_mul_f32 v20, v20, v243\n"
"v_mul_f32 v21, v21, v243\n"
"v_accvgpr_read_b32 v22, a138\n"
"v_mfma_f32_16x16x32_bf16 a[72:75], v[220:223], v[176:179], a[72:75]\n"
"s_waitcnt lgkmcnt(10)\n"
"ds_read_b64_tr_b16 v[220:221], v254 offset:40960\n"
"ds_read_b64_tr_b16 v[222:223], v254 offset:43008\n"
"v_accvgpr_read_b32 v23, a139\n"
"v_mfma_f32_16x16x32_bf16 a[72:75], v[224:227], v[180:183], a[72:75]\n"
"s_waitcnt lgkmcnt(9)\n"
"ds_read_b64_tr_b16 v[224:225], v254 offset:45056\n"
"ds_read_b64_tr_b16 v[226:227], v254 offset:47104\n"
"v_mul_f32 v22, v22, v243\n"
"v_mfma_f32_16x16x32_bf16 a[72:75], v[228:231], v[184:187], a[72:75]\n"
"s_waitcnt lgkmcnt(8)\n"
"v_mul_f32 v23, v23, v243\n"
"v_cvt_pk_bf16_f32 v20, v20, v21\n"
"v_cvt_pk_bf16_f32 v21, v22, v23\n"
"v_mfma_f32_16x16x32_bf16 a[72:75], v[232:235], v[188:191], a[72:75]\n"
"s_add_u32 s83, s35, 0x16000\n"
"v_accvgpr_read_b32 v22, a152\n"
"v_accvgpr_read_b32 v23, a153\n"
"v_mul_f32 v22, v22, v243\n"
"v_mfma_f32_16x16x32_bf16 a[12:15], v[232:235], v[108:111], a[12:15]\n"
"v_mul_f32 v23, v23, v243\n"
"v_cvt_pk_bf16_f32 v22, v22, v23\n"
"v_accvgpr_read_b32 v210, a155\n"
"v_mfma_f32_16x16x32_bf16 a[12:15], v[228:231], v[76:79], a[12:15]\n"
"v_accvgpr_read_b32 v23, a154\n"
"v_mul_f32 v210, v210, v243\n"
"v_mul_f32 v23, v23, v243\n"
"v_mfma_f32_16x16x32_bf16 a[8:11], v[228:231], v[68:71], a[8:11]\n"
"ds_read_b64_tr_b16 v[228:229], v254 offset:49152\n"
"ds_read_b64_tr_b16 v[230:231], v254 offset:51200\n"
"v_cvt_pk_bf16_f32 v23, v23, v210\n"
"v_mfma_f32_16x16x32_bf16 a[8:11], v[232:235], v[100:103], a[8:11]\n"
"s_waitcnt lgkmcnt(8)\n"
"ds_read_b64_tr_b16 v[232:233], v254 offset:53248\n"
"ds_read_b64_tr_b16 v[234:235], v254 offset:55296\n"
"v_accvgpr_read_b32 v28, a140\n"
"v_mfma_f32_16x16x32_bf16 a[108:111], v[216:219], v[0:3], 0\n"
"s_waitcnt lgkmcnt(8)\n"
"buffer_store_dwordx4 v[20:23], v248, s[60:63], s84 offen sc1\n"
"v_accvgpr_read_b32 v29, a141\n"
"v_mfma_f32_16x16x32_bf16 a[108:111], v[212:215], v[32:35], a[108:111]\n"
"s_add_u32 s84, s35, 0x18000\n"
"v_mul_f32 v28, v28, v244\n"
"v_mul_f32 v29, v29, v244\n"
"v_accvgpr_read_b32 v30, a142\n"
"v_mfma_f32_16x16x32_bf16 a[120:123], v[212:215], v[40:43], 0\n"
"ds_read_b64_tr_b16 v[212:213], v254 offset:57344\n"
"ds_read_b64_tr_b16 v[214:215], v254 offset:59392\n"
"v_accvgpr_read_b32 v31, a143\n"
"v_mfma_f32_16x16x32_bf16 a[120:123], v[216:219], v[8:11], a[120:123]\n"
"s_waitcnt lgkmcnt(8)\n"
"ds_read_b64_tr_b16 v[216:217], v254 offset:61440\n"
"ds_read_b64_tr_b16 v[218:219], v254 offset:63488\n"
"v_mul_f32 v30, v30, v244\n"
"v_mul_f32 v31, v31, v244\n"
"v_mfma_f32_16x16x32_bf16 a[120:123], v[220:223], v[72:75], a[120:123]\n"
"s_waitcnt lgkmcnt(8)\n"
"v_cvt_pk_bf16_f32 v28, v28, v29\n"
"v_cvt_pk_bf16_f32 v29, v30, v31\n"
"v_accvgpr_read_b32 v30, a156\n"
"v_mfma_f32_16x16x32_bf16 a[120:123], v[224:227], v[104:107], a[120:123]\n"
"v_accvgpr_read_b32 v31, a157\n"
"v_mul_f32 v30, v30, v244\n"
"v_mul_f32 v31, v31, v244\n"
"v_mfma_f32_16x16x32_bf16 a[108:111], v[224:227], v[96:99], a[108:111]\n"
"ds_read_b64_tr_b16 v[224:225], v249 offset:32768\n"
"ds_read_b64_tr_b16 v[226:227], v249 offset:34816\n"
"v_cvt_pk_bf16_f32 v30, v30, v31\n"
"v_mfma_f32_16x16x32_bf16 a[108:111], v[220:223], v[64:67], a[108:111]\n"
"s_waitcnt lgkmcnt(8)\n"
"ds_read_b64_tr_b16 v[220:221], v249 offset:36864\n"
"ds_read_b64_tr_b16 v[222:223], v249 offset:38912\n"
"v_accvgpr_read_b32 v210, a159\n"
"v_accvgpr_read_b32 v31, a158\n"
"v_mfma_f32_16x16x32_bf16 a[108:111], v[228:231], v[4:7], a[108:111]\n"
"s_waitcnt lgkmcnt(8)\n"
"v_mul_f32 v210, v210, v244\n"
"v_mul_f32 v31, v31, v244\n"
"v_cvt_pk_bf16_f32 v31, v31, v210\n"
"v_accvgpr_read_b32 v52, a168\n"
"v_accvgpr_read_b32 v53, a169\n"
"v_mfma_f32_16x16x32_bf16 a[108:111], v[232:235], v[36:39], a[108:111]\n"
"buffer_store_dwordx4 v[28:31], v248, s[60:63], s67 offen sc1\n"
"v_mul_f32 v52, v52, v243\n"
"v_mfma_f32_16x16x32_bf16 a[120:123], v[232:235], v[44:47], a[120:123]\n"
"s_add_u32 s67, s35, 0x1a000\n"
"ds_read_b64_tr_b16 v[232:233], v249 offset:40960\n"
"ds_read_b64_tr_b16 v[234:235], v249 offset:43008\n"
"v_mul_f32 v53, v53, v243\n"
"v_mfma_f32_16x16x32_bf16 a[120:123], v[228:231], v[12:15], a[120:123]\n"
"s_waitcnt lgkmcnt(8)\n"
"ds_read_b64_tr_b16 v[228:229], v249 offset:45056\n"
"ds_read_b64_tr_b16 v[230:231], v249 offset:47104\n"
"v_accvgpr_read_b32 v54, a170\n"
"v_mfma_f32_16x16x32_bf16 a[120:123], v[212:215], v[76:79], a[120:123]\n"
"s_waitcnt lgkmcnt(8)\n"
"s_waitcnt vmcnt(18)\n"
"s_barrier\n"
"v_mfma_f32_16x16x32_bf16 a[120:123], v[216:219], v[108:111], a[120:123]\n"
"v_accvgpr_read_b32 v55, a171\n"
"v_mul_f32 v54, v54, v243\n"
"v_mul_f32 v55, v55, v243\n"
"v_cvt_pk_bf16_f32 v52, v52, v53\n"
"v_mfma_f32_16x16x32_bf16 a[108:111], v[216:219], v[100:103], a[108:111]\n"
"ds_read_b64_tr_b16 v[216:217], v249 offset:49152\n"
"ds_read_b64_tr_b16 v[218:219], v249 offset:51200\n"
"v_cvt_pk_bf16_f32 v53, v54, v55\n"
"v_mfma_f32_16x16x32_bf16 a[108:111], v[212:215], v[68:71], a[108:111]\n"
"s_waitcnt lgkmcnt(8)\n"
"ds_read_b64_tr_b16 v[212:213], v249 offset:53248\n"
"ds_read_b64_tr_b16 v[214:215], v249 offset:55296\n"
"v_accvgpr_read_b32 v54, a184\n"
"v_mfma_f32_16x16x32_bf16 a[124:127], v[224:227], v[0:3], 0\n"
"s_waitcnt lgkmcnt(8)\n"
"v_accvgpr_read_b32 v55, a185\n"
"v_mul_f32 v54, v54, v243\n"
"v_mul_f32 v55, v55, v243\n"
"v_cvt_pk_bf16_f32 v54, v54, v55\n"
"v_mfma_f32_16x16x32_bf16 a[124:127], v[220:223], v[32:35], a[124:127]\n"
"s_add_u32 m0, s90, 0x5400\n"
"v_accvgpr_read_b32 v210, a187\n"
"v_accvgpr_read_b32 v55, a186\n"
"v_mul_f32 v210, v210, v243\n"
"v_mul_f32 v55, v55, v243\n"
"v_mfma_f32_16x16x32_bf16 a[136:139], v[220:223], v[40:43], 0\n"
"ds_read_b64_tr_b16 v[220:221], v249 offset:57344\n"
"ds_read_b64_tr_b16 v[222:223], v249 offset:59392\n"
"v_cvt_pk_bf16_f32 v55, v55, v210\n"
"v_mfma_f32_16x16x32_bf16 a[136:139], v[224:227], v[8:11], a[136:139]\n"
"s_waitcnt lgkmcnt(8)\n"
"ds_read_b64_tr_b16 v[224:225], v249 offset:61440\n"
"ds_read_b64_tr_b16 v[226:227], v249 offset:63488\n"
"v_accvgpr_read_b32 v60, a172\n"
"v_mfma_f32_16x16x32_bf16 a[136:139], v[232:235], v[72:75], a[136:139]\n"
"Lpada_7:\n"
"s_cbranch_execz Lpada_11\n"
"s_waitcnt lgkmcnt(8)\n"
"buffer_store_dwordx4 v[52:55], v248, s[60:63], s68 offen sc1\n"
"v_accvgpr_read_b32 v61, a173\n"
"v_mfma_f32_16x16x32_bf16 a[136:139], v[228:231], v[104:107], a[136:139]\n"
"s_add_u32 s68, s35, 0x1c000\n"
"v_mul_f32 v60, v60, v244\n"
"v_mul_f32 v61, v61, v244\n"
"v_accvgpr_read_b32 v62, a174\n"
"v_mfma_f32_16x16x32_bf16 a[124:127], v[228:231], v[96:99], a[124:127]\n"
"ds_read_b64_tr_b16 v[228:229], v246 offset:32768\n"
"ds_read_b64_tr_b16 v[230:231], v246 offset:34816\n"
"v_accvgpr_read_b32 v63, a175\n"
"v_mfma_f32_16x16x32_bf16 a[124:127], v[232:235], v[64:67], a[124:127]\n"
"s_waitcnt lgkmcnt(8)\n"
"ds_read_b64_tr_b16 v[232:233], v246 offset:36864\n"
"ds_read_b64_tr_b16 v[234:235], v246 offset:38912\n"
"v_mul_f32 v62, v62, v244\n"
"v_mfma_f32_16x16x32_bf16 a[124:127], v[216:219], v[4:7], a[124:127]\n"
"s_waitcnt lgkmcnt(8)\n"
"buffer_load_dwordx4 v252, s[56:59], s91 offen lds\n"
"v_mul_f32 v63, v63, v244\n"
"v_mfma_f32_16x16x32_bf16 a[124:127], v[212:215], v[36:39], a[124:127]\n"
"s_add_u32 s91, s26, 0x7200\n"
"v_cvt_pk_bf16_f32 v60, v60, v61\n"
"v_cvt_pk_bf16_f32 v61, v62, v63\n"
"v_accvgpr_read_b32 v62, a188\n"
"v_accvgpr_read_b32 v63, a189\n"
"v_mfma_f32_16x16x32_bf16 a[136:139], v[212:215], v[44:47], a[136:139]\n"
"ds_read_b64_tr_b16 v[212:213], v246 offset:40960\n"
"ds_read_b64_tr_b16 v[214:215], v246 offset:43008\n"
"v_mul_f32 v62, v62, v244\n"
"v_mul_f32 v63, v63, v244\n"
"v_mfma_f32_16x16x32_bf16 a[136:139], v[216:219], v[12:15], a[136:139]\n"
"s_waitcnt lgkmcnt(8)\n"
"ds_read_b64_tr_b16 v[216:217], v246 offset:45056\n"
"ds_read_b64_tr_b16 v[218:219], v246 offset:47104\n"
"v_cvt_pk_bf16_f32 v62, v62, v63\n"
"v_accvgpr_read_b32 v210, a191\n"
"v_mfma_f32_16x16x32_bf16 a[136:139], v[220:223], v[76:79], a[136:139]\n"
"s_waitcnt lgkmcnt(8)\n"
"v_accvgpr_read_b32 v63, a190\n"
"v_mul_f32 v210, v210, v244\n"
"v_mul_f32 v63, v63, v244\n"
"v_cvt_pk_bf16_f32 v63, v63, v210\n"
"v_accvgpr_read_b32 v84, a200\n"
"v_accvgpr_read_b32 v85, a201\n"
"v_mfma_f32_16x16x32_bf16 a[136:139], v[224:227], v[108:111], a[136:139]\n"
"buffer_store_dwordx4 v[60:63], v248, s[60:63], s83 offen sc1\n"
"v_mul_f32 v84, v84, v243\n"
"v_mfma_f32_16x16x32_bf16 a[124:127], v[224:227], v[100:103], a[124:127]\n"
"s_add_u32 s83, s35, 0x1e000\n"
"ds_read_b64_tr_b16 v[224:225], v246 offset:49152\n"
"ds_read_b64_tr_b16 v[226:227], v246 offset:51200\n"
"v_mul_f32 v85, v85, v243\n"
"v_mfma_f32_16x16x32_bf16 a[124:127], v[220:223], v[68:71], a[124:127]\n"
"s_waitcnt lgkmcnt(8)\n"
"ds_read_b64_tr_b16 v[220:221], v246 offset:53248\n"
"ds_read_b64_tr_b16 v[222:223], v246 offset:55296\n"
"v_accvgpr_read_b32 v86, a202\n"
"v_mfma_f32_16x16x32_bf16 a[92:95], v[228:231], v[0:3], 0\n"
"s_waitcnt lgkmcnt(8)\n"
"buffer_load_dwordx4 v252, s[56:59], s92 offen offset:1024 lds\n"
"v_accvgpr_read_b32 v87, a203\n"
"v_mfma_f32_16x16x32_bf16 a[92:95], v[232:235], v[32:35], a[92:95]\n"
"s_add_u32 s92, s26, 0x8a00\n"
"v_mul_f32 v86, v86, v243\n"
"v_mul_f32 v87, v87, v243\n"
"v_cvt_pk_bf16_f32 v84, v84, v85\n"
"v_mfma_f32_16x16x32_bf16 a[104:107], v[232:235], v[40:43], 0\n"
"ds_read_b64_tr_b16 v[232:233], v246 offset:57344\n"
"ds_read_b64_tr_b16 v[234:235], v246 offset:59392\n"
"v_cvt_pk_bf16_f32 v85, v86, v87\n"
"v_mfma_f32_16x16x32_bf16 a[104:107], v[228:231], v[8:11], a[104:107]\n"
"s_waitcnt lgkmcnt(8)\n"
"ds_read_b64_tr_b16 v[228:229], v246 offset:61440\n"
"ds_read_b64_tr_b16 v[230:231], v246 offset:63488\n"
"v_accvgpr_read_b32 v86, a216\n"
"v_mfma_f32_16x16x32_bf16 a[104:107], v[212:215], v[72:75], a[104:107]\n"
"s_waitcnt lgkmcnt(8)\n"
"buffer_load_dwordx4 v252, s[56:59], s93 offen offset:2048 lds\n"
"v_accvgpr_read_b32 v87, a217\n"
"v_mul_f32 v86, v86, v243\n"
"v_mfma_f32_16x16x32_bf16 a[104:107], v[216:219], v[104:107], a[104:107]\n"
"s_add_u32 s93, s26, 0xa200\n"
"v_mul_f32 v87, v87, v243\n"
"v_cvt_pk_bf16_f32 v86, v86, v87\n"
"v_accvgpr_read_b32 v210, a219\n"
"v_accvgpr_read_b32 v87, a218\n"
"v_mul_f32 v210, v210, v243\n"
"v_mfma_f32_16x16x32_bf16 a[92:95], v[216:219], v[96:99], a[92:95]\n"
"ds_read_b64_tr_b16 v[216:217], v253 offset:32768\n"
"ds_read_b64_tr_b16 v[218:219], v253 offset:34816\n"
"v_mul_f32 v87, v87, v243\n"
"v_cvt_pk_bf16_f32 v87, v87, v210\n"
"v_mfma_f32_16x16x32_bf16 a[92:95], v[212:215], v[64:67], a[92:95]\n"
"s_waitcnt lgkmcnt(8)\n"
"ds_read_b64_tr_b16 v[212:213], v253 offset:36864\n"
"ds_read_b64_tr_b16 v[214:215], v253 offset:38912\n"
"v_lshlrev_b32 v255, 22, v248\n"
"v_mfma_f32_16x16x32_bf16 a[92:95], v[224:227], v[4:7], a[92:95]\n"
"s_waitcnt lgkmcnt(8)\n"
"buffer_store_dwordx4 v[84:87], v248, s[60:63], s84 offen sc1\n"
"v_accvgpr_read_b32 v92, a204\n"
"v_mfma_f32_16x16x32_bf16 a[92:95], v[220:223], v[36:39], a[92:95]\n"
"s_add_u32 s84, s36, 0\n"
"v_accvgpr_read_b32 v93, a205\n"
"v_mul_f32 v92, v92, v244\n"
"v_mul_f32 v93, v93, v244\n"
"v_mfma_f32_16x16x32_bf16 a[104:107], v[220:223], v[44:47], a[104:107]\n"
"ds_read_b64_tr_b16 v[220:221], v253 offset:40960\n"
"ds_read_b64_tr_b16 v[222:223], v253 offset:43008\n"
"v_accvgpr_read_b32 v94, a206\n"
"v_mfma_f32_16x16x32_bf16 a[104:107], v[224:227], v[12:15], a[104:107]\n"
"s_waitcnt lgkmcnt(8)\n"
"ds_read_b64_tr_b16 v[224:225], v253 offset:45056\n"
"ds_read_b64_tr_b16 v[226:227], v253 offset:47104\n"
"v_accvgpr_read_b32 v95, a207\n"
"v_mfma_f32_16x16x32_bf16 a[104:107], v[232:235], v[76:79], a[104:107]\n"
"s_waitcnt lgkmcnt(8)\n"
"buffer_load_dword v236, v255, s[60:63], s39 offen offset:128 sc1\n"
"v_mul_f32 v94, v94, v244\n"
"v_mul_f32 v95, v95, v244\n"
"v_mfma_f32_16x16x32_bf16 a[104:107], v[228:231], v[108:111], a[104:107]\n"
"v_cvt_pk_bf16_f32 v92, v92, v93\n"
"v_cvt_pk_bf16_f32 v93, v94, v95\n"
"v_accvgpr_read_b32 v94, a220\n"
"v_accvgpr_read_b32 v95, a221\n"
"v_mfma_f32_16x16x32_bf16 a[92:95], v[228:231], v[100:103], a[92:95]\n"
"v_mul_f32 v94, v94, v244\n"
"v_mul_f32 v95, v95, v244\n"
"v_cvt_pk_bf16_f32 v94, v94, v95\n"
"v_accvgpr_read_b32 v210, a223\n"
"v_mfma_f32_16x16x32_bf16 a[92:95], v[232:235], v[68:71], a[92:95]\n"
"s_waitcnt lgkmcnt(6)\n"
"buffer_load_dwordx4 v252, s[56:59], s94 offen offset:3072 lds\n"
"v_accvgpr_read_b32 v95, a222\n"
"v_mfma_f32_16x16x32_bf16 a[76:79], v[216:219], v[0:3], 0\n"
"s_waitcnt lgkmcnt(4)\n"
"ds_read_b64_tr_b16 v[228:229], v253 offset:49152\n"
"ds_read_b64_tr_b16 v[230:231], v253 offset:51200\n"
"v_mul_f32 v210, v210, v244\n"
"v_mul_f32 v95, v95, v244\n"
"v_cvt_pk_bf16_f32 v95, v95, v210\n"
"v_mfma_f32_16x16x32_bf16 a[76:79], v[212:215], v[32:35], a[76:79]\n"
"s_add_u32 m0, s90, 0x6400\n"
"ds_read_b64_tr_b16 v[232:233], v253 offset:53248\n"
"ds_read_b64_tr_b16 v[234:235], v253 offset:55296\n"
"v_accvgpr_read_b32 v116, a232\n"
"v_mfma_f32_16x16x32_bf16 a[88:91], v[212:215], v[40:43], 0\n"
"s_add_u32 s94, s26, 0xba00\n"
"buffer_store_dwordx4 v[92:95], v248, s[60:63], s67 offen sc1\n"
"v_accvgpr_read_b32 v117, a233\n"
"v_mfma_f32_16x16x32_bf16 a[88:91], v[216:219], v[8:11], a[88:91]\n"
"s_add_u32 s67, s36, 0x2000\n"
"v_mul_f32 v116, v116, v243\n"
"v_mul_f32 v117, v117, v243\n"
"v_accvgpr_read_b32 v118, a234\n"
"v_mfma_f32_16x16x32_bf16 a[140:143], v[216:219], v[160:163], 0\n"
"v_accvgpr_read_b32 v119, a235\n"
"v_mul_f32 v118, v118, v243\n"
"v_mul_f32 v119, v119, v243\n"
"v_mfma_f32_16x16x32_bf16 a[140:143], v[212:215], v[164:167], a[140:143]\n"
"s_waitcnt lgkmcnt(6)\n"
"buffer_load_dwordx4 v252, s[56:59], s91 offen lds\n"
"v_cvt_pk_bf16_f32 v116, v116, v117\n"
"v_mfma_f32_16x16x32_bf16 a[140:143], v[220:223], v[168:171], a[140:143]\n"
"s_waitcnt lgkmcnt(4)\n"
"ds_read_b64_tr_b16 v[216:217], v253 offset:57344\n"
"ds_read_b64_tr_b16 v[218:219], v253 offset:59392\n"
"v_cvt_pk_bf16_f32 v117, v118, v119\n"
"v_mfma_f32_16x16x32_bf16 a[140:143], v[224:227], v[172:175], a[140:143]\n"
"s_add_u32 s91, s26, 0x280\n"
"ds_read_b64_tr_b16 v[212:213], v253 offset:61440\n"
"ds_read_b64_tr_b16 v[214:215], v253 offset:63488\n"
"v_accvgpr_read_b32 v118, a248\n"
"v_mfma_f32_16x16x32_bf16 a[88:91], v[224:227], v[104:107], a[88:91]\n"
"v_accvgpr_read_b32 v119, a249\n"
"v_mul_f32 v118, v118, v243\n"
"v_mul_f32 v119, v119, v243\n"
"v_cvt_pk_bf16_f32 v118, v118, v119\n"
"v_mfma_f32_16x16x32_bf16 a[88:91], v[220:223], v[72:75], a[88:91]\n"
"v_accvgpr_read_b32 v210, a251\n"
"v_accvgpr_read_b32 v119, a250\n"
"v_mul_f32 v210, v210, v243\n"
"v_mul_f32 v119, v119, v243\n"
"v_mfma_f32_16x16x32_bf16 a[76:79], v[220:223], v[64:67], a[76:79]\n"
"v_cvt_pk_bf16_f32 v119, v119, v210\n"
"v_add_u32 v254, 0x12000, v254\n"
"v_accvgpr_read_b32 v124, a236\n"
"v_mfma_f32_16x16x32_bf16 a[76:79], v[224:227], v[96:99], a[76:79]\n"
"s_waitcnt lgkmcnt(6)\n"
"buffer_store_dwordx4 v[116:119], v248, s[60:63], s68 offen sc1\n"
"ds_read_b64_tr_b16 v[220:221], v254 offset:0\n"
"v_mfma_f32_16x16x32_bf16 a[76:79], v[228:231], v[4:7], a[76:79]\n"
"s_waitcnt lgkmcnt(5)\n"
"ds_read_b64_tr_b16 v[222:223], v254 offset:2048\n"
"ds_read_b64_tr_b16 v[224:225], v254 offset:4096\n"
"ds_read_b64_tr_b16 v[226:227], v254 offset:6144\n"
"v_mfma_f32_16x16x32_bf16 a[76:79], v[232:235], v[36:39], a[76:79]\n"
"s_add_u32 s68, s36, 0x4000\n"
"v_accvgpr_read_b32 v125, a237\n"
"v_mul_f32 v124, v124, v244\n"
"v_mul_f32 v125, v125, v244\n"
"v_mfma_f32_16x16x32_bf16 a[88:91], v[232:235], v[44:47], a[88:91]\n"
"buffer_load_dwordx4 v252, s[56:59], s92 offen offset:1024 lds\n"
"v_accvgpr_read_b32 v126, a238\n"
"v_mfma_f32_16x16x32_bf16 a[88:91], v[228:231], v[12:15], a[88:91]\n"
"s_add_u32 s92, s26, 0x1a80\n"
"v_accvgpr_read_b32 v127, a239\n"
"v_mul_f32 v126, v126, v244\n"
"v_mul_f32 v127, v127, v244\n"
"v_mfma_f32_16x16x32_bf16 a[140:143], v[228:231], v[176:179], a[140:143]\n"
"ds_read_b64_tr_b16 v[228:229], v254 offset:8192\n"
"ds_read_b64_tr_b16 v[230:231], v254 offset:10240\n"
"v_cvt_pk_bf16_f32 v124, v124, v125\n"
"v_mfma_f32_16x16x32_bf16 a[140:143], v[232:235], v[180:183], a[140:143]\n"
"s_waitcnt lgkmcnt(8)\n"
"ds_read_b64_tr_b16 v[232:233], v254 offset:12288\n"
"ds_read_b64_tr_b16 v[234:235], v254 offset:14336\n"
"v_cvt_pk_bf16_f32 v125, v126, v127\n"
"v_accvgpr_read_b32 v126, a252\n"
"v_mfma_f32_16x16x32_bf16 a[140:143], v[216:219], v[184:187], a[140:143]\n"
"s_waitcnt lgkmcnt(8)\n"
"v_accvgpr_read_b32 v127, a253\n"
"v_mul_f32 v126, v126, v244\n"
"v_mul_f32 v127, v127, v244\n"
"v_cvt_pk_bf16_f32 v126, v126, v127\n"
"v_mfma_f32_16x16x32_bf16 a[140:143], v[212:215], v[188:191], a[140:143]\n"
"v_accvgpr_read_b32 v210, a255\n"
"v_accvgpr_read_b32 v127, a254\n"
"v_mul_f32 v210, v210, v244\n"
"v_mul_f32 v127, v127, v244\n"
"v_mfma_f32_16x16x32_bf16 a[88:91], v[212:215], v[108:111], a[88:91]\n"
"v_cvt_pk_bf16_f32 v127, v127, v210\n"
"v_add_u32 v249, 0x12000, v249\n"
"v_add_u32 v246, 0x12000, v246\n"
"v_mfma_f32_16x16x32_bf16 a[88:91], v[216:219], v[76:79], a[88:91]\n"
"buffer_store_dwordx4 v[124:127], v248, s[60:63], s83 offen sc1\n"
"v_mfma_f32_16x16x32_bf16 a[76:79], v[216:219], v[68:71], a[76:79]\n"
"s_add_u32 s83, s36, 0x6000\n"
"ds_read_b64_tr_b16 v[216:217], v254 offset:16384\n"
"ds_read_b64_tr_b16 v[218:219], v254 offset:18432\n"
"v_mfma_f32_16x16x32_bf16 a[76:79], v[212:215], v[100:103], a[76:79]\n"
"s_waitcnt lgkmcnt(8)\n"
"ds_read_b64_tr_b16 v[212:213], v254 offset:20480\n"
"ds_read_b64_tr_b16 v[214:215], v254 offset:22528\n"
"v_mfma_f32_16x16x32_bf16 a[184:187], v[220:223], v[0:3], 0\n"
"s_waitcnt lgkmcnt(8)\n"
"buffer_load_dwordx4 v252, s[56:59], s93 offen offset:2048 lds\n"
"v_mfma_f32_16x16x32_bf16 a[184:187], v[224:227], v[32:35], a[184:187]\n"
"s_add_u32 s93, s26, 0x3280\n"
"v_add_u32 v253, 0x12000, v253\n"
"v_mfma_f32_16x16x32_bf16 a[188:191], v[224:227], v[40:43], 0\n"
"ds_read_b64_tr_b16 v[224:225], v254 offset:24576\n"
"ds_read_b64_tr_b16 v[226:227], v254 offset:26624\n"
"v_mfma_f32_16x16x32_bf16 a[188:191], v[220:223], v[8:11], a[188:191]\n"
"s_waitcnt lgkmcnt(8)\n"
"ds_read_b64_tr_b16 v[220:221], v254 offset:28672\n"
"ds_read_b64_tr_b16 v[222:223], v254 offset:30720\n"
"v_mfma_f32_16x16x32_bf16 a[188:191], v[228:231], v[72:75], a[188:191]\n"
"s_waitcnt lgkmcnt(8)\n"
"buffer_load_dwordx4 v252, s[56:59], s94 offen offset:3072 lds\n"
"v_mfma_f32_16x16x32_bf16 a[188:191], v[232:235], v[104:107], a[188:191]\n"
"s_add_u32 s94, s26, 0x4a80\n"
"v_mfma_f32_16x16x32_bf16 a[184:187], v[232:235], v[96:99], a[184:187]\n"
"ds_read_b64_tr_b16 v[232:233], v249 offset:0\n"
"ds_read_b64_tr_b16 v[234:235], v249 offset:2048\n"
"v_mfma_f32_16x16x32_bf16 a[184:187], v[228:231], v[64:67], a[184:187]\n"
"s_waitcnt lgkmcnt(8)\n"
"ds_read_b64_tr_b16 v[228:229], v249 offset:4096\n"
"ds_read_b64_tr_b16 v[230:231], v249 offset:6144\n"
"v_mfma_f32_16x16x32_bf16 a[184:187], v[216:219], v[4:7], a[184:187]\n"
"s_waitcnt lgkmcnt(8)\n"
"v_mfma_f32_16x16x32_bf16 a[184:187], v[212:215], v[36:39], a[184:187]\n"
"v_mfma_f32_16x16x32_bf16 a[188:191], v[212:215], v[44:47], a[188:191]\n"
"ds_read_b64_tr_b16 v[212:213], v249 offset:8192\n"
"ds_read_b64_tr_b16 v[214:215], v249 offset:10240\n"
"v_mfma_f32_16x16x32_bf16 a[188:191], v[216:219], v[12:15], a[188:191]\n"
"s_waitcnt lgkmcnt(8)\n"
"ds_read_b64_tr_b16 v[216:217], v249 offset:12288\n"
"ds_read_b64_tr_b16 v[218:219], v249 offset:14336\n"
"v_mfma_f32_16x16x32_bf16 a[188:191], v[224:227], v[76:79], a[188:191]\n"
"s_waitcnt lgkmcnt(8)\n"
"v_mfma_f32_16x16x32_bf16 a[188:191], v[220:223], v[108:111], a[188:191]\n"
"s_waitcnt vmcnt(20)\n"
"s_barrier\n"
"v_mfma_f32_16x16x32_bf16 a[184:187], v[220:223], v[100:103], a[184:187]\n"
"ds_read_b64_tr_b16 v[220:221], v249 offset:16384\n"
"ds_read_b64_tr_b16 v[222:223], v249 offset:18432\n"
"v_mfma_f32_16x16x32_bf16 a[184:187], v[224:227], v[68:71], a[184:187]\n"
"s_waitcnt lgkmcnt(8)\n"
"ds_read_b64_tr_b16 v[224:225], v249 offset:20480\n"
"ds_read_b64_tr_b16 v[226:227], v249 offset:22528\n"
"v_mfma_f32_16x16x32_bf16 a[200:203], v[232:235], v[0:3], 0\n"
"s_waitcnt lgkmcnt(8)\n"
"v_mfma_f32_16x16x32_bf16 a[200:203], v[228:231], v[32:35], a[200:203]\n"
"s_add_u32 m0, s90, 0xd400\n"
"v_mfma_f32_16x16x32_bf16 a[204:207], v[228:231], v[40:43], 0\n"
"ds_read_b64_tr_b16 v[228:229], v249 offset:24576\n"
"ds_read_b64_tr_b16 v[230:231], v249 offset:26624\n"
"v_mfma_f32_16x16x32_bf16 a[204:207], v[232:235], v[8:11], a[204:207]\n"
"s_waitcnt lgkmcnt(8)\n"
"ds_read_b64_tr_b16 v[232:233], v249 offset:28672\n"
"ds_read_b64_tr_b16 v[234:235], v249 offset:30720\n"
"v_mfma_f32_16x16x32_bf16 a[204:207], v[212:215], v[72:75], a[204:207]\n"
"s_waitcnt lgkmcnt(8)\n"
"buffer_load_dwordx4 v252, s[56:59], s91 offen lds\n"
"v_mfma_f32_16x16x32_bf16 a[204:207], v[216:219], v[104:107], a[204:207]\n"
"s_add_u32 s91, s26, 0x7280\n"
"v_mfma_f32_16x16x32_bf16 a[200:203], v[216:219], v[96:99], a[200:203]\n"
"ds_read_b64_tr_b16 v[216:217], v246 offset:0\n"
"ds_read_b64_tr_b16 v[218:219], v246 offset:2048\n"
"v_mfma_f32_16x16x32_bf16 a[200:203], v[212:215], v[64:67], a[200:203]\n"
"s_waitcnt lgkmcnt(8)\n"
"ds_read_b64_tr_b16 v[212:213], v246 offset:4096\n"
"ds_read_b64_tr_b16 v[214:215], v246 offset:6144\n"
"v_mfma_f32_16x16x32_bf16 a[200:203], v[220:223], v[4:7], a[200:203]\n"
"s_waitcnt lgkmcnt(8)\n"
"buffer_load_dwordx4 v252, s[56:59], s92 offen offset:1024 lds\n"
"v_mfma_f32_16x16x32_bf16 a[200:203], v[224:227], v[36:39], a[200:203]\n"
"s_add_u32 s92, s26, 0x8a80\n"
"v_mfma_f32_16x16x32_bf16 a[204:207], v[224:227], v[44:47], a[204:207]\n"
"ds_read_b64_tr_b16 v[224:225], v246 offset:8192\n"
"ds_read_b64_tr_b16 v[226:227], v246 offset:10240\n"
"v_mfma_f32_16x16x32_bf16 a[204:207], v[220:223], v[12:15], a[204:207]\n"
"s_waitcnt lgkmcnt(8)\n"
"ds_read_b64_tr_b16 v[220:221], v246 offset:12288\n"
"ds_read_b64_tr_b16 v[222:223], v246 offset:14336\n"
"v_mfma_f32_16x16x32_bf16 a[204:207], v[228:231], v[76:79], a[204:207]\n"
"s_waitcnt lgkmcnt(8)\n"
"v_mfma_f32_16x16x32_bf16 a[204:207], v[232:235], v[108:111], a[204:207]\n"
"v_mfma_f32_16x16x32_bf16 a[200:203], v[232:235], v[100:103], a[200:203]\n"
"ds_read_b64_tr_b16 v[232:233], v246 offset:16384\n"
"ds_read_b64_tr_b16 v[234:235], v246 offset:18432\n"
"v_mfma_f32_16x16x32_bf16 a[200:203], v[228:231], v[68:71], a[200:203]\n"
"s_waitcnt lgkmcnt(8)\n"
"ds_read_b64_tr_b16 v[228:229], v246 offset:20480\n"
"ds_read_b64_tr_b16 v[230:231], v246 offset:22528\n"
"v_mfma_f32_16x16x32_bf16 a[168:171], v[216:219], v[0:3], 0\n"
"s_waitcnt lgkmcnt(8)\n"
"buffer_load_dwordx4 v252, s[56:59], s93 offen offset:2048 lds\n"
"v_mfma_f32_16x16x32_bf16 a[168:171], v[212:215], v[32:35], a[168:171]\n"
"s_add_u32 s93, s26, 0xa280\n"
"v_mfma_f32_16x16x32_bf16 a[172:175], v[212:215], v[40:43], 0\n"
"ds_read_b64_tr_b16 v[212:213], v246 offset:24576\n"
"ds_read_b64_tr_b16 v[214:215], v246 offset:26624\n"
"v_mfma_f32_16x16x32_bf16 a[172:175], v[216:219], v[8:11], a[172:175]\n"
"s_waitcnt lgkmcnt(8)\n"
"ds_read_b64_tr_b16 v[216:217], v246 offset:28672\n"
"ds_read_b64_tr_b16 v[218:219], v246 offset:30720\n"
"v_mfma_f32_16x16x32_bf16 a[172:175], v[224:227], v[72:75], a[172:175]\n"
"s_waitcnt lgkmcnt(8)\n"
"v_mfma_f32_16x16x32_bf16 a[172:175], v[220:223], v[104:107], a[172:175]\n"
"v_mfma_f32_16x16x32_bf16 a[168:171], v[220:223], v[96:99], a[168:171]\n"
"ds_read_b64_tr_b16 v[220:221], v253 offset:0\n"
"ds_read_b64_tr_b16 v[222:223], v253 offset:2048\n"
"v_mfma_f32_16x16x32_bf16 a[168:171], v[224:227], v[64:67], a[168:171]\n"
"s_waitcnt lgkmcnt(8)\n"
"ds_read_b64_tr_b16 v[224:225], v253 offset:4096\n"
"ds_read_b64_tr_b16 v[226:227], v253 offset:6144\n"
"v_mfma_f32_16x16x32_bf16 a[168:171], v[232:235], v[4:7], a[168:171]\n"
"s_waitcnt lgkmcnt(8)\n"
"buffer_load_dwordx4 v252, s[56:59], s94 offen offset:3072 lds\n"
"v_mfma_f32_16x16x32_bf16 a[168:171], v[228:231], v[36:39], a[168:171]\n"
"s_add_u32 m0, s90, 0xe400\n"
"v_mfma_f32_16x16x32_bf16 a[172:175], v[228:231], v[44:47], a[172:175]\n"
"s_add_u32 s94, s26, 0xba80\n"
"ds_read_b64_tr_b16 v[228:229], v253 offset:8192\n"
"ds_read_b64_tr_b16 v[230:231], v253 offset:10240\n"
"v_mfma_f32_16x16x32_bf16 a[172:175], v[232:235], v[12:15], a[172:175]\n"
"s_waitcnt lgkmcnt(8)\n"
"ds_read_b64_tr_b16 v[232:233], v253 offset:12288\n"
"ds_read_b64_tr_b16 v[234:235], v253 offset:14336\n"
"v_mfma_f32_16x16x32_bf16 a[172:175], v[212:215], v[76:79], a[172:175]\n"
"s_waitcnt lgkmcnt(8)\n"
"v_mfma_f32_16x16x32_bf16 a[172:175], v[216:219], v[108:111], a[172:175]\n"
"v_mfma_f32_16x16x32_bf16 a[168:171], v[216:219], v[100:103], a[168:171]\n"
"ds_read_b64_tr_b16 v[216:217], v253 offset:16384\n"
"ds_read_b64_tr_b16 v[218:219], v253 offset:18432\n"
"v_mfma_f32_16x16x32_bf16 a[168:171], v[212:215], v[68:71], a[168:171]\n"
"s_waitcnt lgkmcnt(8)\n"
"ds_read_b64_tr_b16 v[212:213], v253 offset:20480\n"
"ds_read_b64_tr_b16 v[214:215], v253 offset:22528\n"
"v_mfma_f32_16x16x32_bf16 a[152:155], v[220:223], v[0:3], 0\n"
"s_waitcnt lgkmcnt(8)\n"
"buffer_load_dwordx4 v252, s[56:59], s91 offen lds\n"
"v_mfma_f32_16x16x32_bf16 a[152:155], v[224:227], v[32:35], a[152:155]\n"
"s_add_u32 s91, s26, 0x300\n"
"v_mfma_f32_16x16x32_bf16 a[156:159], v[224:227], v[40:43], 0\n"
"v_mfma_f32_16x16x32_bf16 a[156:159], v[220:223], v[8:11], a[156:159]\n"
"v_mfma_f32_16x16x32_bf16 a[216:219], v[220:223], v[160:163], 0\n"
"ds_read_b64_tr_b16 v[220:221], v253 offset:24576\n"
"ds_read_b64_tr_b16 v[222:223], v253 offset:26624\n"
"v_mfma_f32_16x16x32_bf16 a[216:219], v[224:227], v[164:167], a[216:219]\n"
"s_waitcnt lgkmcnt(8)\n"
"ds_read_b64_tr_b16 v[224:225], v253 offset:28672\n"
"ds_read_b64_tr_b16 v[226:227], v253 offset:30720\n"
"v_mfma_f32_16x16x32_bf16 a[216:219], v[228:231], v[168:171], a[216:219]\n"
"s_waitcnt lgkmcnt(8)\n"
"v_mfma_f32_16x16x32_bf16 a[216:219], v[232:235], v[172:175], a[216:219]\n"
"v_mfma_f32_16x16x32_bf16 a[156:159], v[232:235], v[104:107], a[156:159]\n"
"buffer_load_dwordx4 v252, s[56:59], s92 offen offset:1024 lds\n"
"v_mfma_f32_16x16x32_bf16 a[156:159], v[228:231], v[72:75], a[156:159]\n"
"s_add_u32 s92, s26, 0x1b00\n"
"v_mfma_f32_16x16x32_bf16 a[152:155], v[228:231], v[64:67], a[152:155]\n"
"ds_read_b64_tr_b16 v[228:229], v254 offset:32768\n"
"ds_read_b64_tr_b16 v[230:231], v254 offset:34816\n"
"v_mfma_f32_16x16x32_bf16 a[152:155], v[232:235], v[96:99], a[152:155]\n"
"s_waitcnt lgkmcnt(8)\n"
"ds_read_b64_tr_b16 v[232:233], v254 offset:36864\n"
"ds_read_b64_tr_b16 v[234:235], v254 offset:38912\n"
"v_mfma_f32_16x16x32_bf16 a[152:155], v[216:219], v[4:7], a[152:155]\n"
"s_waitcnt lgkmcnt(8)\n"
"v_mfma_f32_16x16x32_bf16 a[152:155], v[212:215], v[36:39], a[152:155]\n"
"v_mfma_f32_16x16x32_bf16 a[156:159], v[212:215], v[44:47], a[156:159]\n"
"v_mfma_f32_16x16x32_bf16 a[156:159], v[216:219], v[12:15], a[156:159]\n"
"v_mfma_f32_16x16x32_bf16 a[216:219], v[216:219], v[176:179], a[216:219]\n"
"ds_read_b64_tr_b16 v[216:217], v254 offset:40960\n"
"ds_read_b64_tr_b16 v[218:219], v254 offset:43008\n"
"v_mfma_f32_16x16x32_bf16 a[216:219], v[212:215], v[180:183], a[216:219]\n"
"s_waitcnt lgkmcnt(8)\n"
"ds_read_b64_tr_b16 v[212:213], v254 offset:45056\n"
"ds_read_b64_tr_b16 v[214:215], v254 offset:47104\n"
"v_mfma_f32_16x16x32_bf16 a[216:219], v[220:223], v[184:187], a[216:219]\n"
"s_waitcnt lgkmcnt(8)\n"
"buffer_load_dwordx4 v252, s[56:59], s93 offen offset:2048 lds\n"
"v_mfma_f32_16x16x32_bf16 a[216:219], v[224:227], v[188:191], a[216:219]\n"
"s_add_u32 s93, s26, 0x3300\n"
"v_mfma_f32_16x16x32_bf16 a[156:159], v[224:227], v[108:111], a[156:159]\n"
"v_mfma_f32_16x16x32_bf16 a[156:159], v[220:223], v[76:79], a[156:159]\n"
"v_mfma_f32_16x16x32_bf16 a[152:155], v[220:223], v[68:71], a[152:155]\n"
"ds_read_b64_tr_b16 v[220:221], v254 offset:49152\n"
"ds_read_b64_tr_b16 v[222:223], v254 offset:51200\n"
"v_mfma_f32_16x16x32_bf16 a[152:155], v[224:227], v[100:103], a[152:155]\n"
"s_waitcnt lgkmcnt(8)\n"
"ds_read_b64_tr_b16 v[224:225], v254 offset:53248\n"
"ds_read_b64_tr_b16 v[226:227], v254 offset:55296\n"
"v_mfma_f32_16x16x32_bf16 a[252:255], v[228:231], v[0:3], 0\n"
"s_waitcnt lgkmcnt(8)\n"
"buffer_load_dwordx4 v252, s[56:59], s94 offen offset:3072 lds\n"
"v_mfma_f32_16x16x32_bf16 a[252:255], v[232:235], v[32:35], a[252:255]\n"
"s_add_u32 s94, s26, 0x4b00\n"
"v_mfma_f32_16x16x32_bf16 a[0:3], v[232:235], v[40:43], 0\n"
"ds_read_b64_tr_b16 v[232:233], v254 offset:57344\n"
"ds_read_b64_tr_b16 v[234:235], v254 offset:59392\n"
"v_mfma_f32_16x16x32_bf16 a[0:3], v[228:231], v[8:11], a[0:3]\n"
"s_waitcnt lgkmcnt(8)\n"
"ds_read_b64_tr_b16 v[228:229], v254 offset:61440\n"
"ds_read_b64_tr_b16 v[230:231], v254 offset:63488\n"
"v_mfma_f32_16x16x32_bf16 a[0:3], v[216:219], v[72:75], a[0:3]\n"
"s_waitcnt lgkmcnt(8)\n"
"v_subrev_u32 v254, 0x12000, v254\n"
"v_mfma_f32_16x16x32_bf16 a[0:3], v[212:215], v[104:107], a[0:3]\n"
"v_mfma_f32_16x16x32_bf16 a[252:255], v[212:215], v[96:99], a[252:255]\n"
"ds_read_b64_tr_b16 v[212:213], v249 offset:32768\n"
"ds_read_b64_tr_b16 v[214:215], v249 offset:34816\n"
"v_mfma_f32_16x16x32_bf16 a[252:255], v[216:219], v[64:67], a[252:255]\n"
"s_waitcnt lgkmcnt(8)\n"
"ds_read_b64_tr_b16 v[216:217], v249 offset:36864\n"
"ds_read_b64_tr_b16 v[218:219], v249 offset:38912\n"
"v_mfma_f32_16x16x32_bf16 a[252:255], v[220:223], v[4:7], a[252:255]\n"
"s_waitcnt lgkmcnt(8)\n"
"v_mfma_f32_16x16x32_bf16 a[252:255], v[224:227], v[36:39], a[252:255]\n"
"v_mfma_f32_16x16x32_bf16 a[0:3], v[224:227], v[44:47], a[0:3]\n"
"ds_read_b64_tr_b16 v[224:225], v249 offset:40960\n"
"ds_read_b64_tr_b16 v[226:227], v249 offset:43008\n"
"v_mfma_f32_16x16x32_bf16 a[0:3], v[220:223], v[12:15], a[0:3]\n"
"s_waitcnt lgkmcnt(8)\n"
"ds_read_b64_tr_b16 v[220:221], v249 offset:45056\n"
"ds_read_b64_tr_b16 v[222:223], v249 offset:47104\n"
"v_mfma_f32_16x16x32_bf16 a[0:3], v[232:235], v[76:79], a[0:3]\n"
"s_waitcnt lgkmcnt(8)\n"
"v_mfma_f32_16x16x32_bf16 a[0:3], v[228:231], v[108:111], a[0:3]\n"
"s_waitcnt vmcnt(0)\n"
"s_barrier\n"
"v_mfma_f32_16x16x32_bf16 a[252:255], v[228:231], v[100:103], a[252:255]\n"
"ds_read_b64_tr_b16 v[228:229], v249 offset:49152\n"
"ds_read_b64_tr_b16 v[230:231], v249 offset:51200\n"
"v_mfma_f32_16x16x32_bf16 a[252:255], v[232:235], v[68:71], a[252:255]\n"
"s_waitcnt lgkmcnt(8)\n"
"ds_read_b64_tr_b16 v[232:233], v249 offset:53248\n"
"ds_read_b64_tr_b16 v[234:235], v249 offset:55296\n"
"v_mfma_f32_16x16x32_bf16 a[4:7], v[212:215], v[0:3], 0\n"
"s_waitcnt lgkmcnt(8)\n"
"buffer_store_dword v236, v255, s[60:63], s39 offen sc1\n"
"v_mfma_f32_16x16x32_bf16 a[4:7], v[216:219], v[32:35], a[4:7]\n"
"s_add_u32 m0, s90, 0x17400\n"
"v_mfma_f32_16x16x32_bf16 a[16:19], v[216:219], v[40:43], 0\n"
"ds_read_b64_tr_b16 v[216:217], v249 offset:57344\n"
"ds_read_b64_tr_b16 v[218:219], v249 offset:59392\n"
"v_mfma_f32_16x16x32_bf16 a[16:19], v[212:215], v[8:11], a[16:19]\n"
"s_waitcnt lgkmcnt(8)\n"
"ds_read_b64_tr_b16 v[212:213], v249 offset:61440\n"
"ds_read_b64_tr_b16 v[214:215], v249 offset:63488\n"
"v_mfma_f32_16x16x32_bf16 a[16:19], v[224:227], v[72:75], a[16:19]\n"
"s_waitcnt lgkmcnt(8)\n"
"buffer_load_dwordx4 v252, s[56:59], s91 offen lds\n"
"v_mfma_f32_16x16x32_bf16 a[16:19], v[220:223], v[104:107], a[16:19]\n"
"s_add_u32 s91, s26, 0x7300\n"
"v_subrev_u32 v249, 0x12000, v249\n"
"v_mfma_f32_16x16x32_bf16 a[4:7], v[220:223], v[96:99], a[4:7]\n"
"ds_read_b64_tr_b16 v[220:221], v246 offset:32768\n"
"ds_read_b64_tr_b16 v[222:223], v246 offset:34816\n"
"v_mfma_f32_16x16x32_bf16 a[4:7], v[224:227], v[64:67], a[4:7]\n"
"s_waitcnt lgkmcnt(8)\n"
"ds_read_b64_tr_b16 v[224:225], v246 offset:36864\n"
"ds_read_b64_tr_b16 v[226:227], v246 offset:38912\n"
"v_mfma_f32_16x16x32_bf16 a[4:7], v[228:231], v[4:7], a[4:7]\n"
"s_waitcnt lgkmcnt(8)\n"
"buffer_load_dwordx4 v252, s[56:59], s92 offen offset:1024 lds\n"
"v_mfma_f32_16x16x32_bf16 a[4:7], v[232:235], v[36:39], a[4:7]\n"
"s_add_u32 s92, s26, 0x8b00\n"
"v_mfma_f32_16x16x32_bf16 a[16:19], v[232:235], v[44:47], a[16:19]\n"
"ds_read_b64_tr_b16 v[232:233], v246 offset:40960\n"
"ds_read_b64_tr_b16 v[234:235], v246 offset:43008\n"
"v_mfma_f32_16x16x32_bf16 a[16:19], v[228:231], v[12:15], a[16:19]\n"
"s_waitcnt lgkmcnt(8)\n"
"ds_read_b64_tr_b16 v[228:229], v246 offset:45056\n"
"ds_read_b64_tr_b16 v[230:231], v246 offset:47104\n"
"v_mfma_f32_16x16x32_bf16 a[16:19], v[216:219], v[76:79], a[16:19]\n"
"s_waitcnt lgkmcnt(8)\n"
"v_mfma_f32_16x16x32_bf16 a[16:19], v[212:215], v[108:111], a[16:19]\n"
"v_mfma_f32_16x16x32_bf16 a[4:7], v[212:215], v[100:103], a[4:7]\n"
"ds_read_b64_tr_b16 v[212:213], v246 offset:49152\n"
"ds_read_b64_tr_b16 v[214:215], v246 offset:51200\n"
"v_mfma_f32_16x16x32_bf16 a[4:7], v[216:219], v[68:71], a[4:7]\n"
"s_waitcnt lgkmcnt(8)\n"
"ds_read_b64_tr_b16 v[216:217], v246 offset:53248\n"
"ds_read_b64_tr_b16 v[218:219], v246 offset:55296\n"
"v_mfma_f32_16x16x32_bf16 a[236:239], v[220:223], v[0:3], 0\n"
"s_waitcnt lgkmcnt(8)\n"
"buffer_load_dwordx4 v252, s[56:59], s93 offen offset:2048 lds\n"
"v_mfma_f32_16x16x32_bf16 a[236:239], v[224:227], v[32:35], a[236:239]\n"
"s_add_u32 s93, s26, 0xa300\n"
"v_mfma_f32_16x16x32_bf16 a[248:251], v[224:227], v[40:43], 0\n"
"ds_read_b64_tr_b16 v[224:225], v246 offset:57344\n"
"ds_read_b64_tr_b16 v[226:227], v246 offset:59392\n"
"v_mfma_f32_16x16x32_bf16 a[248:251], v[220:223], v[8:11], a[248:251]\n"
"s_waitcnt lgkmcnt(8)\n"
"ds_read_b64_tr_b16 v[220:221], v246 offset:61440\n"
"ds_read_b64_tr_b16 v[222:223], v246 offset:63488\n"
"v_mfma_f32_16x16x32_bf16 a[248:251], v[232:235], v[72:75], a[248:251]\n"
"s_waitcnt lgkmcnt(8)\n"
"v_subrev_u32 v246, 0x12000, v246\n"
"v_mfma_f32_16x16x32_bf16 a[248:251], v[228:231], v[104:107], a[248:251]\n"
"v_mfma_f32_16x16x32_bf16 a[236:239], v[228:231], v[96:99], a[236:239]\n"
"ds_read_b64_tr_b16 v[228:229], v253 offset:32768\n"
"ds_read_b64_tr_b16 v[230:231], v253 offset:34816\n"
"v_mfma_f32_16x16x32_bf16 a[236:239], v[232:235], v[64:67], a[236:239]\n"
"s_waitcnt lgkmcnt(8)\n"
"ds_read_b64_tr_b16 v[232:233], v253 offset:36864\n"
"ds_read_b64_tr_b16 v[234:235], v253 offset:38912\n"
"v_mfma_f32_16x16x32_bf16 a[236:239], v[212:215], v[4:7], a[236:239]\n"
"s_waitcnt lgkmcnt(8)\n"
"buffer_load_dwordx4 v252, s[56:59], s94 offen offset:3072 lds\n"
"v_mfma_f32_16x16x32_bf16 a[236:239], v[216:219], v[36:39], a[236:239]\n"
"s_add_u32 m0, s90, 0x18400\n"
"v_mfma_f32_16x16x32_bf16 a[248:251], v[216:219], v[44:47], a[248:251]\n"
"s_add_u32 s94, s26, 0xbb00\n"
"ds_read_b64_tr_b16 v[216:217], v253 offset:40960\n"
"ds_read_b64_tr_b16 v[218:219], v253 offset:43008\n"
"v_mfma_f32_16x16x32_bf16 a[248:251], v[212:215], v[12:15], a[248:251]\n"
"s_waitcnt lgkmcnt(8)\n"
"ds_read_b64_tr_b16 v[212:213], v253 offset:45056\n"
"ds_read_b64_tr_b16 v[214:215], v253 offset:47104\n"
"v_mfma_f32_16x16x32_bf16 a[248:251], v[224:227], v[76:79], a[248:251]\n"
"s_waitcnt lgkmcnt(8)\n"
"v_mfma_f32_16x16x32_bf16 a[248:251], v[220:223], v[108:111], a[248:251]\n"
"v_mfma_f32_16x16x32_bf16 a[236:239], v[220:223], v[100:103], a[236:239]\n"
"Lpada_8:\n"
"s_cbranch_execz Lpada_12\n"
"ds_read_b64_tr_b16 v[220:221], v253 offset:49152\n"
"ds_read_b64_tr_b16 v[222:223], v253 offset:51200\n"
"v_mfma_f32_16x16x32_bf16 a[236:239], v[224:227], v[68:71], a[236:239]\n"
"s_waitcnt lgkmcnt(8)\n"
"ds_read_b64_tr_b16 v[224:225], v253 offset:53248\n"
"ds_read_b64_tr_b16 v[226:227], v253 offset:55296\n"
"v_mfma_f32_16x16x32_bf16 a[220:223], v[228:231], v[0:3], 0\n"
"s_waitcnt lgkmcnt(8)\n"
"buffer_load_dwordx4 v252, s[56:59], s91 offen lds\n"
"v_mfma_f32_16x16x32_bf16 a[220:223], v[232:235], v[32:35], a[220:223]\n"
"s_add_u32 s91, s27, 0\n"
"v_mfma_f32_16x16x32_bf16 a[232:235], v[232:235], v[40:43], 0\n"
"v_mfma_f32_16x16x32_bf16 a[232:235], v[228:231], v[8:11], a[232:235]\n"
"v_mfma_f32_16x16x32_bf16 a[20:23], v[228:231], v[160:163], 0\n"
"ds_read_b64_tr_b16 v[228:229], v253 offset:57344\n"
"ds_read_b64_tr_b16 v[230:231], v253 offset:59392\n"
"v_mfma_f32_16x16x32_bf16 a[20:23], v[232:235], v[164:167], a[20:23]\n"
"s_waitcnt lgkmcnt(8)\n"
"ds_read_b64_tr_b16 v[232:233], v253 offset:61440\n"
"ds_read_b64_tr_b16 v[234:235], v253 offset:63488\n"
"v_mfma_f32_16x16x32_bf16 a[20:23], v[216:219], v[168:171], a[20:23]\n"
"s_waitcnt lgkmcnt(8)\n"
"v_subrev_u32 v253, 0x12000, v253\n"
"v_mfma_f32_16x16x32_bf16 a[20:23], v[212:215], v[172:175], a[20:23]\n"
"buffer_load_dword v237, v255, s[60:63], s38 offen sc1\n"
"v_mfma_f32_16x16x32_bf16 a[232:235], v[212:215], v[104:107], a[232:235]\n"
"v_mfma_f32_16x16x32_bf16 a[232:235], v[216:219], v[72:75], a[232:235]\n"
"v_mfma_f32_16x16x32_bf16 a[220:223], v[216:219], v[64:67], a[220:223]\n"
"buffer_load_dwordx4 v252, s[56:59], s92 offen offset:1024 lds\n"
"v_mfma_f32_16x16x32_bf16 a[220:223], v[212:215], v[96:99], a[220:223]\n"
"s_waitcnt lgkmcnt(6)\n"
"ds_read_b64_tr_b16 v[216:217], v254 offset:0\n"
"ds_read_b64_tr_b16 v[218:219], v254 offset:2048\n"
"v_mfma_f32_16x16x32_bf16 a[220:223], v[220:223], v[4:7], a[220:223]\n"
"s_waitcnt lgkmcnt(6)\n"
"ds_read_b64_tr_b16 v[212:213], v254 offset:4096\n"
"ds_read_b64_tr_b16 v[214:215], v254 offset:6144\n"
"v_mfma_f32_16x16x32_bf16 a[220:223], v[224:227], v[36:39], a[220:223]\n"
"s_add_u32 s92, s27, 0x1800\n"
"v_mfma_f32_16x16x32_bf16 a[232:235], v[224:227], v[44:47], a[232:235]\n"
"v_mfma_f32_16x16x32_bf16 a[232:235], v[220:223], v[12:15], a[232:235]\n"
"v_mfma_f32_16x16x32_bf16 a[20:23], v[220:223], v[176:179], a[20:23]\n"
"ds_read_b64_tr_b16 v[220:221], v254 offset:8192\n"
"ds_read_b64_tr_b16 v[222:223], v254 offset:10240\n"
"v_mfma_f32_16x16x32_bf16 a[20:23], v[224:227], v[180:183], a[20:23]\n"
"s_waitcnt lgkmcnt(8)\n"
"ds_read_b64_tr_b16 v[224:225], v254 offset:12288\n"
"ds_read_b64_tr_b16 v[226:227], v254 offset:14336\n"
"v_mfma_f32_16x16x32_bf16 a[20:23], v[228:231], v[184:187], a[20:23]\n"
"s_waitcnt lgkmcnt(8)\n"
"buffer_load_dwordx4 v252, s[56:59], s93 offen offset:2048 lds\n"
"v_mfma_f32_16x16x32_bf16 a[20:23], v[232:235], v[188:191], a[20:23]\n"
"s_add_u32 s93, s27, 0x3000\n"
"v_mfma_f32_16x16x32_bf16 a[232:235], v[232:235], v[108:111], a[232:235]\n"
"v_mfma_f32_16x16x32_bf16 a[232:235], v[228:231], v[76:79], a[232:235]\n"
"v_mfma_f32_16x16x32_bf16 a[220:223], v[228:231], v[68:71], a[220:223]\n"
"ds_read_b64_tr_b16 v[228:229], v254 offset:16384\n"
"ds_read_b64_tr_b16 v[230:231], v254 offset:18432\n"
"v_mfma_f32_16x16x32_bf16 a[220:223], v[232:235], v[100:103], a[220:223]\n"
"s_waitcnt lgkmcnt(8)\n"
"ds_read_b64_tr_b16 v[232:233], v254 offset:20480\n"
"ds_read_b64_tr_b16 v[234:235], v254 offset:22528\n"
"v_mfma_f32_16x16x32_bf16 a[64:67], v[216:219], v[0:3], 0\n"
"s_waitcnt lgkmcnt(8)\n"
"buffer_load_dwordx4 v252, s[56:59], s94 offen offset:3072 lds\n"
"v_mfma_f32_16x16x32_bf16 a[64:67], v[212:215], v[32:35], a[64:67]\n"
"s_add_u32 s94, s27, 0x4800\n"
"v_mfma_f32_16x16x32_bf16 a[68:71], v[212:215], v[40:43], 0\n"
"ds_read_b64_tr_b16 v[212:213], v254 offset:24576\n"
"ds_read_b64_tr_b16 v[214:215], v254 offset:26624\n"
"v_mfma_f32_16x16x32_bf16 a[68:71], v[216:219], v[8:11], a[68:71]\n"
"s_waitcnt lgkmcnt(8)\n"
"ds_read_b64_tr_b16 v[216:217], v254 offset:28672\n"
"ds_read_b64_tr_b16 v[218:219], v254 offset:30720\n"
"v_mfma_f32_16x16x32_bf16 a[68:71], v[220:223], v[72:75], a[68:71]\n"
"s_waitcnt lgkmcnt(8)\n"
"v_mfma_f32_16x16x32_bf16 a[68:71], v[224:227], v[104:107], a[68:71]\n"
"v_mfma_f32_16x16x32_bf16 a[64:67], v[224:227], v[96:99], a[64:67]\n"
"ds_read_b64_tr_b16 v[224:225], v249 offset:0\n"
"ds_read_b64_tr_b16 v[226:227], v249 offset:2048\n"
"v_mfma_f32_16x16x32_bf16 a[64:67], v[220:223], v[64:67], a[64:67]\n"
"s_waitcnt lgkmcnt(8)\n"
"ds_read_b64_tr_b16 v[220:221], v249 offset:4096\n"
"ds_read_b64_tr_b16 v[222:223], v249 offset:6144\n"
"v_mfma_f32_16x16x32_bf16 a[64:67], v[228:231], v[4:7], a[64:67]\n"
"s_waitcnt lgkmcnt(8)\n"
"v_mfma_f32_16x16x32_bf16 a[64:67], v[232:235], v[36:39], a[64:67]\n"
"v_mfma_f32_16x16x32_bf16 a[68:71], v[232:235], v[44:47], a[68:71]\n"
"ds_read_b64_tr_b16 v[232:233], v249 offset:8192\n"
"ds_read_b64_tr_b16 v[234:235], v249 offset:10240\n"
"v_mfma_f32_16x16x32_bf16 a[68:71], v[228:231], v[12:15], a[68:71]\n"
"s_waitcnt lgkmcnt(8)\n"
"ds_read_b64_tr_b16 v[228:229], v249 offset:12288\n"
"ds_read_b64_tr_b16 v[230:231], v249 offset:14336\n"
"v_mfma_f32_16x16x32_bf16 a[68:71], v[212:215], v[76:79], a[68:71]\n"
"s_waitcnt lgkmcnt(8)\n"
"s_barrier\n"
"v_mfma_f32_16x16x32_bf16 a[68:71], v[216:219], v[108:111], a[68:71]\n"
"v_mfma_f32_16x16x32_bf16 a[64:67], v[216:219], v[100:103], a[64:67]\n"
"ds_read_b64_tr_b16 v[216:217], v249 offset:16384\n"
"ds_read_b64_tr_b16 v[218:219], v249 offset:18432\n"
"v_mfma_f32_16x16x32_bf16 a[64:67], v[212:215], v[68:71], a[64:67]\n"
"s_waitcnt lgkmcnt(8)\n"
"ds_read_b64_tr_b16 v[212:213], v249 offset:20480\n"
"ds_read_b64_tr_b16 v[214:215], v249 offset:22528\n"
"v_mfma_f32_16x16x32_bf16 a[80:83], v[224:227], v[0:3], 0\n"
"s_waitcnt lgkmcnt(8)\n"
"v_mfma_f32_16x16x32_bf16 a[80:83], v[220:223], v[32:35], a[80:83]\n"
"s_add_u32 m0, s90, 0x1f400\n"
"v_mfma_f32_16x16x32_bf16 a[84:87], v[220:223], v[40:43], 0\n"
"ds_read_b64_tr_b16 v[220:221], v249 offset:24576\n"
"ds_read_b64_tr_b16 v[222:223], v249 offset:26624\n"
"v_mfma_f32_16x16x32_bf16 a[84:87], v[224:227], v[8:11], a[84:87]\n"
"s_waitcnt lgkmcnt(8)\n"
"ds_read_b64_tr_b16 v[224:225], v249 offset:28672\n"
"ds_read_b64_tr_b16 v[226:227], v249 offset:30720\n"
"v_mfma_f32_16x16x32_bf16 a[84:87], v[232:235], v[72:75], a[84:87]\n"
"s_waitcnt lgkmcnt(8)\n"
"buffer_load_dwordx4 v252, s[56:59], s91 offen lds\n"
"v_mfma_f32_16x16x32_bf16 a[84:87], v[228:231], v[104:107], a[84:87]\n"
"s_add_u32 s91, s27, 0x7000\n"
"s_waitcnt vmcnt(4)\n"
"v_readfirstlane_b32 s72, v237\n"
"v_mfma_f32_16x16x32_bf16 a[80:83], v[228:231], v[96:99], a[80:83]\n"
"ds_read_b64_tr_b16 v[228:229], v246 offset:0\n"
"ds_read_b64_tr_b16 v[230:231], v246 offset:2048\n"
"v_mfma_f32_16x16x32_bf16 a[80:83], v[232:235], v[64:67], a[80:83]\n"
"s_waitcnt lgkmcnt(8)\n"
"ds_read_b64_tr_b16 v[232:233], v246 offset:4096\n"
"ds_read_b64_tr_b16 v[234:235], v246 offset:6144\n"
"v_mfma_f32_16x16x32_bf16 a[80:83], v[216:219], v[4:7], a[80:83]\n"
"s_waitcnt lgkmcnt(8)\n"
"s_cmp_lg_u32 s72, s29\n"
"s_cbranch_scc1 Lslow_25\n"
"Lret_25:\n"
"v_mfma_f32_16x16x32_bf16 a[80:83], v[212:215], v[36:39], a[80:83]\n"
"buffer_load_dword v236, v255, s[60:63], s39 offen offset:128 sc1\n"
"v_mfma_f32_16x16x32_bf16 a[84:87], v[212:215], v[44:47], a[84:87]\n"
"ds_read_b64_tr_b16 v[212:213], v246 offset:8192\n"
"ds_read_b64_tr_b16 v[214:215], v246 offset:10240\n"
"v_mfma_f32_16x16x32_bf16 a[84:87], v[216:219], v[12:15], a[84:87]\n"
"s_waitcnt lgkmcnt(8)\n"
"ds_read_b64_tr_b16 v[216:217], v246 offset:12288\n"
"ds_read_b64_tr_b16 v[218:219], v246 offset:14336\n"
"v_mfma_f32_16x16x32_bf16 a[84:87], v[220:223], v[76:79], a[84:87]\n"
"s_waitcnt lgkmcnt(8)\n"
"buffer_load_dwordx4 v252, s[56:59], s92 offen offset:1024 lds\n"
"v_mfma_f32_16x16x32_bf16 a[84:87], v[224:227], v[108:111], a[84:87]\n"
"s_add_u32 s92, s27, 0x8800\n"
"v_mfma_f32_16x16x32_bf16 a[80:83], v[224:227], v[100:103], a[80:83]\n"
"ds_read_b64_tr_b16 v[224:225], v246 offset:16384\n"
"ds_read_b64_tr_b16 v[226:227], v246 offset:18432\n"
"v_mfma_f32_16x16x32_bf16 a[80:83], v[220:223], v[68:71], a[80:83]\n"
"s_waitcnt lgkmcnt(8)\n"
"ds_read_b64_tr_b16 v[220:221], v246 offset:20480\n"
"ds_read_b64_tr_b16 v[222:223], v246 offset:22528\n"
"v_mfma_f32_16x16x32_bf16 a[48:51], v[228:231], v[0:3], 0\n"
"s_waitcnt lgkmcnt(8)\n"
"buffer_load_dwordx4 v[16:19], v248, s[60:63], s84 offen sc1\n"
"v_mfma_f32_16x16x32_bf16 a[48:51], v[232:235], v[32:35], a[48:51]\n"
"s_add_u32 s84, s36, 0x8000\n"
"v_mfma_f32_16x16x32_bf16 a[52:55], v[232:235], v[40:43], 0\n"
"ds_read_b64_tr_b16 v[232:233], v246 offset:24576\n"
"ds_read_b64_tr_b16 v[234:235], v246 offset:26624\n"
"v_mfma_f32_16x16x32_bf16 a[52:55], v[228:231], v[8:11], a[52:55]\n"
"s_waitcnt lgkmcnt(8)\n"
"ds_read_b64_tr_b16 v[228:229], v246 offset:28672\n"
"ds_read_b64_tr_b16 v[230:231], v246 offset:30720\n"
"v_mfma_f32_16x16x32_bf16 a[52:55], v[212:215], v[72:75], a[52:55]\n"
"s_waitcnt lgkmcnt(8)\n"
"buffer_load_dwordx4 v252, s[56:59], s93 offen offset:2048 lds\n"
"v_mfma_f32_16x16x32_bf16 a[52:55], v[216:219], v[104:107], a[52:55]\n"
"s_add_u32 s93, s27, 0xa000\n"
"v_mfma_f32_16x16x32_bf16 a[48:51], v[216:219], v[96:99], a[48:51]\n"
"ds_read_b64_tr_b16 v[216:217], v253 offset:0\n"
"ds_read_b64_tr_b16 v[218:219], v253 offset:2048\n"
"v_mfma_f32_16x16x32_bf16 a[48:51], v[212:215], v[64:67], a[48:51]\n"
"s_waitcnt lgkmcnt(8)\n"
"ds_read_b64_tr_b16 v[212:213], v253 offset:4096\n"
"ds_read_b64_tr_b16 v[214:215], v253 offset:6144\n"
"v_mfma_f32_16x16x32_bf16 a[48:51], v[224:227], v[4:7], a[48:51]\n"
"s_waitcnt lgkmcnt(8)\n"
"buffer_load_dwordx4 v[24:27], v248, s[60:63], s67 offen sc1\n"
"v_mfma_f32_16x16x32_bf16 a[48:51], v[220:223], v[36:39], a[48:51]\n"
"s_add_u32 s67, s36, 0xa000\n"
"v_mfma_f32_16x16x32_bf16 a[52:55], v[220:223], v[44:47], a[52:55]\n"
"ds_read_b64_tr_b16 v[220:221], v253 offset:8192\n"
"ds_read_b64_tr_b16 v[222:223], v253 offset:10240\n"
"v_mfma_f32_16x16x32_bf16 a[52:55], v[224:227], v[12:15], a[52:55]\n"
"s_waitcnt lgkmcnt(8)\n"
"ds_read_b64_tr_b16 v[224:225], v253 offset:12288\n"
"ds_read_b64_tr_b16 v[226:227], v253 offset:14336\n"
"v_mfma_f32_16x16x32_bf16 a[52:55], v[232:235], v[76:79], a[52:55]\n"
"s_waitcnt lgkmcnt(8)\n"
"buffer_load_dwordx4 v252, s[56:59], s94 offen offset:3072 lds\n"
"v_mfma_f32_16x16x32_bf16 a[52:55], v[228:231], v[108:111], a[52:55]\n"
"s_add_u32 m0, s90, 0x20400\n"
"v_mfma_f32_16x16x32_bf16 a[48:51], v[228:231], v[100:103], a[48:51]\n"
"s_add_u32 s94, s27, 0xb800\n"
"ds_read_b64_tr_b16 v[228:229], v253 offset:16384\n"
"ds_read_b64_tr_b16 v[230:231], v253 offset:18432\n"
"v_mfma_f32_16x16x32_bf16 a[48:51], v[232:235], v[68:71], a[48:51]\n"
"s_waitcnt lgkmcnt(8)\n"
"buffer_load_dwordx4 v[48:51], v248, s[60:63], s68 offen sc1\n"
"v_mfma_f32_16x16x32_bf16 a[32:35], v[216:219], v[0:3], 0\n"
"s_waitcnt lgkmcnt(6)\n"
"ds_read_b64_tr_b16 v[232:233], v253 offset:20480\n"
"ds_read_b64_tr_b16 v[234:235], v253 offset:22528\n"
"v_mfma_f32_16x16x32_bf16 a[32:35], v[212:215], v[32:35], a[32:35]\n"
"s_add_u32 s68, s36, 0xc000\n"
"v_mfma_f32_16x16x32_bf16 a[36:39], v[212:215], v[40:43], 0\n"
"buffer_load_dwordx4 v252, s[56:59], s91 offen lds\n"
"v_mfma_f32_16x16x32_bf16 a[36:39], v[216:219], v[8:11], a[36:39]\n"
"s_add_u32 s91, s27, 0x80\n"
"v_mfma_f32_16x16x32_bf16 a[96:99], v[216:219], v[160:163], 0\n"
"ds_read_b64_tr_b16 v[216:217], v253 offset:24576\n"
"ds_read_b64_tr_b16 v[218:219], v253 offset:26624\n"
"v_mfma_f32_16x16x32_bf16 a[96:99], v[212:215], v[164:167], a[96:99]\n"
"s_waitcnt lgkmcnt(8)\n"
"buffer_load_dwordx4 v[56:59], v248, s[60:63], s83 offen sc1\n"
"v_mfma_f32_16x16x32_bf16 a[96:99], v[220:223], v[168:171], a[96:99]\n"
"s_waitcnt lgkmcnt(6)\n"
"ds_read_b64_tr_b16 v[212:213], v253 offset:28672\n"
"ds_read_b64_tr_b16 v[214:215], v253 offset:30720\n"
"v_mfma_f32_16x16x32_bf16 a[96:99], v[224:227], v[172:175], a[96:99]\n"
"s_add_u32 s83, s36, 0xe000\n"
"v_mfma_f32_16x16x32_bf16 a[36:39], v[224:227], v[104:107], a[36:39]\n"
"buffer_load_dwordx4 v252, s[56:59], s92 offen offset:1024 lds\n"
"v_mfma_f32_16x16x32_bf16 a[36:39], v[220:223], v[72:75], a[36:39]\n"
"s_add_u32 s92, s27, 0x1880\n"
"v_mfma_f32_16x16x32_bf16 a[32:35], v[220:223], v[64:67], a[32:35]\n"
"ds_read_b64_tr_b16 v[220:221], v254 offset:32768\n"
"ds_read_b64_tr_b16 v[222:223], v254 offset:34816\n"
"v_mfma_f32_16x16x32_bf16 a[32:35], v[224:227], v[96:99], a[32:35]\n"
"s_waitcnt lgkmcnt(8)\n"
"buffer_load_dwordx4 v[80:83], v248, s[60:63], s84 offen sc1\n"
"v_mfma_f32_16x16x32_bf16 a[32:35], v[228:231], v[4:7], a[32:35]\n"
"s_waitcnt lgkmcnt(6)\n"
"ds_read_b64_tr_b16 v[224:225], v254 offset:36864\n"
"ds_read_b64_tr_b16 v[226:227], v254 offset:38912\n"
"v_mfma_f32_16x16x32_bf16 a[32:35], v[232:235], v[36:39], a[32:35]\n"
"s_add_u32 s84, s36, 0x10000\n"
"v_mfma_f32_16x16x32_bf16 a[36:39], v[232:235], v[44:47], a[36:39]\n"
"v_mfma_f32_16x16x32_bf16 a[36:39], v[228:231], v[12:15], a[36:39]\n"
"v_mfma_f32_16x16x32_bf16 a[96:99], v[228:231], v[176:179], a[96:99]\n"
"ds_read_b64_tr_b16 v[228:229], v254 offset:40960\n"
"ds_read_b64_tr_b16 v[230:231], v254 offset:43008\n"
"v_mfma_f32_16x16x32_bf16 a[96:99], v[232:235], v[180:183], a[96:99]\n"
"s_waitcnt lgkmcnt(8)\n"
"ds_read_b64_tr_b16 v[232:233], v254 offset:45056\n"
"ds_read_b64_tr_b16 v[234:235], v254 offset:47104\n"
"v_mfma_f32_16x16x32_bf16 a[96:99], v[216:219], v[184:187], a[96:99]\n"
"s_waitcnt lgkmcnt(8)\n"
"buffer_load_dwordx4 v252, s[56:59], s93 offen offset:2048 lds\n"
"v_mfma_f32_16x16x32_bf16 a[96:99], v[212:215], v[188:191], a[96:99]\n"
"s_add_u32 s93, s27, 0x3080\n"
"v_mfma_f32_16x16x32_bf16 a[36:39], v[212:215], v[108:111], a[36:39]\n"
"v_mfma_f32_16x16x32_bf16 a[36:39], v[216:219], v[76:79], a[36:39]\n"
"buffer_load_dwordx4 v[88:91], v248, s[60:63], s67 offen sc1\n"
"v_mfma_f32_16x16x32_bf16 a[32:35], v[216:219], v[68:71], a[32:35]\n"
"s_add_u32 s67, s36, 0x12000\n"
"ds_read_b64_tr_b16 v[216:217], v254 offset:49152\n"
"ds_read_b64_tr_b16 v[218:219], v254 offset:51200\n"
"v_mfma_f32_16x16x32_bf16 a[32:35], v[212:215], v[100:103], a[32:35]\n"
"s_waitcnt lgkmcnt(8)\n"
"ds_read_b64_tr_b16 v[212:213], v254 offset:53248\n"
"ds_read_b64_tr_b16 v[214:215], v254 offset:55296\n"
"v_mfma_f32_16x16x32_bf16 a[132:135], v[220:223], v[0:3], 0\n"
"s_waitcnt lgkmcnt(8)\n"
"buffer_load_dwordx4 v252, s[56:59], s94 offen offset:3072 lds\n"
"v_mfma_f32_16x16x32_bf16 a[132:135], v[224:227], v[32:35], a[132:135]\n"
"s_add_u32 s94, s27, 0x4880\n"
"v_mfma_f32_16x16x32_bf16 a[144:147], v[224:227], v[40:43], 0\n"
"ds_read_b64_tr_b16 v[224:225], v254 offset:57344\n"
"ds_read_b64_tr_b16 v[226:227], v254 offset:59392\n"
"v_mfma_f32_16x16x32_bf16 a[144:147], v[220:223], v[8:11], a[144:147]\n"
"s_waitcnt lgkmcnt(8)\n"
"ds_read_b64_tr_b16 v[220:221], v254 offset:61440\n"
"ds_read_b64_tr_b16 v[222:223], v254 offset:63488\n"
"v_mfma_f32_16x16x32_bf16 a[144:147], v[228:231], v[72:75], a[144:147]\n"
"s_waitcnt lgkmcnt(8)\n"
"buffer_load_dwordx4 v[112:115], v248, s[60:63], s68 offen sc1\n"
"v_mfma_f32_16x16x32_bf16 a[144:147], v[232:235], v[104:107], a[144:147]\n"
"s_add_u32 s68, s36, 0x14000\n"
"v_add_u32 v254, 0x12000, v254\n"
"v_mfma_f32_16x16x32_bf16 a[132:135], v[232:235], v[96:99], a[132:135]\n"
"ds_read_b64_tr_b16 v[232:233], v249 offset:32768\n"
"ds_read_b64_tr_b16 v[234:235], v249 offset:34816\n"
"v_mfma_f32_16x16x32_bf16 a[132:135], v[228:231], v[64:67], a[132:135]\n"
"s_waitcnt lgkmcnt(8)\n"
"ds_read_b64_tr_b16 v[228:229], v249 offset:36864\n"
"ds_read_b64_tr_b16 v[230:231], v249 offset:38912\n"
"v_mfma_f32_16x16x32_bf16 a[132:135], v[216:219], v[4:7], a[132:135]\n"
"s_waitcnt lgkmcnt(8)\n"
"v_mfma_f32_16x16x32_bf16 a[132:135], v[212:215], v[36:39], a[132:135]\n"
"v_mfma_f32_16x16x32_bf16 a[144:147], v[212:215], v[44:47], a[144:147]\n"
"ds_read_b64_tr_b16 v[212:213], v249 offset:40960\n"
"ds_read_b64_tr_b16 v[214:215], v249 offset:43008\n"
"v_mfma_f32_16x16x32_bf16 a[144:147], v[216:219], v[12:15], a[144:147]\n"
"s_waitcnt lgkmcnt(8)\n"
"ds_read_b64_tr_b16 v[216:217], v249 offset:45056\n"
"ds_read_b64_tr_b16 v[218:219], v249 offset:47104\n"
"v_mfma_f32_16x16x32_bf16 a[144:147], v[224:227], v[76:79], a[144:147]\n"
"s_waitcnt lgkmcnt(8)\n"
"buffer_load_dwordx4 v[120:123], v248, s[60:63], s83 offen sc1\n"
"v_mfma_f32_16x16x32_bf16 a[144:147], v[220:223], v[108:111], a[144:147]\n"
"s_add_u32 s83, s36, 0x16000\n"
"v_mfma_f32_16x16x32_bf16 a[132:135], v[220:223], v[100:103], a[132:135]\n"
"ds_read_b64_tr_b16 v[220:221], v249 offset:49152\n"
"ds_read_b64_tr_b16 v[222:223], v249 offset:51200\n"
"v_mfma_f32_16x16x32_bf16 a[132:135], v[224:227], v[68:71], a[132:135]\n"
"s_waitcnt lgkmcnt(8)\n"
"ds_read_b64_tr_b16 v[224:225], v249 offset:53248\n"
"ds_read_b64_tr_b16 v[226:227], v249 offset:55296\n"
"v_mfma_f32_16x16x32_bf16 a[148:151], v[232:235], v[0:3], 0\n"
"s_waitcnt lgkmcnt(8)\n"
"v_mfma_f32_16x16x32_bf16 a[148:151], v[228:231], v[32:35], a[148:151]\n"
"s_add_u32 m0, s90, 0x5400\n"
"v_mfma_f32_16x16x32_bf16 a[160:163], v[228:231], v[40:43], 0\n"
"ds_read_b64_tr_b16 v[228:229], v249 offset:57344\n"
"ds_read_b64_tr_b16 v[230:231], v249 offset:59392\n"
"v_mfma_f32_16x16x32_bf16 a[160:163], v[232:235], v[8:11], a[160:163]\n"
"s_waitcnt lgkmcnt(8)\n"
"ds_read_b64_tr_b16 v[232:233], v249 offset:61440\n"
"ds_read_b64_tr_b16 v[234:235], v249 offset:63488\n"
"v_mfma_f32_16x16x32_bf16 a[160:163], v[212:215], v[72:75], a[160:163]\n"
"s_waitcnt lgkmcnt(8)\n"
"buffer_load_dwordx4 v[20:23], v248, s[60:63], s84 offen sc1\n"
"v_mfma_f32_16x16x32_bf16 a[160:163], v[216:219], v[104:107], a[160:163]\n"
"s_add_u32 s84, s36, 0x18000\n"
"v_add_u32 v249, 0x12000, v249\n"
"v_mfma_f32_16x16x32_bf16 a[148:151], v[216:219], v[96:99], a[148:151]\n"
"ds_read_b64_tr_b16 v[216:217], v246 offset:32768\n"
"ds_read_b64_tr_b16 v[218:219], v246 offset:34816\n"
"v_mfma_f32_16x16x32_bf16 a[148:151], v[212:215], v[64:67], a[148:151]\n"
"s_waitcnt lgkmcnt(8)\n"
"ds_read_b64_tr_b16 v[212:213], v246 offset:36864\n"
"ds_read_b64_tr_b16 v[214:215], v246 offset:38912\n"
"v_mfma_f32_16x16x32_bf16 a[148:151], v[220:223], v[4:7], a[148:151]\n"
"s_waitcnt lgkmcnt(8)\n"
"v_mfma_f32_16x16x32_bf16 a[148:151], v[224:227], v[36:39], a[148:151]\n"
"s_waitcnt vmcnt(18)\n"
"s_barrier\n"
"v_mfma_f32_16x16x32_bf16 a[160:163], v[224:227], v[44:47], a[160:163]\n"
"ds_read_b64_tr_b16 v[224:225], v246 offset:40960\n"
"ds_read_b64_tr_b16 v[226:227], v246 offset:43008\n"
"v_mfma_f32_16x16x32_bf16 a[160:163], v[220:223], v[12:15], a[160:163]\n"
"s_waitcnt lgkmcnt(8)\n"
"ds_read_b64_tr_b16 v[220:221], v246 offset:45056\n"
"ds_read_b64_tr_b16 v[222:223], v246 offset:47104\n"
"v_mfma_f32_16x16x32_bf16 a[160:163], v[228:231], v[76:79], a[160:163]\n"
"s_waitcnt lgkmcnt(8)\n"
"buffer_load_dwordx4 v252, s[56:59], s91 offen lds\n"
"v_mfma_f32_16x16x32_bf16 a[160:163], v[232:235], v[108:111], a[160:163]\n"
"s_add_u32 s91, s27, 0x7080\n"
"v_mfma_f32_16x16x32_bf16 a[148:151], v[232:235], v[100:103], a[148:151]\n"
"ds_read_b64_tr_b16 v[232:233], v246 offset:49152\n"
"ds_read_b64_tr_b16 v[234:235], v246 offset:51200\n"
"v_mfma_f32_16x16x32_bf16 a[148:151], v[228:231], v[68:71], a[148:151]\n"
"s_waitcnt lgkmcnt(8)\n"
"ds_read_b64_tr_b16 v[228:229], v246 offset:53248\n"
"ds_read_b64_tr_b16 v[230:231], v246 offset:55296\n"
"v_mfma_f32_16x16x32_bf16 a[116:119], v[216:219], v[0:3], 0\n"
"s_waitcnt lgkmcnt(8)\n"
"buffer_load_dwordx4 v[28:31], v248, s[60:63], s67 offen sc1\n"
"v_mfma_f32_16x16x32_bf16 a[116:119], v[212:215], v[32:35], a[116:119]\n"
"s_add_u32 s67, s36, 0x1a000\n"
"v_mfma_f32_16x16x32_bf16 a[128:131], v[212:215], v[40:43], 0\n"
"ds_read_b64_tr_b16 v[212:213], v246 offset:57344\n"
"ds_read_b64_tr_b16 v[214:215], v246 offset:59392\n"
"v_mfma_f32_16x16x32_bf16 a[128:131], v[216:219], v[8:11], a[128:131]\n"
"s_waitcnt lgkmcnt(8)\n"
"ds_read_b64_tr_b16 v[216:217], v246 offset:61440\n"
"ds_read_b64_tr_b16 v[218:219], v246 offset:63488\n"
"v_mfma_f32_16x16x32_bf16 a[128:131], v[224:227], v[72:75], a[128:131]\n"
"s_waitcnt lgkmcnt(8)\n"
"buffer_load_dwordx4 v252, s[56:59], s92 offen offset:1024 lds\n"
"v_mfma_f32_16x16x32_bf16 a[128:131], v[220:223], v[104:107], a[128:131]\n"
"s_add_u32 s92, s27, 0x8880\n"
"v_add_u32 v246, 0x12000, v246\n"
"v_mfma_f32_16x16x32_bf16 a[116:119], v[220:223], v[96:99], a[116:119]\n"
"ds_read_b64_tr_b16 v[220:221], v253 offset:32768\n"
"ds_read_b64_tr_b16 v[222:223], v253 offset:34816\n"
"v_mfma_f32_16x16x32_bf16 a[116:119], v[224:227], v[64:67], a[116:119]\n"
"s_waitcnt lgkmcnt(8)\n"
"ds_read_b64_tr_b16 v[224:225], v253 offset:36864\n"
"ds_read_b64_tr_b16 v[226:227], v253 offset:38912\n"
"v_mfma_f32_16x16x32_bf16 a[116:119], v[232:235], v[4:7], a[116:119]\n"
"s_waitcnt lgkmcnt(8)\n"
"buffer_load_dwordx4 v[52:55], v248, s[60:63], s68 offen sc1\n"
"v_mfma_f32_16x16x32_bf16 a[116:119], v[228:231], v[36:39], a[116:119]\n"
"s_add_u32 s68, s36, 0x1c000\n"
"v_mfma_f32_16x16x32_bf16 a[128:131], v[228:231], v[44:47], a[128:131]\n"
"ds_read_b64_tr_b16 v[228:229], v253 offset:40960\n"
"ds_read_b64_tr_b16 v[230:231], v253 offset:43008\n"
"v_mfma_f32_16x16x32_bf16 a[128:131], v[232:235], v[12:15], a[128:131]\n"
"s_waitcnt lgkmcnt(8)\n"
"ds_read_b64_tr_b16 v[232:233], v253 offset:45056\n"
"ds_read_b64_tr_b16 v[234:235], v253 offset:47104\n"
"v_mfma_f32_16x16x32_bf16 a[128:131], v[212:215], v[76:79], a[128:131]\n"
"s_waitcnt lgkmcnt(8)\n"
"buffer_load_dwordx4 v252, s[56:59], s93 offen offset:2048 lds\n"
"v_mfma_f32_16x16x32_bf16 a[128:131], v[216:219], v[108:111], a[128:131]\n"
"s_add_u32 s93, s27, 0xa080\n"
"v_mfma_f32_16x16x32_bf16 a[116:119], v[216:219], v[100:103], a[116:119]\n"
"ds_read_b64_tr_b16 v[216:217], v253 offset:49152\n"
"ds_read_b64_tr_b16 v[218:219], v253 offset:51200\n"
"v_mfma_f32_16x16x32_bf16 a[116:119], v[212:215], v[68:71], a[116:119]\n"
"s_waitcnt lgkmcnt(8)\n"
"buffer_load_dwordx4 v[60:63], v248, s[60:63], s83 offen sc1\n"
"v_mfma_f32_16x16x32_bf16 a[100:103], v[220:223], v[0:3], 0\n"
"s_waitcnt lgkmcnt(6)\n"
"ds_read_b64_tr_b16 v[212:213], v253 offset:53248\n"
"ds_read_b64_tr_b16 v[214:215], v253 offset:55296\n"
"v_mfma_f32_16x16x32_bf16 a[100:103], v[224:227], v[32:35], a[100:103]\n"
"s_add_u32 s83, s36, 0x1e000\n"
"v_mfma_f32_16x16x32_bf16 a[112:115], v[224:227], v[40:43], 0\n"
"buffer_load_dwordx4 v252, s[56:59], s94 offen offset:3072 lds\n"
"v_mfma_f32_16x16x32_bf16 a[112:115], v[220:223], v[8:11], a[112:115]\n"
"s_add_u32 m0, s90, 0x6400\n"
"v_mfma_f32_16x16x32_bf16 a[164:167], v[220:223], v[160:163], 0\n"
"s_add_u32 s94, s27, 0xb880\n"
"ds_read_b64_tr_b16 v[220:221], v253 offset:57344\n"
"ds_read_b64_tr_b16 v[222:223], v253 offset:59392\n"
"v_mfma_f32_16x16x32_bf16 a[164:167], v[224:227], v[164:167], a[164:167]\n"
"s_waitcnt lgkmcnt(8)\n"
"buffer_load_dwordx4 v[84:87], v248, s[60:63], s84 offen sc1\n"
"v_mfma_f32_16x16x32_bf16 a[164:167], v[228:231], v[168:171], a[164:167]\n"
"s_waitcnt lgkmcnt(6)\n"
"ds_read_b64_tr_b16 v[224:225], v253 offset:61440\n"
"ds_read_b64_tr_b16 v[226:227], v253 offset:63488\n"
"v_mfma_f32_16x16x32_bf16 a[164:167], v[232:235], v[172:175], a[164:167]\n"
"s_add_u32 s84, s37, 0x20000\n"
"v_add_u32 v253, 0x12000, v253\n"
"v_mfma_f32_16x16x32_bf16 a[112:115], v[232:235], v[104:107], a[112:115]\n"
"buffer_load_dwordx4 v252, s[56:59], s91 offen lds\n"
"v_mfma_f32_16x16x32_bf16 a[112:115], v[228:231], v[72:75], a[112:115]\n"
"s_add_u32 s91, s27, 0x100\n"
"v_mfma_f32_16x16x32_bf16 a[100:103], v[228:231], v[64:67], a[100:103]\n"
"ds_read_b64_tr_b16 v[228:229], v254 offset:0\n"
"ds_read_b64_tr_b16 v[230:231], v254 offset:2048\n"
"v_mfma_f32_16x16x32_bf16 a[100:103], v[232:235], v[96:99], a[100:103]\n"
"s_waitcnt lgkmcnt(8)\n"
"buffer_load_dwordx4 v[92:95], v248, s[60:63], s67 offen sc1\n"
"v_mfma_f32_16x16x32_bf16 a[100:103], v[216:219], v[4:7], a[100:103]\n"
"s_waitcnt lgkmcnt(6)\n"
"ds_read_b64_tr_b16 v[232:233], v254 offset:4096\n"
"ds_read_b64_tr_b16 v[234:235], v254 offset:6144\n"
"v_mfma_f32_16x16x32_bf16 a[100:103], v[212:215], v[36:39], a[100:103]\n"
"s_add_u32 s67, s37, 0x22000\n"
"v_mfma_f32_16x16x32_bf16 a[112:115], v[212:215], v[44:47], a[112:115]\n"
"buffer_load_dwordx4 v252, s[56:59], s92 offen offset:1024 lds\n"
"v_mfma_f32_16x16x32_bf16 a[112:115], v[216:219], v[12:15], a[112:115]\n"
"s_add_u32 s92, s27, 0x1900\n"
"v_mfma_f32_16x16x32_bf16 a[164:167], v[216:219], v[176:179], a[164:167]\n"
"ds_read_b64_tr_b16 v[216:217], v254 offset:8192\n"
"ds_read_b64_tr_b16 v[218:219], v254 offset:10240\n"
"v_mfma_f32_16x16x32_bf16 a[164:167], v[212:215], v[180:183], a[164:167]\n"
"s_waitcnt lgkmcnt(8)\n"
"ds_read_b64_tr_b16 v[212:213], v254 offset:12288\n"
"ds_read_b64_tr_b16 v[214:215], v254 offset:14336\n"
"v_mfma_f32_16x16x32_bf16 a[164:167], v[220:223], v[184:187], a[164:167]\n"
"s_waitcnt lgkmcnt(8)\n"
"buffer_load_dwordx4 v[116:119], v248, s[60:63], s68 offen sc1\n"
"v_mfma_f32_16x16x32_bf16 a[164:167], v[224:227], v[188:191], a[164:167]\n"
"s_add_u32 s68, s37, 0x20400\n"
"v_mfma_f32_16x16x32_bf16 a[112:115], v[224:227], v[108:111], a[112:115]\n"
"v_mfma_f32_16x16x32_bf16 a[112:115], v[220:223], v[76:79], a[112:115]\n"
"buffer_load_dwordx4 v252, s[56:59], s93 offen offset:2048 lds\n"
"v_mfma_f32_16x16x32_bf16 a[100:103], v[220:223], v[68:71], a[100:103]\n"
"s_add_u32 s93, s27, 0x3100\n"
"ds_read_b64_tr_b16 v[220:221], v254 offset:16384\n"
"ds_read_b64_tr_b16 v[222:223], v254 offset:18432\n"
"v_mfma_f32_16x16x32_bf16 a[100:103], v[224:227], v[100:103], a[100:103]\n"
"s_waitcnt lgkmcnt(8)\n"
"ds_read_b64_tr_b16 v[224:225], v254 offset:20480\n"
"ds_read_b64_tr_b16 v[226:227], v254 offset:22528\n"
"v_mfma_f32_16x16x32_bf16 a[208:211], v[228:231], v[0:3], 0\n"
"s_waitcnt lgkmcnt(8)\n"
"buffer_load_dwordx4 v252, s[56:59], s94 offen offset:3072 lds\n"
"v_mfma_f32_16x16x32_bf16 a[208:211], v[232:235], v[32:35], a[208:211]\n"
"s_add_u32 s94, s27, 0x4900\n"
"v_mfma_f32_16x16x32_bf16 a[212:215], v[232:235], v[40:43], 0\n"
"ds_read_b64_tr_b16 v[232:233], v254 offset:24576\n"
"ds_read_b64_tr_b16 v[234:235], v254 offset:26624\n"
"v_mfma_f32_16x16x32_bf16 a[212:215], v[228:231], v[8:11], a[212:215]\n"
"s_waitcnt lgkmcnt(8)\n"
"ds_read_b64_tr_b16 v[228:229], v254 offset:28672\n"
"ds_read_b64_tr_b16 v[230:231], v254 offset:30720\n"
"v_mfma_f32_16x16x32_bf16 a[212:215], v[216:219], v[72:75], a[212:215]\n"
"s_waitcnt lgkmcnt(8)\n"
"buffer_load_dwordx4 v[124:127], v248, s[60:63], s83 offen sc1\n"
"v_mfma_f32_16x16x32_bf16 a[212:215], v[212:215], v[104:107], a[212:215]\n"
"s_add_u32 s83, s37, 0x22400\n"
"v_mfma_f32_16x16x32_bf16 a[208:211], v[212:215], v[96:99], a[208:211]\n"
"ds_read_b64_tr_b16 v[212:213], v249 offset:0\n"
"ds_read_b64_tr_b16 v[214:215], v249 offset:2048\n"
"v_mfma_f32_16x16x32_bf16 a[208:211], v[216:219], v[64:67], a[208:211]\n"
"s_waitcnt lgkmcnt(8)\n"
"ds_read_b64_tr_b16 v[216:217], v249 offset:4096\n"
"ds_read_b64_tr_b16 v[218:219], v249 offset:6144\n"
"v_mfma_f32_16x16x32_bf16 a[208:211], v[220:223], v[4:7], a[208:211]\n"
"s_waitcnt lgkmcnt(8)\n"
"v_mfma_f32_16x16x32_bf16 a[208:211], v[224:227], v[36:39], a[208:211]\n"
"v_mfma_f32_16x16x32_bf16 a[212:215], v[224:227], v[44:47], a[212:215]\n"
"ds_read_b64_tr_b16 v[224:225], v249 offset:8192\n"
"ds_read_b64_tr_b16 v[226:227], v249 offset:10240\n"
"v_mfma_f32_16x16x32_bf16 a[212:215], v[220:223], v[12:15], a[212:215]\n"
"s_waitcnt lgkmcnt(8)\n"
"ds_read_b64_tr_b16 v[220:221], v249 offset:12288\n"
"ds_read_b64_tr_b16 v[222:223], v249 offset:14336\n"
"v_mfma_f32_16x16x32_bf16 a[212:215], v[232:235], v[76:79], a[212:215]\n"
"s_waitcnt lgkmcnt(8)\n"
"buffer_load_dwordx4 v[128:131], v248, s[60:63], s84 offen sc1\n"
"v_mfma_f32_16x16x32_bf16 a[212:215], v[228:231], v[108:111], a[212:215]\n"
"s_add_u32 s84, s37, 0x20800\n"
"v_mfma_f32_16x16x32_bf16 a[208:211], v[228:231], v[100:103], a[208:211]\n"
"ds_read_b64_tr_b16 v[228:229], v249 offset:16384\n"
"ds_read_b64_tr_b16 v[230:231], v249 offset:18432\n"
"v_mfma_f32_16x16x32_bf16 a[208:211], v[232:235], v[68:71], a[208:211]\n"
"s_waitcnt lgkmcnt(8)\n"
"ds_read_b64_tr_b16 v[232:233], v249 offset:20480\n"
"ds_read_b64_tr_b16 v[234:235], v249 offset:22528\n"
"v_mfma_f32_16x16x32_bf16 a[224:227], v[212:215], v[0:3], 0\n"
"s_waitcnt lgkmcnt(8)\n"
"v_mfma_f32_16x16x32_bf16 a[224:227], v[216:219], v[32:35], a[224:227]\n"
"s_add_u32 m0, s90, 0xd400\n"
"v_mfma_f32_16x16x32_bf16 a[228:231], v[216:219], v[40:43], 0\n"
"ds_read_b64_tr_b16 v[216:217], v249 offset:24576\n"
"ds_read_b64_tr_b16 v[218:219], v249 offset:26624\n"
"v_mfma_f32_16x16x32_bf16 a[228:231], v[212:215], v[8:11], a[228:231]\n"
"s_waitcnt lgkmcnt(8)\n"
"ds_read_b64_tr_b16 v[212:213], v249 offset:28672\n"
"ds_read_b64_tr_b16 v[214:215], v249 offset:30720\n"
"v_mfma_f32_16x16x32_bf16 a[228:231], v[224:227], v[72:75], a[228:231]\n"
"s_waitcnt lgkmcnt(8)\n"
"buffer_load_dwordx4 v[132:135], v248, s[60:63], s67 offen sc1\n"
"v_mfma_f32_16x16x32_bf16 a[228:231], v[220:223], v[104:107], a[228:231]\n"
"s_add_u32 s67, s37, 0x22800\n"
"v_mfma_f32_16x16x32_bf16 a[224:227], v[220:223], v[96:99], a[224:227]\n"
"ds_read_b64_tr_b16 v[220:221], v246 offset:0\n"
"ds_read_b64_tr_b16 v[222:223], v246 offset:2048\n"
"v_mfma_f32_16x16x32_bf16 a[224:227], v[224:227], v[64:67], a[224:227]\n"
"s_waitcnt lgkmcnt(8)\n"
"ds_read_b64_tr_b16 v[224:225], v246 offset:4096\n"
"ds_read_b64_tr_b16 v[226:227], v246 offset:6144\n"
"v_mfma_f32_16x16x32_bf16 a[224:227], v[228:231], v[4:7], a[224:227]\n"
"s_waitcnt lgkmcnt(8)\n"
"v_mfma_f32_16x16x32_bf16 a[224:227], v[232:235], v[36:39], a[224:227]\n"
"s_waitcnt vmcnt(20)\n"
"s_barrier\n"
"v_mfma_f32_16x16x32_bf16 a[228:231], v[232:235], v[44:47], a[228:231]\n"
"ds_read_b64_tr_b16 v[232:233], v246 offset:8192\n"
"ds_read_b64_tr_b16 v[234:235], v246 offset:10240\n"
"v_mfma_f32_16x16x32_bf16 a[228:231], v[228:231], v[12:15], a[228:231]\n"
"s_waitcnt lgkmcnt(8)\n"
"ds_read_b64_tr_b16 v[228:229], v246 offset:12288\n"
"ds_read_b64_tr_b16 v[230:231], v246 offset:14336\n"
"v_mfma_f32_16x16x32_bf16 a[228:231], v[216:219], v[76:79], a[228:231]\n"
"s_waitcnt lgkmcnt(8)\n"
"buffer_load_dwordx4 v252, s[56:59], s91 offen lds\n"
"v_mfma_f32_16x16x32_bf16 a[228:231], v[212:215], v[108:111], a[228:231]\n"
"Lpada_9:\n"
"s_cbranch_execz Lpadxa\n"
"s_add_u32 s91, s27, 0x7100\n"
"v_mfma_f32_16x16x32_bf16 a[224:227], v[212:215], v[100:103], a[224:227]\n"
"ds_read_b64_tr_b16 v[212:213], v246 offset:16384\n"
"ds_read_b64_tr_b16 v[214:215], v246 offset:18432\n"
"v_mfma_f32_16x16x32_bf16 a[224:227], v[216:219], v[68:71], a[224:227]\n"
"s_waitcnt lgkmcnt(8)\n"
"ds_read_b64_tr_b16 v[216:217], v246 offset:20480\n"
"ds_read_b64_tr_b16 v[218:219], v246 offset:22528\n"
"v_mfma_f32_16x16x32_bf16 a[192:195], v[220:223], v[0:3], 0\n"
"s_waitcnt lgkmcnt(8)\n"
"buffer_load_dwordx4 v[136:139], v248, s[60:63], s68 offen sc1\n"
"v_mfma_f32_16x16x32_bf16 a[192:195], v[224:227], v[32:35], a[192:195]\n"
"s_add_u32 s68, s37, 0x20c00\n"
"v_mfma_f32_16x16x32_bf16 a[196:199], v[224:227], v[40:43], 0\n"
"ds_read_b64_tr_b16 v[224:225], v246 offset:24576\n"
"ds_read_b64_tr_b16 v[226:227], v246 offset:26624\n"
"v_mfma_f32_16x16x32_bf16 a[196:199], v[220:223], v[8:11], a[196:199]\n"
"s_waitcnt lgkmcnt(8)\n"
"ds_read_b64_tr_b16 v[220:221], v246 offset:28672\n"
"ds_read_b64_tr_b16 v[222:223], v246 offset:30720\n"
"v_mfma_f32_16x16x32_bf16 a[196:199], v[232:235], v[72:75], a[196:199]\n"
"s_waitcnt lgkmcnt(8)\n"
"buffer_load_dwordx4 v252, s[56:59], s92 offen offset:1024 lds\n"
"v_mfma_f32_16x16x32_bf16 a[196:199], v[228:231], v[104:107], a[196:199]\n"
"s_add_u32 s92, s27, 0x8900\n"
"v_mfma_f32_16x16x32_bf16 a[192:195], v[228:231], v[96:99], a[192:195]\n"
"ds_read_b64_tr_b16 v[228:229], v253 offset:0\n"
"ds_read_b64_tr_b16 v[230:231], v253 offset:2048\n"
"v_mfma_f32_16x16x32_bf16 a[192:195], v[232:235], v[64:67], a[192:195]\n"
"s_waitcnt lgkmcnt(8)\n"
"ds_read_b64_tr_b16 v[232:233], v253 offset:4096\n"
"ds_read_b64_tr_b16 v[234:235], v253 offset:6144\n"
"v_mfma_f32_16x16x32_bf16 a[192:195], v[212:215], v[4:7], a[192:195]\n"
"s_waitcnt lgkmcnt(8)\n"
"buffer_store_dword v236, v255, s[60:63], s39 offen sc1\n"
"v_mfma_f32_16x16x32_bf16 a[192:195], v[216:219], v[36:39], a[192:195]\n"
"v_mfma_f32_16x16x32_bf16 a[196:199], v[216:219], v[44:47], a[196:199]\n"
"ds_read_b64_tr_b16 v[216:217], v253 offset:8192\n"
"ds_read_b64_tr_b16 v[218:219], v253 offset:10240\n"
"v_mfma_f32_16x16x32_bf16 a[196:199], v[212:215], v[12:15], a[196:199]\n"
"s_waitcnt lgkmcnt(8)\n"
"ds_read_b64_tr_b16 v[212:213], v253 offset:12288\n"
"ds_read_b64_tr_b16 v[214:215], v253 offset:14336\n"
"v_mfma_f32_16x16x32_bf16 a[196:199], v[224:227], v[76:79], a[196:199]\n"
"s_waitcnt lgkmcnt(8)\n"
"buffer_load_dwordx4 v[140:143], v248, s[60:63], s83 offen sc1\n"
"v_mfma_f32_16x16x32_bf16 a[196:199], v[220:223], v[108:111], a[196:199]\n"
"s_add_u32 s83, s37, 0x22c00\n"
"v_mfma_f32_16x16x32_bf16 a[192:195], v[220:223], v[100:103], a[192:195]\n"
"ds_read_b64_tr_b16 v[220:221], v253 offset:16384\n"
"ds_read_b64_tr_b16 v[222:223], v253 offset:18432\n"
"v_mfma_f32_16x16x32_bf16 a[192:195], v[224:227], v[68:71], a[192:195]\n"
"s_waitcnt lgkmcnt(8)\n"
"buffer_load_dwordx4 v252, s[56:59], s93 offen offset:2048 lds\n"
"v_mfma_f32_16x16x32_bf16 a[176:179], v[228:231], v[0:3], 0\n"
"s_waitcnt lgkmcnt(6)\n"
"ds_read_b64_tr_b16 v[224:225], v253 offset:20480\n"
"ds_read_b64_tr_b16 v[226:227], v253 offset:22528\n"
"v_mfma_f32_16x16x32_bf16 a[176:179], v[232:235], v[32:35], a[176:179]\n"
"s_add_u32 s93, s27, 0xa100\n"
"v_mfma_f32_16x16x32_bf16 a[180:183], v[232:235], v[40:43], 0\n"
"buffer_load_dwordx4 v[144:147], v248, s[60:63], s84 offen sc1\n"
"v_mfma_f32_16x16x32_bf16 a[180:183], v[228:231], v[8:11], a[180:183]\n"
"s_add_u32 s84, s87, 0x200000\n"
"v_mfma_f32_16x16x32_bf16 a[240:243], v[228:231], v[160:163], 0\n"
"ds_read_b64_tr_b16 v[228:229], v253 offset:24576\n"
"ds_read_b64_tr_b16 v[230:231], v253 offset:26624\n"
"v_mfma_f32_16x16x32_bf16 a[240:243], v[232:235], v[164:167], a[240:243]\n"
"s_waitcnt lgkmcnt(8)\n"
"buffer_load_dwordx4 v252, s[56:59], s94 offen offset:3072 lds\n"
"v_mfma_f32_16x16x32_bf16 a[240:243], v[216:219], v[168:171], a[240:243]\n"
"s_waitcnt lgkmcnt(6)\n"
"s_add_u32 m0, s90, 0xe400\n"
"ds_read_b64_tr_b16 v[232:233], v253 offset:28672\n"
"v_mfma_f32_16x16x32_bf16 a[240:243], v[212:215], v[172:175], a[240:243]\n"
"s_add_u32 s94, s27, 0xb900\n"
"ds_read_b64_tr_b16 v[234:235], v253 offset:30720\n"
"v_mfma_f32_16x16x32_bf16 a[180:183], v[212:215], v[104:107], a[180:183]\n"
"buffer_load_dwordx4 v[148:151], v248, s[60:63], s67 offen sc1\n"
"v_mfma_f32_16x16x32_bf16 a[180:183], v[216:219], v[72:75], a[180:183]\n"
"s_add_u32 s67, s87, 0x300000\n"
"v_mfma_f32_16x16x32_bf16 a[176:179], v[216:219], v[64:67], a[176:179]\n"
"ds_read_b64_tr_b16 v[216:217], v254 offset:32768\n"
"ds_read_b64_tr_b16 v[218:219], v254 offset:34816\n"
"v_mfma_f32_16x16x32_bf16 a[176:179], v[212:215], v[96:99], a[176:179]\n"
"s_waitcnt lgkmcnt(8)\n"
"buffer_load_dwordx4 v252, s[56:59], s91 offen lds\n"
"v_mfma_f32_16x16x32_bf16 a[176:179], v[220:223], v[4:7], a[176:179]\n"
"s_waitcnt lgkmcnt(6)\n"
"ds_read_b64_tr_b16 v[212:213], v254 offset:36864\n"
"ds_read_b64_tr_b16 v[214:215], v254 offset:38912\n"
"v_mfma_f32_16x16x32_bf16 a[176:179], v[224:227], v[36:39], a[176:179]\n"
"s_add_u32 s91, s27, 0x180\n"
"v_mfma_f32_16x16x32_bf16 a[180:183], v[224:227], v[44:47], a[180:183]\n"
"buffer_load_dwordx4 v[152:155], v248, s[60:63], s68 offen sc1\n"
"v_mfma_f32_16x16x32_bf16 a[180:183], v[220:223], v[12:15], a[180:183]\n"
"s_add_u32 s68, s87, 0\n"
"v_mfma_f32_16x16x32_bf16 a[240:243], v[220:223], v[176:179], a[240:243]\n"
"ds_read_b64_tr_b16 v[220:221], v254 offset:40960\n"
"ds_read_b64_tr_b16 v[222:223], v254 offset:43008\n"
"v_mfma_f32_16x16x32_bf16 a[240:243], v[224:227], v[180:183], a[240:243]\n"
"s_waitcnt lgkmcnt(8)\n"
"ds_read_b64_tr_b16 v[224:225], v254 offset:45056\n"
"ds_read_b64_tr_b16 v[226:227], v254 offset:47104\n"
"v_mfma_f32_16x16x32_bf16 a[240:243], v[228:231], v[184:187], a[240:243]\n"
"s_waitcnt lgkmcnt(8)\n"
"buffer_load_dwordx4 v252, s[56:59], s92 offen offset:1024 lds\n"
"v_mfma_f32_16x16x32_bf16 a[240:243], v[232:235], v[188:191], a[240:243]\n"
"s_add_u32 s92, s27, 0x1980\n"
"v_mfma_f32_16x16x32_bf16 a[180:183], v[232:235], v[108:111], a[180:183]\n"
"v_mfma_f32_16x16x32_bf16 a[180:183], v[228:231], v[76:79], a[180:183]\n"
"buffer_load_dwordx4 v[156:159], v248, s[60:63], s83 offen sc1\n"
"v_mfma_f32_16x16x32_bf16 a[176:179], v[228:231], v[68:71], a[176:179]\n"
"s_add_u32 s83, s87, 0x100000\n"
"ds_read_b64_tr_b16 v[228:229], v254 offset:49152\n"
"ds_read_b64_tr_b16 v[230:231], v254 offset:51200\n"
"v_mfma_f32_16x16x32_bf16 a[176:179], v[232:235], v[100:103], a[176:179]\n"
"s_waitcnt lgkmcnt(8)\n"
"ds_read_b64_tr_b16 v[232:233], v254 offset:53248\n"
"ds_read_b64_tr_b16 v[234:235], v254 offset:55296\n"
"v_mfma_f32_16x16x32_bf16 a[40:43], v[216:219], v[16:19], a[40:43]\n"
"s_waitcnt lgkmcnt(8)\n"
"buffer_load_dwordx4 v252, s[56:59], s93 offen offset:2048 lds\n"
"v_mfma_f32_16x16x32_bf16 a[40:43], v[212:215], v[48:51], a[40:43]\n"
"s_add_u32 s93, s27, 0x3180\n"
"v_mfma_f32_16x16x32_bf16 a[44:47], v[212:215], v[56:59], a[44:47]\n"
"ds_read_b64_tr_b16 v[212:213], v254 offset:57344\n"
"ds_read_b64_tr_b16 v[214:215], v254 offset:59392\n"
"v_mfma_f32_16x16x32_bf16 a[44:47], v[216:219], v[24:27], a[44:47]\n"
"s_waitcnt lgkmcnt(8)\n"
"ds_read_b64_tr_b16 v[216:217], v254 offset:61440\n"
"ds_read_b64_tr_b16 v[218:219], v254 offset:63488\n"
"v_mfma_f32_16x16x32_bf16 a[44:47], v[220:223], v[88:91], a[44:47]\n"
"s_waitcnt vmcnt(32) lgkmcnt(8)\n"
"buffer_load_dwordx4 v252, s[56:59], s94 offen offset:3072 lds\n"
"v_mfma_f32_16x16x32_bf16 a[44:47], v[224:227], v[120:123], a[44:47]\n"
"s_add_u32 s94, s27, 0x4980\n"
"v_subrev_u32 v254, 0x12000, v254\n"
"v_mfma_f32_16x16x32_bf16 a[40:43], v[224:227], v[112:115], a[40:43]\n"
"ds_read_b64_tr_b16 v[224:225], v249 offset:32768\n"
"ds_read_b64_tr_b16 v[226:227], v249 offset:34816\n"
"v_mfma_f32_16x16x32_bf16 a[40:43], v[220:223], v[80:83], a[40:43]\n"
"s_waitcnt vmcnt(32) lgkmcnt(8)\n"
"ds_read_b64_tr_b16 v[220:221], v249 offset:36864\n"
"ds_read_b64_tr_b16 v[222:223], v249 offset:38912\n"
"v_mfma_f32_16x16x32_bf16 a[40:43], v[228:231], v[20:23], a[40:43]\n"
"s_waitcnt vmcnt(28) lgkmcnt(8)\n"
"v_mfma_f32_16x16x32_bf16 a[40:43], v[232:235], v[52:55], a[40:43]\n"
"s_waitcnt vmcnt(26)\n"
"v_mfma_f32_16x16x32_bf16 a[44:47], v[232:235], v[60:63], a[44:47]\n"
"ds_read_b64_tr_b16 v[232:233], v249 offset:40960\n"
"ds_read_b64_tr_b16 v[234:235], v249 offset:43008\n"
"v_mfma_f32_16x16x32_bf16 a[44:47], v[228:231], v[28:31], a[44:47]\n"
"s_waitcnt vmcnt(22) lgkmcnt(8)\n"
"ds_read_b64_tr_b16 v[228:229], v249 offset:45056\n"
"ds_read_b64_tr_b16 v[230:231], v249 offset:47104\n"
"v_mfma_f32_16x16x32_bf16 a[44:47], v[212:215], v[92:95], a[44:47]\n"
"s_waitcnt vmcnt(17) lgkmcnt(8)\n"
"s_barrier\n"
"v_mfma_f32_16x16x32_bf16 a[44:47], v[216:219], v[124:127], a[44:47]\n"
"v_mfma_f32_16x16x32_bf16 a[40:43], v[216:219], v[116:119], a[40:43]\n"
"ds_read_b64_tr_b16 v[216:217], v249 offset:49152\n"
"ds_read_b64_tr_b16 v[218:219], v249 offset:51200\n"
"v_mfma_f32_16x16x32_bf16 a[40:43], v[212:215], v[84:87], a[40:43]\n"
"s_waitcnt lgkmcnt(8)\n"
"ds_read_b64_tr_b16 v[212:213], v249 offset:53248\n"
"ds_read_b64_tr_b16 v[214:215], v249 offset:55296\n"
"v_mfma_f32_16x16x32_bf16 a[56:59], v[224:227], v[16:19], a[56:59]\n"
"s_waitcnt lgkmcnt(8)\n"
"v_accvgpr_read_b32 v196, a44\n"
"v_accvgpr_read_b32 v197, a45\n"
"v_mfma_f32_16x16x32_bf16 a[56:59], v[220:223], v[48:51], a[56:59]\n"
"s_add_u32 m0, s90, 0x17400\n"
"v_accvgpr_read_b32 v192, a40\n"
"v_accvgpr_read_b32 v193, a41\n"
"v_mfma_f32_16x16x32_bf16 a[60:63], v[220:223], v[56:59], a[60:63]\n"
"ds_read_b64_tr_b16 v[220:221], v249 offset:57344\n"
"ds_read_b64_tr_b16 v[222:223], v249 offset:59392\n"
"v_mfma_f32_16x16x32_bf16 a[60:63], v[224:227], v[24:27], a[60:63]\n"
"s_waitcnt lgkmcnt(8)\n"
"ds_read_b64_tr_b16 v[224:225], v249 offset:61440\n"
"ds_read_b64_tr_b16 v[226:227], v249 offset:63488\n"
"v_mfma_f32_16x16x32_bf16 a[60:63], v[232:235], v[88:91], a[60:63]\n"
"s_waitcnt lgkmcnt(8)\n"
"buffer_load_dwordx4 v252, s[56:59], s91 offen lds\n"
"v_mfma_f32_16x16x32_bf16 a[60:63], v[228:231], v[120:123], a[60:63]\n"
"s_add_u32 s91, s27, 0x7180\n"
"v_subrev_u32 v249, 0x12000, v249\n"
"v_accvgpr_read_b32 v194, a42\n"
"v_mfma_f32_16x16x32_bf16 a[56:59], v[228:231], v[112:115], a[56:59]\n"
"ds_read_b64_tr_b16 v[228:229], v246 offset:32768\n"
"ds_read_b64_tr_b16 v[230:231], v246 offset:34816\n"
"v_mfma_f32_16x16x32_bf16 a[56:59], v[232:235], v[80:83], a[56:59]\n"
"s_waitcnt lgkmcnt(8)\n"
"ds_read_b64_tr_b16 v[232:233], v246 offset:36864\n"
"ds_read_b64_tr_b16 v[234:235], v246 offset:38912\n"
"v_mfma_f32_16x16x32_bf16 a[56:59], v[216:219], v[20:23], a[56:59]\n"
"s_waitcnt lgkmcnt(8)\n"
"buffer_load_dwordx4 v252, s[56:59], s92 offen offset:1024 lds\n"
"v_mfma_f32_16x16x32_bf16 a[56:59], v[212:215], v[52:55], a[56:59]\n"
"s_add_u32 s92, s27, 0x8980\n"
"v_accvgpr_read_b32 v195, a43\n"
"v_cvt_pk_bf16_f32 v192, v192, v193\n"
"v_mfma_f32_16x16x32_bf16 a[60:63], v[212:215], v[60:63], a[60:63]\n"
"ds_read_b64_tr_b16 v[212:213], v246 offset:40960\n"
"ds_read_b64_tr_b16 v[214:215], v246 offset:43008\n"
"v_mfma_f32_16x16x32_bf16 a[60:63], v[216:219], v[28:31], a[60:63]\n"
"s_waitcnt lgkmcnt(8)\n"
"ds_read_b64_tr_b16 v[216:217], v246 offset:45056\n"
"ds_read_b64_tr_b16 v[218:219], v246 offset:47104\n"
"v_mfma_f32_16x16x32_bf16 a[60:63], v[220:223], v[92:95], a[60:63]\n"
"s_waitcnt lgkmcnt(8)\n"
"v_accvgpr_read_b32 v198, a46\n"
"v_accvgpr_read_b32 v199, a47\n"
"v_mfma_f32_16x16x32_bf16 a[60:63], v[224:227], v[124:127], a[60:63]\n"
"v_cvt_pk_bf16_f32 v196, v196, v197\n"
"v_cvt_pk_bf16_f32 v193, v194, v195\n"
"v_mfma_f32_16x16x32_bf16 a[56:59], v[224:227], v[116:119], a[56:59]\n"
"ds_read_b64_tr_b16 v[224:225], v246 offset:49152\n"
"ds_read_b64_tr_b16 v[226:227], v246 offset:51200\n"
"v_mfma_f32_16x16x32_bf16 a[56:59], v[220:223], v[84:87], a[56:59]\n"
"s_waitcnt lgkmcnt(8)\n"
"ds_read_b64_tr_b16 v[220:221], v246 offset:53248\n"
"ds_read_b64_tr_b16 v[222:223], v246 offset:55296\n"
"v_mfma_f32_16x16x32_bf16 a[24:27], v[228:231], v[16:19], a[24:27]\n"
"s_waitcnt lgkmcnt(8)\n"
"buffer_load_dwordx4 v252, s[56:59], s93 offen offset:2048 lds\n"
"v_mfma_f32_16x16x32_bf16 a[24:27], v[232:235], v[48:51], a[24:27]\n"
"s_add_u32 s93, s27, 0xa180\n"
"v_cvt_pk_bf16_f32 v197, v198, v199\n"
"v_accvgpr_read_b32 v194, a56\n"
"v_mfma_f32_16x16x32_bf16 a[28:31], v[232:235], v[56:59], a[28:31]\n"
"ds_read_b64_tr_b16 v[232:233], v246 offset:57344\n"
"ds_read_b64_tr_b16 v[234:235], v246 offset:59392\n"
"v_mfma_f32_16x16x32_bf16 a[28:31], v[228:231], v[24:27], a[28:31]\n"
"s_waitcnt lgkmcnt(8)\n"
"ds_read_b64_tr_b16 v[228:229], v246 offset:61440\n"
"ds_read_b64_tr_b16 v[230:231], v246 offset:63488\n"
"v_mfma_f32_16x16x32_bf16 a[28:31], v[212:215], v[88:91], a[28:31]\n"
"s_waitcnt lgkmcnt(8)\n"
"v_subrev_u32 v246, 0x12000, v246\n"
"v_accvgpr_read_b32 v195, a57\n"
"v_mfma_f32_16x16x32_bf16 a[28:31], v[216:219], v[120:123], a[28:31]\n"
"v_accvgpr_read_b32 v198, a60\n"
"v_accvgpr_read_b32 v199, a61\n"
"v_mfma_f32_16x16x32_bf16 a[24:27], v[216:219], v[112:115], a[24:27]\n"
"ds_read_b64_tr_b16 v[216:217], v253 offset:32768\n"
"ds_read_b64_tr_b16 v[218:219], v253 offset:34816\n"
"v_mfma_f32_16x16x32_bf16 a[24:27], v[212:215], v[80:83], a[24:27]\n"
"s_waitcnt lgkmcnt(8)\n"
"ds_read_b64_tr_b16 v[212:213], v253 offset:36864\n"
"ds_read_b64_tr_b16 v[214:215], v253 offset:38912\n"
"v_mfma_f32_16x16x32_bf16 a[24:27], v[224:227], v[20:23], a[24:27]\n"
"s_waitcnt lgkmcnt(8)\n"
"buffer_load_dwordx4 v252, s[56:59], s94 offen offset:3072 lds\n"
"v_mfma_f32_16x16x32_bf16 a[24:27], v[220:223], v[52:55], a[24:27]\n"
"s_add_u32 m0, s90, 0x18400\n"
"v_cvt_pk_bf16_f32 v194, v194, v195\n"
"v_cvt_pk_bf16_f32 v198, v198, v199\n"
"v_mfma_f32_16x16x32_bf16 a[28:31], v[220:223], v[60:63], a[28:31]\n"
"s_add_u32 s94, s27, 0xb980\n"
"ds_read_b64_tr_b16 v[220:221], v253 offset:40960\n"
"ds_read_b64_tr_b16 v[222:223], v253 offset:43008\n"
"v_mfma_f32_16x16x32_bf16 a[28:31], v[224:227], v[28:31], a[28:31]\n"
"s_waitcnt lgkmcnt(8)\n"
"ds_read_b64_tr_b16 v[224:225], v253 offset:45056\n"
"ds_read_b64_tr_b16 v[226:227], v253 offset:47104\n"
"v_mfma_f32_16x16x32_bf16 a[28:31], v[232:235], v[92:95], a[28:31]\n"
"s_waitcnt lgkmcnt(8)\n"
"v_accvgpr_read_b32 v208, a59\n"
"v_accvgpr_read_b32 v195, a58\n"
"v_mfma_f32_16x16x32_bf16 a[28:31], v[228:231], v[124:127], a[28:31]\n"
"v_accvgpr_read_b32 v209, a63\n"
"v_accvgpr_read_b32 v199, a62\n"
"v_mfma_f32_16x16x32_bf16 a[24:27], v[228:231], v[116:119], a[24:27]\n"
"ds_read_b64_tr_b16 v[228:229], v253 offset:49152\n"
"ds_read_b64_tr_b16 v[230:231], v253 offset:51200\n"
"v_mfma_f32_16x16x32_bf16 a[24:27], v[232:235], v[84:87], a[24:27]\n"
"s_waitcnt lgkmcnt(8)\n"
"ds_read_b64_tr_b16 v[232:233], v253 offset:53248\n"
"ds_read_b64_tr_b16 v[234:235], v253 offset:55296\n"
"v_mfma_f32_16x16x32_bf16 a[8:11], v[216:219], v[16:19], a[8:11]\n"
"s_waitcnt lgkmcnt(8)\n"
"buffer_load_dwordx4 v252, s[56:59], s91 offen lds\n"
"v_mfma_f32_16x16x32_bf16 a[8:11], v[212:215], v[48:51], a[8:11]\n"
"s_add_u32 s91, s27, 0x200\n"
"v_accvgpr_read_b32 v210, a27\n"
"v_accvgpr_read_b32 v211, a31\n"
"v_mfma_f32_16x16x32_bf16 a[12:15], v[212:215], v[56:59], a[12:15]\n"
"v_cvt_pk_bf16_f32 v195, v195, v208\n"
"v_cvt_pk_bf16_f32 v199, v199, v209\n"
"v_mfma_f32_16x16x32_bf16 a[12:15], v[216:219], v[24:27], a[12:15]\n"
"s_waitcnt vmcnt(21)\n"
"buffer_store_dwordx4 v[192:195], v248, s[60:63], s84 offen sc1\n"
"v_mfma_f32_16x16x32_bf16 a[72:75], v[216:219], v[128:131], a[72:75]\n"
"s_waitcnt vmcnt(21)\n"
"ds_read_b64_tr_b16 v[216:217], v253 offset:57344\n"
"ds_read_b64_tr_b16 v[218:219], v253 offset:59392\n"
"v_mfma_f32_16x16x32_bf16 a[72:75], v[212:215], v[132:135], a[72:75]\n"
"s_waitcnt vmcnt(19) lgkmcnt(8)\n"
"ds_read_b64_tr_b16 v[212:213], v253 offset:61440\n"
"ds_read_b64_tr_b16 v[214:215], v253 offset:63488\n"
"v_mfma_f32_16x16x32_bf16 a[72:75], v[220:223], v[136:139], a[72:75]\n"
"s_waitcnt vmcnt(16) lgkmcnt(8)\n"
"v_subrev_u32 v253, 0x12000, v253\n"
"v_mfma_f32_16x16x32_bf16 a[72:75], v[224:227], v[140:143], a[72:75]\n"
"s_add_u32 s84, s87, 0x600000\n"
"v_mfma_f32_16x16x32_bf16 a[12:15], v[224:227], v[120:123], a[12:15]\n"
"buffer_load_dwordx4 v252, s[56:59], s92 offen offset:1024 lds\n"
"v_mfma_f32_16x16x32_bf16 a[12:15], v[220:223], v[88:91], a[12:15]\n"
"s_add_u32 s92, s27, 0x1a00\n"
"v_mfma_f32_16x16x32_bf16 a[8:11], v[220:223], v[80:83], a[8:11]\n"
"ds_read_b64_tr_b16 v[220:221], v254 offset:0\n"
"ds_read_b64_tr_b16 v[222:223], v254 offset:2048\n"
"v_mfma_f32_16x16x32_bf16 a[8:11], v[224:227], v[112:115], a[8:11]\n"
"s_waitcnt lgkmcnt(8)\n"
"ds_read_b64_tr_b16 v[224:225], v254 offset:4096\n"
"ds_read_b64_tr_b16 v[226:227], v254 offset:6144\n"
"v_mfma_f32_16x16x32_bf16 a[8:11], v[228:231], v[20:23], a[8:11]\n"
"s_waitcnt lgkmcnt(8)\n"
"buffer_store_dwordx4 v[196:199], v248, s[60:63], s67 offen sc1\n"
"v_mfma_f32_16x16x32_bf16 a[8:11], v[232:235], v[52:55], a[8:11]\n"
"s_add_u32 s67, s87, 0x700000\n"
"v_mfma_f32_16x16x32_bf16 a[12:15], v[232:235], v[60:63], a[12:15]\n"
"v_mfma_f32_16x16x32_bf16 a[12:15], v[228:231], v[28:31], a[12:15]\n"
"s_waitcnt vmcnt(16)\n"
"v_mfma_f32_16x16x32_bf16 a[72:75], v[228:231], v[144:147], a[72:75]\n"
"s_waitcnt vmcnt(14)\n"
"ds_read_b64_tr_b16 v[228:229], v254 offset:8192\n"
"ds_read_b64_tr_b16 v[230:231], v254 offset:10240\n"
"v_mfma_f32_16x16x32_bf16 a[72:75], v[232:235], v[148:151], a[72:75]\n"
"s_waitcnt vmcnt(12) lgkmcnt(8)\n"
"ds_read_b64_tr_b16 v[232:233], v254 offset:12288\n"
"ds_read_b64_tr_b16 v[234:235], v254 offset:14336\n"
"v_mfma_f32_16x16x32_bf16 a[72:75], v[216:219], v[152:155], a[72:75]\n"
"s_waitcnt vmcnt(10) lgkmcnt(8)\n"
"buffer_load_dwordx4 v252, s[56:59], s93 offen offset:2048 lds\n"
"v_mfma_f32_16x16x32_bf16 a[72:75], v[212:215], v[156:159], a[72:75]\n"
"s_add_u32 s93, s27, 0x3200\n"
"v_mfma_f32_16x16x32_bf16 a[12:15], v[212:215], v[124:127], a[12:15]\n"
"v_mfma_f32_16x16x32_bf16 a[12:15], v[216:219], v[92:95], a[12:15]\n"
"v_mfma_f32_16x16x32_bf16 a[8:11], v[216:219], v[84:87], a[8:11]\n"
"ds_read_b64_tr_b16 v[216:217], v254 offset:16384\n"
"ds_read_b64_tr_b16 v[218:219], v254 offset:18432\n"
"v_mfma_f32_16x16x32_bf16 a[8:11], v[212:215], v[116:119], a[8:11]\n"
"s_waitcnt lgkmcnt(8)\n"
"ds_read_b64_tr_b16 v[212:213], v254 offset:20480\n"
"ds_read_b64_tr_b16 v[214:215], v254 offset:22528\n"
"v_mfma_f32_16x16x32_bf16 a[108:111], v[220:223], v[16:19], a[108:111]\n"
"s_waitcnt lgkmcnt(8)\n"
"buffer_load_dwordx4 v252, s[56:59], s94 offen offset:3072 lds\n"
"v_mfma_f32_16x16x32_bf16 a[108:111], v[224:227], v[48:51], a[108:111]\n"
"s_add_u32 s94, s27, 0x4a00\n"
"v_accvgpr_read_b32 v200, a8\n"
"v_accvgpr_read_b32 v201, a9\n"
"v_mfma_f32_16x16x32_bf16 a[120:123], v[224:227], v[56:59], a[120:123]\n"
"ds_read_b64_tr_b16 v[224:225], v254 offset:24576\n"
"ds_read_b64_tr_b16 v[226:227], v254 offset:26624\n"
"v_mfma_f32_16x16x32_bf16 a[120:123], v[220:223], v[24:27], a[120:123]\n"
"s_waitcnt lgkmcnt(8)\n"
"ds_read_b64_tr_b16 v[220:221], v254 offset:28672\n"
"ds_read_b64_tr_b16 v[222:223], v254 offset:30720\n"
"v_mfma_f32_16x16x32_bf16 a[120:123], v[228:231], v[88:91], a[120:123]\n"
"s_waitcnt lgkmcnt(8)\n"
"v_accvgpr_read_b32 v204, a12\n"
"v_accvgpr_read_b32 v205, a13\n"
"v_mfma_f32_16x16x32_bf16 a[120:123], v[232:235], v[120:123], a[120:123]\n"
"v_accvgpr_read_b32 v202, a10\n"
"v_accvgpr_read_b32 v203, a11\n"
"v_mfma_f32_16x16x32_bf16 a[108:111], v[232:235], v[112:115], a[108:111]\n"
"ds_read_b64_tr_b16 v[232:233], v249 offset:0\n"
"ds_read_b64_tr_b16 v[234:235], v249 offset:2048\n"
"v_mfma_f32_16x16x32_bf16 a[108:111], v[228:231], v[80:83], a[108:111]\n"
"s_waitcnt lgkmcnt(8)\n"
"ds_read_b64_tr_b16 v[228:229], v249 offset:4096\n"
"ds_read_b64_tr_b16 v[230:231], v249 offset:6144\n"
"v_mfma_f32_16x16x32_bf16 a[108:111], v[216:219], v[20:23], a[108:111]\n"
"s_waitcnt lgkmcnt(8)\n"
"v_cvt_pk_bf16_f32 v200, v200, v201\n"
"v_accvgpr_read_b32 v206, a14\n"
"v_mfma_f32_16x16x32_bf16 a[108:111], v[212:215], v[52:55], a[108:111]\n"
"v_accvgpr_read_b32 v207, a15\n"
"v_cvt_pk_bf16_f32 v204, v204, v205\n"
"v_mfma_f32_16x16x32_bf16 a[120:123], v[212:215], v[60:63], a[120:123]\n"
"ds_read_b64_tr_b16 v[212:213], v249 offset:8192\n"
"ds_read_b64_tr_b16 v[214:215], v249 offset:10240\n"
"v_mfma_f32_16x16x32_bf16 a[120:123], v[216:219], v[28:31], a[120:123]\n"
"s_waitcnt lgkmcnt(8)\n"
"ds_read_b64_tr_b16 v[216:217], v249 offset:12288\n"
"ds_read_b64_tr_b16 v[218:219], v249 offset:14336\n"
"v_mfma_f32_16x16x32_bf16 a[120:123], v[224:227], v[92:95], a[120:123]\n"
"s_waitcnt lgkmcnt(8)\n"
"v_cvt_pk_bf16_f32 v201, v202, v203\n"
"v_cvt_pk_bf16_f32 v205, v206, v207\n"
"v_mfma_f32_16x16x32_bf16 a[120:123], v[220:223], v[124:127], a[120:123]\n"
"s_waitcnt vmcnt(10)\n"
"s_barrier\n"
"v_mfma_f32_16x16x32_bf16 a[108:111], v[220:223], v[116:119], a[108:111]\n"
"ds_read_b64_tr_b16 v[220:221], v249 offset:16384\n"
"ds_read_b64_tr_b16 v[222:223], v249 offset:18432\n"
"v_mfma_f32_16x16x32_bf16 a[108:111], v[224:227], v[84:87], a[108:111]\n"
"s_waitcnt lgkmcnt(8)\n"
"ds_read_b64_tr_b16 v[224:225], v249 offset:20480\n"
"ds_read_b64_tr_b16 v[226:227], v249 offset:22528\n"
"v_mfma_f32_16x16x32_bf16 a[124:127], v[232:235], v[16:19], a[124:127]\n"
"s_waitcnt lgkmcnt(8)\n"
"v_accvgpr_read_b32 v202, a24\n"
"v_accvgpr_read_b32 v203, a25\n"
"v_mfma_f32_16x16x32_bf16 a[124:127], v[228:231], v[48:51], a[124:127]\n"
"s_add_u32 m0, s90, 0x1f400\n"
"v_accvgpr_read_b32 v206, a28\n"
"v_accvgpr_read_b32 v207, a29\n"
"v_mfma_f32_16x16x32_bf16 a[136:139], v[228:231], v[56:59], a[136:139]\n"
"ds_read_b64_tr_b16 v[228:229], v249 offset:24576\n"
"ds_read_b64_tr_b16 v[230:231], v249 offset:26624\n"
"v_mfma_f32_16x16x32_bf16 a[136:139], v[232:235], v[24:27], a[136:139]\n"
"s_waitcnt lgkmcnt(8)\n"
"ds_read_b64_tr_b16 v[232:233], v249 offset:28672\n"
"ds_read_b64_tr_b16 v[234:235], v249 offset:30720\n"
"v_mfma_f32_16x16x32_bf16 a[136:139], v[212:215], v[88:91], a[136:139]\n"
"s_waitcnt lgkmcnt(8)\n"
"buffer_load_dwordx4 v252, s[56:59], s91 offen lds\n"
"v_mfma_f32_16x16x32_bf16 a[136:139], v[216:219], v[120:123], a[136:139]\n"
"s_add_u32 s91, s27, 0x7200\n"
"v_cvt_pk_bf16_f32 v202, v202, v203\n"
"v_cvt_pk_bf16_f32 v206, v206, v207\n"
"v_mfma_f32_16x16x32_bf16 a[124:127], v[216:219], v[112:115], a[124:127]\n"
"ds_read_b64_tr_b16 v[216:217], v246 offset:0\n"
"ds_read_b64_tr_b16 v[218:219], v246 offset:2048\n"
"v_mfma_f32_16x16x32_bf16 a[124:127], v[212:215], v[80:83], a[124:127]\n"
"s_waitcnt lgkmcnt(8)\n"
"ds_read_b64_tr_b16 v[212:213], v246 offset:4096\n"
"ds_read_b64_tr_b16 v[214:215], v246 offset:6144\n"
"v_mfma_f32_16x16x32_bf16 a[124:127], v[220:223], v[20:23], a[124:127]\n"
"s_waitcnt lgkmcnt(8)\n"
"buffer_load_dwordx4 v252, s[56:59], s92 offen offset:1024 lds\n"
"v_mfma_f32_16x16x32_bf16 a[124:127], v[224:227], v[52:55], a[124:127]\n"
"s_add_u32 s92, s27, 0x8a00\n"
"v_accvgpr_read_b32 v203, a26\n"
"v_accvgpr_read_b32 v207, a30\n"
"v_mfma_f32_16x16x32_bf16 a[136:139], v[224:227], v[60:63], a[136:139]\n"
"ds_read_b64_tr_b16 v[224:225], v246 offset:8192\n"
"ds_read_b64_tr_b16 v[226:227], v246 offset:10240\n"
"v_mfma_f32_16x16x32_bf16 a[136:139], v[220:223], v[28:31], a[136:139]\n"
"s_waitcnt lgkmcnt(8)\n"
"ds_read_b64_tr_b16 v[220:221], v246 offset:12288\n"
"ds_read_b64_tr_b16 v[222:223], v246 offset:14336\n"
"v_mfma_f32_16x16x32_bf16 a[136:139], v[228:231], v[92:95], a[136:139]\n"
"s_waitcnt lgkmcnt(8)\n"
"v_cvt_pk_bf16_f32 v203, v203, v210\n"
"v_cvt_pk_bf16_f32 v207, v207, v211\n"
"v_mfma_f32_16x16x32_bf16 a[136:139], v[232:235], v[124:127], a[136:139]\n"
"buffer_store_dwordx4 v[200:203], v248, s[60:63], s68 offen sc1\n"
"v_mfma_f32_16x16x32_bf16 a[124:127], v[232:235], v[116:119], a[124:127]\n"
"s_add_u32 s68, s87, 0x400000\n"
"ds_read_b64_tr_b16 v[232:233], v246 offset:16384\n"
"ds_read_b64_tr_b16 v[234:235], v246 offset:18432\n"
"v_mfma_f32_16x16x32_bf16 a[124:127], v[228:231], v[84:87], a[124:127]\n"
"s_waitcnt lgkmcnt(8)\n"
"ds_read_b64_tr_b16 v[228:229], v246 offset:20480\n"
"ds_read_b64_tr_b16 v[230:231], v246 offset:22528\n"
"v_mfma_f32_16x16x32_bf16 a[92:95], v[216:219], v[16:19], a[92:95]\n"
"s_waitcnt lgkmcnt(8)\n"
"buffer_load_dwordx4 v252, s[56:59], s93 offen offset:2048 lds\n"
"v_mfma_f32_16x16x32_bf16 a[92:95], v[212:215], v[48:51], a[92:95]\n"
"s_add_u32 s93, s27, 0xa200\n"
"v_accvgpr_read_b32 v192, a108\n"
"v_accvgpr_read_b32 v193, a109\n"
"v_mfma_f32_16x16x32_bf16 a[104:107], v[212:215], v[56:59], a[104:107]\n"
"ds_read_b64_tr_b16 v[212:213], v246 offset:24576\n"
"ds_read_b64_tr_b16 v[214:215], v246 offset:26624\n"
"v_mfma_f32_16x16x32_bf16 a[104:107], v[216:219], v[24:27], a[104:107]\n"
"s_waitcnt lgkmcnt(8)\n"
"ds_read_b64_tr_b16 v[216:217], v246 offset:28672\n"
"ds_read_b64_tr_b16 v[218:219], v246 offset:30720\n"
"v_mfma_f32_16x16x32_bf16 a[104:107], v[224:227], v[88:91], a[104:107]\n"
"s_waitcnt lgkmcnt(8)\n"
"v_accvgpr_read_b32 v196, a120\n"
"v_accvgpr_read_b32 v197, a121\n"
"v_mfma_f32_16x16x32_bf16 a[104:107], v[220:223], v[120:123], a[104:107]\n"
"buffer_store_dwordx4 v[204:207], v248, s[60:63], s83 offen sc1\n"
"v_mfma_f32_16x16x32_bf16 a[92:95], v[220:223], v[112:115], a[92:95]\n"
"s_add_u32 s83, s87, 0x500000\n"
"ds_read_b64_tr_b16 v[220:221], v253 offset:0\n"
"ds_read_b64_tr_b16 v[222:223], v253 offset:2048\n"
"v_mfma_f32_16x16x32_bf16 a[92:95], v[224:227], v[80:83], a[92:95]\n"
"s_waitcnt lgkmcnt(8)\n"
"ds_read_b64_tr_b16 v[224:225], v253 offset:4096\n"
"ds_read_b64_tr_b16 v[226:227], v253 offset:6144\n"
"v_mfma_f32_16x16x32_bf16 a[92:95], v[232:235], v[20:23], a[92:95]\n"
"s_waitcnt lgkmcnt(8)\n"
"buffer_load_dwordx4 v252, s[56:59], s94 offen offset:3072 lds\n"
"v_mfma_f32_16x16x32_bf16 a[92:95], v[228:231], v[52:55], a[92:95]\n"
"s_add_u32 m0, s90, 0x20400\n"
"v_accvgpr_read_b32 v194, a110\n"
"v_accvgpr_read_b32 v195, a111\n"
"v_mfma_f32_16x16x32_bf16 a[104:107], v[228:231], v[60:63], a[104:107]\n"
"s_add_u32 s94, s27, 0xba00\n"
"ds_read_b64_tr_b16 v[228:229], v253 offset:8192\n"
"ds_read_b64_tr_b16 v[230:231], v253 offset:10240\n"
"v_mfma_f32_16x16x32_bf16 a[104:107], v[232:235], v[28:31], a[104:107]\n"
"s_waitcnt lgkmcnt(8)\n"
"ds_read_b64_tr_b16 v[232:233], v253 offset:12288\n"
"ds_read_b64_tr_b16 v[234:235], v253 offset:14336\n"
"v_mfma_f32_16x16x32_bf16 a[104:107], v[212:215], v[92:95], a[104:107]\n"
"s_waitcnt lgkmcnt(8)\n"
"v_cvt_pk_bf16_f32 v192, v192, v193\n"
"v_accvgpr_read_b32 v198, a122\n"
"v_mfma_f32_16x16x32_bf16 a[104:107], v[216:219], v[124:127], a[104:107]\n"
"v_accvgpr_read_b32 v199, a123\n"
"v_cvt_pk_bf16_f32 v196, v196, v197\n"
"v_mfma_f32_16x16x32_bf16 a[92:95], v[216:219], v[116:119], a[92:95]\n"
"ds_read_b64_tr_b16 v[216:217], v253 offset:16384\n"
"ds_read_b64_tr_b16 v[218:219], v253 offset:18432\n"
"v_mfma_f32_16x16x32_bf16 a[92:95], v[212:215], v[84:87], a[92:95]\n"
"s_waitcnt lgkmcnt(8)\n"
"ds_read_b64_tr_b16 v[212:213], v253 offset:20480\n"
"ds_read_b64_tr_b16 v[214:215], v253 offset:22528\n"
"v_mfma_f32_16x16x32_bf16 a[76:79], v[220:223], v[16:19], a[76:79]\n"
"s_waitcnt lgkmcnt(8)\n"
"buffer_load_dwordx4 v252, s[56:59], s91 offen lds\n"
"v_mfma_f32_16x16x32_bf16 a[76:79], v[224:227], v[48:51], a[76:79]\n"
"s_add_u32 s91, s27, 0x280\n"
"v_cvt_pk_bf16_f32 v193, v194, v195\n"
"v_cvt_pk_bf16_f32 v197, v198, v199\n"
"v_mfma_f32_16x16x32_bf16 a[88:91], v[224:227], v[56:59], a[88:91]\n"
"v_accvgpr_read_b32 v194, a124\n"
"v_accvgpr_read_b32 v195, a125\n"
"v_mfma_f32_16x16x32_bf16 a[88:91], v[220:223], v[24:27], a[88:91]\n"
"v_accvgpr_read_b32 v198, a136\n"
"v_accvgpr_read_b32 v199, a137\n"
"v_mfma_f32_16x16x32_bf16 a[140:143], v[220:223], v[128:131], a[140:143]\n"
"ds_read_b64_tr_b16 v[220:221], v253 offset:24576\n"
"ds_read_b64_tr_b16 v[222:223], v253 offset:26624\n"
"v_mfma_f32_16x16x32_bf16 a[140:143], v[224:227], v[132:135], a[140:143]\n"
"s_waitcnt lgkmcnt(8)\n"
"ds_read_b64_tr_b16 v[224:225], v253 offset:28672\n"
"ds_read_b64_tr_b16 v[226:227], v253 offset:30720\n"
"v_mfma_f32_16x16x32_bf16 a[140:143], v[228:231], v[136:139], a[140:143]\n"
"s_waitcnt lgkmcnt(8)\n"
"v_cvt_pk_bf16_f32 v194, v194, v195\n"
"v_cvt_pk_bf16_f32 v198, v198, v199\n"
"v_mfma_f32_16x16x32_bf16 a[140:143], v[232:235], v[140:143], a[140:143]\n"
"v_accvgpr_read_b32 v208, a127\n"
"v_accvgpr_read_b32 v195, a126\n"
"v_mfma_f32_16x16x32_bf16 a[88:91], v[232:235], v[120:123], a[88:91]\n"
"buffer_load_dwordx4 v252, s[56:59], s92 offen offset:1024 lds\n"
"v_mfma_f32_16x16x32_bf16 a[88:91], v[228:231], v[88:91], a[88:91]\n"
"s_add_u32 s92, s27, 0x1a80\n"
"v_accvgpr_read_b32 v209, a139\n"
"v_accvgpr_read_b32 v199, a138\n"
"v_mfma_f32_16x16x32_bf16 a[76:79], v[228:231], v[80:83], a[76:79]\n"
"ds_read_b64_tr_b16 v[228:229], v254 offset:32768\n"
"ds_read_b64_tr_b16 v[230:231], v254 offset:34816\n"
"v_mfma_f32_16x16x32_bf16 a[76:79], v[232:235], v[112:115], a[76:79]\n"
"Lpada_10:\n"
"s_cbranch_execz Lpadxa\n"
"s_waitcnt lgkmcnt(8)\n"
"ds_read_b64_tr_b16 v[232:233], v254 offset:36864\n"
"ds_read_b64_tr_b16 v[234:235], v254 offset:38912\n"
"v_mfma_f32_16x16x32_bf16 a[76:79], v[216:219], v[20:23], a[76:79]\n"
"s_waitcnt lgkmcnt(8)\n"
"v_accvgpr_read_b32 v210, a95\n"
"v_accvgpr_read_b32 v211, a107\n"
"v_mfma_f32_16x16x32_bf16 a[76:79], v[212:215], v[52:55], a[76:79]\n"
"v_cvt_pk_bf16_f32 v195, v195, v208\n"
"v_cvt_pk_bf16_f32 v199, v199, v209\n"
"v_mfma_f32_16x16x32_bf16 a[88:91], v[212:215], v[60:63], a[88:91]\n"
"buffer_store_dwordx4 v[192:195], v248, s[60:63], s84 offen sc1\n"
"v_mfma_f32_16x16x32_bf16 a[88:91], v[216:219], v[28:31], a[88:91]\n"
"s_add_u32 s84, s87, 0x1c00000\n"
"v_accvgpr_read_b32 v192, a72\n"
"v_accvgpr_read_b32 v193, a73\n"
"v_mfma_f32_16x16x32_bf16 a[140:143], v[216:219], v[144:147], a[140:143]\n"
"ds_read_b64_tr_b16 v[216:217], v254 offset:40960\n"
"ds_read_b64_tr_b16 v[218:219], v254 offset:43008\n"
"v_mfma_f32_16x16x32_bf16 a[140:143], v[212:215], v[148:151], a[140:143]\n"
"s_waitcnt lgkmcnt(8)\n"
"ds_read_b64_tr_b16 v[212:213], v254 offset:45056\n"
"ds_read_b64_tr_b16 v[214:215], v254 offset:47104\n"
"v_mfma_f32_16x16x32_bf16 a[140:143], v[220:223], v[152:155], a[140:143]\n"
"s_waitcnt lgkmcnt(8)\n"
"buffer_load_dwordx4 v252, s[56:59], s93 offen offset:2048 lds\n"
"v_mfma_f32_16x16x32_bf16 a[140:143], v[224:227], v[156:159], a[140:143]\n"
"s_add_u32 s93, s27, 0x3280\n"
"v_accvgpr_read_b32 v194, a74\n"
"v_accvgpr_read_b32 v195, a75\n"
"v_mfma_f32_16x16x32_bf16 a[88:91], v[224:227], v[124:127], a[88:91]\n"
"v_cvt_pk_bf16_f32 v192, v192, v193\n"
"v_cvt_pk_bf16_f32 v193, v194, v195\n"
"v_mfma_f32_16x16x32_bf16 a[88:91], v[220:223], v[92:95], a[88:91]\n"
"v_mfma_f32_16x16x32_bf16 a[76:79], v[220:223], v[84:87], a[76:79]\n"
"ds_read_b64_tr_b16 v[220:221], v254 offset:49152\n"
"ds_read_b64_tr_b16 v[222:223], v254 offset:51200\n"
"v_mfma_f32_16x16x32_bf16 a[76:79], v[224:227], v[116:119], a[76:79]\n"
"s_waitcnt lgkmcnt(8)\n"
"ds_read_b64_tr_b16 v[224:225], v254 offset:53248\n"
"ds_read_b64_tr_b16 v[226:227], v254 offset:55296\n"
"v_mfma_f32_16x16x32_bf16 a[184:187], v[228:231], v[16:19], a[184:187]\n"
"s_waitcnt lgkmcnt(8)\n"
"buffer_load_dwordx4 v252, s[56:59], s94 offen offset:3072 lds\n"
"v_mfma_f32_16x16x32_bf16 a[184:187], v[232:235], v[48:51], a[184:187]\n"
"s_add_u32 s94, s27, 0x4a80\n"
"v_accvgpr_read_b32 v200, a76\n"
"v_accvgpr_read_b32 v201, a77\n"
"v_mfma_f32_16x16x32_bf16 a[188:191], v[232:235], v[56:59], a[188:191]\n"
"ds_read_b64_tr_b16 v[232:233], v254 offset:57344\n"
"ds_read_b64_tr_b16 v[234:235], v254 offset:59392\n"
"v_mfma_f32_16x16x32_bf16 a[188:191], v[228:231], v[24:27], a[188:191]\n"
"s_waitcnt lgkmcnt(8)\n"
"ds_read_b64_tr_b16 v[228:229], v254 offset:61440\n"
"ds_read_b64_tr_b16 v[230:231], v254 offset:63488\n"
"v_mfma_f32_16x16x32_bf16 a[188:191], v[216:219], v[88:91], a[188:191]\n"
"s_waitcnt lgkmcnt(8)\n"
"v_add_u32 v254, 0x12000, v254\n"
"v_accvgpr_read_b32 v204, a88\n"
"v_mfma_f32_16x16x32_bf16 a[188:191], v[212:215], v[120:123], a[188:191]\n"
"v_accvgpr_read_b32 v205, a89\n"
"v_accvgpr_read_b32 v202, a78\n"
"v_mfma_f32_16x16x32_bf16 a[184:187], v[212:215], v[112:115], a[184:187]\n"
"ds_read_b64_tr_b16 v[212:213], v249 offset:32768\n"
"ds_read_b64_tr_b16 v[214:215], v249 offset:34816\n"
"v_mfma_f32_16x16x32_bf16 a[184:187], v[216:219], v[80:83], a[184:187]\n"
"s_waitcnt lgkmcnt(8)\n"
"ds_read_b64_tr_b16 v[216:217], v249 offset:36864\n"
"ds_read_b64_tr_b16 v[218:219], v249 offset:38912\n"
"v_mfma_f32_16x16x32_bf16 a[184:187], v[220:223], v[20:23], a[184:187]\n"
"s_waitcnt lgkmcnt(8)\n"
"v_accvgpr_read_b32 v203, a79\n"
"v_cvt_pk_bf16_f32 v200, v200, v201\n"
"v_mfma_f32_16x16x32_bf16 a[184:187], v[224:227], v[52:55], a[184:187]\n"
"v_accvgpr_read_b32 v206, a90\n"
"v_accvgpr_read_b32 v207, a91\n"
"v_mfma_f32_16x16x32_bf16 a[188:191], v[224:227], v[60:63], a[188:191]\n"
"ds_read_b64_tr_b16 v[224:225], v249 offset:40960\n"
"ds_read_b64_tr_b16 v[226:227], v249 offset:43008\n"
"v_mfma_f32_16x16x32_bf16 a[188:191], v[220:223], v[28:31], a[188:191]\n"
"s_waitcnt lgkmcnt(8)\n"
"ds_read_b64_tr_b16 v[220:221], v249 offset:45056\n"
"ds_read_b64_tr_b16 v[222:223], v249 offset:47104\n"
"v_mfma_f32_16x16x32_bf16 a[188:191], v[232:235], v[92:95], a[188:191]\n"
"s_waitcnt lgkmcnt(8)\n"
"v_cvt_pk_bf16_f32 v204, v204, v205\n"
"v_cvt_pk_bf16_f32 v201, v202, v203\n"
"v_mfma_f32_16x16x32_bf16 a[188:191], v[228:231], v[124:127], a[188:191]\n"
"s_waitcnt vmcnt(11)\n"
"s_barrier\n"
"v_mfma_f32_16x16x32_bf16 a[184:187], v[228:231], v[116:119], a[184:187]\n"
"ds_read_b64_tr_b16 v[228:229], v249 offset:49152\n"
"ds_read_b64_tr_b16 v[230:231], v249 offset:51200\n"
"v_mfma_f32_16x16x32_bf16 a[184:187], v[232:235], v[84:87], a[184:187]\n"
"s_waitcnt lgkmcnt(8)\n"
"ds_read_b64_tr_b16 v[232:233], v249 offset:53248\n"
"ds_read_b64_tr_b16 v[234:235], v249 offset:55296\n"
"v_mfma_f32_16x16x32_bf16 a[200:203], v[212:215], v[16:19], a[200:203]\n"
"s_waitcnt lgkmcnt(8)\n"
"v_cvt_pk_bf16_f32 v205, v206, v207\n"
"v_accvgpr_read_b32 v202, a92\n"
"v_mfma_f32_16x16x32_bf16 a[200:203], v[216:219], v[48:51], a[200:203]\n"
"s_add_u32 m0, s90, 0x5400\n"
"v_accvgpr_read_b32 v203, a93\n"
"v_accvgpr_read_b32 v206, a104\n"
"v_mfma_f32_16x16x32_bf16 a[204:207], v[216:219], v[56:59], a[204:207]\n"
"ds_read_b64_tr_b16 v[216:217], v249 offset:57344\n"
"ds_read_b64_tr_b16 v[218:219], v249 offset:59392\n"
"v_mfma_f32_16x16x32_bf16 a[204:207], v[212:215], v[24:27], a[204:207]\n"
"s_waitcnt lgkmcnt(8)\n"
"ds_read_b64_tr_b16 v[212:213], v249 offset:61440\n"
"ds_read_b64_tr_b16 v[214:215], v249 offset:63488\n"
"v_mfma_f32_16x16x32_bf16 a[204:207], v[224:227], v[88:91], a[204:207]\n"
"s_waitcnt lgkmcnt(8)\n"
"buffer_load_dwordx4 v252, s[56:59], s91 offen lds\n"
"v_mfma_f32_16x16x32_bf16 a[204:207], v[220:223], v[120:123], a[204:207]\n"
"s_add_u32 s91, s27, 0x7280\n"
"v_add_u32 v249, 0x12000, v249\n"
"v_accvgpr_read_b32 v207, a105\n"
"v_mfma_f32_16x16x32_bf16 a[200:203], v[220:223], v[112:115], a[200:203]\n"
"ds_read_b64_tr_b16 v[220:221], v246 offset:32768\n"
"ds_read_b64_tr_b16 v[222:223], v246 offset:34816\n"
"v_mfma_f32_16x16x32_bf16 a[200:203], v[224:227], v[80:83], a[200:203]\n"
"s_waitcnt lgkmcnt(8)\n"
"ds_read_b64_tr_b16 v[224:225], v246 offset:36864\n"
"ds_read_b64_tr_b16 v[226:227], v246 offset:38912\n"
"v_mfma_f32_16x16x32_bf16 a[200:203], v[228:231], v[20:23], a[200:203]\n"
"s_waitcnt lgkmcnt(8)\n"
"buffer_load_dwordx4 v252, s[56:59], s92 offen offset:1024 lds\n"
"v_mfma_f32_16x16x32_bf16 a[200:203], v[232:235], v[52:55], a[200:203]\n"
"s_add_u32 s92, s27, 0x8a80\n"
"v_cvt_pk_bf16_f32 v202, v202, v203\n"
"v_cvt_pk_bf16_f32 v206, v206, v207\n"
"v_mfma_f32_16x16x32_bf16 a[204:207], v[232:235], v[60:63], a[204:207]\n"
"ds_read_b64_tr_b16 v[232:233], v246 offset:40960\n"
"ds_read_b64_tr_b16 v[234:235], v246 offset:43008\n"
"v_mfma_f32_16x16x32_bf16 a[204:207], v[228:231], v[28:31], a[204:207]\n"
"s_waitcnt lgkmcnt(8)\n"
"ds_read_b64_tr_b16 v[228:229], v246 offset:45056\n"
"ds_read_b64_tr_b16 v[230:231], v246 offset:47104\n"
"v_mfma_f32_16x16x32_bf16 a[204:207], v[216:219], v[92:95], a[204:207]\n"
"s_waitcnt lgkmcnt(8)\n"
"v_accvgpr_read_b32 v203, a94\n"
"v_accvgpr_read_b32 v207, a106\n"
"v_mfma_f32_16x16x32_bf16 a[204:207], v[212:215], v[124:127], a[204:207]\n"
"v_cvt_pk_bf16_f32 v203, v203, v210\n"
"v_cvt_pk_bf16_f32 v207, v207, v211\n"
"v_mfma_f32_16x16x32_bf16 a[200:203], v[212:215], v[116:119], a[200:203]\n"
"ds_read_b64_tr_b16 v[212:213], v246 offset:49152\n"
"ds_read_b64_tr_b16 v[214:215], v246 offset:51200\n"
"v_mfma_f32_16x16x32_bf16 a[200:203], v[216:219], v[84:87], a[200:203]\n"
"s_waitcnt lgkmcnt(8)\n"
"ds_read_b64_tr_b16 v[216:217], v246 offset:53248\n"
"ds_read_b64_tr_b16 v[218:219], v246 offset:55296\n"
"v_mfma_f32_16x16x32_bf16 a[168:171], v[220:223], v[16:19], a[168:171]\n"
"s_waitcnt lgkmcnt(8)\n"
"buffer_load_dwordx4 v252, s[56:59], s93 offen offset:2048 lds\n"
"v_mfma_f32_16x16x32_bf16 a[168:171], v[224:227], v[48:51], a[168:171]\n"
"s_add_u32 s93, s27, 0xa280\n"
"v_accvgpr_read_b32 v194, a140\n"
"v_accvgpr_read_b32 v195, a141\n"
"v_mfma_f32_16x16x32_bf16 a[172:175], v[224:227], v[56:59], a[172:175]\n"
"ds_read_b64_tr_b16 v[224:225], v246 offset:57344\n"
"ds_read_b64_tr_b16 v[226:227], v246 offset:59392\n"
"v_mfma_f32_16x16x32_bf16 a[172:175], v[220:223], v[24:27], a[172:175]\n"
"s_waitcnt lgkmcnt(8)\n"
"ds_read_b64_tr_b16 v[220:221], v246 offset:61440\n"
"ds_read_b64_tr_b16 v[222:223], v246 offset:63488\n"
"v_mfma_f32_16x16x32_bf16 a[172:175], v[232:235], v[88:91], a[172:175]\n"
"s_waitcnt lgkmcnt(8)\n"
"v_add_u32 v246, 0x12000, v246\n"
"v_cvt_pk_bf16_f32 v194, v194, v195\n"
"v_mfma_f32_16x16x32_bf16 a[172:175], v[228:231], v[120:123], a[172:175]\n"
"buffer_store_dwordx4 v[196:199], v248, s[60:63], s67 offen sc1\n"
"v_mfma_f32_16x16x32_bf16 a[168:171], v[228:231], v[112:115], a[168:171]\n"
"s_add_u32 s67, s87, 0xa00000\n"
"ds_read_b64_tr_b16 v[228:229], v253 offset:32768\n"
"ds_read_b64_tr_b16 v[230:231], v253 offset:34816\n"
"v_mfma_f32_16x16x32_bf16 a[168:171], v[232:235], v[80:83], a[168:171]\n"
"s_waitcnt lgkmcnt(8)\n"
"ds_read_b64_tr_b16 v[232:233], v253 offset:36864\n"
"ds_read_b64_tr_b16 v[234:235], v253 offset:38912\n"
"v_mfma_f32_16x16x32_bf16 a[168:171], v[212:215], v[20:23], a[168:171]\n"
"s_waitcnt lgkmcnt(8)\n"
"buffer_load_dwordx4 v252, s[56:59], s94 offen offset:3072 lds\n"
"v_mfma_f32_16x16x32_bf16 a[168:171], v[216:219], v[52:55], a[168:171]\n"
"s_add_u32 m0, s90, 0x6400\n"
"v_accvgpr_read_b32 v196, a184\n"
"v_accvgpr_read_b32 v197, a185\n"
"v_mfma_f32_16x16x32_bf16 a[172:175], v[216:219], v[60:63], a[172:175]\n"
"s_add_u32 s94, s27, 0xba80\n"
"ds_read_b64_tr_b16 v[216:217], v253 offset:40960\n"
"ds_read_b64_tr_b16 v[218:219], v253 offset:43008\n"
"v_mfma_f32_16x16x32_bf16 a[172:175], v[212:215], v[28:31], a[172:175]\n"
"s_waitcnt lgkmcnt(8)\n"
"ds_read_b64_tr_b16 v[212:213], v253 offset:45056\n"
"ds_read_b64_tr_b16 v[214:215], v253 offset:47104\n"
"v_mfma_f32_16x16x32_bf16 a[172:175], v[224:227], v[92:95], a[172:175]\n"
"s_waitcnt lgkmcnt(8)\n"
"v_accvgpr_read_b32 v198, a186\n"
"v_accvgpr_read_b32 v199, a187\n"
"v_mfma_f32_16x16x32_bf16 a[172:175], v[220:223], v[124:127], a[172:175]\n"
"buffer_store_dwordx4 v[200:203], v248, s[60:63], s68 offen sc1\n"
"v_mfma_f32_16x16x32_bf16 a[168:171], v[220:223], v[116:119], a[168:171]\n"
"s_add_u32 s68, s87, 0xb00000\n"
"ds_read_b64_tr_b16 v[220:221], v253 offset:49152\n"
"ds_read_b64_tr_b16 v[222:223], v253 offset:51200\n"
"v_mfma_f32_16x16x32_bf16 a[168:171], v[224:227], v[84:87], a[168:171]\n"
"s_waitcnt lgkmcnt(8)\n"
"ds_read_b64_tr_b16 v[224:225], v253 offset:53248\n"
"ds_read_b64_tr_b16 v[226:227], v253 offset:55296\n"
"v_mfma_f32_16x16x32_bf16 a[152:155], v[228:231], v[16:19], a[152:155]\n"
"s_waitcnt lgkmcnt(8)\n"
"buffer_load_dwordx4 v252, s[56:59], s91 offen lds\n"
"v_mfma_f32_16x16x32_bf16 a[152:155], v[232:235], v[48:51], a[152:155]\n"
"s_add_u32 s91, s27, 0x300\n"
"v_accvgpr_read_b32 v200, a188\n"
"v_accvgpr_read_b32 v201, a189\n"
"v_mfma_f32_16x16x32_bf16 a[156:159], v[232:235], v[56:59], a[156:159]\n"
"v_cvt_pk_bf16_f32 v196, v196, v197\n"
"v_accvgpr_read_b32 v202, a190\n"
"v_mfma_f32_16x16x32_bf16 a[156:159], v[228:231], v[24:27], a[156:159]\n"
"v_accvgpr_read_b32 v203, a191\n"
"v_cvt_pk_bf16_f32 v200, v200, v201\n"
"v_mfma_f32_16x16x32_bf16 a[216:219], v[228:231], v[128:131], a[216:219]\n"
"ds_read_b64_tr_b16 v[228:229], v253 offset:57344\n"
"ds_read_b64_tr_b16 v[230:231], v253 offset:59392\n"
"v_mfma_f32_16x16x32_bf16 a[216:219], v[232:235], v[132:135], a[216:219]\n"
"s_waitcnt lgkmcnt(8)\n"
"ds_read_b64_tr_b16 v[232:233], v253 offset:61440\n"
"ds_read_b64_tr_b16 v[234:235], v253 offset:63488\n"
"v_mfma_f32_16x16x32_bf16 a[216:219], v[216:219], v[136:139], a[216:219]\n"
"s_waitcnt lgkmcnt(8)\n"
"v_add_u32 v253, 0x12000, v253\n"
"v_cvt_pk_bf16_f32 v197, v198, v199\n"
"v_mfma_f32_16x16x32_bf16 a[216:219], v[212:215], v[140:143], a[216:219]\n"
"buffer_store_dwordx4 v[204:207], v248, s[60:63], s83 offen sc1\n"
"v_mfma_f32_16x16x32_bf16 a[156:159], v[212:215], v[120:123], a[156:159]\n"
"s_add_u32 s83, s87, 0x800000\n"
"v_cvt_pk_bf16_f32 v201, v202, v203\n"
"v_accvgpr_read_b32 v198, a200\n"
"v_mfma_f32_16x16x32_bf16 a[156:159], v[216:219], v[88:91], a[156:159]\n"
"v_accvgpr_read_b32 v199, a201\n"
"v_accvgpr_read_b32 v202, a204\n"
"v_mfma_f32_16x16x32_bf16 a[152:155], v[216:219], v[80:83], a[152:155]\n"
"buffer_load_dwordx4 v252, s[56:59], s92 offen offset:1024 lds\n"
"v_mfma_f32_16x16x32_bf16 a[152:155], v[212:215], v[112:115], a[152:155]\n"
"s_waitcnt lgkmcnt(6)\n"
"ds_read_b64_tr_b16 v[216:217], v254 offset:0\n"
"ds_read_b64_tr_b16 v[218:219], v254 offset:2048\n"
"v_mfma_f32_16x16x32_bf16 a[152:155], v[220:223], v[20:23], a[152:155]\n"
"s_waitcnt lgkmcnt(6)\n"
"ds_read_b64_tr_b16 v[212:213], v254 offset:4096\n"
"ds_read_b64_tr_b16 v[214:215], v254 offset:6144\n"
"v_mfma_f32_16x16x32_bf16 a[152:155], v[224:227], v[52:55], a[152:155]\n"
"s_add_u32 s92, s27, 0x1b00\n"
"v_accvgpr_read_b32 v203, a205\n"
"v_cvt_pk_bf16_f32 v198, v198, v199\n"
"v_mfma_f32_16x16x32_bf16 a[156:159], v[224:227], v[60:63], a[156:159]\n"
"v_cvt_pk_bf16_f32 v202, v202, v203\n"
"v_accvgpr_read_b32 v208, a143\n"
"v_mfma_f32_16x16x32_bf16 a[156:159], v[220:223], v[28:31], a[156:159]\n"
"v_accvgpr_read_b32 v195, a142\n"
"v_accvgpr_read_b32 v209, a203\n"
"v_mfma_f32_16x16x32_bf16 a[216:219], v[220:223], v[144:147], a[216:219]\n"
"ds_read_b64_tr_b16 v[220:221], v254 offset:8192\n"
"ds_read_b64_tr_b16 v[222:223], v254 offset:10240\n"
"v_mfma_f32_16x16x32_bf16 a[216:219], v[224:227], v[148:151], a[216:219]\n"
"s_waitcnt lgkmcnt(8)\n"
"ds_read_b64_tr_b16 v[224:225], v254 offset:12288\n"
"ds_read_b64_tr_b16 v[226:227], v254 offset:14336\n"
"v_mfma_f32_16x16x32_bf16 a[216:219], v[228:231], v[152:155], a[216:219]\n"
"s_waitcnt lgkmcnt(8)\n"
"buffer_load_dwordx4 v252, s[56:59], s93 offen offset:2048 lds\n"
"v_mfma_f32_16x16x32_bf16 a[216:219], v[232:235], v[156:159], a[216:219]\n"
"s_add_u32 s93, s27, 0x3300\n"
"v_accvgpr_read_b32 v199, a202\n"
"v_accvgpr_read_b32 v210, a207\n"
"v_mfma_f32_16x16x32_bf16 a[156:159], v[232:235], v[124:127], a[156:159]\n"
"v_accvgpr_read_b32 v203, a206\n"
"v_accvgpr_read_b32 v211, a171\n"
"v_mfma_f32_16x16x32_bf16 a[156:159], v[228:231], v[92:95], a[156:159]\n"
"v_cvt_pk_bf16_f32 v195, v195, v208\n"
"v_cvt_pk_bf16_f32 v199, v199, v209\n"
"v_mfma_f32_16x16x32_bf16 a[152:155], v[228:231], v[84:87], a[152:155]\n"
"ds_read_b64_tr_b16 v[228:229], v254 offset:16384\n"
"ds_read_b64_tr_b16 v[230:231], v254 offset:18432\n"
"v_mfma_f32_16x16x32_bf16 a[152:155], v[232:235], v[116:119], a[152:155]\n"
"s_waitcnt lgkmcnt(8)\n"
"ds_read_b64_tr_b16 v[232:233], v254 offset:20480\n"
"ds_read_b64_tr_b16 v[234:235], v254 offset:22528\n"
"v_mfma_f32_16x16x32_bf16 a[252:255], v[216:219], v[16:19], a[252:255]\n"
"s_waitcnt lgkmcnt(8)\n"
"buffer_load_dwordx4 v252, s[56:59], s94 offen offset:3072 lds\n"
"v_mfma_f32_16x16x32_bf16 a[252:255], v[212:215], v[48:51], a[252:255]\n"
"s_add_u32 s94, s27, 0x4b00\n"
"v_accvgpr_read_b32 v204, a152\n"
"v_accvgpr_read_b32 v205, a153\n"
"v_mfma_f32_16x16x32_bf16 a[0:3], v[212:215], v[56:59], a[0:3]\n"
"ds_read_b64_tr_b16 v[212:213], v254 offset:24576\n"
"ds_read_b64_tr_b16 v[214:215], v254 offset:26624\n"
"v_mfma_f32_16x16x32_bf16 a[0:3], v[216:219], v[24:27], a[0:3]\n"
"s_waitcnt lgkmcnt(8)\n"
"ds_read_b64_tr_b16 v[216:217], v254 offset:28672\n"
"ds_read_b64_tr_b16 v[218:219], v254 offset:30720\n"
"v_mfma_f32_16x16x32_bf16 a[0:3], v[220:223], v[88:91], a[0:3]\n"
"s_waitcnt lgkmcnt(8)\n"
"v_accvgpr_read_b32 v206, a154\n"
"v_accvgpr_read_b32 v207, a155\n"
"v_mfma_f32_16x16x32_bf16 a[0:3], v[224:227], v[120:123], a[0:3]\n"
"v_cvt_pk_bf16_f32 v204, v204, v205\n"
"v_cvt_pk_bf16_f32 v205, v206, v207\n"
"v_mfma_f32_16x16x32_bf16 a[252:255], v[224:227], v[112:115], a[252:255]\n"
"ds_read_b64_tr_b16 v[224:225], v249 offset:0\n"
"ds_read_b64_tr_b16 v[226:227], v249 offset:2048\n"
"v_mfma_f32_16x16x32_bf16 a[252:255], v[220:223], v[80:83], a[252:255]\n"
"s_waitcnt lgkmcnt(8)\n"
"ds_read_b64_tr_b16 v[220:221], v249 offset:4096\n"
"ds_read_b64_tr_b16 v[222:223], v249 offset:6144\n"
"v_mfma_f32_16x16x32_bf16 a[252:255], v[228:231], v[20:23], a[252:255]\n"
"s_waitcnt lgkmcnt(8)\n"
"v_accvgpr_read_b32 v206, a168\n"
"v_accvgpr_read_b32 v207, a169\n"
"v_mfma_f32_16x16x32_bf16 a[252:255], v[232:235], v[52:55], a[252:255]\n"
"v_cvt_pk_bf16_f32 v206, v206, v207\n"
"v_accvgpr_read_b32 v207, a170\n"
"v_mfma_f32_16x16x32_bf16 a[0:3], v[232:235], v[60:63], a[0:3]\n"
"ds_read_b64_tr_b16 v[232:233], v249 offset:8192\n"
"ds_read_b64_tr_b16 v[234:235], v249 offset:10240\n"
"v_mfma_f32_16x16x32_bf16 a[0:3], v[228:231], v[28:31], a[0:3]\n"
"s_waitcnt lgkmcnt(8)\n"
"ds_read_b64_tr_b16 v[228:229], v249 offset:12288\n"
"ds_read_b64_tr_b16 v[230:231], v249 offset:14336\n"
"v_mfma_f32_16x16x32_bf16 a[0:3], v[212:215], v[92:95], a[0:3]\n"
"s_waitcnt lgkmcnt(8)\n"
"v_cvt_pk_bf16_f32 v203, v203, v210\n"
"v_cvt_pk_bf16_f32 v207, v207, v211\n"
"v_mfma_f32_16x16x32_bf16 a[0:3], v[216:219], v[124:127], a[0:3]\n"
"s_waitcnt vmcnt(11)\n"
"s_barrier\n"
"v_mfma_f32_16x16x32_bf16 a[252:255], v[216:219], v[116:119], a[252:255]\n"
"ds_read_b64_tr_b16 v[216:217], v249 offset:16384\n"
"ds_read_b64_tr_b16 v[218:219], v249 offset:18432\n"
"v_mfma_f32_16x16x32_bf16 a[252:255], v[212:215], v[84:87], a[252:255]\n"
"s_waitcnt lgkmcnt(8)\n"
"ds_read_b64_tr_b16 v[212:213], v249 offset:20480\n"
"ds_read_b64_tr_b16 v[214:215], v249 offset:22528\n"
"v_mfma_f32_16x16x32_bf16 a[4:7], v[224:227], v[16:19], a[4:7]\n"
"s_waitcnt lgkmcnt(8)\n"
"buffer_store_dwordx4 v[192:195], v248, s[60:63], s84 offen sc1\n"
"v_mfma_f32_16x16x32_bf16 a[4:7], v[220:223], v[48:51], a[4:7]\n"
"s_add_u32 m0, s90, 0xd400\n"
"v_accvgpr_read_b32 v192, a156\n"
"v_accvgpr_read_b32 v193, a157\n"
"v_mfma_f32_16x16x32_bf16 a[16:19], v[220:223], v[56:59], a[16:19]\n"
"s_add_u32 s84, s87, 0x900000\n"
"ds_read_b64_tr_b16 v[220:221], v249 offset:24576\n"
"ds_read_b64_tr_b16 v[222:223], v249 offset:26624\n"
"v_mfma_f32_16x16x32_bf16 a[16:19], v[224:227], v[24:27], a[16:19]\n"
"s_waitcnt lgkmcnt(8)\n"
"ds_read_b64_tr_b16 v[224:225], v249 offset:28672\n"
"ds_read_b64_tr_b16 v[226:227], v249 offset:30720\n"
"v_mfma_f32_16x16x32_bf16 a[16:19], v[232:235], v[88:91], a[16:19]\n"
"s_waitcnt lgkmcnt(8)\n"
"buffer_load_dwordx4 v252, s[56:59], s91 offen lds\n"
"v_mfma_f32_16x16x32_bf16 a[16:19], v[228:231], v[120:123], a[16:19]\n"
"s_add_u32 s91, s27, 0x7300\n"
"v_accvgpr_read_b32 v194, a158\n"
"v_accvgpr_read_b32 v195, a159\n"
"v_mfma_f32_16x16x32_bf16 a[4:7], v[228:231], v[112:115], a[4:7]\n"
"ds_read_b64_tr_b16 v[228:229], v246 offset:0\n"
"ds_read_b64_tr_b16 v[230:231], v246 offset:2048\n"
"v_mfma_f32_16x16x32_bf16 a[4:7], v[232:235], v[80:83], a[4:7]\n"
"s_waitcnt lgkmcnt(8)\n"
"ds_read_b64_tr_b16 v[232:233], v246 offset:4096\n"
"ds_read_b64_tr_b16 v[234:235], v246 offset:6144\n"
"v_mfma_f32_16x16x32_bf16 a[4:7], v[216:219], v[20:23], a[4:7]\n"
"s_waitcnt lgkmcnt(8)\n"
"buffer_load_dwordx4 v252, s[56:59], s92 offen offset:1024 lds\n"
"v_mfma_f32_16x16x32_bf16 a[4:7], v[212:215], v[52:55], a[4:7]\n"
"s_add_u32 s92, s27, 0x8b00\n"
"v_cvt_pk_bf16_f32 v192, v192, v193\n"
"v_cvt_pk_bf16_f32 v193, v194, v195\n"
"v_mfma_f32_16x16x32_bf16 a[16:19], v[212:215], v[60:63], a[16:19]\n"
"ds_read_b64_tr_b16 v[212:213], v246 offset:8192\n"
"ds_read_b64_tr_b16 v[214:215], v246 offset:10240\n"
"v_mfma_f32_16x16x32_bf16 a[16:19], v[216:219], v[28:31], a[16:19]\n"
"s_waitcnt lgkmcnt(8)\n"
"ds_read_b64_tr_b16 v[216:217], v246 offset:12288\n"
"ds_read_b64_tr_b16 v[218:219], v246 offset:14336\n"
"v_mfma_f32_16x16x32_bf16 a[16:19], v[220:223], v[92:95], a[16:19]\n"
"s_waitcnt lgkmcnt(8)\n"
"buffer_store_dwordx4 v[196:199], v248, s[60:63], s67 offen sc1\n"
"v_mfma_f32_16x16x32_bf16 a[16:19], v[224:227], v[124:127], a[16:19]\n"
"s_add_u32 s67, s87, 0xe00000\n"
"v_accvgpr_read_b32 v196, a252\n"
"v_accvgpr_read_b32 v197, a253\n"
"v_mfma_f32_16x16x32_bf16 a[4:7], v[224:227], v[116:119], a[4:7]\n"
"ds_read_b64_tr_b16 v[224:225], v246 offset:16384\n"
"ds_read_b64_tr_b16 v[226:227], v246 offset:18432\n"
"v_mfma_f32_16x16x32_bf16 a[4:7], v[220:223], v[84:87], a[4:7]\n"
"s_waitcnt lgkmcnt(8)\n"
"ds_read_b64_tr_b16 v[220:221], v246 offset:20480\n"
"ds_read_b64_tr_b16 v[222:223], v246 offset:22528\n"
"v_mfma_f32_16x16x32_bf16 a[236:239], v[228:231], v[16:19], a[236:239]\n"
"s_waitcnt lgkmcnt(8)\n"
"buffer_load_dwordx4 v252, s[56:59], s93 offen offset:2048 lds\n"
"v_mfma_f32_16x16x32_bf16 a[236:239], v[232:235], v[48:51], a[236:239]\n"
"s_add_u32 s93, s27, 0xa300\n"
"v_accvgpr_read_b32 v198, a254\n"
"v_accvgpr_read_b32 v199, a255\n"
"v_mfma_f32_16x16x32_bf16 a[248:251], v[232:235], v[56:59], a[248:251]\n"
"ds_read_b64_tr_b16 v[232:233], v246 offset:24576\n"
"ds_read_b64_tr_b16 v[234:235], v246 offset:26624\n"
"v_mfma_f32_16x16x32_bf16 a[248:251], v[228:231], v[24:27], a[248:251]\n"
"s_waitcnt lgkmcnt(8)\n"
"ds_read_b64_tr_b16 v[228:229], v246 offset:28672\n"
"ds_read_b64_tr_b16 v[230:231], v246 offset:30720\n"
"v_mfma_f32_16x16x32_bf16 a[248:251], v[212:215], v[88:91], a[248:251]\n"
"s_waitcnt lgkmcnt(8)\n"
"buffer_store_dwordx4 v[200:203], v248, s[60:63], s68 offen sc1\n"
"v_mfma_f32_16x16x32_bf16 a[248:251], v[216:219], v[120:123], a[248:251]\n"
"s_add_u32 s68, s87, 0xf00000\n"
"v_accvgpr_read_b32 v200, a0\n"
"v_accvgpr_read_b32 v201, a1\n"
"v_mfma_f32_16x16x32_bf16 a[236:239], v[216:219], v[112:115], a[236:239]\n"
"ds_read_b64_tr_b16 v[216:217], v253 offset:0\n"
"ds_read_b64_tr_b16 v[218:219], v253 offset:2048\n"
"v_mfma_f32_16x16x32_bf16 a[236:239], v[212:215], v[80:83], a[236:239]\n"
"s_waitcnt lgkmcnt(8)\n"
"ds_read_b64_tr_b16 v[212:213], v253 offset:4096\n"
"ds_read_b64_tr_b16 v[214:215], v253 offset:6144\n"
"v_mfma_f32_16x16x32_bf16 a[236:239], v[224:227], v[20:23], a[236:239]\n"
"s_waitcnt lgkmcnt(8)\n"
"buffer_load_dwordx4 v252, s[56:59], s94 offen offset:3072 lds\n"
"v_mfma_f32_16x16x32_bf16 a[236:239], v[220:223], v[52:55], a[236:239]\n"
"s_add_u32 m0, s90, 0xe400\n"
"v_cvt_pk_bf16_f32 v196, v196, v197\n"
"v_accvgpr_read_b32 v202, a2\n"
"v_mfma_f32_16x16x32_bf16 a[248:251], v[220:223], v[60:63], a[248:251]\n"
"s_add_u32 s94, s27, 0xbb00\n"
"ds_read_b64_tr_b16 v[220:221], v253 offset:8192\n"
"ds_read_b64_tr_b16 v[222:223], v253 offset:10240\n"
"v_mfma_f32_16x16x32_bf16 a[248:251], v[224:227], v[28:31], a[248:251]\n"
"s_waitcnt lgkmcnt(8)\n"
"ds_read_b64_tr_b16 v[224:225], v253 offset:12288\n"
"ds_read_b64_tr_b16 v[226:227], v253 offset:14336\n"
"v_mfma_f32_16x16x32_bf16 a[248:251], v[232:235], v[92:95], a[248:251]\n"
"s_waitcnt lgkmcnt(8)\n"
"buffer_store_dwordx4 v[204:207], v248, s[60:63], s83 offen sc1\n"
"v_mfma_f32_16x16x32_bf16 a[248:251], v[228:231], v[124:127], a[248:251]\n"
"s_add_u32 s83, s87, 0xc00000\n"
"v_accvgpr_read_b32 v203, a3\n"
"v_cvt_pk_bf16_f32 v200, v200, v201\n"
"v_mfma_f32_16x16x32_bf16 a[236:239], v[228:231], v[116:119], a[236:239]\n"
"ds_read_b64_tr_b16 v[228:229], v253 offset:16384\n"
"ds_read_b64_tr_b16 v[230:231], v253 offset:18432\n"
"v_mfma_f32_16x16x32_bf16 a[236:239], v[232:235], v[84:87], a[236:239]\n"
"s_waitcnt lgkmcnt(8)\n"
"ds_read_b64_tr_b16 v[232:233], v253 offset:20480\n"
"ds_read_b64_tr_b16 v[234:235], v253 offset:22528\n"
"v_mfma_f32_16x16x32_bf16 a[220:223], v[216:219], v[16:19], a[220:223]\n"
"s_waitcnt lgkmcnt(8)\n"
"buffer_load_dwordx4 v252, s[56:59], s91 offen lds\n"
"v_mfma_f32_16x16x32_bf16 a[220:223], v[212:215], v[48:51], a[220:223]\n"
"v_cvt_pk_bf16_f32 v197, v198, v199\n"
"v_cvt_pk_bf16_f32 v201, v202, v203\n"
"v_mfma_f32_16x16x32_bf16 a[232:235], v[212:215], v[56:59], a[232:235]\n"
"v_accvgpr_read_b32 v194, a172\n"
"v_accvgpr_read_b32 v195, a173\n"
"v_mfma_f32_16x16x32_bf16 a[232:235], v[216:219], v[24:27], a[232:235]\n"
"v_accvgpr_read_b32 v198, a4\n"
"v_accvgpr_read_b32 v199, a5\n"
"v_mfma_f32_16x16x32_bf16 a[20:23], v[216:219], v[128:131], a[20:23]\n"
"ds_read_b64_tr_b16 v[216:217], v253 offset:24576\n"
"ds_read_b64_tr_b16 v[218:219], v253 offset:26624\n"
"v_mfma_f32_16x16x32_bf16 a[20:23], v[212:215], v[132:135], a[20:23]\n"
"s_waitcnt lgkmcnt(8)\n"
"ds_read_b64_tr_b16 v[212:213], v253 offset:28672\n"
"ds_read_b64_tr_b16 v[214:215], v253 offset:30720\n"
"v_mfma_f32_16x16x32_bf16 a[20:23], v[220:223], v[136:139], a[20:23]\n"
"s_waitcnt lgkmcnt(8)\n"
"v_accvgpr_read_b32 v202, a16\n"
"v_accvgpr_read_b32 v203, a17\n"
"v_mfma_f32_16x16x32_bf16 a[20:23], v[224:227], v[140:143], a[20:23]\n"
"v_cvt_pk_bf16_f32 v194, v194, v195\n"
"v_cvt_pk_bf16_f32 v198, v198, v199\n"
"v_mfma_f32_16x16x32_bf16 a[232:235], v[224:227], v[120:123], a[232:235]\n"
"buffer_load_dwordx4 v252, s[56:59], s92 offen offset:1024 lds\n"
"v_mfma_f32_16x16x32_bf16 a[232:235], v[220:223], v[88:91], a[232:235]\n"
"v_cvt_pk_bf16_f32 v202, v202, v203\n"
"v_accvgpr_read_b32 v208, a175\n"
"v_mfma_f32_16x16x32_bf16 a[220:223], v[220:223], v[80:83], a[220:223]\n"
"ds_read_b64_tr_b16 v[220:221], v254 offset:32768\n"
"ds_read_b64_tr_b16 v[222:223], v254 offset:34816\n"
"v_mfma_f32_16x16x32_bf16 a[220:223], v[224:227], v[112:115], a[220:223]\n"
"s_waitcnt lgkmcnt(8)\n"
"ds_read_b64_tr_b16 v[224:225], v254 offset:36864\n"
"ds_read_b64_tr_b16 v[226:227], v254 offset:38912\n"
"v_mfma_f32_16x16x32_bf16 a[220:223], v[228:231], v[20:23], a[220:223]\n"
"s_waitcnt lgkmcnt(8)\n"
"v_accvgpr_read_b32 v195, a174\n"
"v_accvgpr_read_b32 v209, a7\n"
"v_mfma_f32_16x16x32_bf16 a[220:223], v[232:235], v[52:55], a[220:223]\n"
"v_accvgpr_read_b32 v199, a6\n"
"v_accvgpr_read_b32 v210, a19\n"
"v_mfma_f32_16x16x32_bf16 a[232:235], v[232:235], v[60:63], a[232:235]\n"
"v_accvgpr_read_b32 v203, a18\n"
"v_accvgpr_read_b32 v211, a239\n"
"v_mfma_f32_16x16x32_bf16 a[232:235], v[228:231], v[28:31], a[232:235]\n"
"v_cvt_pk_bf16_f32 v195, v195, v208\n"
"v_cvt_pk_bf16_f32 v199, v199, v209\n"
"v_mfma_f32_16x16x32_bf16 a[20:23], v[228:231], v[144:147], a[20:23]\n"
"ds_read_b64_tr_b16 v[228:229], v254 offset:40960\n"
"ds_read_b64_tr_b16 v[230:231], v254 offset:43008\n"
"v_mfma_f32_16x16x32_bf16 a[20:23], v[232:235], v[148:151], a[20:23]\n"
"s_waitcnt lgkmcnt(8)\n"
"ds_read_b64_tr_b16 v[232:233], v254 offset:45056\n"
"ds_read_b64_tr_b16 v[234:235], v254 offset:47104\n"
"v_mfma_f32_16x16x32_bf16 a[20:23], v[216:219], v[152:155], a[20:23]\n"
"s_waitcnt lgkmcnt(8)\n"
"buffer_load_dwordx4 v252, s[56:59], s93 offen offset:2048 lds\n"
"v_mfma_f32_16x16x32_bf16 a[20:23], v[212:215], v[156:159], a[20:23]\n"
"v_cvt_pk_bf16_f32 v203, v203, v210\n"
"v_accvgpr_read_b32 v208, a251\n"
"v_mfma_f32_16x16x32_bf16 a[232:235], v[212:215], v[124:127], a[232:235]\n"
"v_mfma_f32_16x16x32_bf16 a[232:235], v[216:219], v[92:95], a[232:235]\n"
"buffer_store_dwordx4 v[192:195], v248, s[60:63], s84 offen sc1\n"
"v_mfma_f32_16x16x32_bf16 a[220:223], v[216:219], v[84:87], a[220:223]\n"
"s_add_u32 s84, s87, 0xd00000\n"
"ds_read_b64_tr_b16 v[216:217], v254 offset:49152\n"
"ds_read_b64_tr_b16 v[218:219], v254 offset:51200\n"
"v_mfma_f32_16x16x32_bf16 a[220:223], v[212:215], v[116:119], a[220:223]\n"
"s_waitcnt lgkmcnt(8)\n"
"ds_read_b64_tr_b16 v[212:213], v254 offset:53248\n"
"ds_read_b64_tr_b16 v[214:215], v254 offset:55296\n"
"v_mfma_f32_16x16x32_bf16 a[64:67], v[220:223], v[16:19], a[64:67]\n"
"s_waitcnt lgkmcnt(8)\n"
"buffer_load_dwordx4 v252, s[56:59], s94 offen offset:3072 lds\n"
"v_mfma_f32_16x16x32_bf16 a[64:67], v[224:227], v[48:51], a[64:67]\n"
"v_accvgpr_read_b32 v192, a232\n"
"v_accvgpr_read_b32 v204, a220\n"
"v_mfma_f32_16x16x32_bf16 a[68:71], v[224:227], v[56:59], a[68:71]\n"
"ds_read_b64_tr_b16 v[224:225], v254 offset:57344\n"
"ds_read_b64_tr_b16 v[226:227], v254 offset:59392\n"
"v_mfma_f32_16x16x32_bf16 a[68:71], v[220:223], v[24:27], a[68:71]\n"
"s_waitcnt lgkmcnt(8)\n"
"ds_read_b64_tr_b16 v[220:221], v254 offset:61440\n"
"ds_read_b64_tr_b16 v[222:223], v254 offset:63488\n"
"v_mfma_f32_16x16x32_bf16 a[68:71], v[228:231], v[88:91], a[68:71]\n"
"Lpada_11:\n"
"s_cbranch_execz Lpadxa\n"
"s_waitcnt lgkmcnt(8)\n"
"v_subrev_u32 v254, 0x12000, v254\n"
"v_accvgpr_read_b32 v205, a221\n"
"v_mfma_f32_16x16x32_bf16 a[68:71], v[232:235], v[120:123], a[68:71]\n"
"v_accvgpr_read_b32 v206, a222\n"
"v_accvgpr_read_b32 v207, a223\n"
"v_mfma_f32_16x16x32_bf16 a[64:67], v[232:235], v[112:115], a[64:67]\n"
"ds_read_b64_tr_b16 v[232:233], v249 offset:32768\n"
"ds_read_b64_tr_b16 v[234:235], v249 offset:34816\n"
"v_mfma_f32_16x16x32_bf16 a[64:67], v[228:231], v[80:83], a[64:67]\n"
"s_waitcnt lgkmcnt(8)\n"
"ds_read_b64_tr_b16 v[228:229], v249 offset:36864\n"
"ds_read_b64_tr_b16 v[230:231], v249 offset:38912\n"
"v_mfma_f32_16x16x32_bf16 a[64:67], v[216:219], v[20:23], a[64:67]\n"
"s_waitcnt lgkmcnt(8)\n"
"v_cvt_pk_bf16_f32 v204, v204, v205\n"
"v_cvt_pk_bf16_f32 v205, v206, v207\n"
"v_mfma_f32_16x16x32_bf16 a[64:67], v[212:215], v[52:55], a[64:67]\n"
"v_accvgpr_read_b32 v206, a236\n"
"v_accvgpr_read_b32 v207, a237\n"
"v_mfma_f32_16x16x32_bf16 a[68:71], v[212:215], v[60:63], a[68:71]\n"
"ds_read_b64_tr_b16 v[212:213], v249 offset:40960\n"
"ds_read_b64_tr_b16 v[214:215], v249 offset:43008\n"
"v_mfma_f32_16x16x32_bf16 a[68:71], v[216:219], v[28:31], a[68:71]\n"
"s_waitcnt lgkmcnt(8)\n"
"ds_read_b64_tr_b16 v[216:217], v249 offset:45056\n"
"ds_read_b64_tr_b16 v[218:219], v249 offset:47104\n"
"v_mfma_f32_16x16x32_bf16 a[68:71], v[224:227], v[92:95], a[68:71]\n"
"s_waitcnt lgkmcnt(8)\n"
"v_cvt_pk_bf16_f32 v206, v206, v207\n"
"v_accvgpr_read_b32 v207, a238\n"
"v_mfma_f32_16x16x32_bf16 a[68:71], v[220:223], v[124:127], a[68:71]\n"
"s_waitcnt vmcnt(13)\n"
"s_barrier\n"
"v_mfma_f32_16x16x32_bf16 a[64:67], v[220:223], v[116:119], a[64:67]\n"
"ds_read_b64_tr_b16 v[220:221], v249 offset:49152\n"
"ds_read_b64_tr_b16 v[222:223], v249 offset:51200\n"
"v_mfma_f32_16x16x32_bf16 a[64:67], v[224:227], v[84:87], a[64:67]\n"
"s_waitcnt lgkmcnt(8)\n"
"ds_read_b64_tr_b16 v[224:225], v249 offset:53248\n"
"ds_read_b64_tr_b16 v[226:227], v249 offset:55296\n"
"v_mfma_f32_16x16x32_bf16 a[80:83], v[232:235], v[16:19], a[80:83]\n"
"s_waitcnt lgkmcnt(8)\n"
"v_cvt_pk_bf16_f32 v207, v207, v211\n"
"v_accvgpr_read_b32 v193, a233\n"
"v_mfma_f32_16x16x32_bf16 a[80:83], v[228:231], v[48:51], a[80:83]\n"
"buffer_store_dwordx4 v[196:199], v248, s[60:63], s67 offen sc1\n"
"v_mfma_f32_16x16x32_bf16 a[84:87], v[228:231], v[56:59], a[84:87]\n"
"s_add_u32 s67, s87, 0x1d00000\n"
"ds_read_b64_tr_b16 v[228:229], v249 offset:57344\n"
"ds_read_b64_tr_b16 v[230:231], v249 offset:59392\n"
"v_mfma_f32_16x16x32_bf16 a[84:87], v[232:235], v[24:27], a[84:87]\n"
"s_waitcnt lgkmcnt(8)\n"
"ds_read_b64_tr_b16 v[232:233], v249 offset:61440\n"
"ds_read_b64_tr_b16 v[234:235], v249 offset:63488\n"
"v_mfma_f32_16x16x32_bf16 a[84:87], v[212:215], v[88:91], a[84:87]\n"
"s_waitcnt lgkmcnt(8)\n"
"v_subrev_u32 v249, 0x12000, v249\n"
"v_accvgpr_read_b32 v196, a216\n"
"v_mfma_f32_16x16x32_bf16 a[84:87], v[216:219], v[120:123], a[84:87]\n"
"v_accvgpr_read_b32 v197, a217\n"
"v_accvgpr_read_b32 v194, a234\n"
"v_mfma_f32_16x16x32_bf16 a[80:83], v[216:219], v[112:115], a[80:83]\n"
"ds_read_b64_tr_b16 v[216:217], v246 offset:32768\n"
"ds_read_b64_tr_b16 v[218:219], v246 offset:34816\n"
"v_mfma_f32_16x16x32_bf16 a[80:83], v[212:215], v[80:83], a[80:83]\n"
"s_waitcnt lgkmcnt(8)\n"
"ds_read_b64_tr_b16 v[212:213], v246 offset:36864\n"
"ds_read_b64_tr_b16 v[214:215], v246 offset:38912\n"
"v_mfma_f32_16x16x32_bf16 a[80:83], v[220:223], v[20:23], a[80:83]\n"
"s_waitcnt lgkmcnt(8)\n"
"v_accvgpr_read_b32 v195, a235\n"
"v_cvt_pk_bf16_f32 v192, v192, v193\n"
"v_mfma_f32_16x16x32_bf16 a[80:83], v[224:227], v[52:55], a[80:83]\n"
"buffer_store_dwordx4 v[200:203], v248, s[60:63], s68 offen sc1\n"
"v_mfma_f32_16x16x32_bf16 a[84:87], v[224:227], v[60:63], a[84:87]\n"
"s_add_u32 s68, s87, 0x1200000\n"
"ds_read_b64_tr_b16 v[224:225], v246 offset:40960\n"
"ds_read_b64_tr_b16 v[226:227], v246 offset:43008\n"
"v_mfma_f32_16x16x32_bf16 a[84:87], v[220:223], v[28:31], a[84:87]\n"
"s_waitcnt lgkmcnt(8)\n"
"ds_read_b64_tr_b16 v[220:221], v246 offset:45056\n"
"ds_read_b64_tr_b16 v[222:223], v246 offset:47104\n"
"v_mfma_f32_16x16x32_bf16 a[84:87], v[228:231], v[92:95], a[84:87]\n"
"s_waitcnt lgkmcnt(8)\n"
"v_accvgpr_read_b32 v200, a64\n"
"v_accvgpr_read_b32 v201, a65\n"
"v_mfma_f32_16x16x32_bf16 a[84:87], v[232:235], v[124:127], a[84:87]\n"
"v_accvgpr_read_b32 v198, a218\n"
"v_accvgpr_read_b32 v199, a219\n"
"v_mfma_f32_16x16x32_bf16 a[80:83], v[232:235], v[116:119], a[80:83]\n"
"ds_read_b64_tr_b16 v[232:233], v246 offset:49152\n"
"ds_read_b64_tr_b16 v[234:235], v246 offset:51200\n"
"v_mfma_f32_16x16x32_bf16 a[80:83], v[228:231], v[84:87], a[80:83]\n"
"s_waitcnt lgkmcnt(8)\n"
"ds_read_b64_tr_b16 v[228:229], v246 offset:53248\n"
"ds_read_b64_tr_b16 v[230:231], v246 offset:55296\n"
"v_mfma_f32_16x16x32_bf16 a[48:51], v[216:219], v[16:19], a[48:51]\n"
"s_waitcnt lgkmcnt(8)\n"
"v_cvt_pk_bf16_f32 v196, v196, v197\n"
"v_accvgpr_read_b32 v202, a66\n"
"v_mfma_f32_16x16x32_bf16 a[48:51], v[212:215], v[48:51], a[48:51]\n"
"buffer_store_dwordx4 v[204:207], v248, s[60:63], s83 offen sc1\n"
"v_mfma_f32_16x16x32_bf16 a[52:55], v[212:215], v[56:59], a[52:55]\n"
"s_add_u32 s83, s87, 0x1300000\n"
"ds_read_b64_tr_b16 v[212:213], v246 offset:57344\n"
"ds_read_b64_tr_b16 v[214:215], v246 offset:59392\n"
"v_mfma_f32_16x16x32_bf16 a[52:55], v[216:219], v[24:27], a[52:55]\n"
"s_waitcnt lgkmcnt(8)\n"
"ds_read_b64_tr_b16 v[216:217], v246 offset:61440\n"
"ds_read_b64_tr_b16 v[218:219], v246 offset:63488\n"
"v_mfma_f32_16x16x32_bf16 a[52:55], v[224:227], v[88:91], a[52:55]\n"
"s_waitcnt lgkmcnt(8)\n"
"v_subrev_u32 v246, 0x12000, v246\n"
"v_accvgpr_read_b32 v204, a68\n"
"v_mfma_f32_16x16x32_bf16 a[52:55], v[220:223], v[120:123], a[52:55]\n"
"v_accvgpr_read_b32 v205, a69\n"
"v_accvgpr_read_b32 v203, a67\n"
"v_mfma_f32_16x16x32_bf16 a[48:51], v[220:223], v[112:115], a[48:51]\n"
"ds_read_b64_tr_b16 v[220:221], v253 offset:32768\n"
"ds_read_b64_tr_b16 v[222:223], v253 offset:34816\n"
"v_mfma_f32_16x16x32_bf16 a[48:51], v[224:227], v[80:83], a[48:51]\n"
"s_waitcnt lgkmcnt(8)\n"
"ds_read_b64_tr_b16 v[224:225], v253 offset:36864\n"
"ds_read_b64_tr_b16 v[226:227], v253 offset:38912\n"
"v_mfma_f32_16x16x32_bf16 a[48:51], v[232:235], v[20:23], a[48:51]\n"
"s_waitcnt lgkmcnt(8)\n"
"v_cvt_pk_bf16_f32 v200, v200, v201\n"
"v_accvgpr_read_b32 v206, a70\n"
"v_mfma_f32_16x16x32_bf16 a[48:51], v[228:231], v[52:55], a[48:51]\n"
"v_accvgpr_read_b32 v207, a71\n"
"v_cvt_pk_bf16_f32 v204, v204, v205\n"
"v_mfma_f32_16x16x32_bf16 a[52:55], v[228:231], v[60:63], a[52:55]\n"
"ds_read_b64_tr_b16 v[228:229], v253 offset:40960\n"
"ds_read_b64_tr_b16 v[230:231], v253 offset:43008\n"
"v_mfma_f32_16x16x32_bf16 a[52:55], v[232:235], v[28:31], a[52:55]\n"
"s_waitcnt lgkmcnt(8)\n"
"ds_read_b64_tr_b16 v[232:233], v253 offset:45056\n"
"ds_read_b64_tr_b16 v[234:235], v253 offset:47104\n"
"v_mfma_f32_16x16x32_bf16 a[52:55], v[212:215], v[92:95], a[52:55]\n"
"s_waitcnt lgkmcnt(8)\n"
"v_cvt_pk_bf16_f32 v193, v194, v195\n"
"v_cvt_pk_bf16_f32 v197, v198, v199\n"
"v_mfma_f32_16x16x32_bf16 a[52:55], v[216:219], v[124:127], a[52:55]\n"
"v_cvt_pk_bf16_f32 v201, v202, v203\n"
"v_cvt_pk_bf16_f32 v205, v206, v207\n"
"v_mfma_f32_16x16x32_bf16 a[48:51], v[216:219], v[116:119], a[48:51]\n"
"ds_read_b64_tr_b16 v[216:217], v253 offset:49152\n"
"ds_read_b64_tr_b16 v[218:219], v253 offset:51200\n"
"v_mfma_f32_16x16x32_bf16 a[48:51], v[212:215], v[84:87], a[48:51]\n"
"s_waitcnt lgkmcnt(8)\n"
"ds_read_b64_tr_b16 v[212:213], v253 offset:53248\n"
"ds_read_b64_tr_b16 v[214:215], v253 offset:55296\n"
"v_mfma_f32_16x16x32_bf16 a[32:35], v[220:223], v[16:19], a[32:35]\n"
"s_waitcnt lgkmcnt(8)\n"
"v_accvgpr_read_b32 v194, a248\n"
"v_accvgpr_read_b32 v195, a249\n"
"v_mfma_f32_16x16x32_bf16 a[32:35], v[224:227], v[48:51], a[32:35]\n"
"v_accvgpr_read_b32 v198, a20\n"
"v_accvgpr_read_b32 v199, a21\n"
"v_mfma_f32_16x16x32_bf16 a[36:39], v[224:227], v[56:59], a[36:39]\n"
"v_accvgpr_read_b32 v202, a80\n"
"v_accvgpr_read_b32 v203, a81\n"
"v_mfma_f32_16x16x32_bf16 a[36:39], v[220:223], v[24:27], a[36:39]\n"
"v_accvgpr_read_b32 v206, a84\n"
"v_accvgpr_read_b32 v207, a85\n"
"v_mfma_f32_16x16x32_bf16 a[96:99], v[220:223], v[128:131], a[96:99]\n"
"ds_read_b64_tr_b16 v[220:221], v253 offset:57344\n"
"ds_read_b64_tr_b16 v[222:223], v253 offset:59392\n"
"v_mfma_f32_16x16x32_bf16 a[96:99], v[224:227], v[132:135], a[96:99]\n"
"s_waitcnt lgkmcnt(8)\n"
"ds_read_b64_tr_b16 v[224:225], v253 offset:61440\n"
"ds_read_b64_tr_b16 v[226:227], v253 offset:63488\n"
"v_mfma_f32_16x16x32_bf16 a[96:99], v[228:231], v[136:139], a[96:99]\n"
"s_waitcnt lgkmcnt(8)\n"
"v_subrev_u32 v253, 0x12000, v253\n"
"v_cvt_pk_bf16_f32 v194, v194, v195\n"
"v_mfma_f32_16x16x32_bf16 a[96:99], v[232:235], v[140:143], a[96:99]\n"
"v_cvt_pk_bf16_f32 v198, v198, v199\n"
"v_cvt_pk_bf16_f32 v202, v202, v203\n"
"v_mfma_f32_16x16x32_bf16 a[36:39], v[232:235], v[120:123], a[36:39]\n"
"v_cvt_pk_bf16_f32 v206, v206, v207\n"
"v_accvgpr_read_b32 v195, a250\n"
"v_mfma_f32_16x16x32_bf16 a[36:39], v[228:231], v[88:91], a[36:39]\n"
"v_accvgpr_read_b32 v209, a23\n"
"v_accvgpr_read_b32 v199, a22\n"
"v_mfma_f32_16x16x32_bf16 a[32:35], v[228:231], v[80:83], a[32:35]\n"
"ds_read_b64_tr_b16 v[228:229], v254 offset:0\n"
"ds_read_b64_tr_b16 v[230:231], v254 offset:2048\n"
"v_mfma_f32_16x16x32_bf16 a[32:35], v[232:235], v[112:115], a[32:35]\n"
"s_waitcnt lgkmcnt(8)\n"
"ds_read_b64_tr_b16 v[232:233], v254 offset:4096\n"
"ds_read_b64_tr_b16 v[234:235], v254 offset:6144\n"
"v_mfma_f32_16x16x32_bf16 a[32:35], v[216:219], v[20:23], a[32:35]\n"
"s_waitcnt lgkmcnt(8)\n"
"v_accvgpr_read_b32 v210, a83\n"
"v_accvgpr_read_b32 v203, a82\n"
"v_mfma_f32_16x16x32_bf16 a[32:35], v[212:215], v[52:55], a[32:35]\n"
"v_accvgpr_read_b32 v211, a87\n"
"v_accvgpr_read_b32 v207, a86\n"
"v_mfma_f32_16x16x32_bf16 a[36:39], v[212:215], v[60:63], a[36:39]\n"
"v_cvt_pk_bf16_f32 v195, v195, v208\n"
"v_cvt_pk_bf16_f32 v199, v199, v209\n"
"v_mfma_f32_16x16x32_bf16 a[36:39], v[216:219], v[28:31], a[36:39]\n"
"v_cvt_pk_bf16_f32 v203, v203, v210\n"
"v_cvt_pk_bf16_f32 v207, v207, v211\n"
"v_mfma_f32_16x16x32_bf16 a[96:99], v[216:219], v[144:147], a[96:99]\n"
"ds_read_b64_tr_b16 v[216:217], v254 offset:8192\n"
"ds_read_b64_tr_b16 v[218:219], v254 offset:10240\n"
"v_mfma_f32_16x16x32_bf16 a[96:99], v[212:215], v[148:151], a[96:99]\n"
"s_waitcnt lgkmcnt(8)\n"
"ds_read_b64_tr_b16 v[212:213], v254 offset:12288\n"
"ds_read_b64_tr_b16 v[214:215], v254 offset:14336\n"
"v_mfma_f32_16x16x32_bf16 a[96:99], v[220:223], v[152:155], a[96:99]\n"
"s_waitcnt lgkmcnt(8)\n"
"buffer_store_dwordx4 v[192:195], v248, s[60:63], s84 offen sc1\n"
"v_mfma_f32_16x16x32_bf16 a[96:99], v[224:227], v[156:159], a[96:99]\n"
"s_add_u32 s84, s87, 0x1000000\n"
"v_accvgpr_read_b32 v208, a51\n"
"v_accvgpr_read_b32 v209, a55\n"
"v_mfma_f32_16x16x32_bf16 a[36:39], v[224:227], v[124:127], a[36:39]\n"
"v_mfma_f32_16x16x32_bf16 a[36:39], v[220:223], v[92:95], a[36:39]\n"
"v_mfma_f32_16x16x32_bf16 a[32:35], v[220:223], v[84:87], a[32:35]\n"
"ds_read_b64_tr_b16 v[220:221], v254 offset:16384\n"
"ds_read_b64_tr_b16 v[222:223], v254 offset:18432\n"
"v_mfma_f32_16x16x32_bf16 a[32:35], v[224:227], v[116:119], a[32:35]\n"
"s_waitcnt lgkmcnt(8)\n"
"ds_read_b64_tr_b16 v[224:225], v254 offset:20480\n"
"ds_read_b64_tr_b16 v[226:227], v254 offset:22528\n"
"v_mfma_f32_16x16x32_bf16 a[132:135], v[228:231], v[16:19], a[132:135]\n"
"s_waitcnt lgkmcnt(8)\n"
"v_mfma_f32_16x16x32_bf16 a[132:135], v[232:235], v[48:51], a[132:135]\n"
"v_mfma_f32_16x16x32_bf16 a[144:147], v[232:235], v[56:59], a[144:147]\n"
"ds_read_b64_tr_b16 v[232:233], v254 offset:24576\n"
"ds_read_b64_tr_b16 v[234:235], v254 offset:26624\n"
"v_mfma_f32_16x16x32_bf16 a[144:147], v[228:231], v[24:27], a[144:147]\n"
"s_waitcnt lgkmcnt(8)\n"
"ds_read_b64_tr_b16 v[228:229], v254 offset:28672\n"
"ds_read_b64_tr_b16 v[230:231], v254 offset:30720\n"
"v_mfma_f32_16x16x32_bf16 a[144:147], v[216:219], v[88:91], a[144:147]\n"
"s_waitcnt lgkmcnt(8)\n"
"buffer_store_dwordx4 v[196:199], v248, s[60:63], s67 offen sc1\n"
"v_mfma_f32_16x16x32_bf16 a[144:147], v[212:215], v[120:123], a[144:147]\n"
"s_add_u32 s67, s87, 0x1100000\n"
"v_accvgpr_read_b32 v192, a32\n"
"v_accvgpr_read_b32 v193, a33\n"
"v_mfma_f32_16x16x32_bf16 a[132:135], v[212:215], v[112:115], a[132:135]\n"
"ds_read_b64_tr_b16 v[212:213], v249 offset:0\n"
"ds_read_b64_tr_b16 v[214:215], v249 offset:2048\n"
"v_mfma_f32_16x16x32_bf16 a[132:135], v[216:219], v[80:83], a[132:135]\n"
"s_waitcnt lgkmcnt(8)\n"
"ds_read_b64_tr_b16 v[216:217], v249 offset:4096\n"
"ds_read_b64_tr_b16 v[218:219], v249 offset:6144\n"
"v_mfma_f32_16x16x32_bf16 a[132:135], v[220:223], v[20:23], a[132:135]\n"
"s_waitcnt lgkmcnt(8)\n"
"v_accvgpr_read_b32 v196, a36\n"
"v_accvgpr_read_b32 v197, a37\n"
"v_mfma_f32_16x16x32_bf16 a[132:135], v[224:227], v[52:55], a[132:135]\n"
"v_accvgpr_read_b32 v194, a34\n"
"v_accvgpr_read_b32 v195, a35\n"
"v_mfma_f32_16x16x32_bf16 a[144:147], v[224:227], v[60:63], a[144:147]\n"
"ds_read_b64_tr_b16 v[224:225], v249 offset:8192\n"
"ds_read_b64_tr_b16 v[226:227], v249 offset:10240\n"
"v_mfma_f32_16x16x32_bf16 a[144:147], v[220:223], v[28:31], a[144:147]\n"
"s_waitcnt lgkmcnt(8)\n"
"ds_read_b64_tr_b16 v[220:221], v249 offset:12288\n"
"ds_read_b64_tr_b16 v[222:223], v249 offset:14336\n"
"v_mfma_f32_16x16x32_bf16 a[144:147], v[232:235], v[92:95], a[144:147]\n"
"s_waitcnt lgkmcnt(8)\n"
"buffer_store_dwordx4 v[200:203], v248, s[60:63], s68 offen sc1\n"
"v_mfma_f32_16x16x32_bf16 a[144:147], v[228:231], v[124:127], a[144:147]\n"
"s_add_u32 s68, s87, 0x1600000\n"
"v_cvt_pk_bf16_f32 v192, v192, v193\n"
"v_accvgpr_read_b32 v198, a38\n"
"v_mfma_f32_16x16x32_bf16 a[132:135], v[228:231], v[116:119], a[132:135]\n"
"ds_read_b64_tr_b16 v[228:229], v249 offset:16384\n"
"ds_read_b64_tr_b16 v[230:231], v249 offset:18432\n"
"v_mfma_f32_16x16x32_bf16 a[132:135], v[232:235], v[84:87], a[132:135]\n"
"s_waitcnt lgkmcnt(8)\n"
"ds_read_b64_tr_b16 v[232:233], v249 offset:20480\n"
"ds_read_b64_tr_b16 v[234:235], v249 offset:22528\n"
"v_mfma_f32_16x16x32_bf16 a[148:151], v[212:215], v[16:19], a[148:151]\n"
"s_waitcnt lgkmcnt(8)\n"
"v_accvgpr_read_b32 v199, a39\n"
"v_cvt_pk_bf16_f32 v196, v196, v197\n"
"v_mfma_f32_16x16x32_bf16 a[148:151], v[216:219], v[48:51], a[148:151]\n"
"s_waitcnt vmcnt(6)\n"
"s_barrier\n"
"v_mfma_f32_16x16x32_bf16 a[160:163], v[216:219], v[56:59], a[160:163]\n"
"ds_read_b64_tr_b16 v[216:217], v249 offset:24576\n"
"ds_read_b64_tr_b16 v[218:219], v249 offset:26624\n"
"v_mfma_f32_16x16x32_bf16 a[160:163], v[212:215], v[24:27], a[160:163]\n"
"s_waitcnt lgkmcnt(8)\n"
"ds_read_b64_tr_b16 v[212:213], v249 offset:28672\n"
"ds_read_b64_tr_b16 v[214:215], v249 offset:30720\n"
"v_mfma_f32_16x16x32_bf16 a[160:163], v[224:227], v[88:91], a[160:163]\n"
"s_waitcnt lgkmcnt(8)\n"
"buffer_store_dwordx4 v[204:207], v248, s[60:63], s83 offen sc1\n"
"v_mfma_f32_16x16x32_bf16 a[160:163], v[220:223], v[120:123], a[160:163]\n"
"s_add_u32 s83, s87, 0x1700000\n"
"v_accvgpr_read_b32 v200, a132\n"
"v_accvgpr_read_b32 v201, a133\n"
"v_mfma_f32_16x16x32_bf16 a[148:151], v[220:223], v[112:115], a[148:151]\n"
"ds_read_b64_tr_b16 v[220:221], v246 offset:0\n"
"ds_read_b64_tr_b16 v[222:223], v246 offset:2048\n"
"v_mfma_f32_16x16x32_bf16 a[148:151], v[224:227], v[80:83], a[148:151]\n"
"s_waitcnt lgkmcnt(8)\n"
"ds_read_b64_tr_b16 v[224:225], v246 offset:4096\n"
"ds_read_b64_tr_b16 v[226:227], v246 offset:6144\n"
"v_mfma_f32_16x16x32_bf16 a[148:151], v[228:231], v[20:23], a[148:151]\n"
"s_waitcnt lgkmcnt(8)\n"
"v_accvgpr_read_b32 v204, a144\n"
"v_accvgpr_read_b32 v205, a145\n"
"v_mfma_f32_16x16x32_bf16 a[148:151], v[232:235], v[52:55], a[148:151]\n"
"v_accvgpr_read_b32 v202, a134\n"
"v_accvgpr_read_b32 v203, a135\n"
"v_mfma_f32_16x16x32_bf16 a[160:163], v[232:235], v[60:63], a[160:163]\n"
"ds_read_b64_tr_b16 v[232:233], v246 offset:8192\n"
"ds_read_b64_tr_b16 v[234:235], v246 offset:10240\n"
"v_mfma_f32_16x16x32_bf16 a[160:163], v[228:231], v[28:31], a[160:163]\n"
"s_waitcnt lgkmcnt(8)\n"
"ds_read_b64_tr_b16 v[228:229], v246 offset:12288\n"
"ds_read_b64_tr_b16 v[230:231], v246 offset:14336\n"
"v_mfma_f32_16x16x32_bf16 a[160:163], v[216:219], v[92:95], a[160:163]\n"
"s_waitcnt lgkmcnt(8)\n"
"v_cvt_pk_bf16_f32 v200, v200, v201\n"
"v_accvgpr_read_b32 v206, a146\n"
"v_mfma_f32_16x16x32_bf16 a[160:163], v[212:215], v[124:127], a[160:163]\n"
"v_accvgpr_read_b32 v207, a147\n"
"v_cvt_pk_bf16_f32 v204, v204, v205\n"
"v_mfma_f32_16x16x32_bf16 a[148:151], v[212:215], v[116:119], a[148:151]\n"
"ds_read_b64_tr_b16 v[212:213], v246 offset:16384\n"
"ds_read_b64_tr_b16 v[214:215], v246 offset:18432\n"
"v_mfma_f32_16x16x32_bf16 a[148:151], v[216:219], v[84:87], a[148:151]\n"
"s_waitcnt lgkmcnt(8)\n"
"ds_read_b64_tr_b16 v[216:217], v246 offset:20480\n"
"ds_read_b64_tr_b16 v[218:219], v246 offset:22528\n"
"v_mfma_f32_16x16x32_bf16 a[116:119], v[220:223], v[16:19], a[116:119]\n"
"s_waitcnt lgkmcnt(8)\n"
"v_cvt_pk_bf16_f32 v193, v194, v195\n"
"v_cvt_pk_bf16_f32 v197, v198, v199\n"
"v_mfma_f32_16x16x32_bf16 a[116:119], v[224:227], v[48:51], a[116:119]\n"
"v_cvt_pk_bf16_f32 v201, v202, v203\n"
"v_cvt_pk_bf16_f32 v205, v206, v207\n"
"v_mfma_f32_16x16x32_bf16 a[128:131], v[224:227], v[56:59], a[128:131]\n"
"ds_read_b64_tr_b16 v[224:225], v246 offset:24576\n"
"ds_read_b64_tr_b16 v[226:227], v246 offset:26624\n"
"v_mfma_f32_16x16x32_bf16 a[128:131], v[220:223], v[24:27], a[128:131]\n"
"s_waitcnt lgkmcnt(8)\n"
"ds_read_b64_tr_b16 v[220:221], v246 offset:28672\n"
"ds_read_b64_tr_b16 v[222:223], v246 offset:30720\n"
"v_mfma_f32_16x16x32_bf16 a[128:131], v[232:235], v[88:91], a[128:131]\n"
"s_waitcnt lgkmcnt(8)\n"
"v_accvgpr_read_b32 v194, a48\n"
"v_accvgpr_read_b32 v195, a49\n"
"v_mfma_f32_16x16x32_bf16 a[128:131], v[228:231], v[120:123], a[128:131]\n"
"v_accvgpr_read_b32 v198, a52\n"
"v_accvgpr_read_b32 v199, a53\n"
"v_mfma_f32_16x16x32_bf16 a[116:119], v[228:231], v[112:115], a[116:119]\n"
"ds_read_b64_tr_b16 v[228:229], v253 offset:0\n"
"ds_read_b64_tr_b16 v[230:231], v253 offset:2048\n"
"v_mfma_f32_16x16x32_bf16 a[116:119], v[232:235], v[80:83], a[116:119]\n"
"s_waitcnt lgkmcnt(8)\n"
"ds_read_b64_tr_b16 v[232:233], v253 offset:4096\n"
"ds_read_b64_tr_b16 v[234:235], v253 offset:6144\n"
"v_mfma_f32_16x16x32_bf16 a[116:119], v[212:215], v[20:23], a[116:119]\n"
"s_waitcnt lgkmcnt(8)\n"
"v_accvgpr_read_b32 v202, a148\n"
"v_accvgpr_read_b32 v203, a149\n"
"v_mfma_f32_16x16x32_bf16 a[116:119], v[216:219], v[52:55], a[116:119]\n"
"v_accvgpr_read_b32 v206, a160\n"
"v_accvgpr_read_b32 v207, a161\n"
"v_mfma_f32_16x16x32_bf16 a[128:131], v[216:219], v[60:63], a[128:131]\n"
"ds_read_b64_tr_b16 v[216:217], v253 offset:8192\n"
"ds_read_b64_tr_b16 v[218:219], v253 offset:10240\n"
"v_mfma_f32_16x16x32_bf16 a[128:131], v[212:215], v[28:31], a[128:131]\n"
"s_waitcnt lgkmcnt(8)\n"
"ds_read_b64_tr_b16 v[212:213], v253 offset:12288\n"
"ds_read_b64_tr_b16 v[214:215], v253 offset:14336\n"
"v_mfma_f32_16x16x32_bf16 a[128:131], v[224:227], v[92:95], a[128:131]\n"
"s_waitcnt lgkmcnt(8)\n"
"v_cvt_pk_bf16_f32 v194, v194, v195\n"
"v_cvt_pk_bf16_f32 v198, v198, v199\n"
"v_mfma_f32_16x16x32_bf16 a[128:131], v[220:223], v[124:127], a[128:131]\n"
"v_cvt_pk_bf16_f32 v202, v202, v203\n"
"v_cvt_pk_bf16_f32 v206, v206, v207\n"
"v_mfma_f32_16x16x32_bf16 a[116:119], v[220:223], v[116:119], a[116:119]\n"
"ds_read_b64_tr_b16 v[220:221], v253 offset:16384\n"
"ds_read_b64_tr_b16 v[222:223], v253 offset:18432\n"
"v_mfma_f32_16x16x32_bf16 a[116:119], v[224:227], v[84:87], a[116:119]\n"
"s_waitcnt lgkmcnt(8)\n"
"ds_read_b64_tr_b16 v[224:225], v253 offset:20480\n"
"ds_read_b64_tr_b16 v[226:227], v253 offset:22528\n"
"v_mfma_f32_16x16x32_bf16 a[100:103], v[228:231], v[16:19], a[100:103]\n"
"s_waitcnt lgkmcnt(8)\n"
"v_accvgpr_read_b32 v195, a50\n"
"v_accvgpr_read_b32 v199, a54\n"
"v_mfma_f32_16x16x32_bf16 a[100:103], v[232:235], v[48:51], a[100:103]\n"
"v_accvgpr_read_b32 v210, a151\n"
"v_accvgpr_read_b32 v203, a150\n"
"v_mfma_f32_16x16x32_bf16 a[112:115], v[232:235], v[56:59], a[112:115]\n"
"v_accvgpr_read_b32 v211, a163\n"
"v_accvgpr_read_b32 v207, a162\n"
"v_mfma_f32_16x16x32_bf16 a[112:115], v[228:231], v[24:27], a[112:115]\n"
"v_cvt_pk_bf16_f32 v195, v195, v208\n"
"v_cvt_pk_bf16_f32 v199, v199, v209\n"
"v_mfma_f32_16x16x32_bf16 a[164:167], v[228:231], v[128:131], a[164:167]\n"
"ds_read_b64_tr_b16 v[228:229], v253 offset:24576\n"
"ds_read_b64_tr_b16 v[230:231], v253 offset:26624\n"
"v_mfma_f32_16x16x32_bf16 a[164:167], v[232:235], v[132:135], a[164:167]\n"
"s_waitcnt lgkmcnt(8)\n"
"ds_read_b64_tr_b16 v[232:233], v253 offset:28672\n"
"ds_read_b64_tr_b16 v[234:235], v253 offset:30720\n"
"v_mfma_f32_16x16x32_bf16 a[164:167], v[216:219], v[136:139], a[164:167]\n"
"s_waitcnt lgkmcnt(8)\n"
"v_cvt_pk_bf16_f32 v203, v203, v210\n"
"v_cvt_pk_bf16_f32 v207, v207, v211\n"
"v_mfma_f32_16x16x32_bf16 a[164:167], v[212:215], v[140:143], a[164:167]\n"
"buffer_store_dwordx4 v[192:195], v248, s[60:63], s84 offen sc1\n"
"v_mfma_f32_16x16x32_bf16 a[112:115], v[212:215], v[120:123], a[112:115]\n"
"s_add_u32 s84, s87, 0x1400000\n"
"v_accvgpr_read_b32 v208, a119\n"
"v_accvgpr_read_b32 v209, a131\n"
"v_mfma_f32_16x16x32_bf16 a[112:115], v[216:219], v[88:91], a[112:115]\n"
"v_mfma_f32_16x16x32_bf16 a[100:103], v[216:219], v[80:83], a[100:103]\n"
"ds_read_b64_tr_b16 v[216:217], v254 offset:32768\n"
"ds_read_b64_tr_b16 v[218:219], v254 offset:34816\n"
"v_mfma_f32_16x16x32_bf16 a[100:103], v[212:215], v[112:115], a[100:103]\n"
"s_waitcnt lgkmcnt(8)\n"
"ds_read_b64_tr_b16 v[212:213], v254 offset:36864\n"
"ds_read_b64_tr_b16 v[214:215], v254 offset:38912\n"
"v_mfma_f32_16x16x32_bf16 a[100:103], v[220:223], v[20:23], a[100:103]\n"
"s_waitcnt lgkmcnt(8)\n"
"v_mfma_f32_16x16x32_bf16 a[100:103], v[224:227], v[52:55], a[100:103]\n"
"v_mfma_f32_16x16x32_bf16 a[112:115], v[224:227], v[60:63], a[112:115]\n"
"v_mfma_f32_16x16x32_bf16 a[112:115], v[220:223], v[28:31], a[112:115]\n"
"buffer_store_dwordx4 v[196:199], v248, s[60:63], s67 offen sc1\n"
"v_mfma_f32_16x16x32_bf16 a[164:167], v[220:223], v[144:147], a[164:167]\n"
"s_add_u32 s67, s87, 0x1500000\n"
"ds_read_b64_tr_b16 v[220:221], v254 offset:40960\n"
"ds_read_b64_tr_b16 v[222:223], v254 offset:43008\n"
"v_mfma_f32_16x16x32_bf16 a[164:167], v[224:227], v[148:151], a[164:167]\n"
"s_waitcnt lgkmcnt(8)\n"
"ds_read_b64_tr_b16 v[224:225], v254 offset:45056\n"
"ds_read_b64_tr_b16 v[226:227], v254 offset:47104\n"
"v_mfma_f32_16x16x32_bf16 a[164:167], v[228:231], v[152:155], a[164:167]\n"
"s_waitcnt lgkmcnt(8)\n"
"v_mfma_f32_16x16x32_bf16 a[164:167], v[232:235], v[156:159], a[164:167]\n"
"v_mfma_f32_16x16x32_bf16 a[112:115], v[232:235], v[124:127], a[112:115]\n"
"v_mfma_f32_16x16x32_bf16 a[112:115], v[228:231], v[92:95], a[112:115]\n"
"v_mfma_f32_16x16x32_bf16 a[100:103], v[228:231], v[84:87], a[100:103]\n"
"ds_read_b64_tr_b16 v[228:229], v254 offset:49152\n"
"ds_read_b64_tr_b16 v[230:231], v254 offset:51200\n"
"v_mfma_f32_16x16x32_bf16 a[100:103], v[232:235], v[116:119], a[100:103]\n"
"s_waitcnt lgkmcnt(8)\n"
"ds_read_b64_tr_b16 v[232:233], v254 offset:53248\n"
"ds_read_b64_tr_b16 v[234:235], v254 offset:55296\n"
"v_mfma_f32_16x16x32_bf16 a[208:211], v[216:219], v[16:19], a[208:211]\n"
"s_waitcnt lgkmcnt(8)\n"
"buffer_store_dwordx4 v[200:203], v248, s[60:63], s68 offen sc1\n"
"v_mfma_f32_16x16x32_bf16 a[208:211], v[212:215], v[48:51], a[208:211]\n"
"s_add_u32 s68, s87, 0x1e00000\n"
"v_accvgpr_read_b32 v192, a100\n"
"v_accvgpr_read_b32 v193, a101\n"
"v_mfma_f32_16x16x32_bf16 a[212:215], v[212:215], v[56:59], a[212:215]\n"
"ds_read_b64_tr_b16 v[212:213], v254 offset:57344\n"
"ds_read_b64_tr_b16 v[214:215], v254 offset:59392\n"
"v_mfma_f32_16x16x32_bf16 a[212:215], v[216:219], v[24:27], a[212:215]\n"
"s_waitcnt lgkmcnt(8)\n"
"ds_read_b64_tr_b16 v[216:217], v254 offset:61440\n"
"ds_read_b64_tr_b16 v[218:219], v254 offset:63488\n"
"v_mfma_f32_16x16x32_bf16 a[212:215], v[220:223], v[88:91], a[212:215]\n"
"s_waitcnt lgkmcnt(8)\n"
"v_accvgpr_read_b32 v196, a112\n"
"v_accvgpr_read_b32 v197, a113\n"
"v_mfma_f32_16x16x32_bf16 a[212:215], v[224:227], v[120:123], a[212:215]\n"
"v_accvgpr_read_b32 v200, a96\n"
"v_accvgpr_read_b32 v201, a97\n"
"v_mfma_f32_16x16x32_bf16 a[208:211], v[224:227], v[112:115], a[208:211]\n"
"ds_read_b64_tr_b16 v[224:225], v249 offset:32768\n"
"ds_read_b64_tr_b16 v[226:227], v249 offset:34816\n"
"v_mfma_f32_16x16x32_bf16 a[208:211], v[220:223], v[80:83], a[208:211]\n"
"s_waitcnt lgkmcnt(8)\n"
"ds_read_b64_tr_b16 v[220:221], v249 offset:36864\n"
"ds_read_b64_tr_b16 v[222:223], v249 offset:38912\n"
"v_mfma_f32_16x16x32_bf16 a[208:211], v[228:231], v[20:23], a[208:211]\n"
"s_waitcnt lgkmcnt(8)\n"
"buffer_store_dwordx4 v[204:207], v248, s[60:63], s83 offen sc1\n"
"v_mfma_f32_16x16x32_bf16 a[208:211], v[232:235], v[52:55], a[208:211]\n"
"s_add_u32 s83, s87, 0x1a00000\n"
"v_accvgpr_read_b32 v194, a102\n"
"v_accvgpr_read_b32 v195, a103\n"
"v_mfma_f32_16x16x32_bf16 a[212:215], v[232:235], v[60:63], a[212:215]\n"
"ds_read_b64_tr_b16 v[232:233], v249 offset:40960\n"
"ds_read_b64_tr_b16 v[234:235], v249 offset:43008\n"
"v_mfma_f32_16x16x32_bf16 a[212:215], v[228:231], v[28:31], a[212:215]\n"
"s_waitcnt lgkmcnt(8)\n"
"ds_read_b64_tr_b16 v[228:229], v249 offset:45056\n"
"ds_read_b64_tr_b16 v[230:231], v249 offset:47104\n"
"v_mfma_f32_16x16x32_bf16 a[212:215], v[212:215], v[92:95], a[212:215]\n"
"s_waitcnt lgkmcnt(8)\n"
"v_cvt_pk_bf16_f32 v192, v192, v193\n"
"v_accvgpr_read_b32 v198, a114\n"
"v_mfma_f32_16x16x32_bf16 a[212:215], v[216:219], v[124:127], a[212:215]\n"
"v_accvgpr_read_b32 v199, a115\n"
"v_cvt_pk_bf16_f32 v196, v196, v197\n"
"v_mfma_f32_16x16x32_bf16 a[208:211], v[216:219], v[116:119], a[208:211]\n"
"ds_read_b64_tr_b16 v[216:217], v249 offset:49152\n"
"ds_read_b64_tr_b16 v[218:219], v249 offset:51200\n"
"v_mfma_f32_16x16x32_bf16 a[208:211], v[212:215], v[84:87], a[208:211]\n"
"s_waitcnt lgkmcnt(8)\n"
"ds_read_b64_tr_b16 v[212:213], v249 offset:53248\n"
"ds_read_b64_tr_b16 v[214:215], v249 offset:55296\n"
"v_mfma_f32_16x16x32_bf16 a[224:227], v[224:227], v[16:19], a[224:227]\n"
"s_waitcnt lgkmcnt(8)\n"
"v_accvgpr_read_b32 v202, a98\n"
"v_accvgpr_read_b32 v203, a99\n"
"v_mfma_f32_16x16x32_bf16 a[224:227], v[220:223], v[48:51], a[224:227]\n"
"v_accvgpr_read_b32 v204, a208\n"
"v_accvgpr_read_b32 v205, a209\n"
"v_mfma_f32_16x16x32_bf16 a[228:231], v[220:223], v[56:59], a[228:231]\n"
"ds_read_b64_tr_b16 v[220:221], v249 offset:57344\n"
"ds_read_b64_tr_b16 v[222:223], v249 offset:59392\n"
"v_mfma_f32_16x16x32_bf16 a[228:231], v[224:227], v[24:27], a[228:231]\n"
"s_waitcnt lgkmcnt(8)\n"
"ds_read_b64_tr_b16 v[224:225], v249 offset:61440\n"
"ds_read_b64_tr_b16 v[226:227], v249 offset:63488\n"
"v_mfma_f32_16x16x32_bf16 a[228:231], v[232:235], v[88:91], a[228:231]\n"
"s_waitcnt lgkmcnt(8)\n"
"v_cvt_pk_bf16_f32 v200, v200, v201\n"
"v_accvgpr_read_b32 v206, a210\n"
"v_mfma_f32_16x16x32_bf16 a[228:231], v[228:231], v[120:123], a[228:231]\n"
"v_accvgpr_read_b32 v207, a211\n"
"v_cvt_pk_bf16_f32 v204, v204, v205\n"
"v_mfma_f32_16x16x32_bf16 a[224:227], v[228:231], v[112:115], a[224:227]\n"
"ds_read_b64_tr_b16 v[228:229], v246 offset:32768\n"
"ds_read_b64_tr_b16 v[230:231], v246 offset:34816\n"
"v_mfma_f32_16x16x32_bf16 a[224:227], v[232:235], v[80:83], a[224:227]\n"
"s_waitcnt lgkmcnt(8)\n"
"ds_read_b64_tr_b16 v[232:233], v246 offset:36864\n"
"ds_read_b64_tr_b16 v[234:235], v246 offset:38912\n"
"v_mfma_f32_16x16x32_bf16 a[224:227], v[216:219], v[20:23], a[224:227]\n"
"s_waitcnt lgkmcnt(8)\n"
"v_cvt_pk_bf16_f32 v193, v194, v195\n"
"v_cvt_pk_bf16_f32 v197, v198, v199\n"
"v_mfma_f32_16x16x32_bf16 a[224:227], v[212:215], v[52:55], a[224:227]\n"
"v_cvt_pk_bf16_f32 v201, v202, v203\n"
"v_cvt_pk_bf16_f32 v205, v206, v207\n"
"v_mfma_f32_16x16x32_bf16 a[228:231], v[212:215], v[60:63], a[228:231]\n"
"Lpada_12:\n"
"s_cbranch_execz Lpadxa\n"
"ds_read_b64_tr_b16 v[212:213], v246 offset:40960\n"
"ds_read_b64_tr_b16 v[214:215], v246 offset:43008\n"
"v_mfma_f32_16x16x32_bf16 a[228:231], v[216:219], v[28:31], a[228:231]\n"
"s_waitcnt lgkmcnt(8)\n"
"ds_read_b64_tr_b16 v[216:217], v246 offset:45056\n"
"ds_read_b64_tr_b16 v[218:219], v246 offset:47104\n"
"v_mfma_f32_16x16x32_bf16 a[228:231], v[220:223], v[92:95], a[228:231]\n"
"s_waitcnt lgkmcnt(8)\n"
"v_accvgpr_read_b32 v194, a116\n"
"v_accvgpr_read_b32 v195, a117\n"
"v_mfma_f32_16x16x32_bf16 a[228:231], v[224:227], v[124:127], a[228:231]\n"
"v_accvgpr_read_b32 v198, a128\n"
"v_accvgpr_read_b32 v199, a129\n"
"v_mfma_f32_16x16x32_bf16 a[224:227], v[224:227], v[116:119], a[224:227]\n"
"ds_read_b64_tr_b16 v[224:225], v246 offset:49152\n"
"ds_read_b64_tr_b16 v[226:227], v246 offset:51200\n"
"v_mfma_f32_16x16x32_bf16 a[224:227], v[220:223], v[84:87], a[224:227]\n"
"s_waitcnt lgkmcnt(8)\n"
"ds_read_b64_tr_b16 v[220:221], v246 offset:53248\n"
"ds_read_b64_tr_b16 v[222:223], v246 offset:55296\n"
"v_mfma_f32_16x16x32_bf16 a[192:195], v[228:231], v[16:19], a[192:195]\n"
"s_waitcnt lgkmcnt(8)\n"
"v_accvgpr_read_b32 v202, a164\n"
"v_accvgpr_read_b32 v203, a165\n"
"v_mfma_f32_16x16x32_bf16 a[192:195], v[232:235], v[48:51], a[192:195]\n"
"v_accvgpr_read_b32 v206, a224\n"
"v_accvgpr_read_b32 v207, a225\n"
"v_mfma_f32_16x16x32_bf16 a[196:199], v[232:235], v[56:59], a[196:199]\n"
"ds_read_b64_tr_b16 v[232:233], v246 offset:57344\n"
"ds_read_b64_tr_b16 v[234:235], v246 offset:59392\n"
"v_mfma_f32_16x16x32_bf16 a[196:199], v[228:231], v[24:27], a[196:199]\n"
"s_waitcnt lgkmcnt(8)\n"
"ds_read_b64_tr_b16 v[228:229], v246 offset:61440\n"
"ds_read_b64_tr_b16 v[230:231], v246 offset:63488\n"
"v_mfma_f32_16x16x32_bf16 a[196:199], v[212:215], v[88:91], a[196:199]\n"
"s_waitcnt lgkmcnt(8)\n"
"v_cvt_pk_bf16_f32 v194, v194, v195\n"
"v_cvt_pk_bf16_f32 v198, v198, v199\n"
"v_mfma_f32_16x16x32_bf16 a[196:199], v[216:219], v[120:123], a[196:199]\n"
"v_cvt_pk_bf16_f32 v202, v202, v203\n"
"v_cvt_pk_bf16_f32 v206, v206, v207\n"
"v_mfma_f32_16x16x32_bf16 a[192:195], v[216:219], v[112:115], a[192:195]\n"
"ds_read_b64_tr_b16 v[216:217], v253 offset:32768\n"
"ds_read_b64_tr_b16 v[218:219], v253 offset:34816\n"
"v_mfma_f32_16x16x32_bf16 a[192:195], v[212:215], v[80:83], a[192:195]\n"
"s_waitcnt lgkmcnt(8)\n"
"ds_read_b64_tr_b16 v[212:213], v253 offset:36864\n"
"ds_read_b64_tr_b16 v[214:215], v253 offset:38912\n"
"v_mfma_f32_16x16x32_bf16 a[192:195], v[224:227], v[20:23], a[192:195]\n"
"s_waitcnt lgkmcnt(8)\n"
"v_accvgpr_read_b32 v195, a118\n"
"v_accvgpr_read_b32 v199, a130\n"
"v_mfma_f32_16x16x32_bf16 a[192:195], v[220:223], v[52:55], a[192:195]\n"
"v_accvgpr_read_b32 v210, a167\n"
"v_accvgpr_read_b32 v203, a166\n"
"v_mfma_f32_16x16x32_bf16 a[196:199], v[220:223], v[60:63], a[196:199]\n"
"ds_read_b64_tr_b16 v[220:221], v253 offset:40960\n"
"ds_read_b64_tr_b16 v[222:223], v253 offset:43008\n"
"v_mfma_f32_16x16x32_bf16 a[196:199], v[224:227], v[28:31], a[196:199]\n"
"s_waitcnt lgkmcnt(8)\n"
"ds_read_b64_tr_b16 v[224:225], v253 offset:45056\n"
"ds_read_b64_tr_b16 v[226:227], v253 offset:47104\n"
"v_mfma_f32_16x16x32_bf16 a[196:199], v[232:235], v[92:95], a[196:199]\n"
"s_waitcnt lgkmcnt(8)\n"
"v_accvgpr_read_b32 v211, a227\n"
"v_accvgpr_read_b32 v207, a226\n"
"v_mfma_f32_16x16x32_bf16 a[196:199], v[228:231], v[124:127], a[196:199]\n"
"v_cvt_pk_bf16_f32 v195, v195, v208\n"
"v_cvt_pk_bf16_f32 v199, v199, v209\n"
"v_mfma_f32_16x16x32_bf16 a[192:195], v[228:231], v[116:119], a[192:195]\n"
"ds_read_b64_tr_b16 v[228:229], v253 offset:49152\n"
"ds_read_b64_tr_b16 v[230:231], v253 offset:51200\n"
"v_mfma_f32_16x16x32_bf16 a[192:195], v[232:235], v[84:87], a[192:195]\n"
"s_waitcnt lgkmcnt(8)\n"
"ds_read_b64_tr_b16 v[232:233], v253 offset:53248\n"
"ds_read_b64_tr_b16 v[234:235], v253 offset:55296\n"
"v_mfma_f32_16x16x32_bf16 a[176:179], v[216:219], v[16:19], a[176:179]\n"
"s_waitcnt lgkmcnt(8)\n"
"v_cvt_pk_bf16_f32 v203, v203, v210\n"
"v_cvt_pk_bf16_f32 v207, v207, v211\n"
"v_mfma_f32_16x16x32_bf16 a[176:179], v[212:215], v[48:51], a[176:179]\n"
"buffer_store_dwordx4 v[192:195], v248, s[60:63], s84 offen sc1\n"
"v_mfma_f32_16x16x32_bf16 a[180:183], v[212:215], v[56:59], a[180:183]\n"
"s_add_u32 s84, s87, 0x1b00000\n"
"v_accvgpr_read_b32 v192, a212\n"
"v_accvgpr_read_b32 v193, a213\n"
"v_mfma_f32_16x16x32_bf16 a[180:183], v[216:219], v[24:27], a[180:183]\n"
"v_accvgpr_read_b32 v194, a214\n"
"v_accvgpr_read_b32 v195, a215\n"
"v_mfma_f32_16x16x32_bf16 a[240:243], v[216:219], v[128:131], a[240:243]\n"
"ds_read_b64_tr_b16 v[216:217], v253 offset:57344\n"
"ds_read_b64_tr_b16 v[218:219], v253 offset:59392\n"
"v_mfma_f32_16x16x32_bf16 a[240:243], v[212:215], v[132:135], a[240:243]\n"
"s_waitcnt lgkmcnt(8)\n"
"ds_read_b64_tr_b16 v[212:213], v253 offset:61440\n"
"ds_read_b64_tr_b16 v[214:215], v253 offset:63488\n"
"v_mfma_f32_16x16x32_bf16 a[240:243], v[220:223], v[136:139], a[240:243]\n"
"s_waitcnt lgkmcnt(8)\n"
"v_cvt_pk_bf16_f32 v192, v192, v193\n"
"v_cvt_pk_bf16_f32 v193, v194, v195\n"
"v_mfma_f32_16x16x32_bf16 a[240:243], v[224:227], v[140:143], a[240:243]\n"
"v_accvgpr_read_b32 v194, a228\n"
"v_accvgpr_read_b32 v195, a229\n"
"v_mfma_f32_16x16x32_bf16 a[180:183], v[224:227], v[120:123], a[180:183]\n"
"v_cvt_pk_bf16_f32 v194, v194, v195\n"
"v_accvgpr_read_b32 v208, a231\n"
"v_mfma_f32_16x16x32_bf16 a[180:183], v[220:223], v[88:91], a[180:183]\n"
"buffer_store_dwordx4 v[196:199], v248, s[60:63], s67 offen sc1\n"
"v_mfma_f32_16x16x32_bf16 a[176:179], v[220:223], v[80:83], a[176:179]\n"
"s_add_u32 s67, s87, 0x1800000\n"
"v_accvgpr_read_b32 v195, a230\n"
"v_accvgpr_read_b32 v209, a195\n"
"v_mfma_f32_16x16x32_bf16 a[176:179], v[224:227], v[112:115], a[176:179]\n"
"s_waitcnt lgkmcnt(6)\n"
"v_accvgpr_read_b32 v210, a199\n"
"v_cvt_pk_bf16_f32 v195, v195, v208\n"
"v_mfma_f32_16x16x32_bf16 a[176:179], v[228:231], v[20:23], a[176:179]\n"
"s_waitcnt lgkmcnt(4)\n"
"v_mfma_f32_16x16x32_bf16 a[176:179], v[232:235], v[52:55], a[176:179]\n"
"v_mfma_f32_16x16x32_bf16 a[180:183], v[232:235], v[60:63], a[180:183]\n"
"v_mfma_f32_16x16x32_bf16 a[180:183], v[228:231], v[28:31], a[180:183]\n"
"v_mfma_f32_16x16x32_bf16 a[240:243], v[228:231], v[144:147], a[240:243]\n"
"v_mfma_f32_16x16x32_bf16 a[240:243], v[232:235], v[148:151], a[240:243]\n"
"s_waitcnt lgkmcnt(2)\n"
"buffer_store_dwordx4 v[200:203], v248, s[60:63], s68 offen sc1\n"
"v_mfma_f32_16x16x32_bf16 a[240:243], v[216:219], v[152:155], a[240:243]\n"
"s_waitcnt lgkmcnt(0)\n"
"v_mfma_f32_16x16x32_bf16 a[240:243], v[212:215], v[156:159], a[240:243]\n"
"s_add_u32 s68, s87, 0x1900000\n"
"v_mfma_f32_16x16x32_bf16 a[180:183], v[212:215], v[124:127], a[180:183]\n"
"v_mfma_f32_16x16x32_bf16 a[180:183], v[216:219], v[92:95], a[180:183]\n"
"v_mfma_f32_16x16x32_bf16 a[176:179], v[216:219], v[84:87], a[176:179]\n"
"v_mfma_f32_16x16x32_bf16 a[176:179], v[212:215], v[116:119], a[176:179]\n"
"buffer_store_dwordx4 v[204:207], v248, s[60:63], s83 offen sc1\n"
"buffer_store_dwordx4 v[192:195], v248, s[60:63], s84 offen sc1\n"
"s_nop 5\n"
"v_accvgpr_read_b32 v196, a176\n"
"v_accvgpr_read_b32 v197, a177\n"
"v_accvgpr_read_b32 v198, a178\n"
"v_accvgpr_read_b32 v199, a179\n"
"v_cvt_pk_bf16_f32 v196, v196, v197\n"
"v_cvt_pk_bf16_f32 v197, v198, v199\n"
"v_accvgpr_read_b32 v198, a192\n"
"v_accvgpr_read_b32 v199, a193\n"
"v_cvt_pk_bf16_f32 v198, v198, v199\n"
"v_accvgpr_read_b32 v199, a194\n"
"v_cvt_pk_bf16_f32 v199, v199, v209\n"
"buffer_store_dwordx4 v[196:199], v248, s[60:63], s67 offen sc1\n"
"v_accvgpr_read_b32 v200, a180\n"
"v_accvgpr_read_b32 v201, a181\n"
"v_accvgpr_read_b32 v202, a182\n"
"v_accvgpr_read_b32 v203, a183\n"
"v_cvt_pk_bf16_f32 v200, v200, v201\n"
"v_cvt_pk_bf16_f32 v201, v202, v203\n"
"v_accvgpr_read_b32 v202, a196\n"
"v_accvgpr_read_b32 v203, a197\n"
"v_cvt_pk_bf16_f32 v202, v202, v203\n"
"v_accvgpr_read_b32 v203, a198\n"
"v_cvt_pk_bf16_f32 v203, v203, v210\n"
"buffer_store_dwordx4 v[200:203], v248, s[60:63], s68 offen sc1\n"
"v_accvgpr_read_b32 v204, a240\n"
"v_accvgpr_read_b32 v205, a241\n"
"v_accvgpr_read_b32 v206, a242\n"
"v_accvgpr_read_b32 v207, a243\n"
"v_cvt_pk_bf16_f32 v204, v204, v205\n"
"v_cvt_pk_bf16_f32 v205, v206, v207\n"
"v_accvgpr_read_b32 v206, a240\n"
"v_accvgpr_read_b32 v207, a241\n"
"v_accvgpr_read_b32 v211, a243\n"
"v_cvt_pk_bf16_f32 v206, v206, v207\n"
"v_accvgpr_read_b32 v207, a242\n"
"v_cvt_pk_bf16_f32 v207, v207, v211\n"
"s_add_u32 s83, s87, 0x1f00000\n"
"buffer_store_dwordx4 v[204:207], v248, s[60:63], s83 offen sc1\n"
"s_branch Lend\n"
"Lslowstart:\n"
"Lslow_0:\n"
"s_call_b64 s[80:81], Lcommon2_0\n"
"s_branch Lret_0\n"
"Lslow_1:\n"
"s_call_b64 s[80:81], Lcommon1\n"
"s_branch Lret_1\n"
"Lslow_2:\n"
"s_call_b64 s[80:81], Lcommon2_1\n"
"s_branch Lret_2\n"
"Lslow_3:\n"
"s_call_b64 s[80:81], Lcommon1\n"
"s_branch Lret_3\n"
"Lslow_4:\n"
"s_call_b64 s[80:81], Lcommon2_0\n"
"s_branch Lret_4\n"
"Lslow_5:\n"
"s_call_b64 s[80:81], Lcommon1\n"
"s_branch Lret_5\n"
"Lslow_6:\n"
"s_call_b64 s[80:81], Lcommon2_1\n"
"s_branch Lret_6\n"
"Lslow_7:\n"
"s_call_b64 s[80:81], Lcommon1\n"
"s_branch Lret_7\n"
"Lslow_8:\n"
"s_call_b64 s[80:81], Lcommon2_0\n"
"s_branch Lret_8\n"
"Lslow_9:\n"
"s_call_b64 s[80:81], Lcommon1\n"
"s_branch Lret_9\n"
"Lslow_10:\n"
"s_call_b64 s[80:81], Lcommon3\n"
"s_branch Lret_10\n"
"Lslow_11:\n"
"s_call_b64 s[80:81], Lcommon4_1\n"
"s_branch Lret_11\n"
"Lslow_12:\n"
"s_call_b64 s[80:81], Lcommon1\n"
"s_branch Lret_12\n"
"Lslow_13:\n"
"s_call_b64 s[80:81], Lcommon3\n"
"s_branch Lret_13\n"
"Lslow_14:\n"
"s_call_b64 s[80:81], Lcommon4_0\n"
"s_branch Lret_14\n"
"Lslow_15:\n"
"s_call_b64 s[80:81], Lcommon1\n"
"s_branch Lret_15\n"
"Lslow_16:\n"
"s_call_b64 s[80:81], Lcommon3\n"
"s_branch Lret_16\n"
"Lslow_17:\n"
"s_call_b64 s[80:81], Lcommon4_1\n"
"s_branch Lret_17\n"
"Lslow_18:\n"
"s_call_b64 s[80:81], Lcommon1\n"
"s_branch Lret_18\n"
"Lslow_19:\n"
"s_call_b64 s[80:81], Lcommon3\n"
"s_branch Lret_19\n"
"Lslow_20:\n"
"s_call_b64 s[80:81], Lcommon4_0\n"
"s_branch Lret_20\n"
"Lslow_21:\n"
"s_call_b64 s[80:81], Lcommon1\n"
"s_branch Lret_21\n"
"Lslow_22:\n"
"s_call_b64 s[80:81], Lcommon2_1\n"
"s_branch Lret_22\n"
"Lslow_23:\n"
"s_call_b64 s[80:81], Lcommon1\n"
"s_branch Lret_23\n"
"Lslow_24:\n"
"s_call_b64 s[80:81], Lcommon3\n"
"s_branch Lret_24\n"
"Lslow_25:\n"
"s_call_b64 s[80:81], Lcommon5\n"
"s_branch Lret_25\n"
"Lcommon5:\n"
"s_mov_b32 s71, 0x20000\n"
"Lspin:\n"
"s_sleep 4\n"
"buffer_load_dword v236, v255, s[60:63], s39 offen offset:128 sc1\n"
"buffer_load_dword v237, v255, s[60:63], s38 offen sc1\n"
"s_waitcnt vmcnt(0)\n"
"buffer_store_dword v236, v255, s[60:63], s39 offen sc1\n"
"v_readfirstlane_b32 s72, v237\n"
"s_nop 3\n"
"s_cmp_eq_u32 s72, s29\n"
"s_cbranch_scc1 Lspun\n"
"s_sub_u32 s71, s71, 1\n"
"s_cmp_lg_u32 s71, 0\n"
"s_cbranch_scc1 Lspin\n"
"Lspun:\n"
"s_setpc_b64 s[80:81]\n"
"Lcommon1:\n"
"s_nop 7\n"
"s_waitcnt lgkmcnt(0)\n"
"s_mov_b32 s75, m0\n"
"s_mov_b32 m0, s82\n"
"v_readfirstlane_b32 s71, v250\n"
"v_max3_f32 v251, v160, v161, v162\n"
"v_max3_f32 v251, v251, v163, v176\n"
"v_max3_f32 v251, v251, v177, v178\n"
"v_max_f32 v251, v251, v179\n"
"v_mov_b32 v250, v251\n"
"s_nop 1\n"
"v_permlane32_swap_b32 v251, v250\n"
"v_max_f32 v251, v251, v250\n"
"v_mov_b32 v250, v251\n"
"s_nop 1\n"
"v_permlane16_swap_b32 v251, v250\n"
"v_max_f32 v251, v251, v250\n"
"v_max_f32 v251, 0, v251\n"
"v_sub_f32 v236, v236, v251\n"
"v_sub_f32 v160, v160, v251\n"
"v_sub_f32 v161, v161, v251\n"
"v_sub_f32 v162, v162, v251\n"
"v_sub_f32 v163, v163, v251\n"
"v_sub_f32 v176, v176, v251\n"
"v_sub_f32 v177, v177, v251\n"
"v_sub_f32 v178, v178, v251\n"
"v_sub_f32 v179, v179, v251\n"
"v_exp_f32 v250, -v251\n"
"s_nop 0\n"
"v_mul_f32 v241, v241, v250\n"
"ds_write_addtid_b32 v250 offset:55296\n"
"v_max3_f32 v251, v164, v165, v166\n"
"v_max3_f32 v251, v251, v167, v180\n"
"v_max3_f32 v251, v251, v181, v182\n"
"v_max_f32 v251, v251, v183\n"
"v_mov_b32 v250, v251\n"
"s_nop 1\n"
"v_permlane32_swap_b32 v251, v250\n"
"v_max_f32 v251, v251, v250\n"
"v_mov_b32 v250, v251\n"
"s_nop 1\n"
"v_permlane16_swap_b32 v251, v250\n"
"v_max_f32 v251, v251, v250\n"
"v_max_f32 v251, 0, v251\n"
"v_sub_f32 v237, v237, v251\n"
"v_sub_f32 v164, v164, v251\n"
"v_sub_f32 v165, v165, v251\n"
"v_sub_f32 v166, v166, v251\n"
"v_sub_f32 v167, v167, v251\n"
"v_sub_f32 v180, v180, v251\n"
"v_sub_f32 v181, v181, v251\n"
"v_sub_f32 v182, v182, v251\n"
"v_sub_f32 v183, v183, v251\n"
"v_exp_f32 v250, -v251\n"
"s_nop 0\n"
"v_mul_f32 v242, v242, v250\n"
"ds_write_addtid_b32 v250 offset:55552\n"
"v_max3_f32 v251, v168, v169, v170\n"
"v_max3_f32 v251, v251, v171, v184\n"
"v_max3_f32 v251, v251, v185, v186\n"
"v_max_f32 v251, v251, v187\n"
"v_mov_b32 v250, v251\n"
"s_nop 1\n"
"v_permlane32_swap_b32 v251, v250\n"
"v_max_f32 v251, v251, v250\n"
"v_mov_b32 v250, v251\n"
"s_nop 1\n"
"v_permlane16_swap_b32 v251, v250\n"
"v_max_f32 v251, v251, v250\n"
"v_max_f32 v251, 0, v251\n"
"v_sub_f32 v238, v238, v251\n"
"v_sub_f32 v168, v168, v251\n"
"v_sub_f32 v169, v169, v251\n"
"v_sub_f32 v170, v170, v251\n"
"v_sub_f32 v171, v171, v251\n"
"v_sub_f32 v184, v184, v251\n"
"v_sub_f32 v185, v185, v251\n"
"v_sub_f32 v186, v186, v251\n"
"v_sub_f32 v187, v187, v251\n"
"v_exp_f32 v250, -v251\n"
"s_nop 0\n"
"v_mul_f32 v243, v243, v250\n"
"ds_write_addtid_b32 v250 offset:55808\n"
"v_max3_f32 v251, v172, v173, v174\n"
"v_max3_f32 v251, v251, v175, v188\n"
"v_max3_f32 v251, v251, v189, v190\n"
"v_max_f32 v251, v251, v191\n"
"v_mov_b32 v250, v251\n"
"s_nop 1\n"
"v_permlane32_swap_b32 v251, v250\n"
"v_max_f32 v251, v251, v250\n"
"v_mov_b32 v250, v251\n"
"s_nop 1\n"
"v_permlane16_swap_b32 v251, v250\n"
"v_max_f32 v251, v251, v250\n"
"v_max_f32 v251, 0, v251\n"
"v_sub_f32 v239, v239, v251\n"
"v_sub_f32 v172, v172, v251\n"
"v_sub_f32 v173, v173, v251\n"
"v_sub_f32 v174, v174, v251\n"
"v_sub_f32 v175, v175, v251\n"
"v_sub_f32 v188, v188, v251\n"
"v_sub_f32 v189, v189, v251\n"
"v_sub_f32 v190, v190, v251\n"
"v_sub_f32 v191, v191, v251\n"
"v_exp_f32 v250, -v251\n"
"s_nop 0\n"
"v_mul_f32 v244, v244, v250\n"
"ds_write_addtid_b32 v250 offset:56064\n"
"s_cmp_eq_u32 s71, 0\n"
"s_cbranch_scc1 Lc1_notail\n"
"v_max3_f32 v251, v208, v209, v210\n"
"v_max_f32 v251, v251, v211\n"
"v_and_b32 v250, 0xf0, v248\n"
"v_add_u32 v250, s86, v250\n"
"ds_max_f32 v250, v251 offset:4\n"
"s_waitcnt lgkmcnt(0)\n"
"s_barrier\n"
"ds_read_b32 v251, v250 offset:4\n"
"s_waitcnt lgkmcnt(0)\n"
"v_sub_f32 v240, v240, v251\n"
"v_sub_f32 v208, v208, v251\n"
"v_sub_f32 v209, v209, v251\n"
"v_sub_f32 v210, v210, v251\n"
"v_sub_f32 v211, v211, v251\n"
"v_exp_f32 v251, -v251\n"
"s_nop 0\n"
"v_mul_f32 v245, v245, v251\n"
"v_and_b32 v250, 0xf0, v248\n"
"ds_read_b32 v251, v250 offset:4\n"
"s_waitcnt lgkmcnt(0)\n"
"v_exp_f32 v251, -v251\n"
"s_nop 0\n"
"ds_write_addtid_b32 v251 offset:56320\n"
"ds_read_b32 v251, v250 offset:260\n"
"s_waitcnt lgkmcnt(0)\n"
"v_exp_f32 v251, -v251\n"
"s_nop 0\n"
"ds_write_addtid_b32 v251 offset:56576\n"
"v_mov_b32 v251, 0\n"
"ds_write_b32 v248, v251 offset:0\n"
"ds_write_b32 v248, v251 offset:12\n"
"s_waitcnt lgkmcnt(0)\n"
"s_barrier\n"
"ds_write_b32 v248, v251 offset:4\n"
"s_waitcnt lgkmcnt(0)\n"
"s_branch Lc1_done\n"
"Lc1_notail:\n"
"v_mov_b32 v251, 1.0\n"
"ds_write_addtid_b32 v251 offset:56320\n"
"ds_write_addtid_b32 v251 offset:56576\n"
"Lc1_done:\n"
"s_waitcnt lgkmcnt(0)\n"
"s_mov_b32 m0, s75\n"
"s_mov_b64 s[76:77], -1\n"
"s_nop 1\n"
"s_setpc_b64 s[80:81]\n"
"Lcommon4_0:\n"
"v_mov_b32 v250, 0x7149f2ca\n"
"ds_write_b32 v248, v250 offset:0\n"
"s_waitcnt lgkmcnt(0)\n"
"s_setpc_b64 s[80:81]\n"
"Lcommon2_0:\n"
"s_cmp_eq_u64 s[78:79], 0\n"
"s_cbranch_scc1 Lcommon3\n"
"v_mov_b32 v250, 0x7149f2ca\n"
"ds_write_b32 v248, v250 offset:0\n"
"s_branch Lcommon3\n"
"Lcommon4_1:\n"
"v_mov_b32 v250, 0x7149f2ca\n"
"ds_write_b32 v248, v250 offset:12\n"
"s_waitcnt lgkmcnt(0)\n"
"s_setpc_b64 s[80:81]\n"
"Lcommon2_1:\n"
"s_cmp_eq_u64 s[78:79], 0\n"
"s_cbranch_scc1 Lcommon3\n"
"v_mov_b32 v250, 0x7149f2ca\n"
"ds_write_b32 v248, v250 offset:12\n"
"s_branch Lcommon3\n"
"Lcommon3:\n"
"s_nop 7\n"
"s_waitcnt lgkmcnt(0)\n"
"s_cmp_eq_u64 s[76:77], 0\n"
"s_cbranch_scc1 Lc2_done\n"
"s_mov_b32 s75, m0\n"
"s_mov_b32 m0, s82\n"
"s_nop 0\n"
"ds_write_addtid_b32 v251 offset:56832\n"
"s_waitcnt lgkmcnt(0)\n"
"ds_read_addtid_b32 v250 offset:55296\n"
"v_accvgpr_read_b32 v251, a0\n"
"s_waitcnt lgkmcnt(0)\n"
"v_mul_f32 v251, v251, v250\n"
"v_accvgpr_write_b32 a0, v251\n"
"v_accvgpr_read_b32 v251, a1\n"
"v_mul_f32 v251, v251, v250\n"
"v_accvgpr_write_b32 a1, v251\n"
"v_accvgpr_read_b32 v251, a2\n"
"v_mul_f32 v251, v251, v250\n"
"v_accvgpr_write_b32 a2, v251\n"
"v_accvgpr_read_b32 v251, a3\n"
"v_mul_f32 v251, v251, v250\n"
"v_accvgpr_write_b32 a3, v251\n"
"v_accvgpr_read_b32 v251, a16\n"
"v_mul_f32 v251, v251, v250\n"
"v_accvgpr_write_b32 a16, v251\n"
"v_accvgpr_read_b32 v251, a17\n"
"v_mul_f32 v251, v251, v250\n"
"v_accvgpr_write_b32 a17, v251\n"
"v_accvgpr_read_b32 v251, a18\n"
"v_mul_f32 v251, v251, v250\n"
"v_accvgpr_write_b32 a18, v251\n"
"v_accvgpr_read_b32 v251, a19\n"
"v_mul_f32 v251, v251, v250\n"
"v_accvgpr_write_b32 a19, v251\n"
"v_accvgpr_read_b32 v251, a32\n"
"v_mul_f32 v251, v251, v250\n"
"v_accvgpr_write_b32 a32, v251\n"
"v_accvgpr_read_b32 v251, a33\n"
"v_mul_f32 v251, v251, v250\n"
"v_accvgpr_write_b32 a33, v251\n"
"v_accvgpr_read_b32 v251, a34\n"
"v_mul_f32 v251, v251, v250\n"
"v_accvgpr_write_b32 a34, v251\n"
"v_accvgpr_read_b32 v251, a35\n"
"v_mul_f32 v251, v251, v250\n"
"v_accvgpr_write_b32 a35, v251\n"
"v_accvgpr_read_b32 v251, a48\n"
"v_mul_f32 v251, v251, v250\n"
"v_accvgpr_write_b32 a48, v251\n"
"v_accvgpr_read_b32 v251, a49\n"
"v_mul_f32 v251, v251, v250\n"
"v_accvgpr_write_b32 a49, v251\n"
"v_accvgpr_read_b32 v251, a50\n"
"v_mul_f32 v251, v251, v250\n"
"v_accvgpr_write_b32 a50, v251\n"
"v_accvgpr_read_b32 v251, a51\n"
"v_mul_f32 v251, v251, v250\n"
"v_accvgpr_write_b32 a51, v251\n"
"v_accvgpr_read_b32 v251, a64\n"
"v_mul_f32 v251, v251, v250\n"
"v_accvgpr_write_b32 a64, v251\n"
"v_accvgpr_read_b32 v251, a65\n"
"v_mul_f32 v251, v251, v250\n"
"v_accvgpr_write_b32 a65, v251\n"
"v_accvgpr_read_b32 v251, a66\n"
"v_mul_f32 v251, v251, v250\n"
"v_accvgpr_write_b32 a66, v251\n"
"v_accvgpr_read_b32 v251, a67\n"
"v_mul_f32 v251, v251, v250\n"
"v_accvgpr_write_b32 a67, v251\n"
"v_accvgpr_read_b32 v251, a80\n"
"v_mul_f32 v251, v251, v250\n"
"v_accvgpr_write_b32 a80, v251\n"
"v_accvgpr_read_b32 v251, a81\n"
"v_mul_f32 v251, v251, v250\n"
"v_accvgpr_write_b32 a81, v251\n"
"v_accvgpr_read_b32 v251, a82\n"
"v_mul_f32 v251, v251, v250\n"
"v_accvgpr_write_b32 a82, v251\n"
"v_accvgpr_read_b32 v251, a83\n"
"v_mul_f32 v251, v251, v250\n"
"v_accvgpr_write_b32 a83, v251\n"
"v_accvgpr_read_b32 v251, a96\n"
"v_mul_f32 v251, v251, v250\n"
"v_accvgpr_write_b32 a96, v251\n"
"v_accvgpr_read_b32 v251, a97\n"
"v_mul_f32 v251, v251, v250\n"
"v_accvgpr_write_b32 a97, v251\n"
"v_accvgpr_read_b32 v251, a98\n"
"v_mul_f32 v251, v251, v250\n"
"v_accvgpr_write_b32 a98, v251\n"
"v_accvgpr_read_b32 v251, a99\n"
"v_mul_f32 v251, v251, v250\n"
"v_accvgpr_write_b32 a99, v251\n"
"v_accvgpr_read_b32 v251, a112\n"
"v_mul_f32 v251, v251, v250\n"
"v_accvgpr_write_b32 a112, v251\n"
"v_accvgpr_read_b32 v251, a113\n"
"v_mul_f32 v251, v251, v250\n"
"v_accvgpr_write_b32 a113, v251\n"
"v_accvgpr_read_b32 v251, a114\n"
"v_mul_f32 v251, v251, v250\n"
"v_accvgpr_write_b32 a114, v251\n"
"v_accvgpr_read_b32 v251, a115\n"
"v_mul_f32 v251, v251, v250\n"
"v_accvgpr_write_b32 a115, v251\n"
"v_accvgpr_read_b32 v251, a128\n"
"v_mul_f32 v251, v251, v250\n"
"v_accvgpr_write_b32 a128, v251\n"
"v_accvgpr_read_b32 v251, a129\n"
"v_mul_f32 v251, v251, v250\n"
"v_accvgpr_write_b32 a129, v251\n"
"v_accvgpr_read_b32 v251, a130\n"
"v_mul_f32 v251, v251, v250\n"
"v_accvgpr_write_b32 a130, v251\n"
"v_accvgpr_read_b32 v251, a131\n"
"v_mul_f32 v251, v251, v250\n"
"v_accvgpr_write_b32 a131, v251\n"
"v_accvgpr_read_b32 v251, a144\n"
"v_mul_f32 v251, v251, v250\n"
"v_accvgpr_write_b32 a144, v251\n"
"v_accvgpr_read_b32 v251, a145\n"
"v_mul_f32 v251, v251, v250\n"
"v_accvgpr_write_b32 a145, v251\n"
"v_accvgpr_read_b32 v251, a146\n"
"v_mul_f32 v251, v251, v250\n"
"v_accvgpr_write_b32 a146, v251\n"
"v_accvgpr_read_b32 v251, a147\n"
"v_mul_f32 v251, v251, v250\n"
"v_accvgpr_write_b32 a147, v251\n"
"v_accvgpr_read_b32 v251, a160\n"
"v_mul_f32 v251, v251, v250\n"
"v_accvgpr_write_b32 a160, v251\n"
"v_accvgpr_read_b32 v251, a161\n"
"v_mul_f32 v251, v251, v250\n"
"v_accvgpr_write_b32 a161, v251\n"
"v_accvgpr_read_b32 v251, a162\n"
"v_mul_f32 v251, v251, v250\n"
"v_accvgpr_write_b32 a162, v251\n"
"v_accvgpr_read_b32 v251, a163\n"
"v_mul_f32 v251, v251, v250\n"
"v_accvgpr_write_b32 a163, v251\n"
"v_accvgpr_read_b32 v251, a176\n"
"v_mul_f32 v251, v251, v250\n"
"v_accvgpr_write_b32 a176, v251\n"
"v_accvgpr_read_b32 v251, a177\n"
"v_mul_f32 v251, v251, v250\n"
"v_accvgpr_write_b32 a177, v251\n"
"v_accvgpr_read_b32 v251, a178\n"
"v_mul_f32 v251, v251, v250\n"
"v_accvgpr_write_b32 a178, v251\n"
"v_accvgpr_read_b32 v251, a179\n"
"v_mul_f32 v251, v251, v250\n"
"v_accvgpr_write_b32 a179, v251\n"
"v_accvgpr_read_b32 v251, a192\n"
"v_mul_f32 v251, v251, v250\n"
"v_accvgpr_write_b32 a192, v251\n"
"v_accvgpr_read_b32 v251, a193\n"
"v_mul_f32 v251, v251, v250\n"
"v_accvgpr_write_b32 a193, v251\n"
"v_accvgpr_read_b32 v251, a194\n"
"v_mul_f32 v251, v251, v250\n"
"v_accvgpr_write_b32 a194, v251\n"
"v_accvgpr_read_b32 v251, a195\n"
"v_mul_f32 v251, v251, v250\n"
"v_accvgpr_write_b32 a195, v251\n"
"v_accvgpr_read_b32 v251, a208\n"
"v_mul_f32 v251, v251, v250\n"
"v_accvgpr_write_b32 a208, v251\n"
"v_accvgpr_read_b32 v251, a209\n"
"v_mul_f32 v251, v251, v250\n"
"v_accvgpr_write_b32 a209, v251\n"
"v_accvgpr_read_b32 v251, a210\n"
"v_mul_f32 v251, v251, v250\n"
"v_accvgpr_write_b32 a210, v251\n"
"v_accvgpr_read_b32 v251, a211\n"
"v_mul_f32 v251, v251, v250\n"
"v_accvgpr_write_b32 a211, v251\n"
"v_accvgpr_read_b32 v251, a224\n"
"v_mul_f32 v251, v251, v250\n"
"v_accvgpr_write_b32 a224, v251\n"
"v_accvgpr_read_b32 v251, a225\n"
"v_mul_f32 v251, v251, v250\n"
"v_accvgpr_write_b32 a225, v251\n"
"v_accvgpr_read_b32 v251, a226\n"
"v_mul_f32 v251, v251, v250\n"
"v_accvgpr_write_b32 a226, v251\n"
"v_accvgpr_read_b32 v251, a227\n"
"v_mul_f32 v251, v251, v250\n"
"v_accvgpr_write_b32 a227, v251\n"
"v_accvgpr_read_b32 v251, a240\n"
"v_mul_f32 v251, v251, v250\n"
"v_accvgpr_write_b32 a240, v251\n"
"v_accvgpr_read_b32 v251, a241\n"
"v_mul_f32 v251, v251, v250\n"
"v_accvgpr_write_b32 a241, v251\n"
"v_accvgpr_read_b32 v251, a242\n"
"v_mul_f32 v251, v251, v250\n"
"v_accvgpr_write_b32 a242, v251\n"
"v_accvgpr_read_b32 v251, a243\n"
"v_mul_f32 v251, v251, v250\n"
"v_accvgpr_write_b32 a243, v251\n"
"ds_read_addtid_b32 v250 offset:55552\n"
"v_accvgpr_read_b32 v251, a4\n"
"s_waitcnt lgkmcnt(0)\n"
"v_mul_f32 v251, v251, v250\n"
"v_accvgpr_write_b32 a4, v251\n"
"v_accvgpr_read_b32 v251, a5\n"
"v_mul_f32 v251, v251, v250\n"
"v_accvgpr_write_b32 a5, v251\n"
"v_accvgpr_read_b32 v251, a6\n"
"v_mul_f32 v251, v251, v250\n"
"v_accvgpr_write_b32 a6, v251\n"
"v_accvgpr_read_b32 v251, a7\n"
"v_mul_f32 v251, v251, v250\n"
"v_accvgpr_write_b32 a7, v251\n"
"v_accvgpr_read_b32 v251, a20\n"
"v_mul_f32 v251, v251, v250\n"
"v_accvgpr_write_b32 a20, v251\n"
"v_accvgpr_read_b32 v251, a21\n"
"v_mul_f32 v251, v251, v250\n"
"v_accvgpr_write_b32 a21, v251\n"
"v_accvgpr_read_b32 v251, a22\n"
"v_mul_f32 v251, v251, v250\n"
"v_accvgpr_write_b32 a22, v251\n"
"v_accvgpr_read_b32 v251, a23\n"
"v_mul_f32 v251, v251, v250\n"
"v_accvgpr_write_b32 a23, v251\n"
"v_accvgpr_read_b32 v251, a36\n"
"v_mul_f32 v251, v251, v250\n"
"v_accvgpr_write_b32 a36, v251\n"
"v_accvgpr_read_b32 v251, a37\n"
"v_mul_f32 v251, v251, v250\n"
"v_accvgpr_write_b32 a37, v251\n"
"v_accvgpr_read_b32 v251, a38\n"
"v_mul_f32 v251, v251, v250\n"
"v_accvgpr_write_b32 a38, v251\n"
"v_accvgpr_read_b32 v251, a39\n"
"v_mul_f32 v251, v251, v250\n"
"v_accvgpr_write_b32 a39, v251\n"
"v_accvgpr_read_b32 v251, a52\n"
"v_mul_f32 v251, v251, v250\n"
"v_accvgpr_write_b32 a52, v251\n"
"v_accvgpr_read_b32 v251, a53\n"
"v_mul_f32 v251, v251, v250\n"
"v_accvgpr_write_b32 a53, v251\n"
"v_accvgpr_read_b32 v251, a54\n"
"v_mul_f32 v251, v251, v250\n"
"v_accvgpr_write_b32 a54, v251\n"
"v_accvgpr_read_b32 v251, a55\n"
"v_mul_f32 v251, v251, v250\n"
"v_accvgpr_write_b32 a55, v251\n"
"v_accvgpr_read_b32 v251, a68\n"
"v_mul_f32 v251, v251, v250\n"
"v_accvgpr_write_b32 a68, v251\n"
"v_accvgpr_read_b32 v251, a69\n"
"v_mul_f32 v251, v251, v250\n"
"v_accvgpr_write_b32 a69, v251\n"
"v_accvgpr_read_b32 v251, a70\n"
"v_mul_f32 v251, v251, v250\n"
"v_accvgpr_write_b32 a70, v251\n"
"v_accvgpr_read_b32 v251, a71\n"
"v_mul_f32 v251, v251, v250\n"
"v_accvgpr_write_b32 a71, v251\n"
"v_accvgpr_read_b32 v251, a84\n"
"v_mul_f32 v251, v251, v250\n"
"v_accvgpr_write_b32 a84, v251\n"
"v_accvgpr_read_b32 v251, a85\n"
"v_mul_f32 v251, v251, v250\n"
"v_accvgpr_write_b32 a85, v251\n"
"v_accvgpr_read_b32 v251, a86\n"
"v_mul_f32 v251, v251, v250\n"
"v_accvgpr_write_b32 a86, v251\n"
"v_accvgpr_read_b32 v251, a87\n"
"v_mul_f32 v251, v251, v250\n"
"v_accvgpr_write_b32 a87, v251\n"
"v_accvgpr_read_b32 v251, a100\n"
"v_mul_f32 v251, v251, v250\n"
"v_accvgpr_write_b32 a100, v251\n"
"v_accvgpr_read_b32 v251, a101\n"
"v_mul_f32 v251, v251, v250\n"
"v_accvgpr_write_b32 a101, v251\n"
"v_accvgpr_read_b32 v251, a102\n"
"v_mul_f32 v251, v251, v250\n"
"v_accvgpr_write_b32 a102, v251\n"
"v_accvgpr_read_b32 v251, a103\n"
"v_mul_f32 v251, v251, v250\n"
"v_accvgpr_write_b32 a103, v251\n"
"v_accvgpr_read_b32 v251, a116\n"
"v_mul_f32 v251, v251, v250\n"
"v_accvgpr_write_b32 a116, v251\n"
"v_accvgpr_read_b32 v251, a117\n"
"v_mul_f32 v251, v251, v250\n"
"v_accvgpr_write_b32 a117, v251\n"
"v_accvgpr_read_b32 v251, a118\n"
"v_mul_f32 v251, v251, v250\n"
"v_accvgpr_write_b32 a118, v251\n"
"v_accvgpr_read_b32 v251, a119\n"
"v_mul_f32 v251, v251, v250\n"
"v_accvgpr_write_b32 a119, v251\n"
"v_accvgpr_read_b32 v251, a132\n"
"v_mul_f32 v251, v251, v250\n"
"v_accvgpr_write_b32 a132, v251\n"
"v_accvgpr_read_b32 v251, a133\n"
"v_mul_f32 v251, v251, v250\n"
"v_accvgpr_write_b32 a133, v251\n"
"v_accvgpr_read_b32 v251, a134\n"
"v_mul_f32 v251, v251, v250\n"
"v_accvgpr_write_b32 a134, v251\n"
"v_accvgpr_read_b32 v251, a135\n"
"v_mul_f32 v251, v251, v250\n"
"v_accvgpr_write_b32 a135, v251\n"
"v_accvgpr_read_b32 v251, a148\n"
"v_mul_f32 v251, v251, v250\n"
"v_accvgpr_write_b32 a148, v251\n"
"v_accvgpr_read_b32 v251, a149\n"
"v_mul_f32 v251, v251, v250\n"
"v_accvgpr_write_b32 a149, v251\n"
"v_accvgpr_read_b32 v251, a150\n"
"v_mul_f32 v251, v251, v250\n"
"v_accvgpr_write_b32 a150, v251\n"
"v_accvgpr_read_b32 v251, a151\n"
"v_mul_f32 v251, v251, v250\n"
"v_accvgpr_write_b32 a151, v251\n"
"v_accvgpr_read_b32 v251, a164\n"
"v_mul_f32 v251, v251, v250\n"
"v_accvgpr_write_b32 a164, v251\n"
"v_accvgpr_read_b32 v251, a165\n"
"v_mul_f32 v251, v251, v250\n"
"v_accvgpr_write_b32 a165, v251\n"
"v_accvgpr_read_b32 v251, a166\n"
"v_mul_f32 v251, v251, v250\n"
"v_accvgpr_write_b32 a166, v251\n"
"v_accvgpr_read_b32 v251, a167\n"
"v_mul_f32 v251, v251, v250\n"
"v_accvgpr_write_b32 a167, v251\n"
"v_accvgpr_read_b32 v251, a180\n"
"v_mul_f32 v251, v251, v250\n"
"v_accvgpr_write_b32 a180, v251\n"
"v_accvgpr_read_b32 v251, a181\n"
"v_mul_f32 v251, v251, v250\n"
"v_accvgpr_write_b32 a181, v251\n"
"v_accvgpr_read_b32 v251, a182\n"
"v_mul_f32 v251, v251, v250\n"
"v_accvgpr_write_b32 a182, v251\n"
"v_accvgpr_read_b32 v251, a183\n"
"v_mul_f32 v251, v251, v250\n"
"v_accvgpr_write_b32 a183, v251\n"
"v_accvgpr_read_b32 v251, a196\n"
"v_mul_f32 v251, v251, v250\n"
"v_accvgpr_write_b32 a196, v251\n"
"v_accvgpr_read_b32 v251, a197\n"
"v_mul_f32 v251, v251, v250\n"
"v_accvgpr_write_b32 a197, v251\n"
"v_accvgpr_read_b32 v251, a198\n"
"v_mul_f32 v251, v251, v250\n"
"v_accvgpr_write_b32 a198, v251\n"
"v_accvgpr_read_b32 v251, a199\n"
"v_mul_f32 v251, v251, v250\n"
"v_accvgpr_write_b32 a199, v251\n"
"v_accvgpr_read_b32 v251, a212\n"
"v_mul_f32 v251, v251, v250\n"
"v_accvgpr_write_b32 a212, v251\n"
"v_accvgpr_read_b32 v251, a213\n"
"v_mul_f32 v251, v251, v250\n"
"v_accvgpr_write_b32 a213, v251\n"
"v_accvgpr_read_b32 v251, a214\n"
"v_mul_f32 v251, v251, v250\n"
"v_accvgpr_write_b32 a214, v251\n"
"v_accvgpr_read_b32 v251, a215\n"
"v_mul_f32 v251, v251, v250\n"
"v_accvgpr_write_b32 a215, v251\n"
"v_accvgpr_read_b32 v251, a228\n"
"v_mul_f32 v251, v251, v250\n"
"v_accvgpr_write_b32 a228, v251\n"
"v_accvgpr_read_b32 v251, a229\n"
"v_mul_f32 v251, v251, v250\n"
"v_accvgpr_write_b32 a229, v251\n"
"v_accvgpr_read_b32 v251, a230\n"
"v_mul_f32 v251, v251, v250\n"
"v_accvgpr_write_b32 a230, v251\n"
"v_accvgpr_read_b32 v251, a231\n"
"v_mul_f32 v251, v251, v250\n"
"v_accvgpr_write_b32 a231, v251\n"
"v_accvgpr_read_b32 v251, a244\n"
"v_mul_f32 v251, v251, v250\n"
"v_accvgpr_write_b32 a244, v251\n"
"v_accvgpr_read_b32 v251, a245\n"
"v_mul_f32 v251, v251, v250\n"
"v_accvgpr_write_b32 a245, v251\n"
"v_accvgpr_read_b32 v251, a246\n"
"v_mul_f32 v251, v251, v250\n"
"v_accvgpr_write_b32 a246, v251\n"
"v_accvgpr_read_b32 v251, a247\n"
"v_mul_f32 v251, v251, v250\n"
"v_accvgpr_write_b32 a247, v251\n"
"ds_read_addtid_b32 v250 offset:55808\n"
"v_accvgpr_read_b32 v251, a8\n"
"s_waitcnt lgkmcnt(0)\n"
"v_mul_f32 v251, v251, v250\n"
"v_accvgpr_write_b32 a8, v251\n"
"v_accvgpr_read_b32 v251, a9\n"
"v_mul_f32 v251, v251, v250\n"
"v_accvgpr_write_b32 a9, v251\n"
"v_accvgpr_read_b32 v251, a10\n"
"v_mul_f32 v251, v251, v250\n"
"v_accvgpr_write_b32 a10, v251\n"
"v_accvgpr_read_b32 v251, a11\n"
"v_mul_f32 v251, v251, v250\n"
"v_accvgpr_write_b32 a11, v251\n"
"v_accvgpr_read_b32 v251, a24\n"
"v_mul_f32 v251, v251, v250\n"
"v_accvgpr_write_b32 a24, v251\n"
"v_accvgpr_read_b32 v251, a25\n"
"v_mul_f32 v251, v251, v250\n"
"v_accvgpr_write_b32 a25, v251\n"
"v_accvgpr_read_b32 v251, a26\n"
"v_mul_f32 v251, v251, v250\n"
"v_accvgpr_write_b32 a26, v251\n"
"v_accvgpr_read_b32 v251, a27\n"
"v_mul_f32 v251, v251, v250\n"
"v_accvgpr_write_b32 a27, v251\n"
"v_accvgpr_read_b32 v251, a40\n"
"v_mul_f32 v251, v251, v250\n"
"v_accvgpr_write_b32 a40, v251\n"
"v_accvgpr_read_b32 v251, a41\n"
"v_mul_f32 v251, v251, v250\n"
"v_accvgpr_write_b32 a41, v251\n"
"v_accvgpr_read_b32 v251, a42\n"
"v_mul_f32 v251, v251, v250\n"
"v_accvgpr_write_b32 a42, v251\n"
"v_accvgpr_read_b32 v251, a43\n"
"v_mul_f32 v251, v251, v250\n"
"v_accvgpr_write_b32 a43, v251\n"
"v_accvgpr_read_b32 v251, a56\n"
"v_mul_f32 v251, v251, v250\n"
"v_accvgpr_write_b32 a56, v251\n"
"v_accvgpr_read_b32 v251, a57\n"
"v_mul_f32 v251, v251, v250\n"
"v_accvgpr_write_b32 a57, v251\n"
"v_accvgpr_read_b32 v251, a58\n"
"v_mul_f32 v251, v251, v250\n"
"v_accvgpr_write_b32 a58, v251\n"
"v_accvgpr_read_b32 v251, a59\n"
"v_mul_f32 v251, v251, v250\n"
"v_accvgpr_write_b32 a59, v251\n"
"v_accvgpr_read_b32 v251, a72\n"
"v_mul_f32 v251, v251, v250\n"
"v_accvgpr_write_b32 a72, v251\n"
"v_accvgpr_read_b32 v251, a73\n"
"v_mul_f32 v251, v251, v250\n"
"v_accvgpr_write_b32 a73, v251\n"
"v_accvgpr_read_b32 v251, a74\n"
"v_mul_f32 v251, v251, v250\n"
"v_accvgpr_write_b32 a74, v251\n"
"v_accvgpr_read_b32 v251, a75\n"
"v_mul_f32 v251, v251, v250\n"
"v_accvgpr_write_b32 a75, v251\n"
"v_accvgpr_read_b32 v251, a88\n"
"v_mul_f32 v251, v251, v250\n"
"v_accvgpr_write_b32 a88, v251\n"
"v_accvgpr_read_b32 v251, a89\n"
"v_mul_f32 v251, v251, v250\n"
"v_accvgpr_write_b32 a89, v251\n"
"v_accvgpr_read_b32 v251, a90\n"
"v_mul_f32 v251, v251, v250\n"
"v_accvgpr_write_b32 a90, v251\n"
"v_accvgpr_read_b32 v251, a91\n"
"v_mul_f32 v251, v251, v250\n"
"v_accvgpr_write_b32 a91, v251\n"
"v_accvgpr_read_b32 v251, a104\n"
"v_mul_f32 v251, v251, v250\n"
"v_accvgpr_write_b32 a104, v251\n"
"v_accvgpr_read_b32 v251, a105\n"
"v_mul_f32 v251, v251, v250\n"
"v_accvgpr_write_b32 a105, v251\n"
"v_accvgpr_read_b32 v251, a106\n"
"v_mul_f32 v251, v251, v250\n"
"v_accvgpr_write_b32 a106, v251\n"
"v_accvgpr_read_b32 v251, a107\n"
"v_mul_f32 v251, v251, v250\n"
"v_accvgpr_write_b32 a107, v251\n"
"v_accvgpr_read_b32 v251, a120\n"
"v_mul_f32 v251, v251, v250\n"
"v_accvgpr_write_b32 a120, v251\n"
"v_accvgpr_read_b32 v251, a121\n"
"v_mul_f32 v251, v251, v250\n"
"v_accvgpr_write_b32 a121, v251\n"
"v_accvgpr_read_b32 v251, a122\n"
"v_mul_f32 v251, v251, v250\n"
"v_accvgpr_write_b32 a122, v251\n"
"v_accvgpr_read_b32 v251, a123\n"
"v_mul_f32 v251, v251, v250\n"
"v_accvgpr_write_b32 a123, v251\n"
"v_accvgpr_read_b32 v251, a136\n"
"v_mul_f32 v251, v251, v250\n"
"v_accvgpr_write_b32 a136, v251\n"
"v_accvgpr_read_b32 v251, a137\n"
"v_mul_f32 v251, v251, v250\n"
"v_accvgpr_write_b32 a137, v251\n"
"v_accvgpr_read_b32 v251, a138\n"
"v_mul_f32 v251, v251, v250\n"
"v_accvgpr_write_b32 a138, v251\n"
"v_accvgpr_read_b32 v251, a139\n"
"v_mul_f32 v251, v251, v250\n"
"v_accvgpr_write_b32 a139, v251\n"
"v_accvgpr_read_b32 v251, a152\n"
"v_mul_f32 v251, v251, v250\n"
"v_accvgpr_write_b32 a152, v251\n"
"v_accvgpr_read_b32 v251, a153\n"
"v_mul_f32 v251, v251, v250\n"
"v_accvgpr_write_b32 a153, v251\n"
"v_accvgpr_read_b32 v251, a154\n"
"v_mul_f32 v251, v251, v250\n"
"v_accvgpr_write_b32 a154, v251\n"
"v_accvgpr_read_b32 v251, a155\n"
"v_mul_f32 v251, v251, v250\n"
"v_accvgpr_write_b32 a155, v251\n"
"v_accvgpr_read_b32 v251, a168\n"
"v_mul_f32 v251, v251, v250\n"
"v_accvgpr_write_b32 a168, v251\n"
"v_accvgpr_read_b32 v251, a169\n"
"v_mul_f32 v251, v251, v250\n"
"v_accvgpr_write_b32 a169, v251\n"
"v_accvgpr_read_b32 v251, a170\n"
"v_mul_f32 v251, v251, v250\n"
"v_accvgpr_write_b32 a170, v251\n"
"v_accvgpr_read_b32 v251, a171\n"
"v_mul_f32 v251, v251, v250\n"
"v_accvgpr_write_b32 a171, v251\n"
"v_accvgpr_read_b32 v251, a184\n"
"v_mul_f32 v251, v251, v250\n"
"v_accvgpr_write_b32 a184, v251\n"
"v_accvgpr_read_b32 v251, a185\n"
"v_mul_f32 v251, v251, v250\n"
"v_accvgpr_write_b32 a185, v251\n"
"v_accvgpr_read_b32 v251, a186\n"
"v_mul_f32 v251, v251, v250\n"
"v_accvgpr_write_b32 a186, v251\n"
"v_accvgpr_read_b32 v251, a187\n"
"v_mul_f32 v251, v251, v250\n"
"v_accvgpr_write_b32 a187, v251\n"
"v_accvgpr_read_b32 v251, a200\n"
"v_mul_f32 v251, v251, v250\n"
"v_accvgpr_write_b32 a200, v251\n"
"v_accvgpr_read_b32 v251, a201\n"
"v_mul_f32 v251, v251, v250\n"
"v_accvgpr_write_b32 a201, v251\n"
"v_accvgpr_read_b32 v251, a202\n"
"v_mul_f32 v251, v251, v250\n"
"v_accvgpr_write_b32 a202, v251\n"
"v_accvgpr_read_b32 v251, a203\n"
"v_mul_f32 v251, v251, v250\n"
"v_accvgpr_write_b32 a203, v251\n"
"v_accvgpr_read_b32 v251, a216\n"
"v_mul_f32 v251, v251, v250\n"
"v_accvgpr_write_b32 a216, v251\n"
"v_accvgpr_read_b32 v251, a217\n"
"v_mul_f32 v251, v251, v250\n"
"v_accvgpr_write_b32 a217, v251\n"
"v_accvgpr_read_b32 v251, a218\n"
"v_mul_f32 v251, v251, v250\n"
"v_accvgpr_write_b32 a218, v251\n"
"v_accvgpr_read_b32 v251, a219\n"
"v_mul_f32 v251, v251, v250\n"
"v_accvgpr_write_b32 a219, v251\n"
"v_accvgpr_read_b32 v251, a232\n"
"v_mul_f32 v251, v251, v250\n"
"v_accvgpr_write_b32 a232, v251\n"
"v_accvgpr_read_b32 v251, a233\n"
"v_mul_f32 v251, v251, v250\n"
"v_accvgpr_write_b32 a233, v251\n"
"v_accvgpr_read_b32 v251, a234\n"
"v_mul_f32 v251, v251, v250\n"
"v_accvgpr_write_b32 a234, v251\n"
"v_accvgpr_read_b32 v251, a235\n"
"v_mul_f32 v251, v251, v250\n"
"v_accvgpr_write_b32 a235, v251\n"
"v_accvgpr_read_b32 v251, a248\n"
"v_mul_f32 v251, v251, v250\n"
"v_accvgpr_write_b32 a248, v251\n"
"v_accvgpr_read_b32 v251, a249\n"
"v_mul_f32 v251, v251, v250\n"
"v_accvgpr_write_b32 a249, v251\n"
"v_accvgpr_read_b32 v251, a250\n"
"v_mul_f32 v251, v251, v250\n"
"v_accvgpr_write_b32 a250, v251\n"
"v_accvgpr_read_b32 v251, a251\n"
"v_mul_f32 v251, v251, v250\n"
"v_accvgpr_write_b32 a251, v251\n"
"ds_read_addtid_b32 v250 offset:56064\n"
"v_accvgpr_read_b32 v251, a12\n"
"s_waitcnt lgkmcnt(0)\n"
"v_mul_f32 v251, v251, v250\n"
"v_accvgpr_write_b32 a12, v251\n"
"v_accvgpr_read_b32 v251, a13\n"
"v_mul_f32 v251, v251, v250\n"
"v_accvgpr_write_b32 a13, v251\n"
"v_accvgpr_read_b32 v251, a14\n"
"v_mul_f32 v251, v251, v250\n"
"v_accvgpr_write_b32 a14, v251\n"
"v_accvgpr_read_b32 v251, a15\n"
"v_mul_f32 v251, v251, v250\n"
"v_accvgpr_write_b32 a15, v251\n"
"v_accvgpr_read_b32 v251, a28\n"
"v_mul_f32 v251, v251, v250\n"
"v_accvgpr_write_b32 a28, v251\n"
"v_accvgpr_read_b32 v251, a29\n"
"v_mul_f32 v251, v251, v250\n"
"v_accvgpr_write_b32 a29, v251\n"
"v_accvgpr_read_b32 v251, a30\n"
"v_mul_f32 v251, v251, v250\n"
"v_accvgpr_write_b32 a30, v251\n"
"v_accvgpr_read_b32 v251, a31\n"
"v_mul_f32 v251, v251, v250\n"
"v_accvgpr_write_b32 a31, v251\n"
"v_accvgpr_read_b32 v251, a44\n"
"v_mul_f32 v251, v251, v250\n"
"v_accvgpr_write_b32 a44, v251\n"
"v_accvgpr_read_b32 v251, a45\n"
"v_mul_f32 v251, v251, v250\n"
"v_accvgpr_write_b32 a45, v251\n"
"v_accvgpr_read_b32 v251, a46\n"
"v_mul_f32 v251, v251, v250\n"
"v_accvgpr_write_b32 a46, v251\n"
"v_accvgpr_read_b32 v251, a47\n"
"v_mul_f32 v251, v251, v250\n"
"v_accvgpr_write_b32 a47, v251\n"
"v_accvgpr_read_b32 v251, a60\n"
"v_mul_f32 v251, v251, v250\n"
"v_accvgpr_write_b32 a60, v251\n"
"v_accvgpr_read_b32 v251, a61\n"
"v_mul_f32 v251, v251, v250\n"
"v_accvgpr_write_b32 a61, v251\n"
"v_accvgpr_read_b32 v251, a62\n"
"v_mul_f32 v251, v251, v250\n"
"v_accvgpr_write_b32 a62, v251\n"
"v_accvgpr_read_b32 v251, a63\n"
"v_mul_f32 v251, v251, v250\n"
"v_accvgpr_write_b32 a63, v251\n"
"v_accvgpr_read_b32 v251, a76\n"
"v_mul_f32 v251, v251, v250\n"
"v_accvgpr_write_b32 a76, v251\n"
"v_accvgpr_read_b32 v251, a77\n"
"v_mul_f32 v251, v251, v250\n"
"v_accvgpr_write_b32 a77, v251\n"
"v_accvgpr_read_b32 v251, a78\n"
"v_mul_f32 v251, v251, v250\n"
"v_accvgpr_write_b32 a78, v251\n"
"v_accvgpr_read_b32 v251, a79\n"
"v_mul_f32 v251, v251, v250\n"
"v_accvgpr_write_b32 a79, v251\n"
"v_accvgpr_read_b32 v251, a92\n"
"v_mul_f32 v251, v251, v250\n"
"v_accvgpr_write_b32 a92, v251\n"
"v_accvgpr_read_b32 v251, a93\n"
"v_mul_f32 v251, v251, v250\n"
"v_accvgpr_write_b32 a93, v251\n"
"v_accvgpr_read_b32 v251, a94\n"
"v_mul_f32 v251, v251, v250\n"
"v_accvgpr_write_b32 a94, v251\n"
"v_accvgpr_read_b32 v251, a95\n"
"v_mul_f32 v251, v251, v250\n"
"v_accvgpr_write_b32 a95, v251\n"
"v_accvgpr_read_b32 v251, a108\n"
"v_mul_f32 v251, v251, v250\n"
"v_accvgpr_write_b32 a108, v251\n"
"v_accvgpr_read_b32 v251, a109\n"
"v_mul_f32 v251, v251, v250\n"
"v_accvgpr_write_b32 a109, v251\n"
"v_accvgpr_read_b32 v251, a110\n"
"v_mul_f32 v251, v251, v250\n"
"v_accvgpr_write_b32 a110, v251\n"
"v_accvgpr_read_b32 v251, a111\n"
"v_mul_f32 v251, v251, v250\n"
"v_accvgpr_write_b32 a111, v251\n"
"v_accvgpr_read_b32 v251, a124\n"
"v_mul_f32 v251, v251, v250\n"
"v_accvgpr_write_b32 a124, v251\n"
"v_accvgpr_read_b32 v251, a125\n"
"v_mul_f32 v251, v251, v250\n"
"v_accvgpr_write_b32 a125, v251\n"
"v_accvgpr_read_b32 v251, a126\n"
"v_mul_f32 v251, v251, v250\n"
"v_accvgpr_write_b32 a126, v251\n"
"v_accvgpr_read_b32 v251, a127\n"
"v_mul_f32 v251, v251, v250\n"
"v_accvgpr_write_b32 a127, v251\n"
"v_accvgpr_read_b32 v251, a140\n"
"v_mul_f32 v251, v251, v250\n"
"v_accvgpr_write_b32 a140, v251\n"
"v_accvgpr_read_b32 v251, a141\n"
"v_mul_f32 v251, v251, v250\n"
"v_accvgpr_write_b32 a141, v251\n"
"v_accvgpr_read_b32 v251, a142\n"
"v_mul_f32 v251, v251, v250\n"
"v_accvgpr_write_b32 a142, v251\n"
"v_accvgpr_read_b32 v251, a143\n"
"v_mul_f32 v251, v251, v250\n"
"v_accvgpr_write_b32 a143, v251\n"
"v_accvgpr_read_b32 v251, a156\n"
"v_mul_f32 v251, v251, v250\n"
"v_accvgpr_write_b32 a156, v251\n"
"v_accvgpr_read_b32 v251, a157\n"
"v_mul_f32 v251, v251, v250\n"
"v_accvgpr_write_b32 a157, v251\n"
"v_accvgpr_read_b32 v251, a158\n"
"v_mul_f32 v251, v251, v250\n"
"v_accvgpr_write_b32 a158, v251\n"
"v_accvgpr_read_b32 v251, a159\n"
"v_mul_f32 v251, v251, v250\n"
"v_accvgpr_write_b32 a159, v251\n"
"v_accvgpr_read_b32 v251, a172\n"
"v_mul_f32 v251, v251, v250\n"
"v_accvgpr_write_b32 a172, v251\n"
"v_accvgpr_read_b32 v251, a173\n"
"v_mul_f32 v251, v251, v250\n"
"v_accvgpr_write_b32 a173, v251\n"
"v_accvgpr_read_b32 v251, a174\n"
"v_mul_f32 v251, v251, v250\n"
"v_accvgpr_write_b32 a174, v251\n"
"v_accvgpr_read_b32 v251, a175\n"
"v_mul_f32 v251, v251, v250\n"
"v_accvgpr_write_b32 a175, v251\n"
"v_accvgpr_read_b32 v251, a188\n"
"v_mul_f32 v251, v251, v250\n"
"v_accvgpr_write_b32 a188, v251\n"
"v_accvgpr_read_b32 v251, a189\n"
"v_mul_f32 v251, v251, v250\n"
"v_accvgpr_write_b32 a189, v251\n"
"v_accvgpr_read_b32 v251, a190\n"
"v_mul_f32 v251, v251, v250\n"
"v_accvgpr_write_b32 a190, v251\n"
"v_accvgpr_read_b32 v251, a191\n"
"v_mul_f32 v251, v251, v250\n"
"v_accvgpr_write_b32 a191, v251\n"
"v_accvgpr_read_b32 v251, a204\n"
"v_mul_f32 v251, v251, v250\n"
"v_accvgpr_write_b32 a204, v251\n"
"v_accvgpr_read_b32 v251, a205\n"
"v_mul_f32 v251, v251, v250\n"
"v_accvgpr_write_b32 a205, v251\n"
"v_accvgpr_read_b32 v251, a206\n"
"v_mul_f32 v251, v251, v250\n"
"v_accvgpr_write_b32 a206, v251\n"
"v_accvgpr_read_b32 v251, a207\n"
"v_mul_f32 v251, v251, v250\n"
"v_accvgpr_write_b32 a207, v251\n"
"v_accvgpr_read_b32 v251, a220\n"
"v_mul_f32 v251, v251, v250\n"
"v_accvgpr_write_b32 a220, v251\n"
"v_accvgpr_read_b32 v251, a221\n"
"v_mul_f32 v251, v251, v250\n"
"v_accvgpr_write_b32 a221, v251\n"
"v_accvgpr_read_b32 v251, a222\n"
"v_mul_f32 v251, v251, v250\n"
"v_accvgpr_write_b32 a222, v251\n"
"v_accvgpr_read_b32 v251, a223\n"
"v_mul_f32 v251, v251, v250\n"
"v_accvgpr_write_b32 a223, v251\n"
"v_accvgpr_read_b32 v251, a236\n"
"v_mul_f32 v251, v251, v250\n"
"v_accvgpr_write_b32 a236, v251\n"
"v_accvgpr_read_b32 v251, a237\n"
"v_mul_f32 v251, v251, v250\n"
"v_accvgpr_write_b32 a237, v251\n"
"v_accvgpr_read_b32 v251, a238\n"
"v_mul_f32 v251, v251, v250\n"
"v_accvgpr_write_b32 a238, v251\n"
"v_accvgpr_read_b32 v251, a239\n"
"v_mul_f32 v251, v251, v250\n"
"v_accvgpr_write_b32 a239, v251\n"
"v_accvgpr_read_b32 v251, a252\n"
"v_mul_f32 v251, v251, v250\n"
"v_accvgpr_write_b32 a252, v251\n"
"v_accvgpr_read_b32 v251, a253\n"
"v_mul_f32 v251, v251, v250\n"
"v_accvgpr_write_b32 a253, v251\n"
"v_accvgpr_read_b32 v251, a254\n"
"v_mul_f32 v251, v251, v250\n"
"v_accvgpr_write_b32 a254, v251\n"
"v_accvgpr_read_b32 v251, a255\n"
"v_mul_f32 v251, v251, v250\n"
"v_accvgpr_write_b32 a255, v251\n"
"ds_read_addtid_b32 v250 offset:56320\n"
"s_waitcnt lgkmcnt(0)\n"
"v_mul_f32 v128, v128, v250\n"
"v_mul_f32 v129, v129, v250\n"
"v_mul_f32 v130, v130, v250\n"
"v_mul_f32 v131, v131, v250\n"
"v_mul_f32 v136, v136, v250\n"
"v_mul_f32 v137, v137, v250\n"
"v_mul_f32 v138, v138, v250\n"
"v_mul_f32 v139, v139, v250\n"
"v_mul_f32 v144, v144, v250\n"
"v_mul_f32 v145, v145, v250\n"
"v_mul_f32 v146, v146, v250\n"
"v_mul_f32 v147, v147, v250\n"
"v_mul_f32 v152, v152, v250\n"
"v_mul_f32 v153, v153, v250\n"
"v_mul_f32 v154, v154, v250\n"
"v_mul_f32 v155, v155, v250\n"
"ds_read_addtid_b32 v250 offset:56576\n"
"s_waitcnt lgkmcnt(0)\n"
"v_mul_f32 v132, v132, v250\n"
"v_mul_f32 v133, v133, v250\n"
"v_mul_f32 v134, v134, v250\n"
"v_mul_f32 v135, v135, v250\n"
"v_mul_f32 v140, v140, v250\n"
"v_mul_f32 v141, v141, v250\n"
"v_mul_f32 v142, v142, v250\n"
"v_mul_f32 v143, v143, v250\n"
"v_mul_f32 v148, v148, v250\n"
"v_mul_f32 v149, v149, v250\n"
"v_mul_f32 v150, v150, v250\n"
"v_mul_f32 v151, v151, v250\n"
"v_mul_f32 v156, v156, v250\n"
"v_mul_f32 v157, v157, v250\n"
"v_mul_f32 v158, v158, v250\n"
"v_mul_f32 v159, v159, v250\n"
"ds_read_addtid_b32 v251 offset:56832\n"
"s_waitcnt lgkmcnt(0)\n"
"s_mov_b32 m0, s75\n"
"s_mov_b64 s[76:77], 0\n"
"Lc2_done:\n"
"s_nop 1\n"
"s_setpc_b64 s[80:81]\n"
"Lend:\n"
      :
      : "s"(x), "s"(wq), "s"(k), "s"(v), "s"(wo), "s"(po), "s"(w), "s"(wqrem), "s"(idx >> 3), "s"(idx & 7u), "s"((((br * 8u + b) * 16u + h) >> 1) * (17u * 8192u))
      : "v0", "v1", "v2", "v3", "v4", "v5", "v6", "v7", "v8", "v9", "v10", "v11", "v12", "v13", "v14", "v15", "v16", "v17", "v18", "v19", "v20", "v21", "v22", "v23",
        "v24", "v25", "v26", "v27", "v28", "v29", "v30", "v31", "v32", "v33", "v34", "v35", "v36", "v37", "v38", "v39", "v40", "v41", "v42", "v43", "v44", "v45", "v46", "v47",
        "v48", "v49", "v50", "v51", "v52", "v53", "v54", "v55", "v56", "v57", "v58", "v59", "v60", "v61", "v62", "v63", "v64", "v65", "v66", "v67", "v68", "v69", "v70", "v71",
        "v72", "v73", "v74", "v75", "v76", "v77", "v78", "v79", "v80", "v81", "v82", "v83", "v84", "v85", "v86", "v87", "v88", "v89", "v90", "v91", "v92", "v93", "v94", "v95",
        "v96", "v97", "v98", "v99", "v100", "v101", "v102", "v103", "v104", "v105", "v106", "v107", "v108", "v109", "v110", "v111", "v112", "v113", "v114", "v115", "v116", "v117", "v118", "v119",
        "v120", "v121", "v122", "v123", "v124", "v125", "v126", "v127", "v128", "v129", "v130", "v131", "v132", "v133", "v134", "v135", "v136", "v137", "v138", "v139", "v140", "v141", "v142", "v143",
        "v144", "v145", "v146", "v147", "v148", "v149", "v150", "v151", "v152", "v153", "v154", "v155", "v156", "v157", "v158", "v159", "v160", "v161", "v162", "v163", "v164", "v165", "v166", "v167",
        "v168", "v169", "v170", "v171", "v172", "v173", "v174", "v175", "v176", "v177", "v178", "v179", "v180", "v181", "v182", "v183", "v184", "v185", "v186", "v187", "v188", "v189", "v190", "v191",
        "v192", "v193", "v194", "v195", "v196", "v197", "v198", "v199", "v200", "v201", "v202", "v203", "v204", "v205", "v206", "v207", "v208", "v209", "v210", "v211", "v212", "v213", "v214", "v215",
        "v216", "v217", "v218", "v219", "v220", "v221", "v222", "v223", "v224", "v225", "v226", "v227", "v228", "v229", "v230", "v231", "v232", "v233", "v234", "v235", "v236", "v237", "v238", "v239",
        "v240", "v241", "v242", "v243", "v244", "v245", "v246", "v247", "v248", "v249", "v250", "v251", "v252", "v253", "v254", "v255", "a0", "a1", "a2", "a3", "a4", "a5", "a6", "a7",
        "a8", "a9", "a10", "a11", "a12", "a13", "a14", "a15", "a16", "a17", "a18", "a19", "a20", "a21", "a22", "a23", "a24", "a25", "a26", "a27", "a28", "a29", "a30", "a31",
        "a32", "a33", "a34", "a35", "a36", "a37", "a38", "a39", "a40", "a41", "a42", "a43", "a44", "a45", "a46", "a47", "a48", "a49", "a50", "a51", "a52", "a53", "a54", "a55",
        "a56", "a57", "a58", "a59", "a60", "a61", "a62", "a63", "a64", "a65", "a66", "a67", "a68", "a69", "a70", "a71", "a72", "a73", "a74", "a75", "a76", "a77", "a78", "a79",
        "a80", "a81", "a82", "a83", "a84", "a85", "a86", "a87", "a88", "a89", "a90", "a91", "a92", "a93", "a94", "a95", "a96", "a97", "a98", "a99", "a100", "a101", "a102", "a103",
        "a104", "a105", "a106", "a107", "a108", "a109", "a110", "a111", "a112", "a113", "a114", "a115", "a116", "a117", "a118", "a119", "a120", "a121", "a122", "a123", "a124", "a125", "a126", "a127",
        "a128", "a129", "a130", "a131", "a132", "a133", "a134", "a135", "a136", "a137", "a138", "a139", "a140", "a141", "a142", "a143", "a144", "a145", "a146", "a147", "a148", "a149", "a150", "a151",
        "a152", "a153", "a154", "a155", "a156", "a157", "a158", "a159", "a160", "a161", "a162", "a163", "a164", "a165", "a166", "a167", "a168", "a169", "a170", "a171", "a172", "a173", "a174", "a175",
        "a176", "a177", "a178", "a179", "a180", "a181", "a182", "a183", "a184", "a185", "a186", "a187", "a188", "a189", "a190", "a191", "a192", "a193", "a194", "a195", "a196", "a197", "a198", "a199",
        "a200", "a201", "a202", "a203", "a204", "a205", "a206", "a207", "a208", "a209", "a210", "a211", "a212", "a213", "a214", "a215", "a216", "a217", "a218", "a219", "a220", "a221", "a222", "a223",
        "a224", "a225", "a226", "a227", "a228", "a229", "a230", "a231", "a232", "a233", "a234", "a235", "a236", "a237", "a238", "a239", "a240", "a241", "a242", "a243", "a244", "a245", "a246", "a247",
        "a248", "a249", "a250", "a251", "a252", "a253", "a254", "a255", "s24", "s25", "s26", "s27", "s28", "s29", "s30", "s31", "s33", "s34", "s35", "s36", "s37", "s38", "s39", "s40",
        "s41", "s42", "s43", "s44", "s45", "s46", "s47", "s48", "s49", "s50", "s51", "s52", "s53", "s54", "s55", "s56", "s57", "s58", "s59", "s60", "s61", "s62", "s63", "s64",
        "s65", "s66", "s67", "s68", "s69", "s70", "s71", "s72", "s73", "s74", "s75", "s76", "s77", "s78", "s79", "s80", "s81", "s82", "s83", "s84", "s85", "s86", "s87", "s88",
        "s89", "s90", "s91", "s92", "s93", "s94", "s95", "s96", "s97", "vcc", "scc", "memory");
}

__device__ __forceinline__ float bflo(unsigned u) { return __uint_as_float(u << 16); }
__device__ __forceinline__ float bfhi(unsigned u) { return __uint_as_float(u & 0xffff0000u); }
__device__ __forceinline__ unsigned pk(float lo, float hi) {
  typedef float f2 __attribute__((ext_vector_type(2)));
  typedef __bf16 b2 __attribute__((ext_vector_type(2)));
  f2 v = {lo, hi};
  b2 r = __builtin_convertvector(v, b2);
  return __builtin_bit_cast(unsigned, r);
}

__global__ __launch_bounds__(256) void head_reduce(const char* ws, u16* out) {
  const unsigned f = blockIdx.x >> 5, grp = (blockIdx.x >> 1) & 15u, r = blockIdx.x & 1u;
  const __amdgpu_buffer_rsrc_t rs = __builtin_amdgcn_make_buffer_rsrc(
      (void*)(ws + ((size_t)f << 20) + (size_t)grp * 65536u + r * 4096u), (short)0, 65536, 0x20000);
  u32x4 v[8];
#pragma unroll
  for (int h = 0; h < 8; ++h) v[h] = __builtin_amdgcn_raw_buffer_load_b128(rs, threadIdx.x * 16u, h * 8192, 0);
  float s[8];
#pragma unroll
  for (int i = 0; i < 8; ++i) s[i] = 0.f;
#pragma unroll
  for (int h = 0; h < 8; ++h) {
#pragma unroll
    for (int i = 0; i < 4; ++i) { s[2 * i] += bflo(v[h][i]); s[2 * i + 1] += bfhi(v[h][i]); }
  }
  const unsigned w = threadIdx.x >> 6, lane = threadIdx.x & 63u, q = lane & 15u, g = lane >> 4;
  const unsigned br = grp >> 3, b = grp & 7u;
  unsigned row, n0, n1;
  if (f < 28u) {
    const unsigned G = f >> 2, pp = ((f >> 1) & 1u) ^ (w >> 1), qt = f & 1u, sw = w & 1u;
    row = w * 64u + r * 32u + qt * 16u + q;
    n0 = G * 64u + pp * 32u + sw * 16u + g * 4u;
    n1 = G * 64u + pp * 32u + (sw ^ 1u) * 16u + g * 4u;
  } else {
    const unsigned G = (f - 28u) * 2u;
    row = 256u + r * 16u + q;
    n0 = G * 64u + w * 16u + g * 4u;
    n1 = G < 6u ? n0 + 64u : n0;
  }
  u16* o = out + ((size_t)b * 288u + row) * 896u + br * 448u;
  u32x2 a = {pk(s[0], s[1]), pk(s[2], s[3])}, d = {pk(s[4], s[5]), pk(s[6], s[7])};
  *(u32x2*)(o + n0) = a;
  *(u32x2*)(o + n1) = d;
}

extern "C" void kernel_launch(void* const* d_in, const int* in_sizes, int n_in, void* d_out, int out_size, void* d_ws,
                              size_t ws_size, hipStream_t stream) {
  (void)in_sizes; (void)n_in; (void)out_size; (void)ws_size;
  (void)hipFuncSetAttribute((const void*)mha_fused, hipFuncAttributeMaxDynamicSharedMemorySize, 163840);
  hipLaunchKernelGGL(mha_fused, dim3(256), dim3(256), 163840, stream,
                     (const char*)d_in[0], (const char*)d_in[1], (const char*)d_in[2], (const char*)d_in[3], (const char*)d_in[4],
                     (const char*)d_in[5], (const char*)d_in[6], (const char*)d_in[7], (const char*)d_in[8], (const char*)d_in[9],
                     (char*)d_ws);
  hipLaunchKernelGGL(head_reduce, dim3(1024), dim3(256), 0, stream, (const char*)d_ws, (u16*)d_out);
}
```
